```python
import torch

def _softmax(x, axis=-1):
    return torch.softmax(x, dim=axis)
def _gelu(x):
    return torch.nn.functional.gelu(x, approximate="tanh")
def _silu(x):
    return torch.nn.functional.silu(x)
def _sigmoid(x):
    return torch.sigmoid(x)
def _layernorm(x, axis=-1, eps=1e-5):
    m = x.mean(dim=axis, keepdim=True)
    v = x.var(dim=axis, keepdim=True, unbiased=False)
    return (x - m) / torch.sqrt(v + eps)
def _scatter_add(tgt, idx, src, axis=0):
    return tgt.clone().index_add_(axis, idx, src)
def _segment_sum(src, seg, n):
    out = torch.zeros((n,) + src.shape[1:], dtype=src.dtype)
    return out.index_add_(0, seg, src)
def _pad(x, pw):
    flat = tuple(v for pair in reversed(pw) for v in pair)
    return torch.nn.functional.pad(x, flat)

kernel_name = 'standalone__kv_cache_quant_int4_int2_kv_with_per_channel_or_per_token_scales__prompt'

def setup_inputs():

    _w = (lambda a: (lambda t: t.to(torch.bfloat16) if t.is_floating_point() else t)(torch.as_tensor(a)))
    B = 1
    Sq = 2048
    Skv = 2048
    Hq = 32
    Hkv = 8
    Dh = 128
    bits = 8
    G = 64
    if G <= 0 or G >= Skv:
        G = max(1, Skv // 2)
    Skv = (Skv // G) * G
    ngk = Skv // G
    qmax = (1 << bits) - 1
    return {
        "q": _w((lambda *s: torch.randn(*s, dtype=torch.bfloat16) * 0.2)(B, Hq, Sq, Dh)),
        "qk": _w(torch.randint(0, qmax + 1, (B, Hkv, Skv, Dh)).to(torch.int32)),
        "qv": _w(torch.randint(0, qmax + 1, (B, Hkv, Skv, Dh)).to(torch.int32)),
        "k_scales": _w(torch.abs((lambda *s: torch.randn(*s, dtype=torch.bfloat16) * 0.2)(B, Hkv, ngk, Dh)) * 0.05 + 0.02),
        "k_zeros": _w((lambda *s: torch.randn(*s, dtype=torch.bfloat16) * 0.2)(B, Hkv, ngk, Dh) + float(qmax) / 2.0),
        "v_scales": _w(torch.abs((lambda *s: torch.randn(*s, dtype=torch.bfloat16) * 0.05)(B, Hkv, Skv, 1)) * 0.05 + 0.02),
        "v_zeros": _w((lambda *s: torch.randn(*s, dtype=torch.bfloat16) * 0.05)(B, Hkv, Skv, 1) + float(qmax) / 2.0),
    }

def reference(q, qk, qv, k_scales, k_zeros, v_scales, v_zeros):
    B, Hq, Sq, Dh = q.shape
    Hkv, Skv = qk.shape[1], qk.shape[2]
    ngk = k_scales.shape[2]
    G = Skv // ngk
    ks = (lambda x, n, axis: torch.repeat_interleave(x, n, dim=axis))(k_scales, G, axis=2)
    kz = (lambda x, n, axis: torch.repeat_interleave(x, n, dim=axis))(k_zeros, G, axis=2)
    k = ((lambda x, dt: x.to(dt))(qk, q.dtype) - kz) * ks
    v = ((lambda x, dt: x.to(dt))(qv, q.dtype) - v_zeros) * v_scales
    rep = Hq // Hkv
    if rep > 1:
        k = (lambda x, n, axis: torch.repeat_interleave(x, n, dim=axis))(k, rep, axis=1)
        v = (lambda x, n, axis: torch.repeat_interleave(x, n, dim=axis))(v, rep, axis=1)
    scale = float(Dh) ** -0.5
    logits = torch.einsum("bhqd,bhkd->bhqk", q, k) * scale
    p = _softmax(logits, axis=-1)
    o = torch.einsum("bhqk,bhkd->bhqd", p, v)
    return o
```

```cpp
#include <hip/hip_runtime.h>
#include <hip/hip_bf16.h>
#include <cstdio>

using bf16 = __hip_bfloat16;
constexpr int   D = 128, NW = 8, QBLK = 32, KVBLK = 64;
constexpr float SCALE = 0.088388347648318440f;
#define ATTN_THR 8.f
constexpr float THR = ATTN_THR;
#define ATTN_LAYOUT 0
constexpr int LAYOUT = ATTN_LAYOUT;
#define ATTN_SDEPTH 2
constexpr int SDEPTH = ATTN_SDEPTH;
static_assert(SDEPTH == 1 || SDEPTH == 2, "SDEPTH is 1 or 2");
#define ATTN_B 1
#define ATTN_HQ 32
#define ATTN_HKV 8
#define ATTN_S 2048
#define ATTN_SKV ATTN_S
constexpr int B = ATTN_B, HQ = ATTN_HQ, HKV = ATTN_HKV, S = ATTN_S, SKV = ATTN_SKV;
constexpr int LDQ = LAYOUT ? HQ * D : D, LDK = LAYOUT ? HKV * D : D, LDO = LDQ;
static_assert(S % (QBLK * NW) == 0 && SKV % (2 * KVBLK) == 0 && HQ % HKV == 0, "S % 256 == 0, SKV % 128 == 0 (even tile count), HQ % HKV == 0");
constexpr size_t SHM_V = KVBLK * D * 2, SHM_K = KVBLK * D * 2, SHM_ATTN = 2 * SHM_V + 2 * SHM_K + NW * 64 * 4;

using bf16x8 = __attribute__((ext_vector_type(8))) short;
using s16x4  = __attribute__((ext_vector_type(4))) short;
using f32x16 = __attribute__((ext_vector_type(16))) float;
using f32x8  = __attribute__((ext_vector_type(8))) float;
using u32x4  = __attribute__((ext_vector_type(4))) unsigned;
#define KSWZ(row, colB) ((row) * 256 + ((colB) ^ (((row) & 7) << 4)))
#define SBAR() __builtin_amdgcn_sched_barrier(0)
__device__ __forceinline__ int crow(int r, int hi) { return (r & 3) + 8 * (r >> 2) + 4 * hi; }
__device__ __forceinline__ unsigned cvtpk(float lo, float hi) {
  unsigned r; asm volatile("v_cvt_pk_bf16_f32 %0, %1, %2" : "=v"(r) : "v"(lo), "v"(hi)); return r;
}
template <typename TIn> struct Stage;
template <> struct Stage<bf16>  { using T = bf16x8;
  __device__ static __forceinline__ T ld8(const bf16* p) { return *reinterpret_cast<const bf16x8*>(p); }
  __device__ static __forceinline__ bf16x8 tobf(T x) { return x; } };
template <> struct Stage<float> { using T = f32x8;
  __device__ static __forceinline__ T ld8(const float* p) { return *reinterpret_cast<const f32x8*>(p); }
  __device__ static __forceinline__ bf16x8 tobf(T x) {
    u32x4 w = {cvtpk(x[0], x[1]), cvtpk(x[2], x[3]), cvtpk(x[4], x[5]), cvtpk(x[6], x[7])}; return *reinterpret_cast<bf16x8*>(&w); } };

__device__ __forceinline__ void partialSM(f32x16& p0, f32x16& p1, float& m_reg, float& mn, float& alpha) {
  constexpr float C = SCALE * 1.4426950408889634f;
  float pmax = p0[0]; for (int r = 1; r < 16; ++r) pmax = fmaxf(pmax, p0[r]); for (int r = 0; r < 16; ++r) pmax = fmaxf(pmax, p1[r]);
  { auto rr = __builtin_amdgcn_permlane32_swap(__float_as_uint(pmax), __float_as_uint(pmax), false, false);
    pmax = fmaxf(__uint_as_float(rr[0]), __uint_as_float(rr[1])); }
  if (__builtin_expect(__all(pmax - m_reg <= THR / SCALE), 1)) { mn = m_reg; alpha = 1.f; }
  else { mn = fmaxf(m_reg, pmax); alpha = __builtin_amdgcn_exp2f((m_reg - mn) * C); m_reg = mn; }
  float mnC = -mn * C;
  for (int r = 0; r < 16; ++r) p0[r] = fmaf(p0[r], C, mnC); for (int r = 0; r < 16; ++r) p1[r] = fmaf(p1[r], C, mnC);
  for (int r = 0; r < 16; ++r) p0[r] = __builtin_amdgcn_exp2f(p0[r]);
}
__device__ __forceinline__ void finishSM(f32x16& p0, f32x16& p1, float alpha, float& l_reg, bf16x8& pa0, bf16x8& pa1, bf16x8& pa2, bf16x8& pa3) {
  for (int r = 0; r < 16; ++r) p1[r] = __builtin_amdgcn_exp2f(p1[r]);
  float ps = 0; for (int r = 0; r < 16; ++r) ps += p0[r]; for (int r = 0; r < 16; ++r) ps += p1[r];
  { auto rr = __builtin_amdgcn_permlane32_swap(__float_as_uint(ps), __float_as_uint(ps), false, false);
    ps = __uint_as_float(rr[0]) + __uint_as_float(rr[1]); }
  l_reg = l_reg * alpha + ps;
#define PK4(P, BASE, OUT) do { unsigned a0 = cvtpk(P[BASE + 0], P[BASE + 1]), a1 = cvtpk(P[BASE + 2], P[BASE + 3]);   \
    unsigned b0 = cvtpk(P[BASE + 4], P[BASE + 5]), b1 = cvtpk(P[BASE + 6], P[BASE + 7]);                              \
    auto r0 = __builtin_amdgcn_permlane32_swap(a0, b0, false, false); auto r1 = __builtin_amdgcn_permlane32_swap(a1, b1, false, false); \
    u32x4 w = {r0[0], r1[0], r0[1], r1[1]}; OUT = *reinterpret_cast<bf16x8*>(&w); } while (0)
  PK4(p0, 0, pa0); PK4(p0, 8, pa1); PK4(p1, 0, pa2); PK4(p1, 8, pa3);
#undef PK4
}
__device__ __forceinline__ void qkt(f32x16& p0, f32x16& p1, const bf16* Ks, const bf16x8* qr, int r32, int hi) {
  p0 = f32x16{}; p1 = f32x16{};
  for (int d0 = 0; d0 < 8; ++d0) { int cb = (d0 * 16 + hi * 8) * 2;
    bf16x8 b0 = *reinterpret_cast<const bf16x8*>((const char*)Ks + KSWZ(r32, cb));
    bf16x8 b1 = *reinterpret_cast<const bf16x8*>((const char*)Ks + KSWZ(32 + r32, cb));
    p0 = __builtin_amdgcn_mfma_f32_32x32x16_bf16(b0, qr[d0], p0, 0, 0, 0);
    p1 = __builtin_amdgcn_mfma_f32_32x32x16_bf16(b1, qr[d0], p1, 0, 0, 0); }
}
__device__ __forceinline__ int v_st(int k, int c) { const int kk = (k & ~0xC) | ((k & 4) << 1) | ((k & 8) >> 1); return ((kk >> 3) * 4 + (c >> 5)) * 512 + ((kk & 7) * 32 + (c & 31)) * 2; }
__device__ __forceinline__ int v_rd_base(int lane) { return ((lane & 3) << 3) | (((lane >> 2) & 3) << 6) | (((lane >> 4) & 1) << 5) | (((lane >> 5) & 1) << 8); }
constexpr int v_rd_off(int d0, int ks, int half) { return d0 * 512 + ks * 4096 + half * 2048; }
template <int OFF> __device__ __forceinline__ s16x4 tr_read(int vb) {
  s16x4 r; asm volatile("ds_read_b64_tr_b16 %0, %1 offset:%2" : "=&v"(r) : "v"(vb), "i"(OFF) : "memory"); return r;
}
template <int D0> __device__ __forceinline__ void pv_one(f32x16& od, int vb, bf16x8 pa0, bf16x8 pa1, bf16x8 pa2, bf16x8 pa3) {
  const s16x4 l0 = tr_read<v_rd_off(D0, 0, 0)>(vb), h0 = tr_read<v_rd_off(D0, 0, 1)>(vb), l1 = tr_read<v_rd_off(D0, 1, 0)>(vb), h1 = tr_read<v_rd_off(D0, 1, 1)>(vb);
  const s16x4 l2 = tr_read<v_rd_off(D0, 2, 0)>(vb), h2 = tr_read<v_rd_off(D0, 2, 1)>(vb), l3 = tr_read<v_rd_off(D0, 3, 0)>(vb), h3 = tr_read<v_rd_off(D0, 3, 1)>(vb);
  asm volatile("s_waitcnt lgkmcnt(0)" ::: "memory"); SBAR();
#define PK(L, H) (bf16x8){L[0], L[1], L[2], L[3], H[0], H[1], H[2], H[3]}
  od = __builtin_amdgcn_mfma_f32_32x32x16_bf16(pa0, PK(l0, h0), od, 0, 0, 0);
  od = __builtin_amdgcn_mfma_f32_32x32x16_bf16(pa1, PK(l1, h1), od, 0, 0, 0);
  od = __builtin_amdgcn_mfma_f32_32x32x16_bf16(pa2, PK(l2, h2), od, 0, 0, 0);
  od = __builtin_amdgcn_mfma_f32_32x32x16_bf16(pa3, PK(l3, h3), od, 0, 0, 0);
#undef PK
}
__device__ __forceinline__ void pv_d0(f32x16* o, int vb, bf16x8 pa0, bf16x8 pa1, bf16x8 pa2, bf16x8 pa3) {
  pv_one<0>(o[0], vb, pa0, pa1, pa2, pa3); pv_one<1>(o[1], vb, pa0, pa1, pa2, pa3); pv_one<2>(o[2], vb, pa0, pa1, pa2, pa3); pv_one<3>(o[3], vb, pa0, pa1, pa2, pa3);
}

template <typename TQ>
__device__ __forceinline__ void attn_dense_body(const TQ* __restrict__ Qb, const bf16* __restrict__ Kh, const bf16* __restrict__ Vh,
                                                bf16* __restrict__ Ob, int seq, char* lds) {
  using St = Stage<bf16>; using SQ = Stage<TQ>;
  const int tid = threadIdx.x, wid = tid >> 6, lane = tid & 63, r32 = lane & 31, hi = lane >> 5;
  bf16* V_lds = (bf16*)lds; bf16* K_lds = (bf16*)(lds + 2 * SHM_V);
  float* ws = (float*)(lds + 2 * SHM_V + 2 * SHM_K) + wid * 64; float* li_l = ws; float* al_l = ws + 32;
  float m_reg = -1e30f, l_reg = 0; f32x16 o[4] = {}; bf16x8 qr[8];
  const TQ* Qw = Qb + (long)(wid * QBLK + r32) * LDQ + hi * 8;
#pragma unroll
  for (int d0 = 0; d0 < 8; ++d0) qr[d0] = SQ::tobf(SQ::ld8(Qw + d0 * 16));
  const int sr = tid >> 4, sc = (tid & 15) * 8, vst0 = v_st(sr, sc), vst1 = v_st(32 + sr, sc);
  const int vb0 = (int)(uintptr_t)V_lds + v_rd_base(lane);
  struct { typename St::T vs0, vs1, ks0, ks1; } sr_[SDEPTH];
#define SLOAD(i, k0) do { sr_[i].vs0 = St::ld8(&Vh[(long)((k0) + sr) * LDK + sc]); sr_[i].vs1 = St::ld8(&Vh[(long)((k0) + 32 + sr) * LDK + sc]); \
    sr_[i].ks0 = St::ld8(&Kh[(long)((k0) + sr) * LDK + sc]); sr_[i].ks1 = St::ld8(&Kh[(long)((k0) + 32 + sr) * LDK + sc]); } while (0)
#define SWRITE(b, i) do { *(bf16x8*)((char*)V_lds + (b) * SHM_V + vst0) = St::tobf(sr_[i].vs0);          \
    *(bf16x8*)((char*)V_lds + (b) * SHM_V + vst1) = St::tobf(sr_[i].vs1); int kc = sc * 2;               \
    *(bf16x8*)((char*)K_lds + (b) * SHM_K + KSWZ(sr, kc)) = St::tobf(sr_[i].ks0);                       \
    *(bf16x8*)((char*)K_lds + (b) * SHM_K + KSWZ(32 + sr, kc)) = St::tobf(sr_[i].ks1); } while (0)
#define SWAIT() do { if constexpr (SDEPTH == 2) asm volatile("s_waitcnt vmcnt(4)" ::: "memory"); else asm volatile("s_waitcnt vmcnt(0)" ::: "memory"); } while (0)
#define RESC(a) do { if (__any((a) < 1.f)) { if (hi == 0) al_l[r32] = (a); asm volatile("s_waitcnt lgkmcnt(0)" ::: "memory"); \
    for (int d = 0; d < 4; ++d) for (int r = 0; r < 16; ++r) o[d][r] *= al_l[crow(r, hi)]; } } while (0)
  f32x16 pA0, pA1, pB0, pB1; float mnA, mnB, alA, alB; bf16x8 pa0, pa1, pa2, pa3; const int NT = seq / KVBLK;
  constexpr int SE = 0, SO = SDEPTH - 1;
  SLOAD(SE, 0); asm volatile("s_waitcnt vmcnt(0)" ::: "memory"); SWRITE(0, SE); __syncthreads();
  qkt(pA0, pA1, K_lds, qr, r32, hi); partialSM(pA0, pA1, m_reg, mnA, alA);
  SLOAD(SO, KVBLK); if constexpr (SDEPTH == 2) { if (2 < NT) SLOAD(SE, 2 * KVBLK); }
  SWAIT(); SWRITE(1, SO); __syncthreads();
  for (int j = 1; j + 1 < NT; j += 2) {
    SBAR(); qkt(pB0, pB1, (bf16*)((char*)K_lds + SHM_K), qr, r32, hi);
    finishSM(pA0, pA1, alA, l_reg, pa0, pa1, pa2, pa3); SBAR();
    SLOAD(SO, (j + SDEPTH) * KVBLK); SBAR();
    pv_d0(o, vb0, pa0, pa1, pa2, pa3); partialSM(pB0, pB1, m_reg, mnB, alB);
    __syncthreads(); SWAIT(); SWRITE(0, SE);
    RESC(alB); __syncthreads();
    SBAR(); qkt(pA0, pA1, K_lds, qr, r32, hi);
    finishSM(pB0, pB1, alB, l_reg, pa0, pa1, pa2, pa3); SBAR();
    if (SDEPTH == 1 || j + 3 < NT) SLOAD(SE, (j + 1 + SDEPTH) * KVBLK); SBAR();
    pv_d0(o, vb0 + (int)SHM_V, pa0, pa1, pa2, pa3); partialSM(pA0, pA1, m_reg, mnA, alA);
    __syncthreads(); SWAIT(); SWRITE(1, SO);
    RESC(alA); __syncthreads();
  }
  SBAR(); qkt(pB0, pB1, (bf16*)((char*)K_lds + SHM_K), qr, r32, hi);
  finishSM(pA0, pA1, alA, l_reg, pa0, pa1, pa2, pa3); SBAR();
  pv_d0(o, vb0, pa0, pa1, pa2, pa3); partialSM(pB0, pB1, m_reg, mnB, alB);
  __syncthreads(); RESC(alB);
  finishSM(pB0, pB1, alB, l_reg, pa0, pa1, pa2, pa3); SBAR();
  pv_d0(o, vb0 + (int)SHM_V, pa0, pa1, pa2, pa3);
  if (hi == 0) li_l[r32] = l_reg; asm volatile("s_waitcnt lgkmcnt(0)" ::: "memory");
  float rli[16];
#pragma unroll
  for (int r = 0; r < 16; ++r) rli[r] = __builtin_amdgcn_rcpf(li_l[crow(r, hi)]);
  bf16* Ow = Ob + (long)(wid * QBLK) * LDO;
#pragma unroll
  for (int r = 0; r < 16; ++r) { int orow = crow(r, hi);
    for (int d0 = 0; d0 < 4; ++d0) Ow[(long)orow * LDO + d0 * 32 + r32] = __float2bfloat16(o[d0][r] * rli[r]); }
#undef SLOAD
#undef SWRITE
#undef SWAIT
#undef RESC
}

__global__ __launch_bounds__(64 * NW, 1)
void attn_fwd_dense(const bf16* __restrict__ Q, const bf16* __restrict__ K, const bf16* __restrict__ V, bf16* __restrict__ O) {
  extern __shared__ __attribute__((aligned(16))) char shm[];
  const int bid = blockIdx.x; const int kvh = bid & 7, rest = bid >> 3; const int h = kvh * 4 + (rest & 3), qb = rest >> 2;
  const long q0 = ((long)h * S + (long)qb * 256) * D, k0 = (long)kvh * SKV * D;
  attn_dense_body<bf16>(Q + q0, K + k0, V + k0, O + q0, SKV, shm);
}
__device__ __forceinline__ float bf2f(unsigned short x) { return __uint_as_float(((unsigned)x) << 16); }
__device__ __forceinline__ float rbf(float x) { return __bfloat162float(__float2bfloat16(x)); }
__global__ __launch_bounds__(256) void dequant(const int* __restrict__ qk, const int* __restrict__ qv, const unsigned short* __restrict__ ks, const unsigned short* __restrict__ kz,
                                               const unsigned short* __restrict__ vs, const unsigned short* __restrict__ vz, bf16* __restrict__ kb, bf16* __restrict__ vb) {
  const long N8 = (long)HKV * SKV * D / 8;
  long i = blockIdx.x * 256L + threadIdx.x;
  if (i >= 2 * N8) return;
  const bool isK = i < N8; const long j = isK ? i : i - N8;
  const long row = j >> 4; const int c = (int)(j & 15) * 8;
  const int* src = (isK ? qk : qv) + j * 8;
  int4 a = *(const int4*)src, b = *(const int4*)(src + 4);
  int x[8] = {a.x, a.y, a.z, a.w, b.x, b.y, b.z, b.w};
  bf16 o[8];
  if (isK) {
    const long g = row >> 6;
#pragma unroll
    for (int e = 0; e < 8; ++e) { float z = bf2f(kz[g * D + c + e]), s = bf2f(ks[g * D + c + e]); o[e] = __float2bfloat16(rbf(rbf((float)x[e]) - z) * s); }
  } else {
    float z = bf2f(vz[row]), s = bf2f(vs[row]);
#pragma unroll
    for (int e = 0; e < 8; ++e) o[e] = __float2bfloat16(rbf(rbf((float)x[e]) - z) * s);
  }
  bf16* dst = (isK ? kb : vb) + j * 8;
  *(uint4*)dst = *(const uint4*)o;
}
constexpr long   NK = (long)B * HKV * SKV * D;
extern "C" void kernel_launch(void* const* d_in, const int* in_sizes, int n_in, void* d_out, int out_size, void* d_ws, size_t ws_size, hipStream_t stream) {
  hipFuncSetAttribute((const void*)attn_fwd_dense, hipFuncAttributeMaxDynamicSharedMemorySize, (int)SHM_ATTN);
  bf16* kb = (bf16*)d_ws; bf16* vb = kb + NK;
  hipLaunchKernelGGL(dequant, dim3((unsigned)(2 * NK / 8 / 256)), dim3(256), 0, stream, (const int*)d_in[1], (const int*)d_in[2], (const unsigned short*)d_in[3], (const unsigned short*)d_in[4],
                     (const unsigned short*)d_in[5], (const unsigned short*)d_in[6], kb, vb);
  hipLaunchKernelGGL(attn_fwd_dense, dim3(256), dim3(64 * NW), SHM_ATTN, stream, (const bf16*)d_in[0], (const bf16*)kb, (const bf16*)vb, (bf16*)d_out);
}
```

```cpp
#include <hip/hip_runtime.h>
#include <stdint.h>

#define KV_STAMP 0
namespace {
constexpr unsigned SQ = 2048, SKV = 2048;
constexpr uint64_t HEADB = 16384 + 17ull * 65536;
}

#define LO(x) "s"((unsigned)(x))
#define HI(x) "s"((unsigned)((x) >> 32))
extern "C" __global__ __launch_bounds__(256, 1) void attn_fused(const char* __restrict__ q, const char* __restrict__ qk, const char* __restrict__ qv,
    const char* __restrict__ ksc, const char* __restrict__ kzr, const char* __restrict__ vsc, const char* __restrict__ vzr,
    char* __restrict__ ws, char* __restrict__ out) {
  __shared__ __attribute__((aligned(1024))) char lds[131072];
  const unsigned bid = blockIdx.x;
  const unsigned w = __builtin_amdgcn_readfirstlane(threadIdx.x >> 6);
  const unsigned g = bid & 7u, rest = bid >> 3, h = g * 4u + (rest & 3u), qb = rest >> 2;
  const unsigned o = rest, kk = w;
  const uint64_t qoff = ((uint64_t)h * SQ + qb * 256u + w * 64u) * 256u;
  const uint64_t qp = (uint64_t)q + qoff, op = (uint64_t)out + qoff, ip = (uint64_t)ws + (uint64_t)g * HEADB;
  const uint64_t kp = (uint64_t)qk + (uint64_t)g * (SKV * 512u), vp = (uint64_t)qv + (uint64_t)g * (SKV * 512u);
  const uint64_t ksp = (uint64_t)ksc + g * 8192u, kzp = (uint64_t)kzr + g * 8192u, vsp = (uint64_t)vsc + g * 4096u, vzp = (uint64_t)vzr + g * 4096u;
  const unsigned T1 = o >> 4;
  const unsigned ksrc0 = o * 4096u + kk * 128u, ksc0 = (o >> 3) * 256u + kk * 64u;
  const unsigned kdst0 = T1 * 65536u + ((o >> 1) & 7u) * 4096u + kk * 1024u + (o & 1u) * 128u;
  const unsigned vkey0 = T1 * 128u + ((o >> 2) & 3u) * 32u + (o & 3u) * 4u;
  const unsigned vsrc0 = vkey0 * 512u + kk * 128u, vsc0 = vkey0 * 2u;
  const unsigned vdst0 = (T1 + 1u) * 65536u + 32768u + kk * 8192u + ((o >> 2) & 3u) * 1024u + (o & 3u) * 256u;
  asm volatile(
    "s_mov_b32 s64, %4\n"
    "s_and_b32 s65, %5, 0xffff\n"
    "s_mov_b32 s66, 0x4000\n"
    "s_mov_b32 s67, 0x20000\n"
    "s_mov_b32 s97, 0x2000\n"
    "buffer_load_dwordx2 v[78:79], off, s[64:67], s97 offset:0 sc1\n"
    "v_mbcnt_lo_u32_b32 v0, -1, 0\n"
    "v_mbcnt_hi_u32_b32 v0, -1, v0\n"
    "s_mov_b32 s68, %8\n"
    "s_and_b32 s69, %9, 0xffff\n"
    "s_mov_b32 s70, 0x100000\n"
    "s_mov_b32 s71, 0x20000\n"
    "s_mov_b32 s76, %12\n"
    "s_and_b32 s77, %13, 0xffff\n"
    "s_mov_b32 s78, 0x2000\n"
    "s_mov_b32 s79, 0x20000\n"
    "s_mov_b32 s80, %14\n"
    "s_and_b32 s81, %15, 0xffff\n"
    "s_mov_b32 s82, 0x2000\n"
    "s_mov_b32 s83, 0x20000\n"
    "v_lshrrev_b32 v8, 3, v0\n"
    "v_and_b32 v9, 7, v0\n"
    "v_lshlrev_b32 v10, 9, v8\n"
    "v_lshlrev_b32 v11, 4, v9\n"
    "v_add3_u32 v88, v10, v11, %21\n"
    "v_lshl_add_u32 v89, v9, 3, %22\n"
    "s_mov_b32 s56, 0x20000\n"
    "s_mov_b32 s57, 1024\n"
    "s_mov_b32 s58, 512\n"
    "buffer_load_dwordx4 v[128:131], v88, s[68:71], 0 offen offset:0\n"
    "buffer_load_dwordx2 v[132:133], v89, s[76:79], 0 offen offset:0\n"
    "buffer_load_dwordx2 v[134:135], v89, s[80:83], 0 offen offset:0\n"
    "s_lshl_b32 s59, %6, 14\n"
    "s_lshr_b32 s60, %6, 1\n"
    "s_lshl_b32 s60, s60, 8\n"
    "v_add3_u32 v124, v10, v11, s59\n"
    "v_lshl_add_u32 v125, v9, 3, s60\n"
    "s_mov_b32 s72, %10\n"
    "s_and_b32 s73, %11, 0xffff\n"
    "s_mov_b32 s74, 0x100000\n"
    "s_mov_b32 s75, 0x20000\n"
    "s_mov_b32 s84, %16\n"
    "s_and_b32 s85, %17, 0xffff\n"
    "s_mov_b32 s86, 0x1000\n"
    "s_mov_b32 s87, 0x20000\n"
    "s_mov_b32 s88, %18\n"
    "s_and_b32 s89, %19, 0xffff\n"
    "s_mov_b32 s90, 0x1000\n"
    "s_mov_b32 s91, 0x20000\n"
    "v_and_b32 v12, 31, v0\n"
    "v_lshrrev_b32 v13, 5, v0\n"
    "v_lshlrev_b32 v10, 13, v13\n"
    "v_lshlrev_b32 v11, 2, v12\n"
    "v_add3_u32 v91, v10, v11, %24\n"
    "v_lshl_add_u32 v92, v13, 5, %25\n"
    "buffer_load_dword v144, v91, s[72:75], 0 offen offset:0\n"
    "buffer_load_dword v145, v91, s[72:75], 0 offen offset:512\n"
    "buffer_load_dword v146, v91, s[72:75], 0 offen offset:1024\n"
    "buffer_load_dword v147, v91, s[72:75], 0 offen offset:1536\n"
    "buffer_load_dwordx2 v[148:149], v92, s[84:87], 0 offen offset:0\n"
    "buffer_load_dwordx2 v[150:151], v92, s[88:91], 0 offen offset:0\n"
    "buffer_load_dwordx4 v[160:163], v88, s[68:71], s56 offen offset:0\n"
    "buffer_load_dwordx2 v[164:165], v89, s[76:79], s57 offen offset:0\n"
    "buffer_load_dwordx2 v[166:167], v89, s[80:83], s57 offen offset:0\n"
    "buffer_load_dword v176, v91, s[72:75], s56 offen offset:0\n"
    "buffer_load_dword v177, v91, s[72:75], s56 offen offset:512\n"
    "buffer_load_dword v178, v91, s[72:75], s56 offen offset:1024\n"
    "buffer_load_dword v179, v91, s[72:75], s56 offen offset:1536\n"
    "buffer_load_dwordx2 v[180:181], v92, s[84:87], s58 offen offset:0\n"
    "buffer_load_dwordx2 v[182:183], v92, s[88:91], s58 offen offset:0\n"
    "s_mov_b32 s49, 0x1000\n"
    "s_mov_b32 s50, 0x2000\n"
    "s_mov_b32 s51, 0x3000\n"
    "buffer_load_dwordx2 v[16:17], v125, s[76:79], 0 offen offset:0\n"
    "buffer_load_dwordx2 v[24:25], v125, s[80:83], 0 offen offset:0\n"
    "buffer_load_dwordx2 v[18:19], v125, s[76:79], 0 offen offset:64\n"
    "buffer_load_dwordx2 v[26:27], v125, s[80:83], 0 offen offset:64\n"
    "buffer_load_dwordx2 v[20:21], v125, s[76:79], 0 offen offset:128\n"
    "buffer_load_dwordx2 v[28:29], v125, s[80:83], 0 offen offset:128\n"
    "buffer_load_dwordx2 v[22:23], v125, s[76:79], 0 offen offset:192\n"
    "buffer_load_dwordx2 v[30:31], v125, s[80:83], 0 offen offset:192\n"
    "buffer_load_dwordx4 v[192:195], v124, s[68:71], 0 offen offset:0\n"
    "buffer_load_dwordx4 v[196:199], v124, s[68:71], 0 offen offset:128\n"
    "buffer_load_dwordx4 v[200:203], v124, s[68:71], 0 offen offset:256\n"
    "buffer_load_dwordx4 v[204:207], v124, s[68:71], 0 offen offset:384\n"
    "buffer_load_dwordx4 v[208:211], v124, s[68:71], s49 offen offset:0\n"
    "buffer_load_dwordx4 v[212:215], v124, s[68:71], s49 offen offset:128\n"
    "buffer_load_dwordx4 v[216:219], v124, s[68:71], s49 offen offset:256\n"
    "buffer_load_dwordx4 v[220:223], v124, s[68:71], s49 offen offset:384\n"
    "buffer_load_dwordx4 v[224:227], v124, s[68:71], s50 offen offset:0\n"
    "buffer_load_dwordx4 v[228:231], v124, s[68:71], s50 offen offset:128\n"
    "buffer_load_dwordx4 v[232:235], v124, s[68:71], s50 offen offset:256\n"
    "buffer_load_dwordx4 v[236:239], v124, s[68:71], s50 offen offset:384\n"
    "buffer_load_dwordx4 v[240:243], v124, s[68:71], s51 offen offset:0\n"
    "buffer_load_dwordx4 v[244:247], v124, s[68:71], s51 offen offset:128\n"
    "buffer_load_dwordx4 v[248:251], v124, s[68:71], s51 offen offset:256\n"
    "buffer_load_dwordx4 v[252:255], v124, s[68:71], s51 offen offset:384\n"
    "s_mov_b32 s36, %0\n"
    "s_and_b32 s37, %1, 0xffff\n"
    "s_mov_b32 s38, 0x4000\n"
    "s_mov_b32 s39, 0x20000\n"
    "v_and_b32 v10, 15, v0\n"
    "v_lshlrev_b32 v10, 8, v10\n"
    "v_lshrrev_b32 v11, 4, v0\n"
    "v_lshlrev_b32 v11, 4, v11\n"
    "v_add_u32 v1, v10, v11\n"
    "buffer_load_dwordx4 a[128:131], v1, s[36:39], 0 offen offset:0\n"
    "buffer_load_dwordx4 a[132:135], v1, s[36:39], 0 offen offset:64\n"
    "buffer_load_dwordx4 a[136:139], v1, s[36:39], 0 offen offset:128\n"
    "buffer_load_dwordx4 a[140:143], v1, s[36:39], 0 offen offset:192\n"
    "v_lshrrev_b32 v10, 1, v9\n"
    "v_lshlrev_b32 v10, 8, v10\n"
    "v_lshlrev_b32 v11, 4, v8\n"
    "v_add_u32 v10, v10, v11\n"
    "v_and_b32 v11, 1, v9\n"
    "v_lshl_add_u32 v10, v11, 3, v10\n"
    "v_add_u32 v90, %23, v10\n"
    "s_lshl_b32 s60, %6, 13\n"
    "s_add_u32 s60, s60, %7\n"
    "v_add_u32 v126, s60, v10\n"
    "v_and_b32 v10, 1, v12\n"
    "v_lshlrev_b32 v10, 12, v10\n"
    "v_lshrrev_b32 v11, 1, v12\n"
    "v_lshlrev_b32 v11, 4, v11\n"
    "v_add3_u32 v93, v10, v11, %26\n"
    "v_lshl_add_u32 v93, v13, 3, v93\n"
    "s_add_u32 s40, %4, 0x4000\n"
    "s_addc_u32 s41, %5, 0\n"
    "s_and_b32 s41, s41, 0xffff\n"
    "s_mov_b32 s42, 0x110000\n"
    "s_mov_b32 s43, 0x20000\n"
    "s_mov_b32 s44, %2\n"
    "s_and_b32 s45, %3, 0xffff\n"
    "s_mov_b32 s46, 0x4000\n"
    "s_mov_b32 s47, 0x20000\n"
    "s_mov_b32 s54, 0x4b800000\n"
    "s_mov_b32 s55, 0x3e0293ee\n"
    "s_mov_b32 s98, 0x20000\n"
    "s_mov_b32 s48, 0x10000\n"
    "v_lshlrev_b32 v76, 4, v0\n"
    "s_lshl_b32 s60, %20, 5\n"
    "v_lshl_add_u32 v94, v12, 3, s60\n"
    "v_lshl_or_b32 v15, %6, 6, v0\n"
    "v_lshlrev_b32 v95, 20, v15\n"
    "v_lshl_add_u32 v14, v15, 8, %20\n"
    "v_lshrrev_b32 v15, 5, v15\n"
    "v_lshl_add_u32 v14, v15, 20, v14\n"
    "v_add_u32 v2, %7, v76\n"
    "v_add_u32 v3, 0x10000, v2\n"
    "s_add_u32 s52, s59, %7\n"
    "s_add_u32 s53, s52, 0x10000\n"
    "v_add_u32 v4, s59, v76\n"
    "v_add_u32 v5, 0x1000, v4\n"
    "v_add_u32 v6, 0x1000, v5\n"
    "v_add_u32 v7, 0x1000, v6\n"
    "v_mov_b32 v64, 0x3f803f80\n"
    "v_mov_b32 v65, 0x3f803f80\n"
    "v_mov_b32 v66, 0x3f803f80\n"
    "v_mov_b32 v67, 0x3f803f80\n"
    "v_mov_b32 v68, 0\n"
    "v_mov_b32 v69, 0\n"
    "v_mov_b32 v70, 0\n"
    "v_mov_b32 v71, 0\n"
    "v_mov_b32 v80, 0\n"
    "v_mov_b32 v81, 0\n"
    "v_mov_b32 v82, 0\n"
    "v_mov_b32 v83, 0\n"
    "v_mov_b32 v98, s55\n"
    "v_mov_b32 v99, s55\n"
    "s_cmp_eq_u32 %20, 16\n"
    "s_cselect_b32 s60, 32, 0\n"
    "s_lshl_b32 s61, %20, 4\n"
    "s_add_u32 s61, s61, 0x2400\n"
    "v_mov_b32 v15, s60\n"
    "buffer_store_dword v15, v95, s[64:67], s61 offen offset:0 sc1\n"
    "s_waitcnt vmcnt(47)\n"
    "v_add_u32 v78, 1, v78\n"
    "v_readfirstlane_b32 s99, v79\n"
    "v_readfirstlane_b32 s96, v78\n"
    "s_waitcnt vmcnt(44)\n"
    "v_cvt_f32_i32 v136, v128\n"
    "v_cvt_f32_i32 v137, v129\n"
    "v_cvt_f32_i32 v138, v130\n"
    "v_cvt_f32_i32 v139, v131\n"
    "v_cvt_pk_bf16_f32 v136, 0, v136\n"
    "v_cvt_pk_bf16_f32 v137, 0, v137\n"
    "v_cvt_pk_bf16_f32 v138, 0, v138\n"
    "v_cvt_pk_bf16_f32 v139, 0, v139\n"
    "v_lshlrev_b32 v140, 16, v134\n"
    "v_and_b32 v141, 0xffff0000, v134\n"
    "v_lshlrev_b32 v142, 16, v135\n"
    "v_and_b32 v143, 0xffff0000, v135\n"
    "v_sub_f32 v136, v136, v140\n"
    "v_sub_f32 v137, v137, v141\n"
    "v_sub_f32 v138, v138, v142\n"
    "v_sub_f32 v139, v139, v143\n"
    "v_cvt_pk_bf16_f32 v136, 0, v136\n"
    "v_cvt_pk_bf16_f32 v137, 0, v137\n"
    "v_cvt_pk_bf16_f32 v138, 0, v138\n"
    "v_cvt_pk_bf16_f32 v139, 0, v139\n"
    "v_lshlrev_b32 v140, 16, v132\n"
    "v_and_b32 v141, 0xffff0000, v132\n"
    "v_lshlrev_b32 v142, 16, v133\n"
    "v_and_b32 v143, 0xffff0000, v133\n"
    "v_mul_f32 v136, v136, v140\n"
    "v_mul_f32 v137, v137, v141\n"
    "v_mul_f32 v138, v138, v142\n"
    "v_mul_f32 v139, v139, v143\n"
    "v_cvt_pk_bf16_f32 v136, 0, v136\n"
    "v_cvt_pk_bf16_f32 v137, 0, v137\n"
    "v_cvt_pk_bf16_f32 v138, 0, v138\n"
    "v_cvt_pk_bf16_f32 v139, 0, v139\n"
    "v_mul_f32 v136, s55, v136\n"
    "v_mul_f32 v137, s55, v137\n"
    "v_mul_f32 v138, s55, v138\n"
    "v_mul_f32 v139, s55, v139\n"
    "v_cvt_pk_bf16_f32 v140, v136, v137\n"
    "v_cvt_pk_bf16_f32 v141, v138, v139\n"
    "buffer_store_dwordx2 v[140:141], v90, s[40:43], 0 offen offset:0 sc1\n"
    "s_waitcnt vmcnt(39)\n"
    "v_cvt_f32_i32 v152, v144\n"
    "v_cvt_f32_i32 v153, v145\n"
    "v_cvt_f32_i32 v154, v146\n"
    "v_cvt_f32_i32 v155, v147\n"
    "v_cvt_pk_bf16_f32 v152, 0, v152\n"
    "v_cvt_pk_bf16_f32 v153, 0, v153\n"
    "v_cvt_pk_bf16_f32 v154, 0, v154\n"
    "v_cvt_pk_bf16_f32 v155, 0, v155\n"
    "v_lshlrev_b32 v156, 16, v150\n"
    "v_and_b32 v157, 0xffff0000, v150\n"
    "v_lshlrev_b32 v158, 16, v151\n"
    "v_and_b32 v159, 0xffff0000, v151\n"
    "v_sub_f32 v152, v152, v156\n"
    "v_sub_f32 v153, v153, v157\n"
    "v_sub_f32 v154, v154, v158\n"
    "v_sub_f32 v155, v155, v159\n"
    "v_cvt_pk_bf16_f32 v152, 0, v152\n"
    "v_cvt_pk_bf16_f32 v153, 0, v153\n"
    "v_cvt_pk_bf16_f32 v154, 0, v154\n"
    "v_cvt_pk_bf16_f32 v155, 0, v155\n"
    "v_lshlrev_b32 v156, 16, v148\n"
    "v_and_b32 v157, 0xffff0000, v148\n"
    "v_lshlrev_b32 v158, 16, v149\n"
    "v_and_b32 v159, 0xffff0000, v149\n"
    "v_mul_f32 v152, v152, v156\n"
    "v_mul_f32 v153, v153, v157\n"
    "v_mul_f32 v154, v154, v158\n"
    "v_mul_f32 v155, v155, v159\n"
    "v_cvt_pk_bf16_f32 v156, v152, v153\n"
    "v_cvt_pk_bf16_f32 v157, v154, v155\n"
    "buffer_store_dwordx2 v[156:157], v93, s[40:43], 0 offen offset:0 sc1\n"
    "s_waitcnt vmcnt(37)\n"
    "v_cvt_f32_i32 v168, v160\n"
    "v_cvt_f32_i32 v169, v161\n"
    "v_cvt_f32_i32 v170, v162\n"
    "v_cvt_f32_i32 v171, v163\n"
    "v_cvt_pk_bf16_f32 v168, 0, v168\n"
    "v_cvt_pk_bf16_f32 v169, 0, v169\n"
    "v_cvt_pk_bf16_f32 v170, 0, v170\n"
    "v_cvt_pk_bf16_f32 v171, 0, v171\n"
    "v_lshlrev_b32 v172, 16, v166\n"
    "v_and_b32 v173, 0xffff0000, v166\n"
    "v_lshlrev_b32 v174, 16, v167\n"
    "v_and_b32 v175, 0xffff0000, v167\n"
    "v_sub_f32 v168, v168, v172\n"
    "v_sub_f32 v169, v169, v173\n"
    "v_sub_f32 v170, v170, v174\n"
    "v_sub_f32 v171, v171, v175\n"
    "v_cvt_pk_bf16_f32 v168, 0, v168\n"
    "v_cvt_pk_bf16_f32 v169, 0, v169\n"
    "v_cvt_pk_bf16_f32 v170, 0, v170\n"
    "v_cvt_pk_bf16_f32 v171, 0, v171\n"
    "v_lshlrev_b32 v172, 16, v164\n"
    "v_and_b32 v173, 0xffff0000, v164\n"
    "v_lshlrev_b32 v174, 16, v165\n"
    "v_and_b32 v175, 0xffff0000, v165\n"
    "v_mul_f32 v168, v168, v172\n"
    "v_mul_f32 v169, v169, v173\n"
    "v_mul_f32 v170, v170, v174\n"
    "v_mul_f32 v171, v171, v175\n"
    "v_cvt_pk_bf16_f32 v168, 0, v168\n"
    "v_cvt_pk_bf16_f32 v169, 0, v169\n"
    "v_cvt_pk_bf16_f32 v170, 0, v170\n"
    "v_cvt_pk_bf16_f32 v171, 0, v171\n"
    "v_mul_f32 v168, s55, v168\n"
    "v_mul_f32 v169, s55, v169\n"
    "v_mul_f32 v170, s55, v170\n"
    "v_mul_f32 v171, s55, v171\n"
    "v_cvt_pk_bf16_f32 v172, v168, v169\n"
    "v_cvt_pk_bf16_f32 v173, v170, v171\n"
    "buffer_store_dwordx2 v[172:173], v90, s[40:43], s56 offen offset:0 sc1\n"
    "s_waitcnt vmcnt(32)\n"
    "v_cvt_f32_i32 v184, v176\n"
    "v_cvt_f32_i32 v185, v177\n"
    "v_cvt_f32_i32 v186, v178\n"
    "v_cvt_f32_i32 v187, v179\n"
    "v_cvt_pk_bf16_f32 v184, 0, v184\n"
    "v_cvt_pk_bf16_f32 v185, 0, v185\n"
    "v_cvt_pk_bf16_f32 v186, 0, v186\n"
    "v_cvt_pk_bf16_f32 v187, 0, v187\n"
    "v_lshlrev_b32 v188, 16, v182\n"
    "v_and_b32 v189, 0xffff0000, v182\n"
    "v_lshlrev_b32 v190, 16, v183\n"
    "v_and_b32 v191, 0xffff0000, v183\n"
    "v_sub_f32 v184, v184, v188\n"
    "v_sub_f32 v185, v185, v189\n"
    "v_sub_f32 v186, v186, v190\n"
    "v_sub_f32 v187, v187, v191\n"
    "v_cvt_pk_bf16_f32 v184, 0, v184\n"
    "v_cvt_pk_bf16_f32 v185, 0, v185\n"
    "v_cvt_pk_bf16_f32 v186, 0, v186\n"
    "v_cvt_pk_bf16_f32 v187, 0, v187\n"
    "v_lshlrev_b32 v188, 16, v180\n"
    "v_and_b32 v189, 0xffff0000, v180\n"
    "v_lshlrev_b32 v190, 16, v181\n"
    "v_and_b32 v191, 0xffff0000, v181\n"
    "v_mul_f32 v184, v184, v188\n"
    "v_mul_f32 v185, v185, v189\n"
    "v_mul_f32 v186, v186, v190\n"
    "v_mul_f32 v187, v187, v191\n"
    "v_cvt_pk_bf16_f32 v188, v184, v185\n"
    "v_cvt_pk_bf16_f32 v189, v186, v187\n"
    "buffer_store_dwordx2 v[188:189], v93, s[40:43], s56 offen offset:0 sc1\n"
    "s_mov_b32 s92, 0x40000\n"
    "s_mov_b32 s93, 2048\n"
    "s_mov_b32 s95, 1024\n"
    "s_mov_b32 s94, 0x60000\n"
    "s_mov_b32 s59, 3072\n"
    "s_waitcnt vmcnt(25)\n"
    "v_lshlrev_b32 v32, 16, v24\n"
    "v_and_b32 v33, 0xffff0000, v24\n"
    "v_lshlrev_b32 v34, 16, v25\n"
    "v_and_b32 v35, 0xffff0000, v25\n"
    "v_lshlrev_b32 v48, 16, v16\n"
    "v_and_b32 v49, 0xffff0000, v16\n"
    "v_lshlrev_b32 v50, 16, v17\n"
    "v_and_b32 v51, 0xffff0000, v17\n"
    "v_lshlrev_b32 v36, 16, v26\n"
    "v_and_b32 v37, 0xffff0000, v26\n"
    "v_lshlrev_b32 v38, 16, v27\n"
    "v_and_b32 v39, 0xffff0000, v27\n"
    "v_lshlrev_b32 v52, 16, v18\n"
    "v_and_b32 v53, 0xffff0000, v18\n"
    "v_lshlrev_b32 v54, 16, v19\n"
    "v_and_b32 v55, 0xffff0000, v19\n"
    "v_lshlrev_b32 v40, 16, v28\n"
    "v_and_b32 v41, 0xffff0000, v28\n"
    "v_lshlrev_b32 v42, 16, v29\n"
    "v_and_b32 v43, 0xffff0000, v29\n"
    "v_lshlrev_b32 v56, 16, v20\n"
    "v_and_b32 v57, 0xffff0000, v20\n"
    "v_lshlrev_b32 v58, 16, v21\n"
    "v_and_b32 v59, 0xffff0000, v21\n"
    "v_lshlrev_b32 v44, 16, v30\n"
    "v_and_b32 v45, 0xffff0000, v30\n"
    "v_lshlrev_b32 v46, 16, v31\n"
    "v_and_b32 v47, 0xffff0000, v31\n"
    "v_lshlrev_b32 v60, 16, v22\n"
    "v_and_b32 v61, 0xffff0000, v22\n"
    "v_lshlrev_b32 v62, 16, v23\n"
    "v_and_b32 v63, 0xffff0000, v23\n"
    "s_waitcnt vmcnt(21)\n"
    "v_cvt_f32_i32 v192, v192\n"
    "v_cvt_f32_i32 v193, v193\n"
    "v_cvt_f32_i32 v194, v194\n"
    "v_cvt_f32_i32 v195, v195\n"
    "v_cvt_f32_i32 v196, v196\n"
    "v_cvt_f32_i32 v197, v197\n"
    "v_cvt_f32_i32 v198, v198\n"
    "v_cvt_f32_i32 v199, v199\n"
    "v_cvt_pk_bf16_f32 v192, 0, v192\n"
    "v_cvt_pk_bf16_f32 v193, 0, v193\n"
    "v_cvt_pk_bf16_f32 v194, 0, v194\n"
    "v_cvt_pk_bf16_f32 v195, 0, v195\n"
    "v_cvt_pk_bf16_f32 v196, 0, v196\n"
    "v_cvt_pk_bf16_f32 v197, 0, v197\n"
    "v_cvt_pk_bf16_f32 v198, 0, v198\n"
    "v_cvt_pk_bf16_f32 v199, 0, v199\n"
    "v_pk_add_f32 v[192:193], v[192:193], v[32:33] neg_lo:[0,1] neg_hi:[0,1]\n"
    "v_pk_add_f32 v[194:195], v[194:195], v[34:35] neg_lo:[0,1] neg_hi:[0,1]\n"
    "v_pk_add_f32 v[196:197], v[196:197], v[36:37] neg_lo:[0,1] neg_hi:[0,1]\n"
    "v_pk_add_f32 v[198:199], v[198:199], v[38:39] neg_lo:[0,1] neg_hi:[0,1]\n"
    "v_cvt_pk_bf16_f32 v192, 0, v192\n"
    "v_cvt_pk_bf16_f32 v193, 0, v193\n"
    "v_cvt_pk_bf16_f32 v194, 0, v194\n"
    "v_cvt_pk_bf16_f32 v195, 0, v195\n"
    "v_cvt_pk_bf16_f32 v196, 0, v196\n"
    "v_cvt_pk_bf16_f32 v197, 0, v197\n"
    "v_cvt_pk_bf16_f32 v198, 0, v198\n"
    "v_cvt_pk_bf16_f32 v199, 0, v199\n"
    "v_pk_mul_f32 v[192:193], v[192:193], v[48:49]\n"
    "v_pk_mul_f32 v[194:195], v[194:195], v[50:51]\n"
    "v_pk_mul_f32 v[196:197], v[196:197], v[52:53]\n"
    "v_pk_mul_f32 v[198:199], v[198:199], v[54:55]\n"
    "v_cvt_pk_bf16_f32 v192, 0, v192\n"
    "v_cvt_pk_bf16_f32 v193, 0, v193\n"
    "v_cvt_pk_bf16_f32 v194, 0, v194\n"
    "v_cvt_pk_bf16_f32 v195, 0, v195\n"
    "v_cvt_pk_bf16_f32 v196, 0, v196\n"
    "v_cvt_pk_bf16_f32 v197, 0, v197\n"
    "v_cvt_pk_bf16_f32 v198, 0, v198\n"
    "v_cvt_pk_bf16_f32 v199, 0, v199\n"
    "v_pk_mul_f32 v[192:193], v[192:193], v[98:99]\n"
    "v_pk_mul_f32 v[194:195], v[194:195], v[98:99]\n"
    "v_pk_mul_f32 v[196:197], v[196:197], v[98:99]\n"
    "v_pk_mul_f32 v[198:199], v[198:199], v[98:99]\n"
    "v_cvt_pk_bf16_f32 v192, v192, v193\n"
    "v_cvt_pk_bf16_f32 v193, v194, v195\n"
    "v_cvt_pk_bf16_f32 v196, v196, v197\n"
    "v_cvt_pk_bf16_f32 v197, v198, v199\n"
    "ds_write_b64 v126, v[192:193] offset:0\n"
    "ds_write_b64 v126, v[196:197] offset:1024\n"
    "v_cvt_f32_i32 v200, v200\n"
    "v_cvt_f32_i32 v201, v201\n"
    "v_cvt_f32_i32 v202, v202\n"
    "v_cvt_f32_i32 v203, v203\n"
    "v_cvt_f32_i32 v204, v204\n"
    "v_cvt_f32_i32 v205, v205\n"
    "v_cvt_f32_i32 v206, v206\n"
    "v_cvt_f32_i32 v207, v207\n"
    "v_cvt_pk_bf16_f32 v200, 0, v200\n"
    "v_cvt_pk_bf16_f32 v201, 0, v201\n"
    "v_cvt_pk_bf16_f32 v202, 0, v202\n"
    "v_cvt_pk_bf16_f32 v203, 0, v203\n"
    "v_cvt_pk_bf16_f32 v204, 0, v204\n"
    "v_cvt_pk_bf16_f32 v205, 0, v205\n"
    "v_cvt_pk_bf16_f32 v206, 0, v206\n"
    "v_cvt_pk_bf16_f32 v207, 0, v207\n"
    "v_pk_add_f32 v[200:201], v[200:201], v[40:41] neg_lo:[0,1] neg_hi:[0,1]\n"
    "v_pk_add_f32 v[202:203], v[202:203], v[42:43] neg_lo:[0,1] neg_hi:[0,1]\n"
    "v_pk_add_f32 v[204:205], v[204:205], v[44:45] neg_lo:[0,1] neg_hi:[0,1]\n"
    "v_pk_add_f32 v[206:207], v[206:207], v[46:47] neg_lo:[0,1] neg_hi:[0,1]\n"
    "v_cvt_pk_bf16_f32 v200, 0, v200\n"
    "v_cvt_pk_bf16_f32 v201, 0, v201\n"
    "v_cvt_pk_bf16_f32 v202, 0, v202\n"
    "v_cvt_pk_bf16_f32 v203, 0, v203\n"
    "v_cvt_pk_bf16_f32 v204, 0, v204\n"
    "v_cvt_pk_bf16_f32 v205, 0, v205\n"
    "v_cvt_pk_bf16_f32 v206, 0, v206\n"
    "v_cvt_pk_bf16_f32 v207, 0, v207\n"
    "v_pk_mul_f32 v[200:201], v[200:201], v[56:57]\n"
    "v_pk_mul_f32 v[202:203], v[202:203], v[58:59]\n"
    "v_pk_mul_f32 v[204:205], v[204:205], v[60:61]\n"
    "v_pk_mul_f32 v[206:207], v[206:207], v[62:63]\n"
    "v_cvt_pk_bf16_f32 v200, 0, v200\n"
    "v_cvt_pk_bf16_f32 v201, 0, v201\n"
    "v_cvt_pk_bf16_f32 v202, 0, v202\n"
    "v_cvt_pk_bf16_f32 v203, 0, v203\n"
    "v_cvt_pk_bf16_f32 v204, 0, v204\n"
    "v_cvt_pk_bf16_f32 v205, 0, v205\n"
    "v_cvt_pk_bf16_f32 v206, 0, v206\n"
    "v_cvt_pk_bf16_f32 v207, 0, v207\n"
    "v_pk_mul_f32 v[200:201], v[200:201], v[98:99]\n"
    "v_pk_mul_f32 v[202:203], v[202:203], v[98:99]\n"
    "v_pk_mul_f32 v[204:205], v[204:205], v[98:99]\n"
    "v_pk_mul_f32 v[206:207], v[206:207], v[98:99]\n"
    "v_cvt_pk_bf16_f32 v200, v200, v201\n"
    "v_cvt_pk_bf16_f32 v201, v202, v203\n"
    "v_cvt_pk_bf16_f32 v204, v204, v205\n"
    "v_cvt_pk_bf16_f32 v205, v206, v207\n"
    "ds_write_b64 v126, v[200:201] offset:2048\n"
    "ds_write_b64 v126, v[204:205] offset:3072\n"
    "s_waitcnt vmcnt(17)\n"
    "v_cvt_f32_i32 v208, v208\n"
    "v_cvt_f32_i32 v209, v209\n"
    "v_cvt_f32_i32 v210, v210\n"
    "v_cvt_f32_i32 v211, v211\n"
    "v_cvt_f32_i32 v212, v212\n"
    "v_cvt_f32_i32 v213, v213\n"
    "v_cvt_f32_i32 v214, v214\n"
    "v_cvt_f32_i32 v215, v215\n"
    "v_cvt_pk_bf16_f32 v208, 0, v208\n"
    "v_cvt_pk_bf16_f32 v209, 0, v209\n"
    "v_cvt_pk_bf16_f32 v210, 0, v210\n"
    "v_cvt_pk_bf16_f32 v211, 0, v211\n"
    "v_cvt_pk_bf16_f32 v212, 0, v212\n"
    "v_cvt_pk_bf16_f32 v213, 0, v213\n"
    "v_cvt_pk_bf16_f32 v214, 0, v214\n"
    "v_cvt_pk_bf16_f32 v215, 0, v215\n"
    "v_pk_add_f32 v[208:209], v[208:209], v[32:33] neg_lo:[0,1] neg_hi:[0,1]\n"
    "v_pk_add_f32 v[210:211], v[210:211], v[34:35] neg_lo:[0,1] neg_hi:[0,1]\n"
    "v_pk_add_f32 v[212:213], v[212:213], v[36:37] neg_lo:[0,1] neg_hi:[0,1]\n"
    "v_pk_add_f32 v[214:215], v[214:215], v[38:39] neg_lo:[0,1] neg_hi:[0,1]\n"
    "v_cvt_pk_bf16_f32 v208, 0, v208\n"
    "v_cvt_pk_bf16_f32 v209, 0, v209\n"
    "v_cvt_pk_bf16_f32 v210, 0, v210\n"
    "v_cvt_pk_bf16_f32 v211, 0, v211\n"
    "v_cvt_pk_bf16_f32 v212, 0, v212\n"
    "v_cvt_pk_bf16_f32 v213, 0, v213\n"
    "v_cvt_pk_bf16_f32 v214, 0, v214\n"
    "v_cvt_pk_bf16_f32 v215, 0, v215\n"
    "v_pk_mul_f32 v[208:209], v[208:209], v[48:49]\n"
    "v_pk_mul_f32 v[210:211], v[210:211], v[50:51]\n"
    "v_pk_mul_f32 v[212:213], v[212:213], v[52:53]\n"
    "v_pk_mul_f32 v[214:215], v[214:215], v[54:55]\n"
    "v_cvt_pk_bf16_f32 v208, 0, v208\n"
    "v_cvt_pk_bf16_f32 v209, 0, v209\n"
    "v_cvt_pk_bf16_f32 v210, 0, v210\n"
    "v_cvt_pk_bf16_f32 v211, 0, v211\n"
    "v_cvt_pk_bf16_f32 v212, 0, v212\n"
    "v_cvt_pk_bf16_f32 v213, 0, v213\n"
    "v_cvt_pk_bf16_f32 v214, 0, v214\n"
    "v_cvt_pk_bf16_f32 v215, 0, v215\n"
    "v_pk_mul_f32 v[208:209], v[208:209], v[98:99]\n"
    "v_pk_mul_f32 v[210:211], v[210:211], v[98:99]\n"
    "v_pk_mul_f32 v[212:213], v[212:213], v[98:99]\n"
    "v_pk_mul_f32 v[214:215], v[214:215], v[98:99]\n"
    "v_cvt_pk_bf16_f32 v208, v208, v209\n"
    "v_cvt_pk_bf16_f32 v209, v210, v211\n"
    "v_cvt_pk_bf16_f32 v212, v212, v213\n"
    "v_cvt_pk_bf16_f32 v213, v214, v215\n"
    "ds_write_b64 v126, v[208:209] offset:128\n"
    "ds_write_b64 v126, v[212:213] offset:1152\n"
    "v_cvt_f32_i32 v216, v216\n"
    "v_cvt_f32_i32 v217, v217\n"
    "v_cvt_f32_i32 v218, v218\n"
    "v_cvt_f32_i32 v219, v219\n"
    "v_cvt_f32_i32 v220, v220\n"
    "v_cvt_f32_i32 v221, v221\n"
    "v_cvt_f32_i32 v222, v222\n"
    "v_cvt_f32_i32 v223, v223\n"
    "v_cvt_pk_bf16_f32 v216, 0, v216\n"
    "v_cvt_pk_bf16_f32 v217, 0, v217\n"
    "v_cvt_pk_bf16_f32 v218, 0, v218\n"
    "v_cvt_pk_bf16_f32 v219, 0, v219\n"
    "v_cvt_pk_bf16_f32 v220, 0, v220\n"
    "v_cvt_pk_bf16_f32 v221, 0, v221\n"
    "v_cvt_pk_bf16_f32 v222, 0, v222\n"
    "v_cvt_pk_bf16_f32 v223, 0, v223\n"
    "v_pk_add_f32 v[216:217], v[216:217], v[40:41] neg_lo:[0,1] neg_hi:[0,1]\n"
    "v_pk_add_f32 v[218:219], v[218:219], v[42:43] neg_lo:[0,1] neg_hi:[0,1]\n"
    "v_pk_add_f32 v[220:221], v[220:221], v[44:45] neg_lo:[0,1] neg_hi:[0,1]\n"
    "v_pk_add_f32 v[222:223], v[222:223], v[46:47] neg_lo:[0,1] neg_hi:[0,1]\n"
    "v_cvt_pk_bf16_f32 v216, 0, v216\n"
    "v_cvt_pk_bf16_f32 v217, 0, v217\n"
    "v_cvt_pk_bf16_f32 v218, 0, v218\n"
    "v_cvt_pk_bf16_f32 v219, 0, v219\n"
    "v_cvt_pk_bf16_f32 v220, 0, v220\n"
    "v_cvt_pk_bf16_f32 v221, 0, v221\n"
    "v_cvt_pk_bf16_f32 v222, 0, v222\n"
    "v_cvt_pk_bf16_f32 v223, 0, v223\n"
    "v_pk_mul_f32 v[216:217], v[216:217], v[56:57]\n"
    "v_pk_mul_f32 v[218:219], v[218:219], v[58:59]\n"
    "v_pk_mul_f32 v[220:221], v[220:221], v[60:61]\n"
    "v_pk_mul_f32 v[222:223], v[222:223], v[62:63]\n"
    "v_cvt_pk_bf16_f32 v216, 0, v216\n"
    "v_cvt_pk_bf16_f32 v217, 0, v217\n"
    "v_cvt_pk_bf16_f32 v218, 0, v218\n"
    "v_cvt_pk_bf16_f32 v219, 0, v219\n"
    "v_cvt_pk_bf16_f32 v220, 0, v220\n"
    "v_cvt_pk_bf16_f32 v221, 0, v221\n"
    "v_cvt_pk_bf16_f32 v222, 0, v222\n"
    "v_cvt_pk_bf16_f32 v223, 0, v223\n"
    "v_pk_mul_f32 v[216:217], v[216:217], v[98:99]\n"
    "v_pk_mul_f32 v[218:219], v[218:219], v[98:99]\n"
    "v_pk_mul_f32 v[220:221], v[220:221], v[98:99]\n"
    "v_pk_mul_f32 v[222:223], v[222:223], v[98:99]\n"
    "v_cvt_pk_bf16_f32 v216, v216, v217\n"
    "v_cvt_pk_bf16_f32 v217, v218, v219\n"
    "v_cvt_pk_bf16_f32 v220, v220, v221\n"
    "v_cvt_pk_bf16_f32 v221, v222, v223\n"
    "ds_write_b64 v126, v[216:217] offset:2176\n"
    "ds_write_b64 v126, v[220:221] offset:3200\n"
    "buffer_load_dwordx4 a[144:147], v1, s[36:39], s49 offen offset:0\n"
    "buffer_load_dwordx4 a[148:151], v1, s[36:39], s49 offen offset:64\n"
    "buffer_load_dwordx4 a[152:155], v1, s[36:39], s49 offen offset:128\n"
    "buffer_load_dwordx4 a[156:159], v1, s[36:39], s49 offen offset:192\n"
    "buffer_load_dwordx4 a[160:163], v1, s[36:39], s50 offen offset:0\n"
    "buffer_load_dwordx4 a[164:167], v1, s[36:39], s50 offen offset:64\n"
    "buffer_load_dwordx4 a[168:171], v1, s[36:39], s50 offen offset:128\n"
    "buffer_load_dwordx4 a[172:175], v1, s[36:39], s50 offen offset:192\n"
    "buffer_load_dwordx4 a[176:179], v1, s[36:39], s51 offen offset:0\n"
    "buffer_load_dwordx4 a[180:183], v1, s[36:39], s51 offen offset:64\n"
    "buffer_load_dwordx4 a[184:187], v1, s[36:39], s51 offen offset:128\n"
    "buffer_load_dwordx4 a[188:191], v1, s[36:39], s51 offen offset:192\n"
    "buffer_load_dwordx4 v[72:75], v88, s[68:71], s92 offen offset:0\n"
    "buffer_load_dwordx2 v[84:85], v89, s[76:79], s93 offen offset:0\n"
    "buffer_load_dwordx2 v[86:87], v89, s[80:83], s93 offen offset:0\n"
    "buffer_load_dword v100, v91, s[72:75], s92 offen offset:0\n"
    "buffer_load_dword v101, v91, s[72:75], s92 offen offset:512\n"
    "buffer_load_dword v102, v91, s[72:75], s92 offen offset:1024\n"
    "buffer_load_dword v103, v91, s[72:75], s92 offen offset:1536\n"
    "buffer_load_dwordx2 v[104:105], v92, s[84:87], s95 offen offset:0\n"
    "buffer_load_dwordx2 v[106:107], v92, s[88:91], s95 offen offset:0\n"
    "buffer_load_dwordx4 v[116:119], v88, s[68:71], s94 offen offset:0\n"
    "buffer_load_dwordx2 v[120:121], v89, s[76:79], s59 offen offset:0\n"
    "buffer_load_dwordx2 v[122:123], v89, s[80:83], s59 offen offset:0\n"
    "s_waitcnt vmcnt(37)\n"
    "v_cvt_f32_i32 v224, v224\n"
    "v_cvt_f32_i32 v225, v225\n"
    "v_cvt_f32_i32 v226, v226\n"
    "v_cvt_f32_i32 v227, v227\n"
    "v_cvt_f32_i32 v228, v228\n"
    "v_cvt_f32_i32 v229, v229\n"
    "v_cvt_f32_i32 v230, v230\n"
    "v_cvt_f32_i32 v231, v231\n"
    "v_cvt_pk_bf16_f32 v224, 0, v224\n"
    "v_cvt_pk_bf16_f32 v225, 0, v225\n"
    "v_cvt_pk_bf16_f32 v226, 0, v226\n"
    "v_cvt_pk_bf16_f32 v227, 0, v227\n"
    "v_cvt_pk_bf16_f32 v228, 0, v228\n"
    "v_cvt_pk_bf16_f32 v229, 0, v229\n"
    "v_cvt_pk_bf16_f32 v230, 0, v230\n"
    "v_cvt_pk_bf16_f32 v231, 0, v231\n"
    "v_pk_add_f32 v[224:225], v[224:225], v[32:33] neg_lo:[0,1] neg_hi:[0,1]\n"
    "v_pk_add_f32 v[226:227], v[226:227], v[34:35] neg_lo:[0,1] neg_hi:[0,1]\n"
    "v_pk_add_f32 v[228:229], v[228:229], v[36:37] neg_lo:[0,1] neg_hi:[0,1]\n"
    "v_pk_add_f32 v[230:231], v[230:231], v[38:39] neg_lo:[0,1] neg_hi:[0,1]\n"
    "v_cvt_pk_bf16_f32 v224, 0, v224\n"
    "v_cvt_pk_bf16_f32 v225, 0, v225\n"
    "v_cvt_pk_bf16_f32 v226, 0, v226\n"
    "v_cvt_pk_bf16_f32 v227, 0, v227\n"
    "v_cvt_pk_bf16_f32 v228, 0, v228\n"
    "v_cvt_pk_bf16_f32 v229, 0, v229\n"
    "v_cvt_pk_bf16_f32 v230, 0, v230\n"
    "v_cvt_pk_bf16_f32 v231, 0, v231\n"
    "v_pk_mul_f32 v[224:225], v[224:225], v[48:49]\n"
    "v_pk_mul_f32 v[226:227], v[226:227], v[50:51]\n"
    "v_pk_mul_f32 v[228:229], v[228:229], v[52:53]\n"
    "v_pk_mul_f32 v[230:231], v[230:231], v[54:55]\n"
    "v_cvt_pk_bf16_f32 v224, 0, v224\n"
    "v_cvt_pk_bf16_f32 v225, 0, v225\n"
    "v_cvt_pk_bf16_f32 v226, 0, v226\n"
    "v_cvt_pk_bf16_f32 v227, 0, v227\n"
    "v_cvt_pk_bf16_f32 v228, 0, v228\n"
    "v_cvt_pk_bf16_f32 v229, 0, v229\n"
    "v_cvt_pk_bf16_f32 v230, 0, v230\n"
    "v_cvt_pk_bf16_f32 v231, 0, v231\n"
    "v_pk_mul_f32 v[224:225], v[224:225], v[98:99]\n"
    "v_pk_mul_f32 v[226:227], v[226:227], v[98:99]\n"
    "v_pk_mul_f32 v[228:229], v[228:229], v[98:99]\n"
    "v_pk_mul_f32 v[230:231], v[230:231], v[98:99]\n"
    "v_cvt_pk_bf16_f32 v224, v224, v225\n"
    "v_cvt_pk_bf16_f32 v225, v226, v227\n"
    "v_cvt_pk_bf16_f32 v228, v228, v229\n"
    "v_cvt_pk_bf16_f32 v229, v230, v231\n"
    "ds_write_b64 v126, v[224:225] offset:4096\n"
    "ds_write_b64 v126, v[228:229] offset:5120\n"
    "v_cvt_f32_i32 v232, v232\n"
    "v_cvt_f32_i32 v233, v233\n"
    "v_cvt_f32_i32 v234, v234\n"
    "v_cvt_f32_i32 v235, v235\n"
    "v_cvt_f32_i32 v236, v236\n"
    "v_cvt_f32_i32 v237, v237\n"
    "v_cvt_f32_i32 v238, v238\n"
    "v_cvt_f32_i32 v239, v239\n"
    "v_cvt_pk_bf16_f32 v232, 0, v232\n"
    "v_cvt_pk_bf16_f32 v233, 0, v233\n"
    "v_cvt_pk_bf16_f32 v234, 0, v234\n"
    "v_cvt_pk_bf16_f32 v235, 0, v235\n"
    "v_cvt_pk_bf16_f32 v236, 0, v236\n"
    "v_cvt_pk_bf16_f32 v237, 0, v237\n"
    "v_cvt_pk_bf16_f32 v238, 0, v238\n"
    "v_cvt_pk_bf16_f32 v239, 0, v239\n"
    "v_pk_add_f32 v[232:233], v[232:233], v[40:41] neg_lo:[0,1] neg_hi:[0,1]\n"
    "v_pk_add_f32 v[234:235], v[234:235], v[42:43] neg_lo:[0,1] neg_hi:[0,1]\n"
    "v_pk_add_f32 v[236:237], v[236:237], v[44:45] neg_lo:[0,1] neg_hi:[0,1]\n"
    "v_pk_add_f32 v[238:239], v[238:239], v[46:47] neg_lo:[0,1] neg_hi:[0,1]\n"
    "v_cvt_pk_bf16_f32 v232, 0, v232\n"
    "v_cvt_pk_bf16_f32 v233, 0, v233\n"
    "v_cvt_pk_bf16_f32 v234, 0, v234\n"
    "v_cvt_pk_bf16_f32 v235, 0, v235\n"
    "v_cvt_pk_bf16_f32 v236, 0, v236\n"
    "v_cvt_pk_bf16_f32 v237, 0, v237\n"
    "v_cvt_pk_bf16_f32 v238, 0, v238\n"
    "v_cvt_pk_bf16_f32 v239, 0, v239\n"
    "v_pk_mul_f32 v[232:233], v[232:233], v[56:57]\n"
    "v_pk_mul_f32 v[234:235], v[234:235], v[58:59]\n"
    "v_pk_mul_f32 v[236:237], v[236:237], v[60:61]\n"
    "v_pk_mul_f32 v[238:239], v[238:239], v[62:63]\n"
    "v_cvt_pk_bf16_f32 v232, 0, v232\n"
    "v_cvt_pk_bf16_f32 v233, 0, v233\n"
    "v_cvt_pk_bf16_f32 v234, 0, v234\n"
    "v_cvt_pk_bf16_f32 v235, 0, v235\n"
    "v_cvt_pk_bf16_f32 v236, 0, v236\n"
    "v_cvt_pk_bf16_f32 v237, 0, v237\n"
    "v_cvt_pk_bf16_f32 v238, 0, v238\n"
    "v_cvt_pk_bf16_f32 v239, 0, v239\n"
    "v_pk_mul_f32 v[232:233], v[232:233], v[98:99]\n"
    "v_pk_mul_f32 v[234:235], v[234:235], v[98:99]\n"
    "v_pk_mul_f32 v[236:237], v[236:237], v[98:99]\n"
    "v_pk_mul_f32 v[238:239], v[238:239], v[98:99]\n"
    "v_cvt_pk_bf16_f32 v232, v232, v233\n"
    "v_cvt_pk_bf16_f32 v233, v234, v235\n"
    "v_cvt_pk_bf16_f32 v236, v236, v237\n"
    "v_cvt_pk_bf16_f32 v237, v238, v239\n"
    "ds_write_b64 v126, v[232:233] offset:6144\n"
    "ds_write_b64 v126, v[236:237] offset:7168\n"
    "s_waitcnt vmcnt(33)\n"
    "v_cvt_f32_i32 v240, v240\n"
    "v_cvt_f32_i32 v241, v241\n"
    "v_cvt_f32_i32 v242, v242\n"
    "v_cvt_f32_i32 v243, v243\n"
    "v_cvt_f32_i32 v244, v244\n"
    "v_cvt_f32_i32 v245, v245\n"
    "v_cvt_f32_i32 v246, v246\n"
    "v_cvt_f32_i32 v247, v247\n"
    "v_cvt_pk_bf16_f32 v240, 0, v240\n"
    "v_cvt_pk_bf16_f32 v241, 0, v241\n"
    "v_cvt_pk_bf16_f32 v242, 0, v242\n"
    "v_cvt_pk_bf16_f32 v243, 0, v243\n"
    "v_cvt_pk_bf16_f32 v244, 0, v244\n"
    "v_cvt_pk_bf16_f32 v245, 0, v245\n"
    "v_cvt_pk_bf16_f32 v246, 0, v246\n"
    "v_cvt_pk_bf16_f32 v247, 0, v247\n"
    "v_pk_add_f32 v[240:241], v[240:241], v[32:33] neg_lo:[0,1] neg_hi:[0,1]\n"
    "v_pk_add_f32 v[242:243], v[242:243], v[34:35] neg_lo:[0,1] neg_hi:[0,1]\n"
    "v_pk_add_f32 v[244:245], v[244:245], v[36:37] neg_lo:[0,1] neg_hi:[0,1]\n"
    "v_pk_add_f32 v[246:247], v[246:247], v[38:39] neg_lo:[0,1] neg_hi:[0,1]\n"
    "v_cvt_pk_bf16_f32 v240, 0, v240\n"
    "v_cvt_pk_bf16_f32 v241, 0, v241\n"
    "v_cvt_pk_bf16_f32 v242, 0, v242\n"
    "v_cvt_pk_bf16_f32 v243, 0, v243\n"
    "v_cvt_pk_bf16_f32 v244, 0, v244\n"
    "v_cvt_pk_bf16_f32 v245, 0, v245\n"
    "v_cvt_pk_bf16_f32 v246, 0, v246\n"
    "v_cvt_pk_bf16_f32 v247, 0, v247\n"
    "v_pk_mul_f32 v[240:241], v[240:241], v[48:49]\n"
    "v_pk_mul_f32 v[242:243], v[242:243], v[50:51]\n"
    "v_pk_mul_f32 v[244:245], v[244:245], v[52:53]\n"
    "v_pk_mul_f32 v[246:247], v[246:247], v[54:55]\n"
    "v_cvt_pk_bf16_f32 v240, 0, v240\n"
    "v_cvt_pk_bf16_f32 v241, 0, v241\n"
    "v_cvt_pk_bf16_f32 v242, 0, v242\n"
    "v_cvt_pk_bf16_f32 v243, 0, v243\n"
    "v_cvt_pk_bf16_f32 v244, 0, v244\n"
    "v_cvt_pk_bf16_f32 v245, 0, v245\n"
    "v_cvt_pk_bf16_f32 v246, 0, v246\n"
    "v_cvt_pk_bf16_f32 v247, 0, v247\n"
    "v_pk_mul_f32 v[240:241], v[240:241], v[98:99]\n"
    "v_pk_mul_f32 v[242:243], v[242:243], v[98:99]\n"
    "v_pk_mul_f32 v[244:245], v[244:245], v[98:99]\n"
    "v_pk_mul_f32 v[246:247], v[246:247], v[98:99]\n"
    "v_cvt_pk_bf16_f32 v240, v240, v241\n"
    "v_cvt_pk_bf16_f32 v241, v242, v243\n"
    "v_cvt_pk_bf16_f32 v244, v244, v245\n"
    "v_cvt_pk_bf16_f32 v245, v246, v247\n"
    "ds_write_b64 v126, v[240:241] offset:4224\n"
    "ds_write_b64 v126, v[244:245] offset:5248\n"
    "v_cvt_f32_i32 v248, v248\n"
    "v_cvt_f32_i32 v249, v249\n"
    "v_cvt_f32_i32 v250, v250\n"
    "v_cvt_f32_i32 v251, v251\n"
    "v_cvt_f32_i32 v252, v252\n"
    "v_cvt_f32_i32 v253, v253\n"
    "v_cvt_f32_i32 v254, v254\n"
    "v_cvt_f32_i32 v255, v255\n"
    "v_cvt_pk_bf16_f32 v248, 0, v248\n"
    "v_cvt_pk_bf16_f32 v249, 0, v249\n"
    "v_cvt_pk_bf16_f32 v250, 0, v250\n"
    "v_cvt_pk_bf16_f32 v251, 0, v251\n"
    "v_cvt_pk_bf16_f32 v252, 0, v252\n"
    "v_cvt_pk_bf16_f32 v253, 0, v253\n"
    "v_cvt_pk_bf16_f32 v254, 0, v254\n"
    "v_cvt_pk_bf16_f32 v255, 0, v255\n"
    "v_pk_add_f32 v[248:249], v[248:249], v[40:41] neg_lo:[0,1] neg_hi:[0,1]\n"
    "v_pk_add_f32 v[250:251], v[250:251], v[42:43] neg_lo:[0,1] neg_hi:[0,1]\n"
    "v_pk_add_f32 v[252:253], v[252:253], v[44:45] neg_lo:[0,1] neg_hi:[0,1]\n"
    "v_pk_add_f32 v[254:255], v[254:255], v[46:47] neg_lo:[0,1] neg_hi:[0,1]\n"
    "v_cvt_pk_bf16_f32 v248, 0, v248\n"
    "v_cvt_pk_bf16_f32 v249, 0, v249\n"
    "v_cvt_pk_bf16_f32 v250, 0, v250\n"
    "v_cvt_pk_bf16_f32 v251, 0, v251\n"
    "v_cvt_pk_bf16_f32 v252, 0, v252\n"
    "v_cvt_pk_bf16_f32 v253, 0, v253\n"
    "v_cvt_pk_bf16_f32 v254, 0, v254\n"
    "v_cvt_pk_bf16_f32 v255, 0, v255\n"
    "v_pk_mul_f32 v[248:249], v[248:249], v[56:57]\n"
    "v_pk_mul_f32 v[250:251], v[250:251], v[58:59]\n"
    "v_pk_mul_f32 v[252:253], v[252:253], v[60:61]\n"
    "v_pk_mul_f32 v[254:255], v[254:255], v[62:63]\n"
    "v_cvt_pk_bf16_f32 v248, 0, v248\n"
    "v_cvt_pk_bf16_f32 v249, 0, v249\n"
    "v_cvt_pk_bf16_f32 v250, 0, v250\n"
    "v_cvt_pk_bf16_f32 v251, 0, v251\n"
    "v_cvt_pk_bf16_f32 v252, 0, v252\n"
    "v_cvt_pk_bf16_f32 v253, 0, v253\n"
    "v_cvt_pk_bf16_f32 v254, 0, v254\n"
    "v_cvt_pk_bf16_f32 v255, 0, v255\n"
    "v_pk_mul_f32 v[248:249], v[248:249], v[98:99]\n"
    "v_pk_mul_f32 v[250:251], v[250:251], v[98:99]\n"
    "v_pk_mul_f32 v[252:253], v[252:253], v[98:99]\n"
    "v_pk_mul_f32 v[254:255], v[254:255], v[98:99]\n"
    "v_cvt_pk_bf16_f32 v248, v248, v249\n"
    "v_cvt_pk_bf16_f32 v249, v250, v251\n"
    "v_cvt_pk_bf16_f32 v252, v252, v253\n"
    "v_cvt_pk_bf16_f32 v253, v254, v255\n"
    "ds_write_b64 v126, v[248:249] offset:6272\n"
    "ds_write_b64 v126, v[252:253] offset:7296\n"
    "s_getpc_b64 s[60:61]\n"
    "L_pc:\n"
    "s_add_u32 s60, s60, L_bodyA-L_pc\n"
    "s_addc_u32 s61, s61, 0\n"
    "s_lshl_b32 s62, %6, 13\n"
    "v_lshl_add_u32 v77, v0, 7, s62\n"
    "global_load_dword v77, v77, s[60:61]\n"
    "s_waitcnt lgkmcnt(0)\n"
    "s_barrier\n"
    "ds_read_b128 a[0:3], v2 offset:0\n"
    "ds_read_b128 a[4:7], v2 offset:1024\n"
    "ds_read_b128 a[8:11], v2 offset:2048\n"
    "ds_read_b128 a[12:15], v2 offset:3072\n"
    "ds_read_b128 a[16:19], v2 offset:4096\n"
    "ds_read_b128 a[20:23], v2 offset:5120\n"
    "ds_read_b128 a[24:27], v2 offset:6144\n"
    "ds_read_b128 a[28:31], v2 offset:7168\n"
    "ds_read_b128 a[32:35], v2 offset:8192\n"
    "ds_read_b128 a[36:39], v2 offset:9216\n"
    "ds_read_b128 a[40:43], v2 offset:10240\n"
    "ds_read_b128 a[44:47], v2 offset:11264\n"
    "s_waitcnt vmcnt(30)\n"
    "s_waitcnt lgkmcnt(8)\n"
    "v_mfma_f32_16x16x32_bf16 v[8:11], a[0:3], a[128:131], 0\n"
    "v_mfma_f32_16x16x32_bf16 v[8:11], a[4:7], a[132:135], v[8:11]\n"
    "v_mfma_f32_16x16x32_bf16 v[8:11], a[8:11], a[136:139], v[8:11]\n"
    "v_mfma_f32_16x16x32_bf16 v[8:11], a[12:15], a[140:143], v[8:11]\n"
    "s_nop 7\n"
    "v_max3_f32 v12, v8, v9, v10\n"
    "v_max_f32 v12, v11, v12\n"
    "v_mov_b32 v13, v12\n"
    "s_nop 1\n"
    "v_permlane16_swap_b32 v13, v12\n"
    "v_max_f32 v12, v13, v12\n"
    "v_mov_b32 v13, v12\n"
    "s_nop 1\n"
    "v_permlane32_swap_b32 v13, v12\n"
    "v_max_f32 v12, v13, v12\n"
    "v_sub_f32 v32, 0, v12\n"
    "v_sub_f32 v33, 0, v12\n"
    "v_sub_f32 v34, 0, v12\n"
    "v_sub_f32 v35, 0, v12\n"
    "s_nop 1\n"
    "v_mfma_f32_16x16x32_bf16 v[192:195], a[0:3], a[128:131], v[32:35]\n"
    "ds_read_b128 a[48:51], v2 offset:12288\n"
    "ds_read_b128 a[52:55], v2 offset:13312\n"
    "ds_read_b128 a[56:59], v2 offset:14336\n"
    "ds_read_b128 a[60:63], v2 offset:15360\n"
    "v_mfma_f32_16x16x32_bf16 v[192:195], a[4:7], a[132:135], v[192:195]\n"
    "v_mfma_f32_16x16x32_bf16 v[192:195], a[8:11], a[136:139], v[192:195]\n"
    "v_mfma_f32_16x16x32_bf16 v[192:195], a[12:15], a[140:143], v[192:195]\n"
    "s_waitcnt lgkmcnt(8)\n"
    "v_mfma_f32_16x16x32_bf16 v[196:199], a[16:19], a[128:131], v[32:35]\n"
    "ds_read_b128 a[64:67], v2 offset:16384\n"
    "ds_read_b128 a[68:71], v2 offset:17408\n"
    "ds_read_b128 a[72:75], v2 offset:18432\n"
    "ds_read_b128 a[76:79], v2 offset:19456\n"
    "v_mfma_f32_16x16x32_bf16 v[196:199], a[20:23], a[132:135], v[196:199]\n"
    "v_mfma_f32_16x16x32_bf16 v[196:199], a[24:27], a[136:139], v[196:199]\n"
    "v_mfma_f32_16x16x32_bf16 v[196:199], a[28:31], a[140:143], v[196:199]\n"
    "s_waitcnt lgkmcnt(8)\n"
    "v_mfma_f32_16x16x32_bf16 v[200:203], a[32:35], a[128:131], v[32:35]\n"
    "ds_read_b128 a[80:83], v2 offset:20480\n"
    "ds_read_b128 a[84:87], v2 offset:21504\n"
    "ds_read_b128 a[88:91], v2 offset:22528\n"
    "ds_read_b128 a[92:95], v2 offset:23552\n"
    "v_exp_f32 v192, v192\n"
    "v_mfma_f32_16x16x32_bf16 v[200:203], a[36:39], a[132:135], v[200:203]\n"
    "v_exp_f32 v193, v193\n"
    "v_exp_f32 v194, v194\n"
    "v_mfma_f32_16x16x32_bf16 v[200:203], a[40:43], a[136:139], v[200:203]\n"
    "v_cvt_pk_bf16_f32 v128, v192, v193\n"
    "v_exp_f32 v195, v195\n"
    "v_mfma_f32_16x16x32_bf16 v[200:203], a[44:47], a[140:143], v[200:203]\n"
    "v_cvt_pk_bf16_f32 v129, v194, v195\n"
    "s_waitcnt lgkmcnt(8)\n"
    "v_mfma_f32_16x16x32_bf16 v[204:207], a[48:51], a[128:131], v[32:35]\n"
    "ds_read_b128 a[96:99], v2 offset:24576\n"
    "ds_read_b128 a[100:103], v2 offset:25600\n"
    "ds_read_b128 a[104:107], v2 offset:26624\n"
    "ds_read_b128 a[108:111], v2 offset:27648\n"
    "v_exp_f32 v196, v196\n"
    "v_mfma_f32_16x16x32_bf16 v[204:207], a[52:55], a[132:135], v[204:207]\n"
    "v_exp_f32 v197, v197\n"
    "v_exp_f32 v198, v198\n"
    "v_mfma_f32_16x16x32_bf16 v[204:207], a[56:59], a[136:139], v[204:207]\n"
    "v_cvt_pk_bf16_f32 v130, v196, v197\n"
    "v_exp_f32 v199, v199\n"
    "v_mfma_f32_16x16x32_bf16 v[204:207], a[60:63], a[140:143], v[204:207]\n"
    "v_cvt_pk_bf16_f32 v131, v198, v199\n"
    "s_waitcnt lgkmcnt(8)\n"
    "v_mfma_f32_16x16x32_bf16 v[208:211], a[64:67], a[128:131], v[32:35]\n"
    "ds_read_b128 a[112:115], v2 offset:28672\n"
    "ds_read_b128 a[116:119], v2 offset:29696\n"
    "ds_read_b128 a[120:123], v2 offset:30720\n"
    "ds_read_b128 a[124:127], v2 offset:31744\n"
    "v_exp_f32 v200, v200\n"
    "v_mfma_f32_16x16x32_bf16 v[208:211], a[68:71], a[132:135], v[208:211]\n"
    "v_exp_f32 v201, v201\n"
    "v_exp_f32 v202, v202\n"
    "v_mfma_f32_16x16x32_bf16 v[208:211], a[72:75], a[136:139], v[208:211]\n"
    "v_cvt_pk_bf16_f32 v132, v200, v201\n"
    "v_exp_f32 v203, v203\n"
    "v_mfma_f32_16x16x32_bf16 v[208:211], a[76:79], a[140:143], v[208:211]\n"
    "v_cvt_pk_bf16_f32 v133, v202, v203\n"
    "v_mfma_f32_16x16x32_bf16 v[48:51], v[64:67], v[128:131], 0\n"
    "s_waitcnt lgkmcnt(8)\n"
    "v_mfma_f32_16x16x32_bf16 v[212:215], a[80:83], a[128:131], v[32:35]\n"
    "v_exp_f32 v204, v204\n"
    "v_mfma_f32_16x16x32_bf16 v[212:215], a[84:87], a[132:135], v[212:215]\n"
    "v_exp_f32 v205, v205\n"
    "v_exp_f32 v206, v206\n"
    "v_mfma_f32_16x16x32_bf16 v[212:215], a[88:91], a[136:139], v[212:215]\n"
    "v_cvt_pk_bf16_f32 v134, v204, v205\n"
    "v_exp_f32 v207, v207\n"
    "v_mfma_f32_16x16x32_bf16 v[212:215], a[92:95], a[140:143], v[212:215]\n"
    "v_cvt_pk_bf16_f32 v135, v206, v207\n"
    "s_waitcnt lgkmcnt(4)\n"
    "v_mfma_f32_16x16x32_bf16 v[216:219], a[96:99], a[128:131], v[32:35]\n"
    "v_exp_f32 v208, v208\n"
    "v_mfma_f32_16x16x32_bf16 v[216:219], a[100:103], a[132:135], v[216:219]\n"
    "v_exp_f32 v209, v209\n"
    "v_exp_f32 v210, v210\n"
    "v_mfma_f32_16x16x32_bf16 v[216:219], a[104:107], a[136:139], v[216:219]\n"
    "v_cvt_pk_bf16_f32 v136, v208, v209\n"
    "v_exp_f32 v211, v211\n"
    "v_mfma_f32_16x16x32_bf16 v[216:219], a[108:111], a[140:143], v[216:219]\n"
    "v_cvt_pk_bf16_f32 v137, v210, v211\n"
    "v_mfma_f32_16x16x32_bf16 v[48:51], v[64:67], v[132:135], v[48:51]\n"
    "s_waitcnt lgkmcnt(0)\n"
    "v_mfma_f32_16x16x32_bf16 v[220:223], a[112:115], a[128:131], v[32:35]\n"
    "v_exp_f32 v212, v212\n"
    "v_mfma_f32_16x16x32_bf16 v[220:223], a[116:119], a[132:135], v[220:223]\n"
    "v_exp_f32 v213, v213\n"
    "v_exp_f32 v214, v214\n"
    "v_mfma_f32_16x16x32_bf16 v[220:223], a[120:123], a[136:139], v[220:223]\n"
    "v_cvt_pk_bf16_f32 v138, v212, v213\n"
    "v_exp_f32 v215, v215\n"
    "v_mfma_f32_16x16x32_bf16 v[220:223], a[124:127], a[140:143], v[220:223]\n"
    "v_cvt_pk_bf16_f32 v139, v214, v215\n"
    "s_waitcnt vmcnt(25)\n"
    "s_barrier\n"
    "buffer_store_dwordx2 v[78:79], v14, s[64:67], 0 offen offset:0 sc1\n"
    "s_waitcnt vmcnt(22)\n"
    "v_mfma_f32_16x16x32_bf16 v[8:11], a[0:3], a[144:147], 0\n"
    "v_exp_f32 v216, v216\n"
    "v_mfma_f32_16x16x32_bf16 v[8:11], a[4:7], a[148:151], v[8:11]\n"
    "v_exp_f32 v217, v217\n"
    "v_mfma_f32_16x16x32_bf16 v[8:11], a[8:11], a[152:155], v[8:11]\n"
    "v_exp_f32 v218, v218\n"
    "v_mfma_f32_16x16x32_bf16 v[8:11], a[12:15], a[156:159], v[8:11]\n"
    "v_cvt_pk_bf16_f32 v140, v216, v217\n"
    "v_exp_f32 v219, v219\n"
    "s_nop 0\n"
    "v_cvt_pk_bf16_f32 v141, v218, v219\n"
    "v_mfma_f32_16x16x32_bf16 v[48:51], v[64:67], v[136:139], v[48:51]\n"
    "v_exp_f32 v220, v220\n"
    "v_exp_f32 v221, v221\n"
    "v_exp_f32 v222, v222\n"
    "v_cvt_pk_bf16_f32 v142, v220, v221\n"
    "v_exp_f32 v223, v223\n"
    "s_nop 0\n"
    "v_cvt_pk_bf16_f32 v143, v222, v223\n"
    "s_nop 1\n"
    "v_mfma_f32_16x16x32_bf16 v[48:51], v[64:67], v[140:143], v[48:51]\n"
    "v_max3_f32 v12, v8, v9, v10\n"
    "v_max_f32 v12, v11, v12\n"
    "v_mov_b32 v13, v12\n"
    "s_nop 1\n"
    "v_permlane16_swap_b32 v13, v12\n"
    "v_max_f32 v12, v13, v12\n"
    "v_mov_b32 v13, v12\n"
    "s_nop 1\n"
    "v_permlane32_swap_b32 v13, v12\n"
    "v_max_f32 v12, v13, v12\n"
    "v_sub_f32 v36, 0, v12\n"
    "v_sub_f32 v37, 0, v12\n"
    "v_sub_f32 v38, 0, v12\n"
    "v_sub_f32 v39, 0, v12\n"
    "s_nop 1\n"
    "v_mfma_f32_16x16x32_bf16 v[224:227], a[0:3], a[144:147], v[36:39]\n"
    "v_mfma_f32_16x16x32_bf16 v[224:227], a[4:7], a[148:151], v[224:227]\n"
    "v_mfma_f32_16x16x32_bf16 v[224:227], a[8:11], a[152:155], v[224:227]\n"
    "v_mfma_f32_16x16x32_bf16 v[224:227], a[12:15], a[156:159], v[224:227]\n"
    "v_mfma_f32_16x16x32_bf16 v[228:231], a[16:19], a[144:147], v[36:39]\n"
    "v_mfma_f32_16x16x32_bf16 v[228:231], a[20:23], a[148:151], v[228:231]\n"
    "v_mfma_f32_16x16x32_bf16 v[228:231], a[24:27], a[152:155], v[228:231]\n"
    "v_mfma_f32_16x16x32_bf16 v[228:231], a[28:31], a[156:159], v[228:231]\n"
    "v_mfma_f32_16x16x32_bf16 v[232:235], a[32:35], a[144:147], v[36:39]\n"
    "s_nop 2\n"
    "v_exp_f32 v224, v224\n"
    "v_mfma_f32_16x16x32_bf16 v[232:235], a[36:39], a[148:151], v[232:235]\n"
    "v_exp_f32 v225, v225\n"
    "v_exp_f32 v226, v226\n"
    "v_mfma_f32_16x16x32_bf16 v[232:235], a[40:43], a[152:155], v[232:235]\n"
    "v_cvt_pk_bf16_f32 v144, v224, v225\n"
    "v_exp_f32 v227, v227\n"
    "v_mfma_f32_16x16x32_bf16 v[232:235], a[44:47], a[156:159], v[232:235]\n"
    "v_cvt_pk_bf16_f32 v145, v226, v227\n"
    "v_mfma_f32_16x16x32_bf16 v[236:239], a[48:51], a[144:147], v[36:39]\n"
    "v_exp_f32 v228, v228\n"
    "v_mfma_f32_16x16x32_bf16 v[236:239], a[52:55], a[148:151], v[236:239]\n"
    "v_exp_f32 v229, v229\n"
    "v_exp_f32 v230, v230\n"
    "v_mfma_f32_16x16x32_bf16 v[236:239], a[56:59], a[152:155], v[236:239]\n"
    "v_cvt_pk_bf16_f32 v146, v228, v229\n"
    "v_exp_f32 v231, v231\n"
    "v_mfma_f32_16x16x32_bf16 v[236:239], a[60:63], a[156:159], v[236:239]\n"
    "v_cvt_pk_bf16_f32 v147, v230, v231\n"
    "v_mfma_f32_16x16x32_bf16 v[240:243], a[64:67], a[144:147], v[36:39]\n"
    "v_exp_f32 v232, v232\n"
    "v_mfma_f32_16x16x32_bf16 v[240:243], a[68:71], a[148:151], v[240:243]\n"
    "v_exp_f32 v233, v233\n"
    "v_exp_f32 v234, v234\n"
    "v_mfma_f32_16x16x32_bf16 v[240:243], a[72:75], a[152:155], v[240:243]\n"
    "v_cvt_pk_bf16_f32 v148, v232, v233\n"
    "v_exp_f32 v235, v235\n"
    "v_mfma_f32_16x16x32_bf16 v[240:243], a[76:79], a[156:159], v[240:243]\n"
    "v_cvt_pk_bf16_f32 v149, v234, v235\n"
    "v_mfma_f32_16x16x32_bf16 v[52:55], v[64:67], v[144:147], 0\n"
    "v_mfma_f32_16x16x32_bf16 v[244:247], a[80:83], a[144:147], v[36:39]\n"
    "v_exp_f32 v236, v236\n"
    "v_mfma_f32_16x16x32_bf16 v[244:247], a[84:87], a[148:151], v[244:247]\n"
    "v_exp_f32 v237, v237\n"
    "v_exp_f32 v238, v238\n"
    "v_mfma_f32_16x16x32_bf16 v[244:247], a[88:91], a[152:155], v[244:247]\n"
    "v_cvt_pk_bf16_f32 v150, v236, v237\n"
    "v_exp_f32 v239, v239\n"
    "v_mfma_f32_16x16x32_bf16 v[244:247], a[92:95], a[156:159], v[244:247]\n"
    "v_cvt_pk_bf16_f32 v151, v238, v239\n"
    "v_mfma_f32_16x16x32_bf16 v[248:251], a[96:99], a[144:147], v[36:39]\n"
    "v_exp_f32 v240, v240\n"
    "v_mfma_f32_16x16x32_bf16 v[248:251], a[100:103], a[148:151], v[248:251]\n"
    "v_exp_f32 v241, v241\n"
    "v_exp_f32 v242, v242\n"
    "v_mfma_f32_16x16x32_bf16 v[248:251], a[104:107], a[152:155], v[248:251]\n"
    "v_cvt_pk_bf16_f32 v152, v240, v241\n"
    "v_exp_f32 v243, v243\n"
    "v_mfma_f32_16x16x32_bf16 v[248:251], a[108:111], a[156:159], v[248:251]\n"
    "v_cvt_pk_bf16_f32 v153, v242, v243\n"
    "v_mfma_f32_16x16x32_bf16 v[52:55], v[64:67], v[148:151], v[52:55]\n"
    "v_mfma_f32_16x16x32_bf16 v[252:255], a[112:115], a[144:147], v[36:39]\n"
    "v_exp_f32 v244, v244\n"
    "v_mfma_f32_16x16x32_bf16 v[252:255], a[116:119], a[148:151], v[252:255]\n"
    "v_exp_f32 v245, v245\n"
    "v_exp_f32 v246, v246\n"
    "v_mfma_f32_16x16x32_bf16 v[252:255], a[120:123], a[152:155], v[252:255]\n"
    "v_cvt_pk_bf16_f32 v154, v244, v245\n"
    "v_exp_f32 v247, v247\n"
    "v_mfma_f32_16x16x32_bf16 v[252:255], a[124:127], a[156:159], v[252:255]\n"
    "v_cvt_pk_bf16_f32 v155, v246, v247\n"
    "s_cmp_lg_u32 %6, 0\n"
    "s_cbranch_scc1 L_np1\n"
    "buffer_load_dwordx2 v[96:97], v94, s[64:67], 0 offen offset:0 sc1\n"
    "L_np1:\n"
    "s_waitcnt vmcnt(18)\n"
    "v_mfma_f32_16x16x32_bf16 v[8:11], a[0:3], a[160:163], 0\n"
    "v_exp_f32 v248, v248\n"
    "v_mfma_f32_16x16x32_bf16 v[8:11], a[4:7], a[164:167], v[8:11]\n"
    "v_exp_f32 v249, v249\n"
    "v_mfma_f32_16x16x32_bf16 v[8:11], a[8:11], a[168:171], v[8:11]\n"
    "v_exp_f32 v250, v250\n"
    "v_mfma_f32_16x16x32_bf16 v[8:11], a[12:15], a[172:175], v[8:11]\n"
    "v_cvt_pk_bf16_f32 v156, v248, v249\n"
    "v_exp_f32 v251, v251\n"
    "s_nop 0\n"
    "v_cvt_pk_bf16_f32 v157, v250, v251\n"
    "v_mfma_f32_16x16x32_bf16 v[52:55], v[64:67], v[152:155], v[52:55]\n"
    "v_exp_f32 v252, v252\n"
    "v_exp_f32 v253, v253\n"
    "v_exp_f32 v254, v254\n"
    "v_cvt_pk_bf16_f32 v158, v252, v253\n"
    "v_exp_f32 v255, v255\n"
    "s_nop 0\n"
    "v_cvt_pk_bf16_f32 v159, v254, v255\n"
    "s_nop 1\n"
    "v_mfma_f32_16x16x32_bf16 v[52:55], v[64:67], v[156:159], v[52:55]\n"
    "v_max3_f32 v12, v8, v9, v10\n"
    "v_max_f32 v12, v11, v12\n"
    "v_mov_b32 v13, v12\n"
    "s_nop 1\n"
    "v_permlane16_swap_b32 v13, v12\n"
    "v_max_f32 v12, v13, v12\n"
    "v_mov_b32 v13, v12\n"
    "s_nop 1\n"
    "v_permlane32_swap_b32 v13, v12\n"
    "v_max_f32 v12, v13, v12\n"
    "v_sub_f32 v40, 0, v12\n"
    "v_sub_f32 v41, 0, v12\n"
    "v_sub_f32 v42, 0, v12\n"
    "v_sub_f32 v43, 0, v12\n"
    "s_nop 1\n"
    "v_mfma_f32_16x16x32_bf16 v[192:195], a[0:3], a[160:163], v[40:43]\n"
    "v_mfma_f32_16x16x32_bf16 v[192:195], a[4:7], a[164:167], v[192:195]\n"
    "v_mfma_f32_16x16x32_bf16 v[192:195], a[8:11], a[168:171], v[192:195]\n"
    "v_mfma_f32_16x16x32_bf16 v[192:195], a[12:15], a[172:175], v[192:195]\n"
    "v_mfma_f32_16x16x32_bf16 v[196:199], a[16:19], a[160:163], v[40:43]\n"
    "v_mfma_f32_16x16x32_bf16 v[196:199], a[20:23], a[164:167], v[196:199]\n"
    "v_mfma_f32_16x16x32_bf16 v[196:199], a[24:27], a[168:171], v[196:199]\n"
    "v_mfma_f32_16x16x32_bf16 v[196:199], a[28:31], a[172:175], v[196:199]\n"
    "v_mfma_f32_16x16x32_bf16 v[200:203], a[32:35], a[160:163], v[40:43]\n"
    "s_nop 2\n"
    "v_exp_f32 v192, v192\n"
    "v_mfma_f32_16x16x32_bf16 v[200:203], a[36:39], a[164:167], v[200:203]\n"
    "v_exp_f32 v193, v193\n"
    "v_exp_f32 v194, v194\n"
    "v_mfma_f32_16x16x32_bf16 v[200:203], a[40:43], a[168:171], v[200:203]\n"
    "v_cvt_pk_bf16_f32 v160, v192, v193\n"
    "v_exp_f32 v195, v195\n"
    "v_mfma_f32_16x16x32_bf16 v[200:203], a[44:47], a[172:175], v[200:203]\n"
    "v_cvt_pk_bf16_f32 v161, v194, v195\n"
    "v_mfma_f32_16x16x32_bf16 v[204:207], a[48:51], a[160:163], v[40:43]\n"
    "v_exp_f32 v196, v196\n"
    "v_mfma_f32_16x16x32_bf16 v[204:207], a[52:55], a[164:167], v[204:207]\n"
    "v_exp_f32 v197, v197\n"
    "v_exp_f32 v198, v198\n"
    "v_mfma_f32_16x16x32_bf16 v[204:207], a[56:59], a[168:171], v[204:207]\n"
    "v_cvt_pk_bf16_f32 v162, v196, v197\n"
    "v_exp_f32 v199, v199\n"
    "v_mfma_f32_16x16x32_bf16 v[204:207], a[60:63], a[172:175], v[204:207]\n"
    "v_cvt_pk_bf16_f32 v163, v198, v199\n"
    "v_mfma_f32_16x16x32_bf16 v[208:211], a[64:67], a[160:163], v[40:43]\n"
    "v_exp_f32 v200, v200\n"
    "v_mfma_f32_16x16x32_bf16 v[208:211], a[68:71], a[164:167], v[208:211]\n"
    "v_exp_f32 v201, v201\n"
    "v_exp_f32 v202, v202\n"
    "v_mfma_f32_16x16x32_bf16 v[208:211], a[72:75], a[168:171], v[208:211]\n"
    "v_cvt_pk_bf16_f32 v164, v200, v201\n"
    "v_exp_f32 v203, v203\n"
    "v_mfma_f32_16x16x32_bf16 v[208:211], a[76:79], a[172:175], v[208:211]\n"
    "v_cvt_pk_bf16_f32 v165, v202, v203\n"
    "v_mfma_f32_16x16x32_bf16 v[56:59], v[64:67], v[160:163], 0\n"
    "v_mfma_f32_16x16x32_bf16 v[212:215], a[80:83], a[160:163], v[40:43]\n"
    "v_exp_f32 v204, v204\n"
    "v_mfma_f32_16x16x32_bf16 v[212:215], a[84:87], a[164:167], v[212:215]\n"
    "v_exp_f32 v205, v205\n"
    "v_exp_f32 v206, v206\n"
    "v_mfma_f32_16x16x32_bf16 v[212:215], a[88:91], a[168:171], v[212:215]\n"
    "v_cvt_pk_bf16_f32 v166, v204, v205\n"
    "v_exp_f32 v207, v207\n"
    "v_mfma_f32_16x16x32_bf16 v[212:215], a[92:95], a[172:175], v[212:215]\n"
    "v_cvt_pk_bf16_f32 v167, v206, v207\n"
    "v_mfma_f32_16x16x32_bf16 v[216:219], a[96:99], a[160:163], v[40:43]\n"
    "v_exp_f32 v208, v208\n"
    "v_mfma_f32_16x16x32_bf16 v[216:219], a[100:103], a[164:167], v[216:219]\n"
    "v_exp_f32 v209, v209\n"
    "v_exp_f32 v210, v210\n"
    "v_mfma_f32_16x16x32_bf16 v[216:219], a[104:107], a[168:171], v[216:219]\n"
    "v_cvt_pk_bf16_f32 v168, v208, v209\n"
    "v_exp_f32 v211, v211\n"
    "v_mfma_f32_16x16x32_bf16 v[216:219], a[108:111], a[172:175], v[216:219]\n"
    "v_cvt_pk_bf16_f32 v169, v210, v211\n"
    "v_mfma_f32_16x16x32_bf16 v[56:59], v[64:67], v[164:167], v[56:59]\n"
    "v_mfma_f32_16x16x32_bf16 v[24:27], a[112:115], a[160:163], v[40:43]\n"
    "v_exp_f32 v212, v212\n"
    "v_mfma_f32_16x16x32_bf16 v[24:27], a[116:119], a[164:167], v[24:27]\n"
    "v_exp_f32 v213, v213\n"
    "v_exp_f32 v214, v214\n"
    "v_mfma_f32_16x16x32_bf16 v[24:27], a[120:123], a[168:171], v[24:27]\n"
    "v_cvt_pk_bf16_f32 v170, v212, v213\n"
    "v_exp_f32 v215, v215\n"
    "v_mfma_f32_16x16x32_bf16 v[24:27], a[124:127], a[172:175], v[24:27]\n"
    "v_cvt_pk_bf16_f32 v171, v214, v215\n"
    "s_waitcnt vmcnt(5)\n"
    "v_cvt_f32_i32 v108, v72\n"
    "v_cvt_f32_i32 v109, v73\n"
    "v_cvt_f32_i32 v110, v74\n"
    "v_cvt_f32_i32 v111, v75\n"
    "v_cvt_pk_bf16_f32 v108, 0, v108\n"
    "v_cvt_pk_bf16_f32 v109, 0, v109\n"
    "v_cvt_pk_bf16_f32 v110, 0, v110\n"
    "v_cvt_pk_bf16_f32 v111, 0, v111\n"
    "v_lshlrev_b32 v112, 16, v86\n"
    "v_and_b32 v113, 0xffff0000, v86\n"
    "v_lshlrev_b32 v114, 16, v87\n"
    "v_and_b32 v115, 0xffff0000, v87\n"
    "v_sub_f32 v108, v108, v112\n"
    "v_sub_f32 v109, v109, v113\n"
    "v_sub_f32 v110, v110, v114\n"
    "v_sub_f32 v111, v111, v115\n"
    "v_cvt_pk_bf16_f32 v108, 0, v108\n"
    "v_cvt_pk_bf16_f32 v109, 0, v109\n"
    "v_cvt_pk_bf16_f32 v110, 0, v110\n"
    "v_cvt_pk_bf16_f32 v111, 0, v111\n"
    "v_lshlrev_b32 v112, 16, v84\n"
    "v_and_b32 v113, 0xffff0000, v84\n"
    "v_lshlrev_b32 v114, 16, v85\n"
    "v_and_b32 v115, 0xffff0000, v85\n"
    "v_mul_f32 v108, v108, v112\n"
    "v_mul_f32 v109, v109, v113\n"
    "v_mul_f32 v110, v110, v114\n"
    "v_mul_f32 v111, v111, v115\n"
    "v_cvt_pk_bf16_f32 v108, 0, v108\n"
    "v_cvt_pk_bf16_f32 v109, 0, v109\n"
    "v_cvt_pk_bf16_f32 v110, 0, v110\n"
    "v_cvt_pk_bf16_f32 v111, 0, v111\n"
    "v_mul_f32 v108, s55, v108\n"
    "v_mul_f32 v109, s55, v109\n"
    "v_mul_f32 v110, s55, v110\n"
    "v_mul_f32 v111, s55, v111\n"
    "v_cvt_pk_bf16_f32 v112, v108, v109\n"
    "v_cvt_pk_bf16_f32 v113, v110, v111\n"
    "buffer_store_dwordx2 v[112:113], v90, s[40:43], s92 offen offset:0 sc1\n"
    "v_cvt_f32_i32 v108, v100\n"
    "v_cvt_f32_i32 v109, v101\n"
    "v_cvt_f32_i32 v110, v102\n"
    "v_cvt_f32_i32 v111, v103\n"
    "v_cvt_pk_bf16_f32 v108, 0, v108\n"
    "v_cvt_pk_bf16_f32 v109, 0, v109\n"
    "v_cvt_pk_bf16_f32 v110, 0, v110\n"
    "v_cvt_pk_bf16_f32 v111, 0, v111\n"
    "v_lshlrev_b32 v112, 16, v106\n"
    "v_and_b32 v113, 0xffff0000, v106\n"
    "v_lshlrev_b32 v114, 16, v107\n"
    "v_and_b32 v115, 0xffff0000, v107\n"
    "v_sub_f32 v108, v108, v112\n"
    "v_sub_f32 v109, v109, v113\n"
    "v_sub_f32 v110, v110, v114\n"
    "v_sub_f32 v111, v111, v115\n"
    "v_cvt_pk_bf16_f32 v108, 0, v108\n"
    "v_cvt_pk_bf16_f32 v109, 0, v109\n"
    "v_cvt_pk_bf16_f32 v110, 0, v110\n"
    "v_cvt_pk_bf16_f32 v111, 0, v111\n"
    "v_lshlrev_b32 v112, 16, v104\n"
    "v_and_b32 v113, 0xffff0000, v104\n"
    "v_lshlrev_b32 v114, 16, v105\n"
    "v_and_b32 v115, 0xffff0000, v105\n"
    "v_mul_f32 v108, v108, v112\n"
    "v_mul_f32 v109, v109, v113\n"
    "v_mul_f32 v110, v110, v114\n"
    "v_mul_f32 v111, v111, v115\n"
    "v_cvt_pk_bf16_f32 v112, v108, v109\n"
    "v_cvt_pk_bf16_f32 v113, v110, v111\n"
    "buffer_store_dwordx2 v[112:113], v93, s[40:43], s92 offen offset:0 sc1\n"
    "s_mov_b32 s92, 0x60000\n"
    "s_mov_b32 s93, 3072\n"
    "s_mov_b32 s95, 1536\n"
    "s_cmp_lg_u32 %6, 0\n"
    "s_cbranch_scc1 L_polled\n"
    "s_waitcnt vmcnt(2)\n"
    "v_xor_b32 v96, s96, v96\n"
    "v_xor_b32 v97, s99, v97\n"
    "v_or_b32 v96, v96, v97\n"
    "v_cmp_ne_u32 vcc, 0, v96\n"
    "s_nop 1\n"
    "s_cbranch_vccz L_ok\n"
    "L_poll:\n"
    "buffer_load_dwordx2 v[96:97], v94, s[64:67], 0 offen offset:0 sc1\n"
    "s_waitcnt vmcnt(0)\n"
    "v_xor_b32 v96, s96, v96\n"
    "v_xor_b32 v97, s99, v97\n"
    "v_or_b32 v96, v96, v97\n"
    "v_cmp_ne_u32 vcc, 0, v96\n"
    "s_nop 1\n"
    "s_cbranch_vccz L_ok\n"
    "s_sleep 1\n"
    "s_sub_u32 s98, s98, 1\n"
    "s_cmp_gt_i32 s98, 0\n"
    "s_cbranch_scc1 L_poll\n"
    "L_ok:\n"
    "s_cmp_lg_u32 %20, 0\n"
    "s_cbranch_scc1 L_polled\n"
    "buffer_store_dword v78, v95, s[64:67], s97 offen offset:0 sc1\n"
    "L_polled:\n"
    "s_barrier\n"
    "s_mov_b32 s97, 0x2580\n"
    "s_mov_b32 s96, 0x2500\n"
    "v_mov_b32 v78, 1\n"
    "s_mov_b32 m0, s53\n"
    "s_nop 0\n"
    "buffer_load_dwordx4 v4, s[40:43], s48 offen offset:0 sc1 lds\n"
    "buffer_load_dwordx4 v4, s[40:43], s48 offen offset:1024 sc1 lds\n"
    "buffer_load_dwordx4 v4, s[40:43], s48 offen offset:2048 sc1 lds\n"
    "buffer_load_dwordx4 v4, s[40:43], s48 offen offset:3072 sc1 lds\n"
    "s_waitcnt vmcnt(20)\n"
    "v_mfma_f32_16x16x32_bf16 v[8:11], a[0:3], a[176:179], 0\n"
    "v_exp_f32 v216, v216\n"
    "v_mfma_f32_16x16x32_bf16 v[8:11], a[4:7], a[180:183], v[8:11]\n"
    "v_exp_f32 v217, v217\n"
    "v_mfma_f32_16x16x32_bf16 v[8:11], a[8:11], a[184:187], v[8:11]\n"
    "v_exp_f32 v218, v218\n"
    "v_mfma_f32_16x16x32_bf16 v[8:11], a[12:15], a[188:191], v[8:11]\n"
    "v_cvt_pk_bf16_f32 v172, v216, v217\n"
    "v_exp_f32 v219, v219\n"
    "s_nop 0\n"
    "v_cvt_pk_bf16_f32 v173, v218, v219\n"
    "v_mfma_f32_16x16x32_bf16 v[56:59], v[64:67], v[168:171], v[56:59]\n"
    "v_exp_f32 v24, v24\n"
    "v_exp_f32 v25, v25\n"
    "v_exp_f32 v26, v26\n"
    "v_cvt_pk_bf16_f32 v174, v24, v25\n"
    "v_exp_f32 v27, v27\n"
    "v_max3_f32 v12, v8, v9, v10\n"
    "v_max_f32 v12, v11, v12\n"
    "v_mov_b32 v13, v12\n"
    "s_nop 1\n"
    "v_permlane16_swap_b32 v13, v12\n"
    "v_max_f32 v12, v13, v12\n"
    "v_mov_b32 v13, v12\n"
    "s_nop 1\n"
    "v_permlane32_swap_b32 v13, v12\n"
    "v_max_f32 v12, v13, v12\n"
    "v_sub_f32 v44, 0, v12\n"
    "v_sub_f32 v45, 0, v12\n"
    "v_sub_f32 v46, 0, v12\n"
    "v_sub_f32 v47, 0, v12\n"
    "s_nop 1\n"
    "v_mfma_f32_16x16x32_bf16 v[224:227], a[0:3], a[176:179], v[44:47]\n"
    "s_add_u32 m0, m0, 0x1000\n"
    "s_nop 0\n"
    "buffer_load_dwordx4 v5, s[40:43], s48 offen offset:0 sc1 lds\n"
    "v_mfma_f32_16x16x32_bf16 v[224:227], a[4:7], a[180:183], v[224:227]\n"
    "buffer_load_dwordx4 v5, s[40:43], s48 offen offset:1024 sc1 lds\n"
    "v_mfma_f32_16x16x32_bf16 v[224:227], a[8:11], a[184:187], v[224:227]\n"
    "buffer_load_dwordx4 v5, s[40:43], s48 offen offset:2048 sc1 lds\n"
    "v_mfma_f32_16x16x32_bf16 v[224:227], a[12:15], a[188:191], v[224:227]\n"
    "buffer_load_dwordx4 v5, s[40:43], s48 offen offset:3072 sc1 lds\n"
    "v_mfma_f32_16x16x32_bf16 v[228:231], a[16:19], a[176:179], v[44:47]\n"
    "s_add_u32 m0, m0, 0x1000\n"
    "s_nop 0\n"
    "buffer_load_dwordx4 v6, s[40:43], s48 offen offset:0 sc1 lds\n"
    "v_mfma_f32_16x16x32_bf16 v[228:231], a[20:23], a[180:183], v[228:231]\n"
    "buffer_load_dwordx4 v6, s[40:43], s48 offen offset:1024 sc1 lds\n"
    "v_mfma_f32_16x16x32_bf16 v[228:231], a[24:27], a[184:187], v[228:231]\n"
    "buffer_load_dwordx4 v6, s[40:43], s48 offen offset:2048 sc1 lds\n"
    "v_mfma_f32_16x16x32_bf16 v[228:231], a[28:31], a[188:191], v[228:231]\n"
    "buffer_load_dwordx4 v6, s[40:43], s48 offen offset:3072 sc1 lds\n"
    "v_mfma_f32_32x32x16_bf16 a[0:15], v[80:83], v[80:83], 0\n"
    "v_mfma_f32_16x16x32_bf16 v[232:235], a[32:35], a[176:179], v[44:47]\n"
    "s_add_u32 m0, m0, 0x1000\n"
    "s_nop 0\n"
    "buffer_load_dwordx4 v7, s[40:43], s48 offen offset:0 sc1 lds\n"
    "v_exp_f32 v224, v224\n"
    "v_mfma_f32_16x16x32_bf16 v[232:235], a[36:39], a[180:183], v[232:235]\n"
    "buffer_load_dwordx4 v7, s[40:43], s48 offen offset:1024 sc1 lds\n"
    "v_exp_f32 v225, v225\n"
    "v_exp_f32 v226, v226\n"
    "v_mfma_f32_16x16x32_bf16 v[232:235], a[40:43], a[184:187], v[232:235]\n"
    "buffer_load_dwordx4 v7, s[40:43], s48 offen offset:2048 sc1 lds\n"
    "v_cvt_pk_bf16_f32 v176, v224, v225\n"
    "v_exp_f32 v227, v227\n"
    "v_mfma_f32_16x16x32_bf16 v[232:235], a[44:47], a[188:191], v[232:235]\n"
    "buffer_load_dwordx4 v7, s[40:43], s48 offen offset:3072 sc1 lds\n"
    "v_cvt_pk_bf16_f32 v177, v226, v227\n"
    "v_mfma_f32_32x32x16_bf16 a[16:31], v[80:83], v[80:83], 0\n"
    "v_mfma_f32_16x16x32_bf16 v[236:239], a[48:51], a[176:179], v[44:47]\n"
    "v_exp_f32 v228, v228\n"
    "v_mfma_f32_16x16x32_bf16 v[236:239], a[52:55], a[180:183], v[236:239]\n"
    "v_exp_f32 v229, v229\n"
    "v_exp_f32 v230, v230\n"
    "v_mfma_f32_16x16x32_bf16 v[236:239], a[56:59], a[184:187], v[236:239]\n"
    "v_cvt_pk_bf16_f32 v178, v228, v229\n"
    "v_exp_f32 v231, v231\n"
    "v_mfma_f32_16x16x32_bf16 v[236:239], a[60:63], a[188:191], v[236:239]\n"
    "v_cvt_pk_bf16_f32 v179, v230, v231\n"
    "v_mfma_f32_32x32x16_bf16 a[32:47], v[80:83], v[80:83], 0\n"
    "v_mfma_f32_16x16x32_bf16 v[240:243], a[64:67], a[176:179], v[44:47]\n"
    "v_exp_f32 v232, v232\n"
    "v_mfma_f32_16x16x32_bf16 v[240:243], a[68:71], a[180:183], v[240:243]\n"
    "v_exp_f32 v233, v233\n"
    "v_exp_f32 v234, v234\n"
    "v_mfma_f32_16x16x32_bf16 v[240:243], a[72:75], a[184:187], v[240:243]\n"
    "v_cvt_pk_bf16_f32 v180, v232, v233\n"
    "v_exp_f32 v235, v235\n"
    "v_mfma_f32_16x16x32_bf16 v[240:243], a[76:79], a[188:191], v[240:243]\n"
    "v_cvt_pk_bf16_f32 v181, v234, v235\n"
    "v_mfma_f32_16x16x32_bf16 v[60:63], v[64:67], v[176:179], 0\n"
    "v_mfma_f32_32x32x16_bf16 a[48:63], v[80:83], v[80:83], 0\n"
    "v_mfma_f32_16x16x32_bf16 v[244:247], a[80:83], a[176:179], v[44:47]\n"
    "v_exp_f32 v236, v236\n"
    "v_mfma_f32_16x16x32_bf16 v[244:247], a[84:87], a[180:183], v[244:247]\n"
    "v_exp_f32 v237, v237\n"
    "v_exp_f32 v238, v238\n"
    "v_mfma_f32_16x16x32_bf16 v[244:247], a[88:91], a[184:187], v[244:247]\n"
    "v_cvt_pk_bf16_f32 v182, v236, v237\n"
    "v_exp_f32 v239, v239\n"
    "v_mfma_f32_16x16x32_bf16 v[244:247], a[92:95], a[188:191], v[244:247]\n"
    "v_cvt_pk_bf16_f32 v183, v238, v239\n"
    "v_mfma_f32_32x32x16_bf16 a[64:79], v[80:83], v[80:83], 0\n"
    "v_mfma_f32_16x16x32_bf16 v[248:251], a[96:99], a[176:179], v[44:47]\n"
    "v_exp_f32 v240, v240\n"
    "v_mfma_f32_16x16x32_bf16 v[248:251], a[100:103], a[180:183], v[248:251]\n"
    "v_exp_f32 v241, v241\n"
    "v_exp_f32 v242, v242\n"
    "v_mfma_f32_16x16x32_bf16 v[248:251], a[104:107], a[184:187], v[248:251]\n"
    "v_cvt_pk_bf16_f32 v184, v240, v241\n"
    "v_exp_f32 v243, v243\n"
    "v_mfma_f32_16x16x32_bf16 v[248:251], a[108:111], a[188:191], v[248:251]\n"
    "v_cvt_pk_bf16_f32 v185, v242, v243\n"
    "v_mfma_f32_16x16x32_bf16 v[60:63], v[64:67], v[180:183], v[60:63]\n"
    "v_mfma_f32_32x32x16_bf16 a[80:95], v[80:83], v[80:83], 0\n"
    "v_mfma_f32_16x16x32_bf16 v[28:31], a[112:115], a[176:179], v[44:47]\n"
    "v_exp_f32 v244, v244\n"
    "v_mfma_f32_16x16x32_bf16 v[28:31], a[116:119], a[180:183], v[28:31]\n"
    "v_exp_f32 v245, v245\n"
    "v_exp_f32 v246, v246\n"
    "v_mfma_f32_16x16x32_bf16 v[28:31], a[120:123], a[184:187], v[28:31]\n"
    "v_cvt_pk_bf16_f32 v186, v244, v245\n"
    "v_exp_f32 v247, v247\n"
    "v_mfma_f32_16x16x32_bf16 v[28:31], a[124:127], a[188:191], v[28:31]\n"
    "v_cvt_pk_bf16_f32 v187, v246, v247\n"
    "v_mfma_f32_32x32x16_bf16 a[96:111], v[80:83], v[80:83], 0\n"
    "s_waitcnt vmcnt(0)\n"
    "s_barrier\n"
    "ds_read_b128 a[192:195], v3 offset:0\n"
    "ds_read_b128 a[196:199], v3 offset:1024\n"
    "ds_read_b128 a[200:203], v3 offset:2048\n"
    "ds_read_b128 a[204:207], v3 offset:3072\n"
    "s_mov_b32 s48, 0x20000\n"
    "s_mov_b32 m0, s52\n"
    "v_exp_f32 v248, v248\n"
    "v_exp_f32 v249, v249\n"
    "v_mfma_f32_32x32x16_bf16 a[112:127], v[80:83], v[80:83], 0\n"
    "v_exp_f32 v250, v250\n"
    "v_cvt_pk_bf16_f32 v188, v248, v249\n"
    "v_exp_f32 v251, v251\n"
    "s_nop 0\n"
    "v_cvt_pk_bf16_f32 v189, v250, v251\n"
    "v_mfma_f32_16x16x32_bf16 v[60:63], v[64:67], v[184:187], v[60:63]\n"
    "s_waitcnt lgkmcnt(0)\n"
    "ds_read_b128 a[216:219], v3 offset:34816\n"
    "ds_read_b128 a[220:223], v3 offset:35840\n"
    "v_and_b32 v99, 48, v0\n"
    ".p2align 6\n"
    "L_bodyA:\n"
    "v_mfma_f32_16x16x32_bf16 v[16:19], a[192:195], a[128:131], v[32:35]\n"
    "v_cvt_pk_bf16_f32 v175, v26, v27\n"
    "s_waitcnt vmcnt(0)\n"
    "v_mfma_f32_16x16x32_bf16 v[16:19], a[196:199], a[132:135], v[16:19]\n"
    "v_exp_f32 v28, v28\n"
    "v_mfma_f32_16x16x32_bf16 v[16:19], a[200:203], a[136:139], v[16:19]\n"
    "ds_read_b128 a[208:211], v3 offset:32768\n"
    "ds_read_b128 a[212:215], v3 offset:33792\n"
    "v_mfma_f32_16x16x32_bf16 v[16:19], a[204:207], a[140:143], v[16:19]\n"
    "v_exp_f32 v29, v29\n"
    "v_mfma_f32_16x16x32_bf16 v[56:59], v[64:67], v[172:175], v[56:59]\n"
    "buffer_load_dwordx4 v4, s[40:43], s48 offen offset:0 sc1 lds\n"
    "v_cvt_f32_i32 v108, v116\n"
    "v_mfma_f32_16x16x32_bf16 v[20:23], a[204:207], a[156:159], v[36:39]\n"
    "v_cvt_pk_bf16_f32 v190, v28, v29\n"
    "v_cmp_nge_f32 vcc, s54, v48\n"
    "v_mfma_f32_16x16x32_bf16 v[20:23], a[200:203], a[152:155], v[20:23]\n"
    "v_exp_f32 v30, v30\n"
    "v_mfma_f32_16x16x32_bf16 v[20:23], a[196:199], a[148:151], v[20:23]\n"
    "s_cbranch_vccnz L_rare_A0\n"
    "L_ret_A0:\n"
    "v_add_f32 v68, v48, v68\n"
    "v_mfma_f32_16x16x32_bf16 v[20:23], a[192:195], a[144:147], v[20:23]\n"
    "v_exp_f32 v31, v31\n"
    "v_mfma_f32_16x16x32_bf16 v[24:27], a[192:195], a[160:163], v[40:43]\n"
    "v_cvt_pk_bf16_f32 v191, v30, v31\n"
    "v_cmp_nge_f32 vcc, s54, v52\n"
    "v_mfma_f32_16x16x32_bf16 v[24:27], a[196:199], a[164:167], v[24:27]\n"
    "v_exp_f32 v16, v16\n"
    "v_mfma_f32_16x16x32_bf16 v[24:27], a[200:203], a[168:171], v[24:27]\n"
    "s_cbranch_vccnz L_rare_A1\n"
    "L_ret_A1:\n"
    "v_add_f32 v69, v52, v69\n"
    "v_mfma_f32_16x16x32_bf16 v[24:27], a[204:207], a[172:175], v[24:27]\n"
    "v_exp_f32 v17, v17\n"
    "v_mfma_f32_16x16x32_bf16 v[60:63], v[64:67], v[188:191], v[60:63]\n"
    "buffer_load_dwordx4 v4, s[40:43], s48 offen offset:1024 sc1 lds\n"
    "v_cvt_f32_i32 v109, v117\n"
    "v_mfma_f32_16x16x32_bf16 v[28:31], a[204:207], a[188:191], v[44:47]\n"
    "v_cvt_pk_bf16_f32 v192, v16, v17\n"
    "s_waitcnt lgkmcnt(0)\n"
    "v_mfma_f32_16x16x32_bf16 v[28:31], a[200:203], a[184:187], v[28:31]\n"
    "v_exp_f32 v18, v18\n"
    "v_mfma_f32_16x16x32_bf16 v[28:31], a[196:199], a[180:183], v[28:31]\n"
    "ds_read_b128 a[200:203], v3 offset:6144\n"
    "ds_read_b128 a[204:207], v3 offset:7168\n"
    "v_mfma_f32_16x16x32_bf16 v[28:31], a[192:195], a[176:179], v[28:31]\n"
    "v_exp_f32 v19, v19\n"
    "v_mfma_f32_16x16x32_bf16 a[0:3], v[128:131], a[208:211], a[0:3]\n"
    "v_cvt_pk_bf16_f32 v193, v18, v19\n"
    "v_cvt_f32_i32 v110, v118\n"
    "v_mfma_f32_16x16x32_bf16 a[0:3], v[132:135], a[212:215], a[0:3]\n"
    "v_exp_f32 v20, v20\n"
    "v_mfma_f32_16x16x32_bf16 a[0:3], v[136:139], a[216:219], a[0:3]\n"
    "ds_read_b128 a[192:195], v3 offset:4096\n"
    "ds_read_b128 a[196:199], v3 offset:5120\n"
    "v_mfma_f32_16x16x32_bf16 a[0:3], v[140:143], a[220:223], a[0:3]\n"
    "v_exp_f32 v21, v21\n"
    "v_mfma_f32_16x16x32_bf16 a[4:7], v[156:159], a[220:223], a[4:7]\n"
    "v_cvt_pk_bf16_f32 v208, v20, v21\n"
    "v_cmp_nge_f32 vcc, s54, v56\n"
    "v_mfma_f32_16x16x32_bf16 a[4:7], v[152:155], a[216:219], a[4:7]\n"
    "v_exp_f32 v22, v22\n"
    "v_mfma_f32_16x16x32_bf16 a[4:7], v[148:151], a[212:215], a[4:7]\n"
    "s_cbranch_vccnz L_rare_A2\n"
    "L_ret_A2:\n"
    "v_add_f32 v70, v56, v70\n"
    "v_mfma_f32_16x16x32_bf16 a[4:7], v[144:147], a[208:211], a[4:7]\n"
    "v_exp_f32 v23, v23\n"
    "v_mfma_f32_16x16x32_bf16 a[8:11], v[160:163], a[208:211], a[8:11]\n"
    "v_cvt_pk_bf16_f32 v209, v22, v23\n"
    "v_cmp_nge_f32 vcc, s54, v60\n"
    "v_mfma_f32_16x16x32_bf16 a[8:11], v[164:167], a[212:215], a[8:11]\n"
    "v_exp_f32 v24, v24\n"
    "v_mfma_f32_16x16x32_bf16 a[8:11], v[168:171], a[216:219], a[8:11]\n"
    "s_cbranch_vccnz L_rare_A3\n"
    "L_ret_A3:\n"
    "v_add_f32 v71, v60, v71\n"
    "v_mfma_f32_16x16x32_bf16 a[8:11], v[172:175], a[220:223], a[8:11]\n"
    "v_exp_f32 v25, v25\n"
    "v_mfma_f32_16x16x32_bf16 a[12:15], v[188:191], a[220:223], a[12:15]\n"
    "v_cvt_pk_bf16_f32 v224, v24, v25\n"
    "s_waitcnt lgkmcnt(0)\n"
    "v_mfma_f32_16x16x32_bf16 a[12:15], v[184:187], a[216:219], a[12:15]\n"
    "v_exp_f32 v26, v26\n"
    "v_mfma_f32_16x16x32_bf16 a[12:15], v[180:183], a[212:215], a[12:15]\n"
    "ds_read_b128 a[216:219], v3 offset:38912\n"
    "ds_read_b128 a[220:223], v3 offset:39936\n"
    "v_mfma_f32_16x16x32_bf16 a[12:15], v[176:179], a[208:211], a[12:15]\n"
    "v_exp_f32 v27, v27\n"
    "v_mfma_f32_16x16x32_bf16 v[16:19], a[192:195], a[128:131], v[32:35]\n"
    "v_cvt_pk_bf16_f32 v225, v26, v27\n"
    "v_cvt_f32_i32 v111, v119\n"
    "v_mfma_f32_16x16x32_bf16 v[16:19], a[196:199], a[132:135], v[16:19]\n"
    "v_exp_f32 v28, v28\n"
    "v_mfma_f32_16x16x32_bf16 v[16:19], a[200:203], a[136:139], v[16:19]\n"
    "ds_read_b128 a[208:211], v3 offset:36864\n"
    "ds_read_b128 a[212:215], v3 offset:37888\n"
    "v_mfma_f32_16x16x32_bf16 v[16:19], a[204:207], a[140:143], v[16:19]\n"
    "v_exp_f32 v29, v29\n"
    "v_mfma_f32_16x16x32_bf16 v[20:23], a[204:207], a[156:159], v[36:39]\n"
    "v_cvt_pk_bf16_f32 v240, v28, v29\n"
    "v_cvt_pk_bf16_f32 v108, 0, v108\n"
    "v_mfma_f32_16x16x32_bf16 v[20:23], a[200:203], a[152:155], v[20:23]\n"
    "v_exp_f32 v30, v30\n"
    "v_mfma_f32_16x16x32_bf16 v[20:23], a[196:199], a[148:151], v[20:23]\n"
    "buffer_load_dwordx4 v4, s[40:43], s48 offen offset:2048 sc1 lds\n"
    "v_cvt_pk_bf16_f32 v109, 0, v109\n"
    "v_mfma_f32_16x16x32_bf16 v[20:23], a[192:195], a[144:147], v[20:23]\n"
    "v_exp_f32 v31, v31\n"
    "v_mfma_f32_16x16x32_bf16 v[24:27], a[192:195], a[160:163], v[40:43]\n"
    "v_cvt_pk_bf16_f32 v241, v30, v31\n"
    "v_cvt_pk_bf16_f32 v110, 0, v110\n"
    "v_mfma_f32_16x16x32_bf16 v[24:27], a[196:199], a[164:167], v[24:27]\n"
    "v_exp_f32 v16, v16\n"
    "v_mfma_f32_16x16x32_bf16 v[24:27], a[200:203], a[168:171], v[24:27]\n"
    "buffer_load_dwordx4 v4, s[40:43], s48 offen offset:3072 sc1 lds\n"
    "s_add_u32 m0, m0, 0x1000\n"
    "v_mfma_f32_16x16x32_bf16 v[24:27], a[204:207], a[172:175], v[24:27]\n"
    "v_exp_f32 v17, v17\n"
    "v_mfma_f32_16x16x32_bf16 v[28:31], a[204:207], a[188:191], v[44:47]\n"
    "v_cvt_pk_bf16_f32 v194, v16, v17\n"
    "s_waitcnt lgkmcnt(0)\n"
    "v_mfma_f32_16x16x32_bf16 v[28:31], a[200:203], a[184:187], v[28:31]\n"
    "v_exp_f32 v18, v18\n"
    "v_mfma_f32_16x16x32_bf16 v[28:31], a[196:199], a[180:183], v[28:31]\n"
    "ds_read_b128 a[200:203], v3 offset:10240\n"
    "ds_read_b128 a[204:207], v3 offset:11264\n"
    "v_mfma_f32_16x16x32_bf16 v[28:31], a[192:195], a[176:179], v[28:31]\n"
    "v_exp_f32 v19, v19\n"
    "v_mfma_f32_16x16x32_bf16 a[16:19], v[128:131], a[208:211], a[16:19]\n"
    "v_cvt_pk_bf16_f32 v195, v18, v19\n"
    "v_cvt_pk_bf16_f32 v111, 0, v111\n"
    "v_mfma_f32_16x16x32_bf16 a[16:19], v[132:135], a[212:215], a[16:19]\n"
    "v_exp_f32 v20, v20\n"
    "v_mfma_f32_16x16x32_bf16 a[16:19], v[136:139], a[216:219], a[16:19]\n"
    "ds_read_b128 a[192:195], v3 offset:8192\n"
    "ds_read_b128 a[196:199], v3 offset:9216\n"
    "v_mfma_f32_16x16x32_bf16 a[16:19], v[140:143], a[220:223], a[16:19]\n"
    "v_exp_f32 v21, v21\n"
    "v_mfma_f32_16x16x32_bf16 v[48:51], v[64:67], v[192:195], 0\n"
    "buffer_load_dwordx4 v5, s[40:43], s48 offen offset:0 sc1 lds\n"
    "v_lshlrev_b32 v112, 16, v122\n"
    "v_mfma_f32_16x16x32_bf16 a[20:23], v[156:159], a[220:223], a[20:23]\n"
    "v_cvt_pk_bf16_f32 v210, v20, v21\n"
    "v_and_b32 v113, 0xffff0000, v122\n"
    "v_mfma_f32_16x16x32_bf16 a[20:23], v[152:155], a[216:219], a[20:23]\n"
    "v_exp_f32 v22, v22\n"
    "v_mfma_f32_16x16x32_bf16 a[20:23], v[148:151], a[212:215], a[20:23]\n"
    "buffer_load_dwordx4 v5, s[40:43], s48 offen offset:1024 sc1 lds\n"
    "v_lshlrev_b32 v114, 16, v123\n"
    "v_mfma_f32_16x16x32_bf16 a[20:23], v[144:147], a[208:211], a[20:23]\n"
    "v_exp_f32 v23, v23\n"
    "v_mfma_f32_16x16x32_bf16 a[24:27], v[160:163], a[208:211], a[24:27]\n"
    "v_cvt_pk_bf16_f32 v211, v22, v23\n"
    "v_and_b32 v115, 0xffff0000, v123\n"
    "v_mfma_f32_16x16x32_bf16 a[24:27], v[164:167], a[212:215], a[24:27]\n"
    "v_exp_f32 v24, v24\n"
    "v_mfma_f32_16x16x32_bf16 a[24:27], v[168:171], a[216:219], a[24:27]\n"
    "buffer_load_dwordx4 v5, s[40:43], s48 offen offset:2048 sc1 lds\n"
    "v_sub_f32 v108, v108, v112\n"
    "v_mfma_f32_16x16x32_bf16 a[24:27], v[172:175], a[220:223], a[24:27]\n"
    "v_exp_f32 v25, v25\n"
    "v_mfma_f32_16x16x32_bf16 v[52:55], v[64:67], v[208:211], 0\n"
    "buffer_load_dwordx4 v5, s[40:43], s48 offen offset:3072 sc1 lds\n"
    "s_add_u32 m0, m0, 0x1000\n"
    "v_mfma_f32_16x16x32_bf16 a[28:31], v[188:191], a[220:223], a[28:31]\n"
    "v_cvt_pk_bf16_f32 v226, v24, v25\n"
    "s_waitcnt lgkmcnt(0)\n"
    "v_mfma_f32_16x16x32_bf16 a[28:31], v[184:187], a[216:219], a[28:31]\n"
    "v_exp_f32 v26, v26\n"
    "v_mfma_f32_16x16x32_bf16 a[28:31], v[180:183], a[212:215], a[28:31]\n"
    "ds_read_b128 a[216:219], v3 offset:43008\n"
    "ds_read_b128 a[220:223], v3 offset:44032\n"
    "v_mfma_f32_16x16x32_bf16 a[28:31], v[176:179], a[208:211], a[28:31]\n"
    "v_exp_f32 v27, v27\n"
    "v_mfma_f32_16x16x32_bf16 v[16:19], a[192:195], a[128:131], v[32:35]\n"
    "v_cvt_pk_bf16_f32 v227, v26, v27\n"
    "v_sub_f32 v109, v109, v113\n"
    "v_mfma_f32_16x16x32_bf16 v[16:19], a[196:199], a[132:135], v[16:19]\n"
    "v_exp_f32 v28, v28\n"
    "v_mfma_f32_16x16x32_bf16 v[16:19], a[200:203], a[136:139], v[16:19]\n"
    "ds_read_b128 a[208:211], v3 offset:40960\n"
    "ds_read_b128 a[212:215], v3 offset:41984\n"
    "v_mfma_f32_16x16x32_bf16 v[16:19], a[204:207], a[140:143], v[16:19]\n"
    "v_exp_f32 v29, v29\n"
    "v_mfma_f32_16x16x32_bf16 v[56:59], v[64:67], v[224:227], 0\n"
    "v_sub_f32 v110, v110, v114\n"
    "v_sub_f32 v111, v111, v115\n"
    "v_mfma_f32_16x16x32_bf16 v[20:23], a[204:207], a[156:159], v[36:39]\n"
    "v_cvt_pk_bf16_f32 v242, v28, v29\n"
    "v_cvt_pk_bf16_f32 v108, 0, v108\n"
    "v_mfma_f32_16x16x32_bf16 v[20:23], a[200:203], a[152:155], v[20:23]\n"
    "v_exp_f32 v30, v30\n"
    "v_mfma_f32_16x16x32_bf16 v[20:23], a[196:199], a[148:151], v[20:23]\n"
    "buffer_load_dwordx4 v6, s[40:43], s48 offen offset:0 sc1 lds\n"
    "v_cvt_pk_bf16_f32 v109, 0, v109\n"
    "v_mfma_f32_16x16x32_bf16 v[20:23], a[192:195], a[144:147], v[20:23]\n"
    "v_exp_f32 v31, v31\n"
    "v_mfma_f32_16x16x32_bf16 v[24:27], a[192:195], a[160:163], v[40:43]\n"
    "v_cvt_pk_bf16_f32 v243, v30, v31\n"
    "v_cvt_pk_bf16_f32 v110, 0, v110\n"
    "v_mfma_f32_16x16x32_bf16 v[24:27], a[196:199], a[164:167], v[24:27]\n"
    "v_exp_f32 v16, v16\n"
    "v_mfma_f32_16x16x32_bf16 v[24:27], a[200:203], a[168:171], v[24:27]\n"
    "buffer_load_dwordx4 v6, s[40:43], s48 offen offset:1024 sc1 lds\n"
    "v_cvt_pk_bf16_f32 v111, 0, v111\n"
    "v_mfma_f32_16x16x32_bf16 v[24:27], a[204:207], a[172:175], v[24:27]\n"
    "v_exp_f32 v17, v17\n"
    "v_mfma_f32_16x16x32_bf16 v[60:63], v[64:67], v[240:243], 0\n"
    "v_lshlrev_b32 v112, 16, v120\n"
    "v_and_b32 v113, 0xffff0000, v120\n"
    "v_mfma_f32_16x16x32_bf16 v[28:31], a[204:207], a[188:191], v[44:47]\n"
    "v_cvt_pk_bf16_f32 v196, v16, v17\n"
    "s_waitcnt lgkmcnt(0)\n"
    "v_mfma_f32_16x16x32_bf16 v[28:31], a[200:203], a[184:187], v[28:31]\n"
    "v_exp_f32 v18, v18\n"
    "v_mfma_f32_16x16x32_bf16 v[28:31], a[196:199], a[180:183], v[28:31]\n"
    "ds_read_b128 a[200:203], v3 offset:14336\n"
    "ds_read_b128 a[204:207], v3 offset:15360\n"
    "v_mfma_f32_16x16x32_bf16 v[28:31], a[192:195], a[176:179], v[28:31]\n"
    "v_exp_f32 v19, v19\n"
    "v_mfma_f32_16x16x32_bf16 a[32:35], v[128:131], a[208:211], a[32:35]\n"
    "v_cvt_pk_bf16_f32 v197, v18, v19\n"
    "v_lshlrev_b32 v114, 16, v121\n"
    "v_mfma_f32_16x16x32_bf16 a[32:35], v[132:135], a[212:215], a[32:35]\n"
    "v_exp_f32 v20, v20\n"
    "v_mfma_f32_16x16x32_bf16 a[32:35], v[136:139], a[216:219], a[32:35]\n"
    "ds_read_b128 a[192:195], v3 offset:12288\n"
    "ds_read_b128 a[196:199], v3 offset:13312\n"
    "v_mfma_f32_16x16x32_bf16 a[32:35], v[140:143], a[220:223], a[32:35]\n"
    "v_exp_f32 v21, v21\n"
    "v_mfma_f32_16x16x32_bf16 a[36:39], v[156:159], a[220:223], a[36:39]\n"
    "v_cvt_pk_bf16_f32 v212, v20, v21\n"
    "v_and_b32 v115, 0xffff0000, v121\n"
    "v_mfma_f32_16x16x32_bf16 a[36:39], v[152:155], a[216:219], a[36:39]\n"
    "v_exp_f32 v22, v22\n"
    "v_mfma_f32_16x16x32_bf16 a[36:39], v[148:151], a[212:215], a[36:39]\n"
    "buffer_load_dwordx4 v6, s[40:43], s48 offen offset:2048 sc1 lds\n"
    "v_mul_f32 v108, v108, v112\n"
    "v_mfma_f32_16x16x32_bf16 a[36:39], v[144:147], a[208:211], a[36:39]\n"
    "v_exp_f32 v23, v23\n"
    "v_mfma_f32_16x16x32_bf16 a[40:43], v[160:163], a[208:211], a[40:43]\n"
    "v_cvt_pk_bf16_f32 v213, v22, v23\n"
    "v_mul_f32 v109, v109, v113\n"
    "v_mfma_f32_16x16x32_bf16 a[40:43], v[164:167], a[212:215], a[40:43]\n"
    "v_exp_f32 v24, v24\n"
    "v_mfma_f32_16x16x32_bf16 a[40:43], v[168:171], a[216:219], a[40:43]\n"
    "buffer_load_dwordx4 v6, s[40:43], s48 offen offset:3072 sc1 lds\n"
    "s_add_u32 m0, m0, 0x1000\n"
    "v_mfma_f32_16x16x32_bf16 a[40:43], v[172:175], a[220:223], a[40:43]\n"
    "v_exp_f32 v25, v25\n"
    "v_mfma_f32_16x16x32_bf16 a[44:47], v[188:191], a[220:223], a[44:47]\n"
    "v_cvt_pk_bf16_f32 v228, v24, v25\n"
    "s_waitcnt lgkmcnt(0)\n"
    "v_mfma_f32_16x16x32_bf16 a[44:47], v[184:187], a[216:219], a[44:47]\n"
    "v_exp_f32 v26, v26\n"
    "v_mfma_f32_16x16x32_bf16 a[44:47], v[180:183], a[212:215], a[44:47]\n"
    "ds_read_b128 a[216:219], v3 offset:47104\n"
    "ds_read_b128 a[220:223], v3 offset:48128\n"
    "v_mfma_f32_16x16x32_bf16 a[44:47], v[176:179], a[208:211], a[44:47]\n"
    "v_exp_f32 v27, v27\n"
    "v_mfma_f32_16x16x32_bf16 v[16:19], a[192:195], a[128:131], v[32:35]\n"
    "v_cvt_pk_bf16_f32 v229, v26, v27\n"
    "v_mul_f32 v110, v110, v114\n"
    "v_mfma_f32_16x16x32_bf16 v[16:19], a[196:199], a[132:135], v[16:19]\n"
    "v_exp_f32 v28, v28\n"
    "v_mfma_f32_16x16x32_bf16 v[16:19], a[200:203], a[136:139], v[16:19]\n"
    "ds_read_b128 a[208:211], v3 offset:45056\n"
    "ds_read_b128 a[212:215], v3 offset:46080\n"
    "v_mfma_f32_16x16x32_bf16 v[16:19], a[204:207], a[140:143], v[16:19]\n"
    "v_exp_f32 v29, v29\n"
    "v_mfma_f32_16x16x32_bf16 v[20:23], a[204:207], a[156:159], v[36:39]\n"
    "v_cvt_pk_bf16_f32 v244, v28, v29\n"
    "v_mul_f32 v111, v111, v115\n"
    "v_mfma_f32_16x16x32_bf16 v[20:23], a[200:203], a[152:155], v[20:23]\n"
    "v_exp_f32 v30, v30\n"
    "v_mfma_f32_16x16x32_bf16 v[20:23], a[196:199], a[148:151], v[20:23]\n"
    "buffer_load_dwordx4 v7, s[40:43], s48 offen offset:0 sc1 lds\n"
    "v_cvt_pk_bf16_f32 v108, 0, v108\n"
    "v_mfma_f32_16x16x32_bf16 v[20:23], a[192:195], a[144:147], v[20:23]\n"
    "v_exp_f32 v31, v31\n"
    "v_mfma_f32_16x16x32_bf16 v[24:27], a[192:195], a[160:163], v[40:43]\n"
    "v_cvt_pk_bf16_f32 v245, v30, v31\n"
    "v_cvt_pk_bf16_f32 v109, 0, v109\n"
    "v_mfma_f32_16x16x32_bf16 v[24:27], a[196:199], a[164:167], v[24:27]\n"
    "v_exp_f32 v16, v16\n"
    "v_mfma_f32_16x16x32_bf16 v[24:27], a[200:203], a[168:171], v[24:27]\n"
    "buffer_load_dwordx4 v7, s[40:43], s48 offen offset:1024 sc1 lds\n"
    "v_cvt_pk_bf16_f32 v110, 0, v110\n"
    "v_mfma_f32_16x16x32_bf16 v[24:27], a[204:207], a[172:175], v[24:27]\n"
    "v_exp_f32 v17, v17\n"
    "v_mfma_f32_16x16x32_bf16 v[28:31], a[204:207], a[188:191], v[44:47]\n"
    "v_cvt_pk_bf16_f32 v198, v16, v17\n"
    "s_waitcnt lgkmcnt(0)\n"
    "v_mfma_f32_16x16x32_bf16 v[28:31], a[200:203], a[184:187], v[28:31]\n"
    "v_exp_f32 v18, v18\n"
    "v_mfma_f32_16x16x32_bf16 v[28:31], a[196:199], a[180:183], v[28:31]\n"
    "ds_read_b128 a[200:203], v3 offset:18432\n"
    "ds_read_b128 a[204:207], v3 offset:19456\n"
    "v_mfma_f32_16x16x32_bf16 v[28:31], a[192:195], a[176:179], v[28:31]\n"
    "v_exp_f32 v19, v19\n"
    "v_mfma_f32_16x16x32_bf16 a[48:51], v[128:131], a[208:211], a[48:51]\n"
    "v_cvt_pk_bf16_f32 v199, v18, v19\n"
    "v_cvt_pk_bf16_f32 v111, 0, v111\n"
    "v_mfma_f32_16x16x32_bf16 a[48:51], v[132:135], a[212:215], a[48:51]\n"
    "v_exp_f32 v20, v20\n"
    "v_mfma_f32_16x16x32_bf16 a[48:51], v[136:139], a[216:219], a[48:51]\n"
    "ds_read_b128 a[192:195], v3 offset:16384\n"
    "ds_read_b128 a[196:199], v3 offset:17408\n"
    "v_mfma_f32_16x16x32_bf16 a[48:51], v[140:143], a[220:223], a[48:51]\n"
    "v_exp_f32 v21, v21\n"
    "v_mfma_f32_16x16x32_bf16 v[48:51], v[64:67], v[196:199], v[48:51]\n"
    "v_mul_f32 v108, s55, v108\n"
    "v_mul_f32 v109, s55, v109\n"
    "v_mfma_f32_16x16x32_bf16 a[52:55], v[156:159], a[220:223], a[52:55]\n"
    "v_cvt_pk_bf16_f32 v214, v20, v21\n"
    "v_mul_f32 v110, s55, v110\n"
    "v_mfma_f32_16x16x32_bf16 a[52:55], v[152:155], a[216:219], a[52:55]\n"
    "v_exp_f32 v22, v22\n"
    "v_mfma_f32_16x16x32_bf16 a[52:55], v[148:151], a[212:215], a[52:55]\n"
    "buffer_load_dwordx4 v7, s[40:43], s48 offen offset:2048 sc1 lds\n"
    "v_mul_f32 v111, s55, v111\n"
    "v_mfma_f32_16x16x32_bf16 a[52:55], v[144:147], a[208:211], a[52:55]\n"
    "v_exp_f32 v23, v23\n"
    "v_mfma_f32_16x16x32_bf16 a[56:59], v[160:163], a[208:211], a[56:59]\n"
    "v_cvt_pk_bf16_f32 v215, v22, v23\n"
    "v_cvt_pk_bf16_f32 v112, v108, v109\n"
    "v_mfma_f32_16x16x32_bf16 a[56:59], v[164:167], a[212:215], a[56:59]\n"
    "v_exp_f32 v24, v24\n"
    "v_mfma_f32_16x16x32_bf16 a[56:59], v[168:171], a[216:219], a[56:59]\n"
    "buffer_load_dwordx4 v7, s[40:43], s48 offen offset:3072 sc1 lds\n"
    "v_cvt_pk_bf16_f32 v113, v110, v111\n"
    "v_mfma_f32_16x16x32_bf16 a[56:59], v[172:175], a[220:223], a[56:59]\n"
    "v_exp_f32 v25, v25\n"
    "v_mfma_f32_16x16x32_bf16 v[52:55], v[64:67], v[212:215], v[52:55]\n"
    "v_mfma_f32_16x16x32_bf16 a[60:63], v[188:191], a[220:223], a[60:63]\n"
    "v_cvt_pk_bf16_f32 v230, v24, v25\n"
    "s_waitcnt lgkmcnt(0)\n"
    "v_mfma_f32_16x16x32_bf16 a[60:63], v[184:187], a[216:219], a[60:63]\n"
    "v_exp_f32 v26, v26\n"
    "v_mfma_f32_16x16x32_bf16 a[60:63], v[180:183], a[212:215], a[60:63]\n"
    "ds_read_b128 a[216:219], v3 offset:51200\n"
    "ds_read_b128 a[220:223], v3 offset:52224\n"
    "v_mfma_f32_16x16x32_bf16 a[60:63], v[176:179], a[208:211], a[60:63]\n"
    "v_exp_f32 v27, v27\n"
    "v_mfma_f32_16x16x32_bf16 v[16:19], a[192:195], a[128:131], v[32:35]\n"
    "v_cvt_pk_bf16_f32 v231, v26, v27\n"
    "v_mfma_f32_16x16x32_bf16 v[16:19], a[196:199], a[132:135], v[16:19]\n"
    "v_exp_f32 v28, v28\n"
    "v_mfma_f32_16x16x32_bf16 v[16:19], a[200:203], a[136:139], v[16:19]\n"
    "ds_read_b128 a[208:211], v3 offset:49152\n"
    "ds_read_b128 a[212:215], v3 offset:50176\n"
    "v_mfma_f32_16x16x32_bf16 v[16:19], a[204:207], a[140:143], v[16:19]\n"
    "v_exp_f32 v29, v29\n"
    "v_mfma_f32_16x16x32_bf16 v[56:59], v[64:67], v[228:231], v[56:59]\n"
    "buffer_load_dword v96, off, s[64:67], s96 offset:0 sc1\n"
    "buffer_atomic_add v78, v95, s[64:67], s97 offen\n"
    "v_mfma_f32_16x16x32_bf16 v[20:23], a[204:207], a[156:159], v[36:39]\n"
    "buffer_store_dwordx2 v[112:113], v90, s[40:43], s92 offen offset:0 sc1\n"
    "v_cvt_pk_bf16_f32 v246, v28, v29\n"
    "v_mfma_f32_16x16x32_bf16 v[20:23], a[200:203], a[152:155], v[20:23]\n"
    "v_exp_f32 v30, v30\n"
    "v_mfma_f32_16x16x32_bf16 v[20:23], a[196:199], a[148:151], v[20:23]\n"
    "buffer_load_dword v100, v91, s[72:75], s94 offen offset:0\n"
    "v_mfma_f32_16x16x32_bf16 v[20:23], a[192:195], a[144:147], v[20:23]\n"
    "v_exp_f32 v31, v31\n"
    "v_mfma_f32_16x16x32_bf16 v[24:27], a[192:195], a[160:163], v[40:43]\n"
    "buffer_load_dword v101, v91, s[72:75], s94 offen offset:512\n"
    "v_cvt_pk_bf16_f32 v247, v30, v31\n"
    "v_mfma_f32_16x16x32_bf16 v[24:27], a[196:199], a[164:167], v[24:27]\n"
    "v_exp_f32 v16, v16\n"
    "v_mfma_f32_16x16x32_bf16 v[24:27], a[200:203], a[168:171], v[24:27]\n"
    "buffer_load_dword v102, v91, s[72:75], s94 offen offset:1024\n"
    "v_mfma_f32_16x16x32_bf16 v[24:27], a[204:207], a[172:175], v[24:27]\n"
    "v_exp_f32 v17, v17\n"
    "v_mfma_f32_16x16x32_bf16 v[60:63], v[64:67], v[244:247], v[60:63]\n"
    "buffer_load_dword v103, v91, s[72:75], s94 offen offset:1536\n"
    "v_mfma_f32_16x16x32_bf16 v[28:31], a[204:207], a[188:191], v[44:47]\n"
    "v_cvt_pk_bf16_f32 v200, v16, v17\n"
    "s_waitcnt lgkmcnt(0)\n"
    "v_mfma_f32_16x16x32_bf16 v[28:31], a[200:203], a[184:187], v[28:31]\n"
    "v_exp_f32 v18, v18\n"
    "v_mfma_f32_16x16x32_bf16 v[28:31], a[196:199], a[180:183], v[28:31]\n"
    "ds_read_b128 a[200:203], v3 offset:22528\n"
    "ds_read_b128 a[204:207], v3 offset:23552\n"
    "v_mfma_f32_16x16x32_bf16 v[28:31], a[192:195], a[176:179], v[28:31]\n"
    "v_exp_f32 v19, v19\n"
    "v_mfma_f32_16x16x32_bf16 a[64:67], v[128:131], a[208:211], a[64:67]\n"
    "buffer_load_dwordx2 v[104:105], v92, s[84:87], s95 offen offset:0\n"
    "v_cvt_pk_bf16_f32 v201, v18, v19\n"
    "v_mfma_f32_16x16x32_bf16 a[64:67], v[132:135], a[212:215], a[64:67]\n"
    "v_exp_f32 v20, v20\n"
    "v_mfma_f32_16x16x32_bf16 a[64:67], v[136:139], a[216:219], a[64:67]\n"
    "ds_read_b128 a[192:195], v3 offset:20480\n"
    "ds_read_b128 a[196:199], v3 offset:21504\n"
    "v_mfma_f32_16x16x32_bf16 a[64:67], v[140:143], a[220:223], a[64:67]\n"
    "v_exp_f32 v21, v21\n"
    "v_mfma_f32_16x16x32_bf16 a[68:71], v[156:159], a[220:223], a[68:71]\n"
    "buffer_load_dwordx2 v[106:107], v92, s[88:91], s95 offen offset:0\n"
    "v_cvt_pk_bf16_f32 v216, v20, v21\n"
    "v_mfma_f32_16x16x32_bf16 a[68:71], v[152:155], a[216:219], a[68:71]\n"
    "v_exp_f32 v22, v22\n"
    "v_mfma_f32_16x16x32_bf16 a[68:71], v[148:151], a[212:215], a[68:71]\n"
    "s_add_u32 s92, s92, 0x20000\n"
    "s_add_u32 s93, s93, 0x400\n"
    "v_mfma_f32_16x16x32_bf16 a[68:71], v[144:147], a[208:211], a[68:71]\n"
    "v_exp_f32 v23, v23\n"
    "v_mfma_f32_16x16x32_bf16 a[72:75], v[160:163], a[208:211], a[72:75]\n"
    "v_cvt_pk_bf16_f32 v217, v22, v23\n"
    "s_min_u32 s92, s92, 0xe0000\n"
    "v_mfma_f32_16x16x32_bf16 a[72:75], v[164:167], a[212:215], a[72:75]\n"
    "v_exp_f32 v24, v24\n"
    "v_mfma_f32_16x16x32_bf16 a[72:75], v[168:171], a[216:219], a[72:75]\n"
    "s_min_u32 s93, s93, 0x1c00\n"
    "v_mfma_f32_16x16x32_bf16 a[72:75], v[172:175], a[220:223], a[72:75]\n"
    "v_exp_f32 v25, v25\n"
    "v_mfma_f32_16x16x32_bf16 a[76:79], v[188:191], a[220:223], a[76:79]\n"
    "v_cvt_pk_bf16_f32 v232, v24, v25\n"
    "s_waitcnt lgkmcnt(0)\n"
    "v_mfma_f32_16x16x32_bf16 a[76:79], v[184:187], a[216:219], a[76:79]\n"
    "v_exp_f32 v26, v26\n"
    "v_mfma_f32_16x16x32_bf16 a[76:79], v[180:183], a[212:215], a[76:79]\n"
    "ds_read_b128 a[216:219], v3 offset:55296\n"
    "ds_read_b128 a[220:223], v3 offset:56320\n"
    "v_mfma_f32_16x16x32_bf16 a[76:79], v[176:179], a[208:211], a[76:79]\n"
    "v_exp_f32 v27, v27\n"
    "v_mfma_f32_16x16x32_bf16 v[16:19], a[192:195], a[128:131], v[32:35]\n"
    "v_cvt_pk_bf16_f32 v233, v26, v27\n"
    "v_mfma_f32_16x16x32_bf16 v[16:19], a[196:199], a[132:135], v[16:19]\n"
    "v_exp_f32 v28, v28\n"
    "v_mfma_f32_16x16x32_bf16 v[16:19], a[200:203], a[136:139], v[16:19]\n"
    "ds_read_b128 a[208:211], v3 offset:53248\n"
    "ds_read_b128 a[212:215], v3 offset:54272\n"
    "v_mfma_f32_16x16x32_bf16 v[16:19], a[204:207], a[140:143], v[16:19]\n"
    "v_exp_f32 v29, v29\n"
    "v_mfma_f32_16x16x32_bf16 v[20:23], a[204:207], a[156:159], v[36:39]\n"
    "v_cvt_pk_bf16_f32 v248, v28, v29\n"
    "v_mfma_f32_16x16x32_bf16 v[20:23], a[200:203], a[152:155], v[20:23]\n"
    "v_exp_f32 v30, v30\n"
    "v_mfma_f32_16x16x32_bf16 v[20:23], a[196:199], a[148:151], v[20:23]\n"
    "v_mfma_f32_16x16x32_bf16 v[20:23], a[192:195], a[144:147], v[20:23]\n"
    "v_exp_f32 v31, v31\n"
    "v_mfma_f32_16x16x32_bf16 v[24:27], a[192:195], a[160:163], v[40:43]\n"
    "v_cvt_pk_bf16_f32 v249, v30, v31\n"
    "v_mfma_f32_16x16x32_bf16 v[24:27], a[196:199], a[164:167], v[24:27]\n"
    "v_exp_f32 v16, v16\n"
    "v_mfma_f32_16x16x32_bf16 v[24:27], a[200:203], a[168:171], v[24:27]\n"
    "v_mfma_f32_16x16x32_bf16 v[24:27], a[204:207], a[172:175], v[24:27]\n"
    "v_exp_f32 v17, v17\n"
    "v_mfma_f32_16x16x32_bf16 v[28:31], a[204:207], a[188:191], v[44:47]\n"
    "v_cvt_pk_bf16_f32 v202, v16, v17\n"
    "s_waitcnt lgkmcnt(0)\n"
    "v_mfma_f32_16x16x32_bf16 v[28:31], a[200:203], a[184:187], v[28:31]\n"
    "v_exp_f32 v18, v18\n"
    "v_mfma_f32_16x16x32_bf16 v[28:31], a[196:199], a[180:183], v[28:31]\n"
    "ds_read_b128 a[200:203], v3 offset:26624\n"
    "ds_read_b128 a[204:207], v3 offset:27648\n"
    "v_mfma_f32_16x16x32_bf16 v[28:31], a[192:195], a[176:179], v[28:31]\n"
    "v_exp_f32 v19, v19\n"
    "v_mfma_f32_16x16x32_bf16 a[80:83], v[128:131], a[208:211], a[80:83]\n"
    "v_cvt_pk_bf16_f32 v203, v18, v19\n"
    "v_mfma_f32_16x16x32_bf16 a[80:83], v[132:135], a[212:215], a[80:83]\n"
    "v_exp_f32 v20, v20\n"
    "v_mfma_f32_16x16x32_bf16 a[80:83], v[136:139], a[216:219], a[80:83]\n"
    "ds_read_b128 a[192:195], v3 offset:24576\n"
    "ds_read_b128 a[196:199], v3 offset:25600\n"
    "v_mfma_f32_16x16x32_bf16 a[80:83], v[140:143], a[220:223], a[80:83]\n"
    "v_exp_f32 v21, v21\n"
    "v_mfma_f32_16x16x32_bf16 v[48:51], v[64:67], v[200:203], v[48:51]\n"
    "v_mfma_f32_16x16x32_bf16 a[84:87], v[156:159], a[220:223], a[84:87]\n"
    "v_cvt_pk_bf16_f32 v218, v20, v21\n"
    "v_mfma_f32_16x16x32_bf16 a[84:87], v[152:155], a[216:219], a[84:87]\n"
    "v_exp_f32 v22, v22\n"
    "v_mfma_f32_16x16x32_bf16 a[84:87], v[148:151], a[212:215], a[84:87]\n"
    "v_mfma_f32_16x16x32_bf16 a[84:87], v[144:147], a[208:211], a[84:87]\n"
    "v_exp_f32 v23, v23\n"
    "v_mfma_f32_16x16x32_bf16 a[88:91], v[160:163], a[208:211], a[88:91]\n"
    "v_cvt_pk_bf16_f32 v219, v22, v23\n"
    "v_mfma_f32_16x16x32_bf16 a[88:91], v[164:167], a[212:215], a[88:91]\n"
    "v_exp_f32 v24, v24\n"
    "v_mfma_f32_16x16x32_bf16 a[88:91], v[168:171], a[216:219], a[88:91]\n"
    "v_mfma_f32_16x16x32_bf16 a[88:91], v[172:175], a[220:223], a[88:91]\n"
    "v_exp_f32 v25, v25\n"
    "v_mfma_f32_16x16x32_bf16 v[52:55], v[64:67], v[216:219], v[52:55]\n"
    "v_mfma_f32_16x16x32_bf16 a[92:95], v[188:191], a[220:223], a[92:95]\n"
    "v_cvt_pk_bf16_f32 v234, v24, v25\n"
    "s_waitcnt lgkmcnt(0)\n"
    "v_mfma_f32_16x16x32_bf16 a[92:95], v[184:187], a[216:219], a[92:95]\n"
    "v_exp_f32 v26, v26\n"
    "v_mfma_f32_16x16x32_bf16 a[92:95], v[180:183], a[212:215], a[92:95]\n"
    "ds_read_b128 a[216:219], v3 offset:59392\n"
    "ds_read_b128 a[220:223], v3 offset:60416\n"
    "v_mfma_f32_16x16x32_bf16 a[92:95], v[176:179], a[208:211], a[92:95]\n"
    "v_exp_f32 v27, v27\n"
    "v_mfma_f32_16x16x32_bf16 v[16:19], a[192:195], a[128:131], v[32:35]\n"
    "v_cvt_pk_bf16_f32 v235, v26, v27\n"
    "v_mfma_f32_16x16x32_bf16 v[16:19], a[196:199], a[132:135], v[16:19]\n"
    "v_exp_f32 v28, v28\n"
    "v_mfma_f32_16x16x32_bf16 v[16:19], a[200:203], a[136:139], v[16:19]\n"
    "ds_read_b128 a[208:211], v3 offset:57344\n"
    "ds_read_b128 a[212:215], v3 offset:58368\n"
    "v_mfma_f32_16x16x32_bf16 v[16:19], a[204:207], a[140:143], v[16:19]\n"
    "v_exp_f32 v29, v29\n"
    "v_mfma_f32_16x16x32_bf16 v[56:59], v[64:67], v[232:235], v[56:59]\n"
    "v_mfma_f32_16x16x32_bf16 v[20:23], a[204:207], a[156:159], v[36:39]\n"
    "v_cvt_pk_bf16_f32 v250, v28, v29\n"
    "v_mfma_f32_16x16x32_bf16 v[20:23], a[200:203], a[152:155], v[20:23]\n"
    "v_exp_f32 v30, v30\n"
    "v_mfma_f32_16x16x32_bf16 v[20:23], a[196:199], a[148:151], v[20:23]\n"
    "v_mfma_f32_16x16x32_bf16 v[20:23], a[192:195], a[144:147], v[20:23]\n"
    "v_exp_f32 v31, v31\n"
    "v_mfma_f32_16x16x32_bf16 v[24:27], a[192:195], a[160:163], v[40:43]\n"
    "v_cvt_pk_bf16_f32 v251, v30, v31\n"
    "v_mfma_f32_16x16x32_bf16 v[24:27], a[196:199], a[164:167], v[24:27]\n"
    "v_exp_f32 v16, v16\n"
    "v_mfma_f32_16x16x32_bf16 v[24:27], a[200:203], a[168:171], v[24:27]\n"
    "v_mfma_f32_16x16x32_bf16 v[24:27], a[204:207], a[172:175], v[24:27]\n"
    "v_exp_f32 v17, v17\n"
    "v_mfma_f32_16x16x32_bf16 v[60:63], v[64:67], v[248:251], v[60:63]\n"
    "v_mfma_f32_16x16x32_bf16 v[28:31], a[204:207], a[188:191], v[44:47]\n"
    "v_cvt_pk_bf16_f32 v204, v16, v17\n"
    "s_waitcnt lgkmcnt(0)\n"
    "v_mfma_f32_16x16x32_bf16 v[28:31], a[200:203], a[184:187], v[28:31]\n"
    "v_exp_f32 v18, v18\n"
    "v_mfma_f32_16x16x32_bf16 v[28:31], a[196:199], a[180:183], v[28:31]\n"
    "ds_read_b128 a[200:203], v3 offset:30720\n"
    "ds_read_b128 a[204:207], v3 offset:31744\n"
    "v_mfma_f32_16x16x32_bf16 v[28:31], a[192:195], a[176:179], v[28:31]\n"
    "v_exp_f32 v19, v19\n"
    "v_mfma_f32_16x16x32_bf16 a[96:99], v[128:131], a[208:211], a[96:99]\n"
    "v_cvt_pk_bf16_f32 v205, v18, v19\n"
    "v_mfma_f32_16x16x32_bf16 a[96:99], v[132:135], a[212:215], a[96:99]\n"
    "v_exp_f32 v20, v20\n"
    "v_mfma_f32_16x16x32_bf16 a[96:99], v[136:139], a[216:219], a[96:99]\n"
    "ds_read_b128 a[192:195], v3 offset:28672\n"
    "ds_read_b128 a[196:199], v3 offset:29696\n"
    "v_mfma_f32_16x16x32_bf16 a[96:99], v[140:143], a[220:223], a[96:99]\n"
    "v_exp_f32 v21, v21\n"
    "v_mfma_f32_16x16x32_bf16 a[100:103], v[156:159], a[220:223], a[100:103]\n"
    "v_cvt_pk_bf16_f32 v220, v20, v21\n"
    "v_mfma_f32_16x16x32_bf16 a[100:103], v[152:155], a[216:219], a[100:103]\n"
    "v_exp_f32 v22, v22\n"
    "v_mfma_f32_16x16x32_bf16 a[100:103], v[148:151], a[212:215], a[100:103]\n"
    "v_mfma_f32_16x16x32_bf16 a[100:103], v[144:147], a[208:211], a[100:103]\n"
    "v_exp_f32 v23, v23\n"
    "v_mfma_f32_16x16x32_bf16 a[104:107], v[160:163], a[208:211], a[104:107]\n"
    "v_cvt_pk_bf16_f32 v221, v22, v23\n"
    "v_mfma_f32_16x16x32_bf16 a[104:107], v[164:167], a[212:215], a[104:107]\n"
    "v_exp_f32 v24, v24\n"
    "v_mfma_f32_16x16x32_bf16 a[104:107], v[168:171], a[216:219], a[104:107]\n"
    "v_mfma_f32_16x16x32_bf16 a[104:107], v[172:175], a[220:223], a[104:107]\n"
    "v_exp_f32 v25, v25\n"
    "v_mfma_f32_16x16x32_bf16 a[108:111], v[188:191], a[220:223], a[108:111]\n"
    "v_cvt_pk_bf16_f32 v236, v24, v25\n"
    "s_waitcnt lgkmcnt(0)\n"
    "v_mfma_f32_16x16x32_bf16 a[108:111], v[184:187], a[216:219], a[108:111]\n"
    "v_exp_f32 v26, v26\n"
    "v_mfma_f32_16x16x32_bf16 a[108:111], v[180:183], a[212:215], a[108:111]\n"
    "ds_read_b128 a[216:219], v3 offset:63488\n"
    "ds_read_b128 a[220:223], v3 offset:64512\n"
    "v_mfma_f32_16x16x32_bf16 a[108:111], v[176:179], a[208:211], a[108:111]\n"
    "v_exp_f32 v27, v27\n"
    "v_mfma_f32_16x16x32_bf16 v[16:19], a[192:195], a[128:131], v[32:35]\n"
    "v_cvt_pk_bf16_f32 v237, v26, v27\n"
    "v_mfma_f32_16x16x32_bf16 v[16:19], a[196:199], a[132:135], v[16:19]\n"
    "v_exp_f32 v28, v28\n"
    "v_mfma_f32_16x16x32_bf16 v[16:19], a[200:203], a[136:139], v[16:19]\n"
    "ds_read_b128 a[208:211], v3 offset:61440\n"
    "ds_read_b128 a[212:215], v3 offset:62464\n"
    "v_mfma_f32_16x16x32_bf16 v[16:19], a[204:207], a[140:143], v[16:19]\n"
    "v_exp_f32 v29, v29\n"
    "v_mfma_f32_16x16x32_bf16 v[20:23], a[204:207], a[156:159], v[36:39]\n"
    "v_cvt_pk_bf16_f32 v252, v28, v29\n"
    "v_mfma_f32_16x16x32_bf16 v[20:23], a[200:203], a[152:155], v[20:23]\n"
    "v_exp_f32 v30, v30\n"
    "v_mfma_f32_16x16x32_bf16 v[20:23], a[196:199], a[148:151], v[20:23]\n"
    "v_mfma_f32_16x16x32_bf16 v[20:23], a[192:195], a[144:147], v[20:23]\n"
    "v_exp_f32 v31, v31\n"
    "v_mfma_f32_16x16x32_bf16 v[24:27], a[192:195], a[160:163], v[40:43]\n"
    "v_cvt_pk_bf16_f32 v253, v30, v31\n"
    "v_mfma_f32_16x16x32_bf16 v[24:27], a[196:199], a[164:167], v[24:27]\n"
    "v_exp_f32 v16, v16\n"
    "v_mfma_f32_16x16x32_bf16 v[24:27], a[200:203], a[168:171], v[24:27]\n"
    "v_mfma_f32_16x16x32_bf16 v[24:27], a[204:207], a[172:175], v[24:27]\n"
    "v_exp_f32 v17, v17\n"
    "v_mfma_f32_16x16x32_bf16 v[28:31], a[204:207], a[188:191], v[44:47]\n"
    "v_cvt_pk_bf16_f32 v206, v16, v17\n"
    "s_waitcnt vmcnt(8) lgkmcnt(0)\n"
    "v_mfma_f32_16x16x32_bf16 v[28:31], a[200:203], a[184:187], v[28:31]\n"
    "v_exp_f32 v18, v18\n"
    "s_barrier\n"
    "v_mfma_f32_16x16x32_bf16 v[28:31], a[196:199], a[180:183], v[28:31]\n"
    "ds_read_b128 a[200:203], v2 offset:2048\n"
    "ds_read_b128 a[204:207], v2 offset:3072\n"
    "v_mfma_f32_16x16x32_bf16 v[28:31], a[192:195], a[176:179], v[28:31]\n"
    "v_exp_f32 v19, v19\n"
    "v_mfma_f32_16x16x32_bf16 a[112:115], v[128:131], a[208:211], a[112:115]\n"
    "v_cvt_pk_bf16_f32 v207, v18, v19\n"
    "s_add_u32 s48, s48, 0x10000\n"
    "v_mfma_f32_16x16x32_bf16 a[112:115], v[132:135], a[212:215], a[112:115]\n"
    "v_exp_f32 v20, v20\n"
    "v_mfma_f32_16x16x32_bf16 a[112:115], v[136:139], a[216:219], a[112:115]\n"
    "ds_read_b128 a[192:195], v2 offset:0\n"
    "ds_read_b128 a[196:199], v2 offset:1024\n"
    "v_mfma_f32_16x16x32_bf16 a[112:115], v[140:143], a[220:223], a[112:115]\n"
    "v_exp_f32 v21, v21\n"
    "v_mfma_f32_16x16x32_bf16 v[48:51], v[64:67], v[204:207], v[48:51]\n"
    "v_cmp_gt_u32 vcc, 32, v96\n"
    "v_mfma_f32_16x16x32_bf16 a[116:119], v[156:159], a[220:223], a[116:119]\n"
    "v_cvt_pk_bf16_f32 v222, v20, v21\n"
    "s_add_u32 s97, s97, 128\n"
    "v_mfma_f32_16x16x32_bf16 a[116:119], v[152:155], a[216:219], a[116:119]\n"
    "v_exp_f32 v22, v22\n"
    "v_mfma_f32_16x16x32_bf16 a[116:119], v[148:151], a[212:215], a[116:119]\n"
    "v_mfma_f32_16x16x32_bf16 a[116:119], v[144:147], a[208:211], a[116:119]\n"
    "v_exp_f32 v23, v23\n"
    "v_mfma_f32_16x16x32_bf16 a[120:123], v[160:163], a[208:211], a[120:123]\n"
    "v_cvt_pk_bf16_f32 v223, v22, v23\n"
    "s_mov_b32 m0, s53\n"
    "v_mfma_f32_16x16x32_bf16 a[120:123], v[164:167], a[212:215], a[120:123]\n"
    "v_exp_f32 v24, v24\n"
    "v_mfma_f32_16x16x32_bf16 a[120:123], v[168:171], a[216:219], a[120:123]\n"
    "s_cbranch_vccnz L_spin_A\n"
    "L_spinret_A:\n"
    "v_mfma_f32_16x16x32_bf16 a[120:123], v[172:175], a[220:223], a[120:123]\n"
    "v_exp_f32 v25, v25\n"
    "v_mfma_f32_16x16x32_bf16 v[52:55], v[64:67], v[220:223], v[52:55]\n"
    "s_add_u32 s96, s96, 128\n"
    "s_min_u32 s96, s96, 0x2b00\n"
    "v_mfma_f32_16x16x32_bf16 a[124:127], v[188:191], a[220:223], a[124:127]\n"
    "v_cvt_pk_bf16_f32 v238, v24, v25\n"
    "s_waitcnt lgkmcnt(0)\n"
    "v_mfma_f32_16x16x32_bf16 a[124:127], v[184:187], a[216:219], a[124:127]\n"
    "v_exp_f32 v26, v26\n"
    "v_mfma_f32_16x16x32_bf16 a[124:127], v[180:183], a[212:215], a[124:127]\n"
    "ds_read_b128 a[216:219], v2 offset:34816\n"
    "ds_read_b128 a[220:223], v2 offset:35840\n"
    "v_mfma_f32_16x16x32_bf16 a[124:127], v[176:179], a[208:211], a[124:127]\n"
    "v_exp_f32 v27, v27\n"
    "v_mfma_f32_16x16x32_bf16 v[16:19], a[192:195], a[128:131], v[32:35]\n"
    "v_cvt_pk_bf16_f32 v239, v26, v27\n"
    "s_waitcnt vmcnt(0)\n"
    "v_mfma_f32_16x16x32_bf16 v[16:19], a[196:199], a[132:135], v[16:19]\n"
    "v_exp_f32 v28, v28\n"
    "v_mfma_f32_16x16x32_bf16 v[16:19], a[200:203], a[136:139], v[16:19]\n"
    "ds_read_b128 a[208:211], v2 offset:32768\n"
    "ds_read_b128 a[212:215], v2 offset:33792\n"
    "v_mfma_f32_16x16x32_bf16 v[16:19], a[204:207], a[140:143], v[16:19]\n"
    "v_exp_f32 v29, v29\n"
    "v_mfma_f32_16x16x32_bf16 v[56:59], v[64:67], v[236:239], v[56:59]\n"
    "buffer_load_dwordx4 v4, s[40:43], s48 offen offset:0 sc1 lds\n"
    "v_cvt_f32_i32 v108, v100\n"
    "v_mfma_f32_16x16x32_bf16 v[20:23], a[204:207], a[156:159], v[36:39]\n"
    "v_cvt_pk_bf16_f32 v254, v28, v29\n"
    "v_cmp_nge_f32 vcc, s54, v48\n"
    "v_mfma_f32_16x16x32_bf16 v[20:23], a[200:203], a[152:155], v[20:23]\n"
    "v_exp_f32 v30, v30\n"
    "v_mfma_f32_16x16x32_bf16 v[20:23], a[196:199], a[148:151], v[20:23]\n"
    "s_cbranch_vccnz L_rare_B0\n"
    "L_ret_B0:\n"
    "v_add_f32 v68, v48, v68\n"
    "v_mfma_f32_16x16x32_bf16 v[20:23], a[192:195], a[144:147], v[20:23]\n"
    "v_exp_f32 v31, v31\n"
    "v_mfma_f32_16x16x32_bf16 v[24:27], a[192:195], a[160:163], v[40:43]\n"
    "v_cvt_pk_bf16_f32 v255, v30, v31\n"
    "v_cmp_nge_f32 vcc, s54, v52\n"
    "v_mfma_f32_16x16x32_bf16 v[24:27], a[196:199], a[164:167], v[24:27]\n"
    "v_exp_f32 v16, v16\n"
    "v_mfma_f32_16x16x32_bf16 v[24:27], a[200:203], a[168:171], v[24:27]\n"
    "s_cbranch_vccnz L_rare_B1\n"
    "L_ret_B1:\n"
    "v_add_f32 v69, v52, v69\n"
    "v_mfma_f32_16x16x32_bf16 v[24:27], a[204:207], a[172:175], v[24:27]\n"
    "v_exp_f32 v17, v17\n"
    "v_mfma_f32_16x16x32_bf16 v[60:63], v[64:67], v[252:255], v[60:63]\n"
    "buffer_load_dwordx4 v4, s[40:43], s48 offen offset:1024 sc1 lds\n"
    "v_cvt_f32_i32 v109, v101\n"
    "v_mfma_f32_16x16x32_bf16 v[28:31], a[204:207], a[188:191], v[44:47]\n"
    "v_cvt_pk_bf16_f32 v128, v16, v17\n"
    "s_waitcnt lgkmcnt(0)\n"
    "v_mfma_f32_16x16x32_bf16 v[28:31], a[200:203], a[184:187], v[28:31]\n"
    "v_exp_f32 v18, v18\n"
    "v_mfma_f32_16x16x32_bf16 v[28:31], a[196:199], a[180:183], v[28:31]\n"
    "ds_read_b128 a[200:203], v2 offset:6144\n"
    "ds_read_b128 a[204:207], v2 offset:7168\n"
    "v_mfma_f32_16x16x32_bf16 v[28:31], a[192:195], a[176:179], v[28:31]\n"
    "v_exp_f32 v19, v19\n"
    "v_mfma_f32_16x16x32_bf16 a[0:3], v[192:195], a[208:211], a[0:3]\n"
    "v_cvt_pk_bf16_f32 v129, v18, v19\n"
    "v_cvt_f32_i32 v110, v102\n"
    "v_mfma_f32_16x16x32_bf16 a[0:3], v[196:199], a[212:215], a[0:3]\n"
    "v_exp_f32 v20, v20\n"
    "v_mfma_f32_16x16x32_bf16 a[0:3], v[200:203], a[216:219], a[0:3]\n"
    "ds_read_b128 a[192:195], v2 offset:4096\n"
    "ds_read_b128 a[196:199], v2 offset:5120\n"
    "v_mfma_f32_16x16x32_bf16 a[0:3], v[204:207], a[220:223], a[0:3]\n"
    "v_exp_f32 v21, v21\n"
    "v_mfma_f32_16x16x32_bf16 a[4:7], v[220:223], a[220:223], a[4:7]\n"
    "v_cvt_pk_bf16_f32 v144, v20, v21\n"
    "v_cmp_nge_f32 vcc, s54, v56\n"
    "v_mfma_f32_16x16x32_bf16 a[4:7], v[216:219], a[216:219], a[4:7]\n"
    "v_exp_f32 v22, v22\n"
    "v_mfma_f32_16x16x32_bf16 a[4:7], v[212:215], a[212:215], a[4:7]\n"
    "s_cbranch_vccnz L_rare_B2\n"
    "L_ret_B2:\n"
    "v_add_f32 v70, v56, v70\n"
    "v_mfma_f32_16x16x32_bf16 a[4:7], v[208:211], a[208:211], a[4:7]\n"
    "v_exp_f32 v23, v23\n"
    "v_mfma_f32_16x16x32_bf16 a[8:11], v[224:227], a[208:211], a[8:11]\n"
    "v_cvt_pk_bf16_f32 v145, v22, v23\n"
    "v_cmp_nge_f32 vcc, s54, v60\n"
    "v_mfma_f32_16x16x32_bf16 a[8:11], v[228:231], a[212:215], a[8:11]\n"
    "v_exp_f32 v24, v24\n"
    "v_mfma_f32_16x16x32_bf16 a[8:11], v[232:235], a[216:219], a[8:11]\n"
    "s_cbranch_vccnz L_rare_B3\n"
    "L_ret_B3:\n"
    "v_add_f32 v71, v60, v71\n"
    "v_mfma_f32_16x16x32_bf16 a[8:11], v[236:239], a[220:223], a[8:11]\n"
    "v_exp_f32 v25, v25\n"
    "v_mfma_f32_16x16x32_bf16 a[12:15], v[252:255], a[220:223], a[12:15]\n"
    "v_cvt_pk_bf16_f32 v160, v24, v25\n"
    "s_waitcnt lgkmcnt(0)\n"
    "v_mfma_f32_16x16x32_bf16 a[12:15], v[248:251], a[216:219], a[12:15]\n"
    "v_exp_f32 v26, v26\n"
    "v_mfma_f32_16x16x32_bf16 a[12:15], v[244:247], a[212:215], a[12:15]\n"
    "ds_read_b128 a[216:219], v2 offset:38912\n"
    "ds_read_b128 a[220:223], v2 offset:39936\n"
    "v_mfma_f32_16x16x32_bf16 a[12:15], v[240:243], a[208:211], a[12:15]\n"
    "v_exp_f32 v27, v27\n"
    "v_mfma_f32_16x16x32_bf16 v[16:19], a[192:195], a[128:131], v[32:35]\n"
    "v_cvt_pk_bf16_f32 v161, v26, v27\n"
    "v_cvt_f32_i32 v111, v103\n"
    "v_mfma_f32_16x16x32_bf16 v[16:19], a[196:199], a[132:135], v[16:19]\n"
    "v_exp_f32 v28, v28\n"
    "v_mfma_f32_16x16x32_bf16 v[16:19], a[200:203], a[136:139], v[16:19]\n"
    "ds_read_b128 a[208:211], v2 offset:36864\n"
    "ds_read_b128 a[212:215], v2 offset:37888\n"
    "v_mfma_f32_16x16x32_bf16 v[16:19], a[204:207], a[140:143], v[16:19]\n"
    "v_exp_f32 v29, v29\n"
    "v_mfma_f32_16x16x32_bf16 v[20:23], a[204:207], a[156:159], v[36:39]\n"
    "v_cvt_pk_bf16_f32 v176, v28, v29\n"
    "v_cvt_pk_bf16_f32 v108, 0, v108\n"
    "v_mfma_f32_16x16x32_bf16 v[20:23], a[200:203], a[152:155], v[20:23]\n"
    "v_exp_f32 v30, v30\n"
    "v_mfma_f32_16x16x32_bf16 v[20:23], a[196:199], a[148:151], v[20:23]\n"
    "buffer_load_dwordx4 v4, s[40:43], s48 offen offset:2048 sc1 lds\n"
    "v_cvt_pk_bf16_f32 v109, 0, v109\n"
    "v_mfma_f32_16x16x32_bf16 v[20:23], a[192:195], a[144:147], v[20:23]\n"
    "v_exp_f32 v31, v31\n"
    "v_mfma_f32_16x16x32_bf16 v[24:27], a[192:195], a[160:163], v[40:43]\n"
    "v_cvt_pk_bf16_f32 v177, v30, v31\n"
    "v_cvt_pk_bf16_f32 v110, 0, v110\n"
    "v_mfma_f32_16x16x32_bf16 v[24:27], a[196:199], a[164:167], v[24:27]\n"
    "v_exp_f32 v16, v16\n"
    "v_mfma_f32_16x16x32_bf16 v[24:27], a[200:203], a[168:171], v[24:27]\n"
    "buffer_load_dwordx4 v4, s[40:43], s48 offen offset:3072 sc1 lds\n"
    "s_add_u32 m0, m0, 0x1000\n"
    "v_mfma_f32_16x16x32_bf16 v[24:27], a[204:207], a[172:175], v[24:27]\n"
    "v_exp_f32 v17, v17\n"
    "v_mfma_f32_16x16x32_bf16 v[28:31], a[204:207], a[188:191], v[44:47]\n"
    "v_cvt_pk_bf16_f32 v130, v16, v17\n"
    "s_waitcnt lgkmcnt(0)\n"
    "v_mfma_f32_16x16x32_bf16 v[28:31], a[200:203], a[184:187], v[28:31]\n"
    "v_exp_f32 v18, v18\n"
    "v_mfma_f32_16x16x32_bf16 v[28:31], a[196:199], a[180:183], v[28:31]\n"
    "ds_read_b128 a[200:203], v2 offset:10240\n"
    "ds_read_b128 a[204:207], v2 offset:11264\n"
    "v_mfma_f32_16x16x32_bf16 v[28:31], a[192:195], a[176:179], v[28:31]\n"
    "v_exp_f32 v19, v19\n"
    "v_mfma_f32_16x16x32_bf16 a[16:19], v[192:195], a[208:211], a[16:19]\n"
    "v_cvt_pk_bf16_f32 v131, v18, v19\n"
    "v_cvt_pk_bf16_f32 v111, 0, v111\n"
    "v_mfma_f32_16x16x32_bf16 a[16:19], v[196:199], a[212:215], a[16:19]\n"
    "v_exp_f32 v20, v20\n"
    "v_mfma_f32_16x16x32_bf16 a[16:19], v[200:203], a[216:219], a[16:19]\n"
    "ds_read_b128 a[192:195], v2 offset:8192\n"
    "ds_read_b128 a[196:199], v2 offset:9216\n"
    "v_mfma_f32_16x16x32_bf16 a[16:19], v[204:207], a[220:223], a[16:19]\n"
    "v_exp_f32 v21, v21\n"
    "v_mfma_f32_16x16x32_bf16 v[48:51], v[64:67], v[128:131], 0\n"
    "buffer_load_dwordx4 v5, s[40:43], s48 offen offset:0 sc1 lds\n"
    "v_lshlrev_b32 v112, 16, v106\n"
    "v_mfma_f32_16x16x32_bf16 a[20:23], v[220:223], a[220:223], a[20:23]\n"
    "v_cvt_pk_bf16_f32 v146, v20, v21\n"
    "v_and_b32 v113, 0xffff0000, v106\n"
    "v_mfma_f32_16x16x32_bf16 a[20:23], v[216:219], a[216:219], a[20:23]\n"
    "v_exp_f32 v22, v22\n"
    "v_mfma_f32_16x16x32_bf16 a[20:23], v[212:215], a[212:215], a[20:23]\n"
    "buffer_load_dwordx4 v5, s[40:43], s48 offen offset:1024 sc1 lds\n"
    "v_lshlrev_b32 v114, 16, v107\n"
    "v_mfma_f32_16x16x32_bf16 a[20:23], v[208:211], a[208:211], a[20:23]\n"
    "v_exp_f32 v23, v23\n"
    "v_mfma_f32_16x16x32_bf16 a[24:27], v[224:227], a[208:211], a[24:27]\n"
    "v_cvt_pk_bf16_f32 v147, v22, v23\n"
    "v_and_b32 v115, 0xffff0000, v107\n"
    "v_mfma_f32_16x16x32_bf16 a[24:27], v[228:231], a[212:215], a[24:27]\n"
    "v_exp_f32 v24, v24\n"
    "v_mfma_f32_16x16x32_bf16 a[24:27], v[232:235], a[216:219], a[24:27]\n"
    "buffer_load_dwordx4 v5, s[40:43], s48 offen offset:2048 sc1 lds\n"
    "v_sub_f32 v108, v108, v112\n"
    "v_mfma_f32_16x16x32_bf16 a[24:27], v[236:239], a[220:223], a[24:27]\n"
    "v_exp_f32 v25, v25\n"
    "v_mfma_f32_16x16x32_bf16 v[52:55], v[64:67], v[144:147], 0\n"
    "buffer_load_dwordx4 v5, s[40:43], s48 offen offset:3072 sc1 lds\n"
    "s_add_u32 m0, m0, 0x1000\n"
    "v_mfma_f32_16x16x32_bf16 a[28:31], v[252:255], a[220:223], a[28:31]\n"
    "v_cvt_pk_bf16_f32 v162, v24, v25\n"
    "s_waitcnt lgkmcnt(0)\n"
    "v_mfma_f32_16x16x32_bf16 a[28:31], v[248:251], a[216:219], a[28:31]\n"
    "v_exp_f32 v26, v26\n"
    "v_mfma_f32_16x16x32_bf16 a[28:31], v[244:247], a[212:215], a[28:31]\n"
    "ds_read_b128 a[216:219], v2 offset:43008\n"
    "ds_read_b128 a[220:223], v2 offset:44032\n"
    "v_mfma_f32_16x16x32_bf16 a[28:31], v[240:243], a[208:211], a[28:31]\n"
    "v_exp_f32 v27, v27\n"
    "v_mfma_f32_16x16x32_bf16 v[16:19], a[192:195], a[128:131], v[32:35]\n"
    "v_cvt_pk_bf16_f32 v163, v26, v27\n"
    "v_sub_f32 v109, v109, v113\n"
    "v_mfma_f32_16x16x32_bf16 v[16:19], a[196:199], a[132:135], v[16:19]\n"
    "v_exp_f32 v28, v28\n"
    "v_mfma_f32_16x16x32_bf16 v[16:19], a[200:203], a[136:139], v[16:19]\n"
    "ds_read_b128 a[208:211], v2 offset:40960\n"
    "ds_read_b128 a[212:215], v2 offset:41984\n"
    "v_mfma_f32_16x16x32_bf16 v[16:19], a[204:207], a[140:143], v[16:19]\n"
    "v_exp_f32 v29, v29\n"
    "v_mfma_f32_16x16x32_bf16 v[56:59], v[64:67], v[160:163], 0\n"
    "v_sub_f32 v110, v110, v114\n"
    "v_sub_f32 v111, v111, v115\n"
    "v_mfma_f32_16x16x32_bf16 v[20:23], a[204:207], a[156:159], v[36:39]\n"
    "v_cvt_pk_bf16_f32 v178, v28, v29\n"
    "v_cvt_pk_bf16_f32 v108, 0, v108\n"
    "v_mfma_f32_16x16x32_bf16 v[20:23], a[200:203], a[152:155], v[20:23]\n"
    "v_exp_f32 v30, v30\n"
    "v_mfma_f32_16x16x32_bf16 v[20:23], a[196:199], a[148:151], v[20:23]\n"
    "buffer_load_dwordx4 v6, s[40:43], s48 offen offset:0 sc1 lds\n"
    "v_cvt_pk_bf16_f32 v109, 0, v109\n"
    "v_mfma_f32_16x16x32_bf16 v[20:23], a[192:195], a[144:147], v[20:23]\n"
    "v_exp_f32 v31, v31\n"
    "v_mfma_f32_16x16x32_bf16 v[24:27], a[192:195], a[160:163], v[40:43]\n"
    "v_cvt_pk_bf16_f32 v179, v30, v31\n"
    "v_cvt_pk_bf16_f32 v110, 0, v110\n"
    "v_mfma_f32_16x16x32_bf16 v[24:27], a[196:199], a[164:167], v[24:27]\n"
    "v_exp_f32 v16, v16\n"
    "v_mfma_f32_16x16x32_bf16 v[24:27], a[200:203], a[168:171], v[24:27]\n"
    "buffer_load_dwordx4 v6, s[40:43], s48 offen offset:1024 sc1 lds\n"
    "v_cvt_pk_bf16_f32 v111, 0, v111\n"
    "v_mfma_f32_16x16x32_bf16 v[24:27], a[204:207], a[172:175], v[24:27]\n"
    "v_exp_f32 v17, v17\n"
    "v_mfma_f32_16x16x32_bf16 v[60:63], v[64:67], v[176:179], 0\n"
    "v_lshlrev_b32 v112, 16, v104\n"
    "v_and_b32 v113, 0xffff0000, v104\n"
    "v_mfma_f32_16x16x32_bf16 v[28:31], a[204:207], a[188:191], v[44:47]\n"
    "v_cvt_pk_bf16_f32 v132, v16, v17\n"
    "s_waitcnt lgkmcnt(0)\n"
    "v_mfma_f32_16x16x32_bf16 v[28:31], a[200:203], a[184:187], v[28:31]\n"
    "v_exp_f32 v18, v18\n"
    "v_mfma_f32_16x16x32_bf16 v[28:31], a[196:199], a[180:183], v[28:31]\n"
    "ds_read_b128 a[200:203], v2 offset:14336\n"
    "ds_read_b128 a[204:207], v2 offset:15360\n"
    "v_mfma_f32_16x16x32_bf16 v[28:31], a[192:195], a[176:179], v[28:31]\n"
    "v_exp_f32 v19, v19\n"
    "v_mfma_f32_16x16x32_bf16 a[32:35], v[192:195], a[208:211], a[32:35]\n"
    "v_cvt_pk_bf16_f32 v133, v18, v19\n"
    "v_lshlrev_b32 v114, 16, v105\n"
    "v_mfma_f32_16x16x32_bf16 a[32:35], v[196:199], a[212:215], a[32:35]\n"
    "v_exp_f32 v20, v20\n"
    "v_mfma_f32_16x16x32_bf16 a[32:35], v[200:203], a[216:219], a[32:35]\n"
    "ds_read_b128 a[192:195], v2 offset:12288\n"
    "ds_read_b128 a[196:199], v2 offset:13312\n"
    "v_mfma_f32_16x16x32_bf16 a[32:35], v[204:207], a[220:223], a[32:35]\n"
    "v_exp_f32 v21, v21\n"
    "v_mfma_f32_16x16x32_bf16 a[36:39], v[220:223], a[220:223], a[36:39]\n"
    "v_cvt_pk_bf16_f32 v148, v20, v21\n"
    "v_and_b32 v115, 0xffff0000, v105\n"
    "v_mfma_f32_16x16x32_bf16 a[36:39], v[216:219], a[216:219], a[36:39]\n"
    "v_exp_f32 v22, v22\n"
    "v_mfma_f32_16x16x32_bf16 a[36:39], v[212:215], a[212:215], a[36:39]\n"
    "buffer_load_dwordx4 v6, s[40:43], s48 offen offset:2048 sc1 lds\n"
    "v_mul_f32 v108, v108, v112\n"
    "v_mfma_f32_16x16x32_bf16 a[36:39], v[208:211], a[208:211], a[36:39]\n"
    "v_exp_f32 v23, v23\n"
    "v_mfma_f32_16x16x32_bf16 a[40:43], v[224:227], a[208:211], a[40:43]\n"
    "v_cvt_pk_bf16_f32 v149, v22, v23\n"
    "v_mul_f32 v109, v109, v113\n"
    "v_mfma_f32_16x16x32_bf16 a[40:43], v[228:231], a[212:215], a[40:43]\n"
    "v_exp_f32 v24, v24\n"
    "v_mfma_f32_16x16x32_bf16 a[40:43], v[232:235], a[216:219], a[40:43]\n"
    "buffer_load_dwordx4 v6, s[40:43], s48 offen offset:3072 sc1 lds\n"
    "s_add_u32 m0, m0, 0x1000\n"
    "v_mfma_f32_16x16x32_bf16 a[40:43], v[236:239], a[220:223], a[40:43]\n"
    "v_exp_f32 v25, v25\n"
    "v_mfma_f32_16x16x32_bf16 a[44:47], v[252:255], a[220:223], a[44:47]\n"
    "v_cvt_pk_bf16_f32 v164, v24, v25\n"
    "s_waitcnt lgkmcnt(0)\n"
    "v_mfma_f32_16x16x32_bf16 a[44:47], v[248:251], a[216:219], a[44:47]\n"
    "v_exp_f32 v26, v26\n"
    "v_mfma_f32_16x16x32_bf16 a[44:47], v[244:247], a[212:215], a[44:47]\n"
    "ds_read_b128 a[216:219], v2 offset:47104\n"
    "ds_read_b128 a[220:223], v2 offset:48128\n"
    "v_mfma_f32_16x16x32_bf16 a[44:47], v[240:243], a[208:211], a[44:47]\n"
    "v_exp_f32 v27, v27\n"
    "v_mfma_f32_16x16x32_bf16 v[16:19], a[192:195], a[128:131], v[32:35]\n"
    "v_cvt_pk_bf16_f32 v165, v26, v27\n"
    "v_mul_f32 v110, v110, v114\n"
    "v_mfma_f32_16x16x32_bf16 v[16:19], a[196:199], a[132:135], v[16:19]\n"
    "v_exp_f32 v28, v28\n"
    "v_mfma_f32_16x16x32_bf16 v[16:19], a[200:203], a[136:139], v[16:19]\n"
    "ds_read_b128 a[208:211], v2 offset:45056\n"
    "ds_read_b128 a[212:215], v2 offset:46080\n"
    "v_mfma_f32_16x16x32_bf16 v[16:19], a[204:207], a[140:143], v[16:19]\n"
    "v_exp_f32 v29, v29\n"
    "v_mfma_f32_16x16x32_bf16 v[20:23], a[204:207], a[156:159], v[36:39]\n"
    "v_cvt_pk_bf16_f32 v180, v28, v29\n"
    "v_mul_f32 v111, v111, v115\n"
    "v_mfma_f32_16x16x32_bf16 v[20:23], a[200:203], a[152:155], v[20:23]\n"
    "v_exp_f32 v30, v30\n"
    "v_mfma_f32_16x16x32_bf16 v[20:23], a[196:199], a[148:151], v[20:23]\n"
    "buffer_load_dwordx4 v7, s[40:43], s48 offen offset:0 sc1 lds\n"
    "v_cvt_pk_bf16_f32 v112, v108, v109\n"
    "v_mfma_f32_16x16x32_bf16 v[20:23], a[192:195], a[144:147], v[20:23]\n"
    "v_exp_f32 v31, v31\n"
    "v_mfma_f32_16x16x32_bf16 v[24:27], a[192:195], a[160:163], v[40:43]\n"
    "v_cvt_pk_bf16_f32 v181, v30, v31\n"
    "v_cvt_pk_bf16_f32 v113, v110, v111\n"
    "v_mfma_f32_16x16x32_bf16 v[24:27], a[196:199], a[164:167], v[24:27]\n"
    "v_exp_f32 v16, v16\n"
    "v_mfma_f32_16x16x32_bf16 v[24:27], a[200:203], a[168:171], v[24:27]\n"
    "buffer_load_dwordx4 v7, s[40:43], s48 offen offset:1024 sc1 lds\n"
    "v_mfma_f32_16x16x32_bf16 v[24:27], a[204:207], a[172:175], v[24:27]\n"
    "v_exp_f32 v17, v17\n"
    "v_mfma_f32_16x16x32_bf16 v[28:31], a[204:207], a[188:191], v[44:47]\n"
    "v_cvt_pk_bf16_f32 v134, v16, v17\n"
    "s_waitcnt lgkmcnt(0)\n"
    "v_mfma_f32_16x16x32_bf16 v[28:31], a[200:203], a[184:187], v[28:31]\n"
    "v_exp_f32 v18, v18\n"
    "v_mfma_f32_16x16x32_bf16 v[28:31], a[196:199], a[180:183], v[28:31]\n"
    "ds_read_b128 a[200:203], v2 offset:18432\n"
    "ds_read_b128 a[204:207], v2 offset:19456\n"
    "v_mfma_f32_16x16x32_bf16 v[28:31], a[192:195], a[176:179], v[28:31]\n"
    "v_exp_f32 v19, v19\n"
    "v_mfma_f32_16x16x32_bf16 a[48:51], v[192:195], a[208:211], a[48:51]\n"
    "v_cvt_pk_bf16_f32 v135, v18, v19\n"
    "v_mfma_f32_16x16x32_bf16 a[48:51], v[196:199], a[212:215], a[48:51]\n"
    "v_exp_f32 v20, v20\n"
    "v_mfma_f32_16x16x32_bf16 a[48:51], v[200:203], a[216:219], a[48:51]\n"
    "ds_read_b128 a[192:195], v2 offset:16384\n"
    "ds_read_b128 a[196:199], v2 offset:17408\n"
    "v_mfma_f32_16x16x32_bf16 a[48:51], v[204:207], a[220:223], a[48:51]\n"
    "v_exp_f32 v21, v21\n"
    "v_mfma_f32_16x16x32_bf16 v[48:51], v[64:67], v[132:135], v[48:51]\n"
    "v_mfma_f32_16x16x32_bf16 a[52:55], v[220:223], a[220:223], a[52:55]\n"
    "v_cvt_pk_bf16_f32 v150, v20, v21\n"
    "v_mfma_f32_16x16x32_bf16 a[52:55], v[216:219], a[216:219], a[52:55]\n"
    "v_exp_f32 v22, v22\n"
    "v_mfma_f32_16x16x32_bf16 a[52:55], v[212:215], a[212:215], a[52:55]\n"
    "buffer_load_dwordx4 v7, s[40:43], s48 offen offset:2048 sc1 lds\n"
    "v_mfma_f32_16x16x32_bf16 a[52:55], v[208:211], a[208:211], a[52:55]\n"
    "v_exp_f32 v23, v23\n"
    "v_mfma_f32_16x16x32_bf16 a[56:59], v[224:227], a[208:211], a[56:59]\n"
    "v_cvt_pk_bf16_f32 v151, v22, v23\n"
    "v_mfma_f32_16x16x32_bf16 a[56:59], v[228:231], a[212:215], a[56:59]\n"
    "v_exp_f32 v24, v24\n"
    "v_mfma_f32_16x16x32_bf16 a[56:59], v[232:235], a[216:219], a[56:59]\n"
    "buffer_load_dwordx4 v7, s[40:43], s48 offen offset:3072 sc1 lds\n"
    "v_mfma_f32_16x16x32_bf16 a[56:59], v[236:239], a[220:223], a[56:59]\n"
    "v_exp_f32 v25, v25\n"
    "v_mfma_f32_16x16x32_bf16 v[52:55], v[64:67], v[148:151], v[52:55]\n"
    "v_mfma_f32_16x16x32_bf16 a[60:63], v[252:255], a[220:223], a[60:63]\n"
    "v_cvt_pk_bf16_f32 v166, v24, v25\n"
    "s_waitcnt lgkmcnt(0)\n"
    "v_mfma_f32_16x16x32_bf16 a[60:63], v[248:251], a[216:219], a[60:63]\n"
    "v_exp_f32 v26, v26\n"
    "v_mfma_f32_16x16x32_bf16 a[60:63], v[244:247], a[212:215], a[60:63]\n"
    "ds_read_b128 a[216:219], v2 offset:51200\n"
    "ds_read_b128 a[220:223], v2 offset:52224\n"
    "v_mfma_f32_16x16x32_bf16 a[60:63], v[240:243], a[208:211], a[60:63]\n"
    "v_exp_f32 v27, v27\n"
    "v_mfma_f32_16x16x32_bf16 v[16:19], a[192:195], a[128:131], v[32:35]\n"
    "v_cvt_pk_bf16_f32 v167, v26, v27\n"
    "v_mfma_f32_16x16x32_bf16 v[16:19], a[196:199], a[132:135], v[16:19]\n"
    "v_exp_f32 v28, v28\n"
    "v_mfma_f32_16x16x32_bf16 v[16:19], a[200:203], a[136:139], v[16:19]\n"
    "ds_read_b128 a[208:211], v2 offset:49152\n"
    "ds_read_b128 a[212:215], v2 offset:50176\n"
    "v_mfma_f32_16x16x32_bf16 v[16:19], a[204:207], a[140:143], v[16:19]\n"
    "v_exp_f32 v29, v29\n"
    "v_mfma_f32_16x16x32_bf16 v[56:59], v[64:67], v[164:167], v[56:59]\n"
    "buffer_load_dword v96, off, s[64:67], s96 offset:0 sc1\n"
    "buffer_atomic_add v78, v95, s[64:67], s97 offen\n"
    "v_mfma_f32_16x16x32_bf16 v[20:23], a[204:207], a[156:159], v[36:39]\n"
    "buffer_store_dwordx2 v[112:113], v93, s[40:43], s94 offen offset:0 sc1\n"
    "v_cvt_pk_bf16_f32 v182, v28, v29\n"
    "v_mfma_f32_16x16x32_bf16 v[20:23], a[200:203], a[152:155], v[20:23]\n"
    "v_exp_f32 v30, v30\n"
    "v_mfma_f32_16x16x32_bf16 v[20:23], a[196:199], a[148:151], v[20:23]\n"
    "buffer_load_dwordx4 v[116:119], v88, s[68:71], s92 offen offset:0\n"
    "v_mfma_f32_16x16x32_bf16 v[20:23], a[192:195], a[144:147], v[20:23]\n"
    "v_exp_f32 v31, v31\n"
    "v_mfma_f32_16x16x32_bf16 v[24:27], a[192:195], a[160:163], v[40:43]\n"
    "buffer_load_dwordx2 v[120:121], v89, s[76:79], s93 offen offset:0\n"
    "v_cvt_pk_bf16_f32 v183, v30, v31\n"
    "v_mfma_f32_16x16x32_bf16 v[24:27], a[196:199], a[164:167], v[24:27]\n"
    "v_exp_f32 v16, v16\n"
    "v_mfma_f32_16x16x32_bf16 v[24:27], a[200:203], a[168:171], v[24:27]\n"
    "buffer_load_dwordx2 v[122:123], v89, s[80:83], s93 offen offset:0\n"
    "s_add_u32 s94, s94, 0x20000\n"
    "v_mfma_f32_16x16x32_bf16 v[24:27], a[204:207], a[172:175], v[24:27]\n"
    "v_exp_f32 v17, v17\n"
    "v_mfma_f32_16x16x32_bf16 v[60:63], v[64:67], v[180:183], v[60:63]\n"
    "s_add_u32 s95, s95, 0x200\n"
    "v_mfma_f32_16x16x32_bf16 v[28:31], a[204:207], a[188:191], v[44:47]\n"
    "v_cvt_pk_bf16_f32 v136, v16, v17\n"
    "s_waitcnt lgkmcnt(0)\n"
    "v_mfma_f32_16x16x32_bf16 v[28:31], a[200:203], a[184:187], v[28:31]\n"
    "v_exp_f32 v18, v18\n"
    "v_mfma_f32_16x16x32_bf16 v[28:31], a[196:199], a[180:183], v[28:31]\n"
    "ds_read_b128 a[200:203], v2 offset:22528\n"
    "ds_read_b128 a[204:207], v2 offset:23552\n"
    "v_mfma_f32_16x16x32_bf16 v[28:31], a[192:195], a[176:179], v[28:31]\n"
    "v_exp_f32 v19, v19\n"
    "v_mfma_f32_16x16x32_bf16 a[64:67], v[192:195], a[208:211], a[64:67]\n"
    "v_cvt_pk_bf16_f32 v137, v18, v19\n"
    "v_mfma_f32_16x16x32_bf16 a[64:67], v[196:199], a[212:215], a[64:67]\n"
    "v_exp_f32 v20, v20\n"
    "v_mfma_f32_16x16x32_bf16 a[64:67], v[200:203], a[216:219], a[64:67]\n"
    "ds_read_b128 a[192:195], v2 offset:20480\n"
    "ds_read_b128 a[196:199], v2 offset:21504\n"
    "v_mfma_f32_16x16x32_bf16 a[64:67], v[204:207], a[220:223], a[64:67]\n"
    "v_exp_f32 v21, v21\n"
    "v_mfma_f32_16x16x32_bf16 a[68:71], v[220:223], a[220:223], a[68:71]\n"
    "v_cvt_pk_bf16_f32 v152, v20, v21\n"
    "v_mfma_f32_16x16x32_bf16 a[68:71], v[216:219], a[216:219], a[68:71]\n"
    "v_exp_f32 v22, v22\n"
    "v_mfma_f32_16x16x32_bf16 a[68:71], v[212:215], a[212:215], a[68:71]\n"
    "v_mfma_f32_16x16x32_bf16 a[68:71], v[208:211], a[208:211], a[68:71]\n"
    "v_exp_f32 v23, v23\n"
    "v_mfma_f32_16x16x32_bf16 a[72:75], v[224:227], a[208:211], a[72:75]\n"
    "v_cvt_pk_bf16_f32 v153, v22, v23\n"
    "v_mfma_f32_16x16x32_bf16 a[72:75], v[228:231], a[212:215], a[72:75]\n"
    "v_exp_f32 v24, v24\n"
    "v_mfma_f32_16x16x32_bf16 a[72:75], v[232:235], a[216:219], a[72:75]\n"
    "v_mfma_f32_16x16x32_bf16 a[72:75], v[236:239], a[220:223], a[72:75]\n"
    "v_exp_f32 v25, v25\n"
    "v_mfma_f32_16x16x32_bf16 a[76:79], v[252:255], a[220:223], a[76:79]\n"
    "v_cvt_pk_bf16_f32 v168, v24, v25\n"
    "s_waitcnt lgkmcnt(0)\n"
    "v_mfma_f32_16x16x32_bf16 a[76:79], v[248:251], a[216:219], a[76:79]\n"
    "v_exp_f32 v26, v26\n"
    "v_mfma_f32_16x16x32_bf16 a[76:79], v[244:247], a[212:215], a[76:79]\n"
    "ds_read_b128 a[216:219], v2 offset:55296\n"
    "ds_read_b128 a[220:223], v2 offset:56320\n"
    "v_mfma_f32_16x16x32_bf16 a[76:79], v[240:243], a[208:211], a[76:79]\n"
    "v_exp_f32 v27, v27\n"
    "v_mfma_f32_16x16x32_bf16 v[16:19], a[192:195], a[128:131], v[32:35]\n"
    "v_cvt_pk_bf16_f32 v169, v26, v27\n"
    "v_mfma_f32_16x16x32_bf16 v[16:19], a[196:199], a[132:135], v[16:19]\n"
    "v_exp_f32 v28, v28\n"
    "v_mfma_f32_16x16x32_bf16 v[16:19], a[200:203], a[136:139], v[16:19]\n"
    "ds_read_b128 a[208:211], v2 offset:53248\n"
    "ds_read_b128 a[212:215], v2 offset:54272\n"
    "v_mfma_f32_16x16x32_bf16 v[16:19], a[204:207], a[140:143], v[16:19]\n"
    "v_exp_f32 v29, v29\n"
    "v_mfma_f32_16x16x32_bf16 v[20:23], a[204:207], a[156:159], v[36:39]\n"
    "v_cvt_pk_bf16_f32 v184, v28, v29\n"
    "v_mfma_f32_16x16x32_bf16 v[20:23], a[200:203], a[152:155], v[20:23]\n"
    "v_exp_f32 v30, v30\n"
    "v_mfma_f32_16x16x32_bf16 v[20:23], a[196:199], a[148:151], v[20:23]\n"
    "v_mfma_f32_16x16x32_bf16 v[20:23], a[192:195], a[144:147], v[20:23]\n"
    "v_exp_f32 v31, v31\n"
    "v_mfma_f32_16x16x32_bf16 v[24:27], a[192:195], a[160:163], v[40:43]\n"
    "v_cvt_pk_bf16_f32 v185, v30, v31\n"
    "v_mfma_f32_16x16x32_bf16 v[24:27], a[196:199], a[164:167], v[24:27]\n"
    "v_exp_f32 v16, v16\n"
    "v_mfma_f32_16x16x32_bf16 v[24:27], a[200:203], a[168:171], v[24:27]\n"
    "v_mfma_f32_16x16x32_bf16 v[24:27], a[204:207], a[172:175], v[24:27]\n"
    "v_exp_f32 v17, v17\n"
    "v_mfma_f32_16x16x32_bf16 v[28:31], a[204:207], a[188:191], v[44:47]\n"
    "v_cvt_pk_bf16_f32 v138, v16, v17\n"
    "s_waitcnt lgkmcnt(0)\n"
    "v_mfma_f32_16x16x32_bf16 v[28:31], a[200:203], a[184:187], v[28:31]\n"
    "v_exp_f32 v18, v18\n"
    "v_mfma_f32_16x16x32_bf16 v[28:31], a[196:199], a[180:183], v[28:31]\n"
    "ds_read_b128 a[200:203], v2 offset:26624\n"
    "ds_read_b128 a[204:207], v2 offset:27648\n"
    "v_mfma_f32_16x16x32_bf16 v[28:31], a[192:195], a[176:179], v[28:31]\n"
    "v_exp_f32 v19, v19\n"
    "v_mfma_f32_16x16x32_bf16 a[80:83], v[192:195], a[208:211], a[80:83]\n"
    "v_cvt_pk_bf16_f32 v139, v18, v19\n"
    "v_mfma_f32_16x16x32_bf16 a[80:83], v[196:199], a[212:215], a[80:83]\n"
    "v_exp_f32 v20, v20\n"
    "v_mfma_f32_16x16x32_bf16 a[80:83], v[200:203], a[216:219], a[80:83]\n"
    "ds_read_b128 a[192:195], v2 offset:24576\n"
    "ds_read_b128 a[196:199], v2 offset:25600\n"
    "v_mfma_f32_16x16x32_bf16 a[80:83], v[204:207], a[220:223], a[80:83]\n"
    "v_exp_f32 v21, v21\n"
    "v_mfma_f32_16x16x32_bf16 v[48:51], v[64:67], v[136:139], v[48:51]\n"
    "v_mfma_f32_16x16x32_bf16 a[84:87], v[220:223], a[220:223], a[84:87]\n"
    "v_cvt_pk_bf16_f32 v154, v20, v21\n"
    "v_mfma_f32_16x16x32_bf16 a[84:87], v[216:219], a[216:219], a[84:87]\n"
    "v_exp_f32 v22, v22\n"
    "v_mfma_f32_16x16x32_bf16 a[84:87], v[212:215], a[212:215], a[84:87]\n"
    "v_mfma_f32_16x16x32_bf16 a[84:87], v[208:211], a[208:211], a[84:87]\n"
    "v_exp_f32 v23, v23\n"
    "v_mfma_f32_16x16x32_bf16 a[88:91], v[224:227], a[208:211], a[88:91]\n"
    "v_cvt_pk_bf16_f32 v155, v22, v23\n"
    "v_mfma_f32_16x16x32_bf16 a[88:91], v[228:231], a[212:215], a[88:91]\n"
    "v_exp_f32 v24, v24\n"
    "v_mfma_f32_16x16x32_bf16 a[88:91], v[232:235], a[216:219], a[88:91]\n"
    "v_mfma_f32_16x16x32_bf16 a[88:91], v[236:239], a[220:223], a[88:91]\n"
    "v_exp_f32 v25, v25\n"
    "v_mfma_f32_16x16x32_bf16 v[52:55], v[64:67], v[152:155], v[52:55]\n"
    "v_mfma_f32_16x16x32_bf16 a[92:95], v[252:255], a[220:223], a[92:95]\n"
    "v_cvt_pk_bf16_f32 v170, v24, v25\n"
    "s_waitcnt lgkmcnt(0)\n"
    "v_mfma_f32_16x16x32_bf16 a[92:95], v[248:251], a[216:219], a[92:95]\n"
    "v_exp_f32 v26, v26\n"
    "v_mfma_f32_16x16x32_bf16 a[92:95], v[244:247], a[212:215], a[92:95]\n"
    "ds_read_b128 a[216:219], v2 offset:59392\n"
    "ds_read_b128 a[220:223], v2 offset:60416\n"
    "v_mfma_f32_16x16x32_bf16 a[92:95], v[240:243], a[208:211], a[92:95]\n"
    "v_exp_f32 v27, v27\n"
    "v_mfma_f32_16x16x32_bf16 v[16:19], a[192:195], a[128:131], v[32:35]\n"
    "v_cvt_pk_bf16_f32 v171, v26, v27\n"
    "v_mfma_f32_16x16x32_bf16 v[16:19], a[196:199], a[132:135], v[16:19]\n"
    "v_exp_f32 v28, v28\n"
    "v_mfma_f32_16x16x32_bf16 v[16:19], a[200:203], a[136:139], v[16:19]\n"
    "ds_read_b128 a[208:211], v2 offset:57344\n"
    "ds_read_b128 a[212:215], v2 offset:58368\n"
    "v_mfma_f32_16x16x32_bf16 v[16:19], a[204:207], a[140:143], v[16:19]\n"
    "v_exp_f32 v29, v29\n"
    "v_mfma_f32_16x16x32_bf16 v[56:59], v[64:67], v[168:171], v[56:59]\n"
    "v_mfma_f32_16x16x32_bf16 v[20:23], a[204:207], a[156:159], v[36:39]\n"
    "v_cvt_pk_bf16_f32 v186, v28, v29\n"
    "v_mfma_f32_16x16x32_bf16 v[20:23], a[200:203], a[152:155], v[20:23]\n"
    "v_exp_f32 v30, v30\n"
    "v_mfma_f32_16x16x32_bf16 v[20:23], a[196:199], a[148:151], v[20:23]\n"
    "v_mfma_f32_16x16x32_bf16 v[20:23], a[192:195], a[144:147], v[20:23]\n"
    "v_exp_f32 v31, v31\n"
    "v_mfma_f32_16x16x32_bf16 v[24:27], a[192:195], a[160:163], v[40:43]\n"
    "v_cvt_pk_bf16_f32 v187, v30, v31\n"
    "v_mfma_f32_16x16x32_bf16 v[24:27], a[196:199], a[164:167], v[24:27]\n"
    "v_exp_f32 v16, v16\n"
    "v_mfma_f32_16x16x32_bf16 v[24:27], a[200:203], a[168:171], v[24:27]\n"
    "v_mfma_f32_16x16x32_bf16 v[24:27], a[204:207], a[172:175], v[24:27]\n"
    "v_exp_f32 v17, v17\n"
    "v_mfma_f32_16x16x32_bf16 v[60:63], v[64:67], v[184:187], v[60:63]\n"
    "v_mfma_f32_16x16x32_bf16 v[28:31], a[204:207], a[188:191], v[44:47]\n"
    "v_cvt_pk_bf16_f32 v140, v16, v17\n"
    "s_waitcnt lgkmcnt(0)\n"
    "v_mfma_f32_16x16x32_bf16 v[28:31], a[200:203], a[184:187], v[28:31]\n"
    "v_exp_f32 v18, v18\n"
    "v_mfma_f32_16x16x32_bf16 v[28:31], a[196:199], a[180:183], v[28:31]\n"
    "ds_read_b128 a[200:203], v2 offset:30720\n"
    "ds_read_b128 a[204:207], v2 offset:31744\n"
    "v_mfma_f32_16x16x32_bf16 v[28:31], a[192:195], a[176:179], v[28:31]\n"
    "v_exp_f32 v19, v19\n"
    "v_mfma_f32_16x16x32_bf16 a[96:99], v[192:195], a[208:211], a[96:99]\n"
    "v_cvt_pk_bf16_f32 v141, v18, v19\n"
    "v_mfma_f32_16x16x32_bf16 a[96:99], v[196:199], a[212:215], a[96:99]\n"
    "v_exp_f32 v20, v20\n"
    "v_mfma_f32_16x16x32_bf16 a[96:99], v[200:203], a[216:219], a[96:99]\n"
    "ds_read_b128 a[192:195], v2 offset:28672\n"
    "ds_read_b128 a[196:199], v2 offset:29696\n"
    "v_mfma_f32_16x16x32_bf16 a[96:99], v[204:207], a[220:223], a[96:99]\n"
    "v_exp_f32 v21, v21\n"
    "v_mfma_f32_16x16x32_bf16 a[100:103], v[220:223], a[220:223], a[100:103]\n"
    "v_cvt_pk_bf16_f32 v156, v20, v21\n"
    "v_mfma_f32_16x16x32_bf16 a[100:103], v[216:219], a[216:219], a[100:103]\n"
    "v_exp_f32 v22, v22\n"
    "v_mfma_f32_16x16x32_bf16 a[100:103], v[212:215], a[212:215], a[100:103]\n"
    "v_mfma_f32_16x16x32_bf16 a[100:103], v[208:211], a[208:211], a[100:103]\n"
    "v_exp_f32 v23, v23\n"
    "v_mfma_f32_16x16x32_bf16 a[104:107], v[224:227], a[208:211], a[104:107]\n"
    "v_cvt_pk_bf16_f32 v157, v22, v23\n"
    "v_mfma_f32_16x16x32_bf16 a[104:107], v[228:231], a[212:215], a[104:107]\n"
    "v_exp_f32 v24, v24\n"
    "v_mfma_f32_16x16x32_bf16 a[104:107], v[232:235], a[216:219], a[104:107]\n"
    "v_mfma_f32_16x16x32_bf16 a[104:107], v[236:239], a[220:223], a[104:107]\n"
    "v_exp_f32 v25, v25\n"
    "v_mfma_f32_16x16x32_bf16 a[108:111], v[252:255], a[220:223], a[108:111]\n"
    "v_cvt_pk_bf16_f32 v172, v24, v25\n"
    "s_waitcnt lgkmcnt(0)\n"
    "v_mfma_f32_16x16x32_bf16 a[108:111], v[248:251], a[216:219], a[108:111]\n"
    "v_exp_f32 v26, v26\n"
    "v_mfma_f32_16x16x32_bf16 a[108:111], v[244:247], a[212:215], a[108:111]\n"
    "ds_read_b128 a[216:219], v2 offset:63488\n"
    "ds_read_b128 a[220:223], v2 offset:64512\n"
    "v_mfma_f32_16x16x32_bf16 a[108:111], v[240:243], a[208:211], a[108:111]\n"
    "v_exp_f32 v27, v27\n"
    "v_mfma_f32_16x16x32_bf16 v[16:19], a[192:195], a[128:131], v[32:35]\n"
    "v_cvt_pk_bf16_f32 v173, v26, v27\n"
    "v_mfma_f32_16x16x32_bf16 v[16:19], a[196:199], a[132:135], v[16:19]\n"
    "v_exp_f32 v28, v28\n"
    "v_mfma_f32_16x16x32_bf16 v[16:19], a[200:203], a[136:139], v[16:19]\n"
    "ds_read_b128 a[208:211], v2 offset:61440\n"
    "ds_read_b128 a[212:215], v2 offset:62464\n"
    "v_mfma_f32_16x16x32_bf16 v[16:19], a[204:207], a[140:143], v[16:19]\n"
    "v_exp_f32 v29, v29\n"
    "v_mfma_f32_16x16x32_bf16 v[20:23], a[204:207], a[156:159], v[36:39]\n"
    "v_cvt_pk_bf16_f32 v188, v28, v29\n"
    "v_mfma_f32_16x16x32_bf16 v[20:23], a[200:203], a[152:155], v[20:23]\n"
    "v_exp_f32 v30, v30\n"
    "v_mfma_f32_16x16x32_bf16 v[20:23], a[196:199], a[148:151], v[20:23]\n"
    "v_mfma_f32_16x16x32_bf16 v[20:23], a[192:195], a[144:147], v[20:23]\n"
    "v_exp_f32 v31, v31\n"
    "v_mfma_f32_16x16x32_bf16 v[24:27], a[192:195], a[160:163], v[40:43]\n"
    "v_cvt_pk_bf16_f32 v189, v30, v31\n"
    "v_mfma_f32_16x16x32_bf16 v[24:27], a[196:199], a[164:167], v[24:27]\n"
    "v_exp_f32 v16, v16\n"
    "v_mfma_f32_16x16x32_bf16 v[24:27], a[200:203], a[168:171], v[24:27]\n"
    "v_mfma_f32_16x16x32_bf16 v[24:27], a[204:207], a[172:175], v[24:27]\n"
    "v_exp_f32 v17, v17\n"
    "v_mfma_f32_16x16x32_bf16 v[28:31], a[204:207], a[188:191], v[44:47]\n"
    "v_cvt_pk_bf16_f32 v142, v16, v17\n"
    "s_waitcnt vmcnt(5) lgkmcnt(0)\n"
    "v_mfma_f32_16x16x32_bf16 v[28:31], a[200:203], a[184:187], v[28:31]\n"
    "v_exp_f32 v18, v18\n"
    "s_barrier\n"
    "v_mfma_f32_16x16x32_bf16 v[28:31], a[196:199], a[180:183], v[28:31]\n"
    "ds_read_b128 a[200:203], v3 offset:2048\n"
    "ds_read_b128 a[204:207], v3 offset:3072\n"
    "v_mfma_f32_16x16x32_bf16 v[28:31], a[192:195], a[176:179], v[28:31]\n"
    "v_exp_f32 v19, v19\n"
    "v_mfma_f32_16x16x32_bf16 a[112:115], v[192:195], a[208:211], a[112:115]\n"
    "v_cvt_pk_bf16_f32 v143, v18, v19\n"
    "s_add_u32 s48, s48, 0x10000\n"
    "v_mfma_f32_16x16x32_bf16 a[112:115], v[196:199], a[212:215], a[112:115]\n"
    "v_exp_f32 v20, v20\n"
    "v_mfma_f32_16x16x32_bf16 a[112:115], v[200:203], a[216:219], a[112:115]\n"
    "ds_read_b128 a[192:195], v3 offset:0\n"
    "ds_read_b128 a[196:199], v3 offset:1024\n"
    "v_mfma_f32_16x16x32_bf16 a[112:115], v[204:207], a[220:223], a[112:115]\n"
    "v_exp_f32 v21, v21\n"
    "v_mfma_f32_16x16x32_bf16 v[48:51], v[64:67], v[140:143], v[48:51]\n"
    "v_cmp_gt_u32 vcc, 32, v96\n"
    "v_mfma_f32_16x16x32_bf16 a[116:119], v[220:223], a[220:223], a[116:119]\n"
    "v_cvt_pk_bf16_f32 v158, v20, v21\n"
    "s_add_u32 s97, s97, 128\n"
    "v_mfma_f32_16x16x32_bf16 a[116:119], v[216:219], a[216:219], a[116:119]\n"
    "v_exp_f32 v22, v22\n"
    "v_mfma_f32_16x16x32_bf16 a[116:119], v[212:215], a[212:215], a[116:119]\n"
    "v_mfma_f32_16x16x32_bf16 a[116:119], v[208:211], a[208:211], a[116:119]\n"
    "v_exp_f32 v23, v23\n"
    "v_mfma_f32_16x16x32_bf16 a[120:123], v[224:227], a[208:211], a[120:123]\n"
    "v_cvt_pk_bf16_f32 v159, v22, v23\n"
    "s_mov_b32 m0, s52\n"
    "v_mfma_f32_16x16x32_bf16 a[120:123], v[228:231], a[212:215], a[120:123]\n"
    "v_exp_f32 v24, v24\n"
    "v_mfma_f32_16x16x32_bf16 a[120:123], v[232:235], a[216:219], a[120:123]\n"
    "s_cbranch_vccnz L_spin_B\n"
    "L_spinret_B:\n"
    "v_mfma_f32_16x16x32_bf16 a[120:123], v[236:239], a[220:223], a[120:123]\n"
    "v_exp_f32 v25, v25\n"
    "v_mfma_f32_16x16x32_bf16 v[52:55], v[64:67], v[156:159], v[52:55]\n"
    "s_add_u32 s96, s96, 128\n"
    "s_min_u32 s96, s96, 0x2b00\n"
    "v_mfma_f32_16x16x32_bf16 a[124:127], v[252:255], a[220:223], a[124:127]\n"
    "v_cvt_pk_bf16_f32 v174, v24, v25\n"
    "s_waitcnt lgkmcnt(0)\n"
    "v_mfma_f32_16x16x32_bf16 a[124:127], v[248:251], a[216:219], a[124:127]\n"
    "v_exp_f32 v26, v26\n"
    "v_mfma_f32_16x16x32_bf16 a[124:127], v[244:247], a[212:215], a[124:127]\n"
    "ds_read_b128 a[216:219], v3 offset:34816\n"
    "ds_read_b128 a[220:223], v3 offset:35840\n"
    "v_mfma_f32_16x16x32_bf16 a[124:127], v[240:243], a[208:211], a[124:127]\n"
    "v_exp_f32 v27, v27\n"
    "s_cmp_lg_u32 s48, 0xc0000\n"
    "s_cbranch_scc1 L_bodyA\n"
    ".p2align 6\n"
    "L_bodyC:\n"
    "v_mfma_f32_16x16x32_bf16 v[16:19], a[192:195], a[128:131], v[32:35]\n"
    "v_cvt_pk_bf16_f32 v175, v26, v27\n"
    "v_mfma_f32_16x16x32_bf16 v[16:19], a[196:199], a[132:135], v[16:19]\n"
    "v_exp_f32 v28, v28\n"
    "v_mfma_f32_16x16x32_bf16 v[16:19], a[200:203], a[136:139], v[16:19]\n"
    "ds_read_b128 a[208:211], v3 offset:32768\n"
    "ds_read_b128 a[212:215], v3 offset:33792\n"
    "v_mfma_f32_16x16x32_bf16 v[16:19], a[204:207], a[140:143], v[16:19]\n"
    "v_exp_f32 v29, v29\n"
    "v_mfma_f32_16x16x32_bf16 v[56:59], v[64:67], v[172:175], v[56:59]\n"
    "buffer_load_dwordx4 v4, s[40:43], s48 offen offset:0 sc1 lds\n"
    "v_mfma_f32_16x16x32_bf16 v[20:23], a[204:207], a[156:159], v[36:39]\n"
    "v_cvt_pk_bf16_f32 v190, v28, v29\n"
    "v_cmp_nge_f32 vcc, s54, v48\n"
    "v_mfma_f32_16x16x32_bf16 v[20:23], a[200:203], a[152:155], v[20:23]\n"
    "v_exp_f32 v30, v30\n"
    "v_mfma_f32_16x16x32_bf16 v[20:23], a[196:199], a[148:151], v[20:23]\n"
    "s_cbranch_vccnz L_rare_C0\n"
    "L_ret_C0:\n"
    "v_add_f32 v68, v48, v68\n"
    "v_mfma_f32_16x16x32_bf16 v[20:23], a[192:195], a[144:147], v[20:23]\n"
    "v_exp_f32 v31, v31\n"
    "v_mfma_f32_16x16x32_bf16 v[24:27], a[192:195], a[160:163], v[40:43]\n"
    "v_cvt_pk_bf16_f32 v191, v30, v31\n"
    "v_cmp_nge_f32 vcc, s54, v52\n"
    "v_mfma_f32_16x16x32_bf16 v[24:27], a[196:199], a[164:167], v[24:27]\n"
    "v_exp_f32 v16, v16\n"
    "v_mfma_f32_16x16x32_bf16 v[24:27], a[200:203], a[168:171], v[24:27]\n"
    "s_cbranch_vccnz L_rare_C1\n"
    "L_ret_C1:\n"
    "v_add_f32 v69, v52, v69\n"
    "v_mfma_f32_16x16x32_bf16 v[24:27], a[204:207], a[172:175], v[24:27]\n"
    "v_exp_f32 v17, v17\n"
    "v_mfma_f32_16x16x32_bf16 v[60:63], v[64:67], v[188:191], v[60:63]\n"
    "buffer_load_dwordx4 v4, s[40:43], s48 offen offset:1024 sc1 lds\n"
    "v_mfma_f32_16x16x32_bf16 v[28:31], a[204:207], a[188:191], v[44:47]\n"
    "v_cvt_pk_bf16_f32 v192, v16, v17\n"
    "s_waitcnt lgkmcnt(0)\n"
    "v_mfma_f32_16x16x32_bf16 v[28:31], a[200:203], a[184:187], v[28:31]\n"
    "v_exp_f32 v18, v18\n"
    "v_mfma_f32_16x16x32_bf16 v[28:31], a[196:199], a[180:183], v[28:31]\n"
    "ds_read_b128 a[200:203], v3 offset:6144\n"
    "ds_read_b128 a[204:207], v3 offset:7168\n"
    "v_mfma_f32_16x16x32_bf16 v[28:31], a[192:195], a[176:179], v[28:31]\n"
    "v_exp_f32 v19, v19\n"
    "v_mfma_f32_16x16x32_bf16 a[0:3], v[128:131], a[208:211], a[0:3]\n"
    "v_cvt_pk_bf16_f32 v193, v18, v19\n"
    "v_mfma_f32_16x16x32_bf16 a[0:3], v[132:135], a[212:215], a[0:3]\n"
    "v_exp_f32 v20, v20\n"
    "v_mfma_f32_16x16x32_bf16 a[0:3], v[136:139], a[216:219], a[0:3]\n"
    "ds_read_b128 a[192:195], v3 offset:4096\n"
    "ds_read_b128 a[196:199], v3 offset:5120\n"
    "v_mfma_f32_16x16x32_bf16 a[0:3], v[140:143], a[220:223], a[0:3]\n"
    "v_exp_f32 v21, v21\n"
    "v_mfma_f32_16x16x32_bf16 a[4:7], v[156:159], a[220:223], a[4:7]\n"
    "v_cvt_pk_bf16_f32 v208, v20, v21\n"
    "v_cmp_nge_f32 vcc, s54, v56\n"
    "v_mfma_f32_16x16x32_bf16 a[4:7], v[152:155], a[216:219], a[4:7]\n"
    "v_exp_f32 v22, v22\n"
    "v_mfma_f32_16x16x32_bf16 a[4:7], v[148:151], a[212:215], a[4:7]\n"
    "s_cbranch_vccnz L_rare_C2\n"
    "L_ret_C2:\n"
    "v_add_f32 v70, v56, v70\n"
    "v_mfma_f32_16x16x32_bf16 a[4:7], v[144:147], a[208:211], a[4:7]\n"
    "v_exp_f32 v23, v23\n"
    "v_mfma_f32_16x16x32_bf16 a[8:11], v[160:163], a[208:211], a[8:11]\n"
    "v_cvt_pk_bf16_f32 v209, v22, v23\n"
    "v_cmp_nge_f32 vcc, s54, v60\n"
    "v_mfma_f32_16x16x32_bf16 a[8:11], v[164:167], a[212:215], a[8:11]\n"
    "v_exp_f32 v24, v24\n"
    "v_mfma_f32_16x16x32_bf16 a[8:11], v[168:171], a[216:219], a[8:11]\n"
    "s_cbranch_vccnz L_rare_C3\n"
    "L_ret_C3:\n"
    "v_add_f32 v71, v60, v71\n"
    "v_mfma_f32_16x16x32_bf16 a[8:11], v[172:175], a[220:223], a[8:11]\n"
    "v_exp_f32 v25, v25\n"
    "v_mfma_f32_16x16x32_bf16 a[12:15], v[188:191], a[220:223], a[12:15]\n"
    "v_cvt_pk_bf16_f32 v224, v24, v25\n"
    "s_waitcnt lgkmcnt(0)\n"
    "v_mfma_f32_16x16x32_bf16 a[12:15], v[184:187], a[216:219], a[12:15]\n"
    "v_exp_f32 v26, v26\n"
    "v_mfma_f32_16x16x32_bf16 a[12:15], v[180:183], a[212:215], a[12:15]\n"
    "ds_read_b128 a[216:219], v3 offset:38912\n"
    "ds_read_b128 a[220:223], v3 offset:39936\n"
    "v_mfma_f32_16x16x32_bf16 a[12:15], v[176:179], a[208:211], a[12:15]\n"
    "v_exp_f32 v27, v27\n"
    "v_mfma_f32_16x16x32_bf16 v[16:19], a[192:195], a[128:131], v[32:35]\n"
    "v_cvt_pk_bf16_f32 v225, v26, v27\n"
    "v_mfma_f32_16x16x32_bf16 v[16:19], a[196:199], a[132:135], v[16:19]\n"
    "v_exp_f32 v28, v28\n"
    "v_mfma_f32_16x16x32_bf16 v[16:19], a[200:203], a[136:139], v[16:19]\n"
    "ds_read_b128 a[208:211], v3 offset:36864\n"
    "ds_read_b128 a[212:215], v3 offset:37888\n"
    "v_mfma_f32_16x16x32_bf16 v[16:19], a[204:207], a[140:143], v[16:19]\n"
    "v_exp_f32 v29, v29\n"
    "v_mfma_f32_16x16x32_bf16 v[20:23], a[204:207], a[156:159], v[36:39]\n"
    "v_cvt_pk_bf16_f32 v240, v28, v29\n"
    "v_mfma_f32_16x16x32_bf16 v[20:23], a[200:203], a[152:155], v[20:23]\n"
    "v_exp_f32 v30, v30\n"
    "v_mfma_f32_16x16x32_bf16 v[20:23], a[196:199], a[148:151], v[20:23]\n"
    "buffer_load_dwordx4 v4, s[40:43], s48 offen offset:2048 sc1 lds\n"
    "v_mfma_f32_16x16x32_bf16 v[20:23], a[192:195], a[144:147], v[20:23]\n"
    "v_exp_f32 v31, v31\n"
    "v_mfma_f32_16x16x32_bf16 v[24:27], a[192:195], a[160:163], v[40:43]\n"
    "v_cvt_pk_bf16_f32 v241, v30, v31\n"
    "v_mfma_f32_16x16x32_bf16 v[24:27], a[196:199], a[164:167], v[24:27]\n"
    "v_exp_f32 v16, v16\n"
    "v_mfma_f32_16x16x32_bf16 v[24:27], a[200:203], a[168:171], v[24:27]\n"
    "buffer_load_dwordx4 v4, s[40:43], s48 offen offset:3072 sc1 lds\n"
    "s_add_u32 m0, m0, 0x1000\n"
    "v_mfma_f32_16x16x32_bf16 v[24:27], a[204:207], a[172:175], v[24:27]\n"
    "v_exp_f32 v17, v17\n"
    "v_mfma_f32_16x16x32_bf16 v[28:31], a[204:207], a[188:191], v[44:47]\n"
    "v_cvt_pk_bf16_f32 v194, v16, v17\n"
    "s_waitcnt lgkmcnt(0)\n"
    "v_mfma_f32_16x16x32_bf16 v[28:31], a[200:203], a[184:187], v[28:31]\n"
    "v_exp_f32 v18, v18\n"
    "v_mfma_f32_16x16x32_bf16 v[28:31], a[196:199], a[180:183], v[28:31]\n"
    "ds_read_b128 a[200:203], v3 offset:10240\n"
    "ds_read_b128 a[204:207], v3 offset:11264\n"
    "v_mfma_f32_16x16x32_bf16 v[28:31], a[192:195], a[176:179], v[28:31]\n"
    "v_exp_f32 v19, v19\n"
    "v_mfma_f32_16x16x32_bf16 a[16:19], v[128:131], a[208:211], a[16:19]\n"
    "v_cvt_pk_bf16_f32 v195, v18, v19\n"
    "v_mfma_f32_16x16x32_bf16 a[16:19], v[132:135], a[212:215], a[16:19]\n"
    "v_exp_f32 v20, v20\n"
    "v_mfma_f32_16x16x32_bf16 a[16:19], v[136:139], a[216:219], a[16:19]\n"
    "ds_read_b128 a[192:195], v3 offset:8192\n"
    "ds_read_b128 a[196:199], v3 offset:9216\n"
    "v_mfma_f32_16x16x32_bf16 a[16:19], v[140:143], a[220:223], a[16:19]\n"
    "v_exp_f32 v21, v21\n"
    "v_mfma_f32_16x16x32_bf16 v[48:51], v[64:67], v[192:195], 0\n"
    "buffer_load_dwordx4 v5, s[40:43], s48 offen offset:0 sc1 lds\n"
    "v_mfma_f32_16x16x32_bf16 a[20:23], v[156:159], a[220:223], a[20:23]\n"
    "v_cvt_pk_bf16_f32 v210, v20, v21\n"
    "v_mfma_f32_16x16x32_bf16 a[20:23], v[152:155], a[216:219], a[20:23]\n"
    "v_exp_f32 v22, v22\n"
    "v_mfma_f32_16x16x32_bf16 a[20:23], v[148:151], a[212:215], a[20:23]\n"
    "buffer_load_dwordx4 v5, s[40:43], s48 offen offset:1024 sc1 lds\n"
    "v_mfma_f32_16x16x32_bf16 a[20:23], v[144:147], a[208:211], a[20:23]\n"
    "v_exp_f32 v23, v23\n"
    "v_mfma_f32_16x16x32_bf16 a[24:27], v[160:163], a[208:211], a[24:27]\n"
    "v_cvt_pk_bf16_f32 v211, v22, v23\n"
    "v_mfma_f32_16x16x32_bf16 a[24:27], v[164:167], a[212:215], a[24:27]\n"
    "v_exp_f32 v24, v24\n"
    "v_mfma_f32_16x16x32_bf16 a[24:27], v[168:171], a[216:219], a[24:27]\n"
    "buffer_load_dwordx4 v5, s[40:43], s48 offen offset:2048 sc1 lds\n"
    "v_mfma_f32_16x16x32_bf16 a[24:27], v[172:175], a[220:223], a[24:27]\n"
    "v_exp_f32 v25, v25\n"
    "v_mfma_f32_16x16x32_bf16 v[52:55], v[64:67], v[208:211], 0\n"
    "buffer_load_dwordx4 v5, s[40:43], s48 offen offset:3072 sc1 lds\n"
    "s_add_u32 m0, m0, 0x1000\n"
    "v_mfma_f32_16x16x32_bf16 a[28:31], v[188:191], a[220:223], a[28:31]\n"
    "v_cvt_pk_bf16_f32 v226, v24, v25\n"
    "s_waitcnt lgkmcnt(0)\n"
    "v_mfma_f32_16x16x32_bf16 a[28:31], v[184:187], a[216:219], a[28:31]\n"
    "v_exp_f32 v26, v26\n"
    "v_mfma_f32_16x16x32_bf16 a[28:31], v[180:183], a[212:215], a[28:31]\n"
    "ds_read_b128 a[216:219], v3 offset:43008\n"
    "ds_read_b128 a[220:223], v3 offset:44032\n"
    "v_mfma_f32_16x16x32_bf16 a[28:31], v[176:179], a[208:211], a[28:31]\n"
    "v_exp_f32 v27, v27\n"
    "v_mfma_f32_16x16x32_bf16 v[16:19], a[192:195], a[128:131], v[32:35]\n"
    "v_cvt_pk_bf16_f32 v227, v26, v27\n"
    "v_mfma_f32_16x16x32_bf16 v[16:19], a[196:199], a[132:135], v[16:19]\n"
    "v_exp_f32 v28, v28\n"
    "v_mfma_f32_16x16x32_bf16 v[16:19], a[200:203], a[136:139], v[16:19]\n"
    "ds_read_b128 a[208:211], v3 offset:40960\n"
    "ds_read_b128 a[212:215], v3 offset:41984\n"
    "v_mfma_f32_16x16x32_bf16 v[16:19], a[204:207], a[140:143], v[16:19]\n"
    "v_exp_f32 v29, v29\n"
    "v_mfma_f32_16x16x32_bf16 v[56:59], v[64:67], v[224:227], 0\n"
    "v_mfma_f32_16x16x32_bf16 v[20:23], a[204:207], a[156:159], v[36:39]\n"
    "v_cvt_pk_bf16_f32 v242, v28, v29\n"
    "v_mfma_f32_16x16x32_bf16 v[20:23], a[200:203], a[152:155], v[20:23]\n"
    "v_exp_f32 v30, v30\n"
    "v_mfma_f32_16x16x32_bf16 v[20:23], a[196:199], a[148:151], v[20:23]\n"
    "buffer_load_dwordx4 v6, s[40:43], s48 offen offset:0 sc1 lds\n"
    "v_mfma_f32_16x16x32_bf16 v[20:23], a[192:195], a[144:147], v[20:23]\n"
    "v_exp_f32 v31, v31\n"
    "v_mfma_f32_16x16x32_bf16 v[24:27], a[192:195], a[160:163], v[40:43]\n"
    "v_cvt_pk_bf16_f32 v243, v30, v31\n"
    "v_mfma_f32_16x16x32_bf16 v[24:27], a[196:199], a[164:167], v[24:27]\n"
    "v_exp_f32 v16, v16\n"
    "v_mfma_f32_16x16x32_bf16 v[24:27], a[200:203], a[168:171], v[24:27]\n"
    "buffer_load_dwordx4 v6, s[40:43], s48 offen offset:1024 sc1 lds\n"
    "v_mfma_f32_16x16x32_bf16 v[24:27], a[204:207], a[172:175], v[24:27]\n"
    "v_exp_f32 v17, v17\n"
    "v_mfma_f32_16x16x32_bf16 v[60:63], v[64:67], v[240:243], 0\n"
    "v_mfma_f32_16x16x32_bf16 v[28:31], a[204:207], a[188:191], v[44:47]\n"
    "v_cvt_pk_bf16_f32 v196, v16, v17\n"
    "s_waitcnt lgkmcnt(0)\n"
    "v_mfma_f32_16x16x32_bf16 v[28:31], a[200:203], a[184:187], v[28:31]\n"
    "v_exp_f32 v18, v18\n"
    "v_mfma_f32_16x16x32_bf16 v[28:31], a[196:199], a[180:183], v[28:31]\n"
    "ds_read_b128 a[200:203], v3 offset:14336\n"
    "ds_read_b128 a[204:207], v3 offset:15360\n"
    "v_mfma_f32_16x16x32_bf16 v[28:31], a[192:195], a[176:179], v[28:31]\n"
    "v_exp_f32 v19, v19\n"
    "v_mfma_f32_16x16x32_bf16 a[32:35], v[128:131], a[208:211], a[32:35]\n"
    "v_cvt_pk_bf16_f32 v197, v18, v19\n"
    "v_mfma_f32_16x16x32_bf16 a[32:35], v[132:135], a[212:215], a[32:35]\n"
    "v_exp_f32 v20, v20\n"
    "v_mfma_f32_16x16x32_bf16 a[32:35], v[136:139], a[216:219], a[32:35]\n"
    "ds_read_b128 a[192:195], v3 offset:12288\n"
    "ds_read_b128 a[196:199], v3 offset:13312\n"
    "v_mfma_f32_16x16x32_bf16 a[32:35], v[140:143], a[220:223], a[32:35]\n"
    "v_exp_f32 v21, v21\n"
    "v_mfma_f32_16x16x32_bf16 a[36:39], v[156:159], a[220:223], a[36:39]\n"
    "v_cvt_pk_bf16_f32 v212, v20, v21\n"
    "v_mfma_f32_16x16x32_bf16 a[36:39], v[152:155], a[216:219], a[36:39]\n"
    "v_exp_f32 v22, v22\n"
    "v_mfma_f32_16x16x32_bf16 a[36:39], v[148:151], a[212:215], a[36:39]\n"
    "buffer_load_dwordx4 v6, s[40:43], s48 offen offset:2048 sc1 lds\n"
    "v_mfma_f32_16x16x32_bf16 a[36:39], v[144:147], a[208:211], a[36:39]\n"
    "v_exp_f32 v23, v23\n"
    "v_mfma_f32_16x16x32_bf16 a[40:43], v[160:163], a[208:211], a[40:43]\n"
    "v_cvt_pk_bf16_f32 v213, v22, v23\n"
    "v_mfma_f32_16x16x32_bf16 a[40:43], v[164:167], a[212:215], a[40:43]\n"
    "v_exp_f32 v24, v24\n"
    "v_mfma_f32_16x16x32_bf16 a[40:43], v[168:171], a[216:219], a[40:43]\n"
    "buffer_load_dwordx4 v6, s[40:43], s48 offen offset:3072 sc1 lds\n"
    "s_add_u32 m0, m0, 0x1000\n"
    "v_mfma_f32_16x16x32_bf16 a[40:43], v[172:175], a[220:223], a[40:43]\n"
    "v_exp_f32 v25, v25\n"
    "v_mfma_f32_16x16x32_bf16 a[44:47], v[188:191], a[220:223], a[44:47]\n"
    "v_cvt_pk_bf16_f32 v228, v24, v25\n"
    "s_waitcnt lgkmcnt(0)\n"
    "v_mfma_f32_16x16x32_bf16 a[44:47], v[184:187], a[216:219], a[44:47]\n"
    "v_exp_f32 v26, v26\n"
    "v_mfma_f32_16x16x32_bf16 a[44:47], v[180:183], a[212:215], a[44:47]\n"
    "ds_read_b128 a[216:219], v3 offset:47104\n"
    "ds_read_b128 a[220:223], v3 offset:48128\n"
    "v_mfma_f32_16x16x32_bf16 a[44:47], v[176:179], a[208:211], a[44:47]\n"
    "v_exp_f32 v27, v27\n"
    "v_mfma_f32_16x16x32_bf16 v[16:19], a[192:195], a[128:131], v[32:35]\n"
    "v_cvt_pk_bf16_f32 v229, v26, v27\n"
    "v_mfma_f32_16x16x32_bf16 v[16:19], a[196:199], a[132:135], v[16:19]\n"
    "v_exp_f32 v28, v28\n"
    "v_mfma_f32_16x16x32_bf16 v[16:19], a[200:203], a[136:139], v[16:19]\n"
    "ds_read_b128 a[208:211], v3 offset:45056\n"
    "ds_read_b128 a[212:215], v3 offset:46080\n"
    "v_mfma_f32_16x16x32_bf16 v[16:19], a[204:207], a[140:143], v[16:19]\n"
    "v_exp_f32 v29, v29\n"
    "v_mfma_f32_16x16x32_bf16 v[20:23], a[204:207], a[156:159], v[36:39]\n"
    "v_cvt_pk_bf16_f32 v244, v28, v29\n"
    "v_mfma_f32_16x16x32_bf16 v[20:23], a[200:203], a[152:155], v[20:23]\n"
    "v_exp_f32 v30, v30\n"
    "v_mfma_f32_16x16x32_bf16 v[20:23], a[196:199], a[148:151], v[20:23]\n"
    "buffer_load_dwordx4 v7, s[40:43], s48 offen offset:0 sc1 lds\n"
    "v_mfma_f32_16x16x32_bf16 v[20:23], a[192:195], a[144:147], v[20:23]\n"
    "v_exp_f32 v31, v31\n"
    "v_mfma_f32_16x16x32_bf16 v[24:27], a[192:195], a[160:163], v[40:43]\n"
    "v_cvt_pk_bf16_f32 v245, v30, v31\n"
    "v_mfma_f32_16x16x32_bf16 v[24:27], a[196:199], a[164:167], v[24:27]\n"
    "v_exp_f32 v16, v16\n"
    "v_mfma_f32_16x16x32_bf16 v[24:27], a[200:203], a[168:171], v[24:27]\n"
    "buffer_load_dwordx4 v7, s[40:43], s48 offen offset:1024 sc1 lds\n"
    "v_mfma_f32_16x16x32_bf16 v[24:27], a[204:207], a[172:175], v[24:27]\n"
    "v_exp_f32 v17, v17\n"
    "v_mfma_f32_16x16x32_bf16 v[28:31], a[204:207], a[188:191], v[44:47]\n"
    "v_cvt_pk_bf16_f32 v198, v16, v17\n"
    "s_waitcnt lgkmcnt(0)\n"
    "v_mfma_f32_16x16x32_bf16 v[28:31], a[200:203], a[184:187], v[28:31]\n"
    "v_exp_f32 v18, v18\n"
    "v_mfma_f32_16x16x32_bf16 v[28:31], a[196:199], a[180:183], v[28:31]\n"
    "ds_read_b128 a[200:203], v3 offset:18432\n"
    "ds_read_b128 a[204:207], v3 offset:19456\n"
    "v_mfma_f32_16x16x32_bf16 v[28:31], a[192:195], a[176:179], v[28:31]\n"
    "v_exp_f32 v19, v19\n"
    "v_mfma_f32_16x16x32_bf16 a[48:51], v[128:131], a[208:211], a[48:51]\n"
    "v_cvt_pk_bf16_f32 v199, v18, v19\n"
    "v_mfma_f32_16x16x32_bf16 a[48:51], v[132:135], a[212:215], a[48:51]\n"
    "v_exp_f32 v20, v20\n"
    "v_mfma_f32_16x16x32_bf16 a[48:51], v[136:139], a[216:219], a[48:51]\n"
    "ds_read_b128 a[192:195], v3 offset:16384\n"
    "ds_read_b128 a[196:199], v3 offset:17408\n"
    "v_mfma_f32_16x16x32_bf16 a[48:51], v[140:143], a[220:223], a[48:51]\n"
    "v_exp_f32 v21, v21\n"
    "v_mfma_f32_16x16x32_bf16 v[48:51], v[64:67], v[196:199], v[48:51]\n"
    "v_mfma_f32_16x16x32_bf16 a[52:55], v[156:159], a[220:223], a[52:55]\n"
    "v_cvt_pk_bf16_f32 v214, v20, v21\n"
    "v_mfma_f32_16x16x32_bf16 a[52:55], v[152:155], a[216:219], a[52:55]\n"
    "v_exp_f32 v22, v22\n"
    "v_mfma_f32_16x16x32_bf16 a[52:55], v[148:151], a[212:215], a[52:55]\n"
    "buffer_load_dwordx4 v7, s[40:43], s48 offen offset:2048 sc1 lds\n"
    "v_mfma_f32_16x16x32_bf16 a[52:55], v[144:147], a[208:211], a[52:55]\n"
    "v_exp_f32 v23, v23\n"
    "v_mfma_f32_16x16x32_bf16 a[56:59], v[160:163], a[208:211], a[56:59]\n"
    "v_cvt_pk_bf16_f32 v215, v22, v23\n"
    "v_mfma_f32_16x16x32_bf16 a[56:59], v[164:167], a[212:215], a[56:59]\n"
    "v_exp_f32 v24, v24\n"
    "v_mfma_f32_16x16x32_bf16 a[56:59], v[168:171], a[216:219], a[56:59]\n"
    "buffer_load_dwordx4 v7, s[40:43], s48 offen offset:3072 sc1 lds\n"
    "v_mfma_f32_16x16x32_bf16 a[56:59], v[172:175], a[220:223], a[56:59]\n"
    "v_exp_f32 v25, v25\n"
    "v_mfma_f32_16x16x32_bf16 v[52:55], v[64:67], v[212:215], v[52:55]\n"
    "v_mfma_f32_16x16x32_bf16 a[60:63], v[188:191], a[220:223], a[60:63]\n"
    "v_cvt_pk_bf16_f32 v230, v24, v25\n"
    "s_waitcnt lgkmcnt(0)\n"
    "v_mfma_f32_16x16x32_bf16 a[60:63], v[184:187], a[216:219], a[60:63]\n"
    "v_exp_f32 v26, v26\n"
    "v_mfma_f32_16x16x32_bf16 a[60:63], v[180:183], a[212:215], a[60:63]\n"
    "ds_read_b128 a[216:219], v3 offset:51200\n"
    "ds_read_b128 a[220:223], v3 offset:52224\n"
    "v_mfma_f32_16x16x32_bf16 a[60:63], v[176:179], a[208:211], a[60:63]\n"
    "v_exp_f32 v27, v27\n"
    "v_mfma_f32_16x16x32_bf16 v[16:19], a[192:195], a[128:131], v[32:35]\n"
    "v_cvt_pk_bf16_f32 v231, v26, v27\n"
    "v_mfma_f32_16x16x32_bf16 v[16:19], a[196:199], a[132:135], v[16:19]\n"
    "v_exp_f32 v28, v28\n"
    "v_mfma_f32_16x16x32_bf16 v[16:19], a[200:203], a[136:139], v[16:19]\n"
    "ds_read_b128 a[208:211], v3 offset:49152\n"
    "ds_read_b128 a[212:215], v3 offset:50176\n"
    "v_mfma_f32_16x16x32_bf16 v[16:19], a[204:207], a[140:143], v[16:19]\n"
    "v_exp_f32 v29, v29\n"
    "v_mfma_f32_16x16x32_bf16 v[56:59], v[64:67], v[228:231], v[56:59]\n"
    "buffer_load_dword v96, off, s[64:67], s96 offset:0 sc1\n"
    "buffer_atomic_add v78, v95, s[64:67], s97 offen\n"
    "v_mfma_f32_16x16x32_bf16 v[20:23], a[204:207], a[156:159], v[36:39]\n"
    "v_cvt_pk_bf16_f32 v246, v28, v29\n"
    "v_mfma_f32_16x16x32_bf16 v[20:23], a[200:203], a[152:155], v[20:23]\n"
    "v_exp_f32 v30, v30\n"
    "v_mfma_f32_16x16x32_bf16 v[20:23], a[196:199], a[148:151], v[20:23]\n"
    "v_mfma_f32_16x16x32_bf16 v[20:23], a[192:195], a[144:147], v[20:23]\n"
    "v_exp_f32 v31, v31\n"
    "v_mfma_f32_16x16x32_bf16 v[24:27], a[192:195], a[160:163], v[40:43]\n"
    "v_cvt_pk_bf16_f32 v247, v30, v31\n"
    "v_mfma_f32_16x16x32_bf16 v[24:27], a[196:199], a[164:167], v[24:27]\n"
    "v_exp_f32 v16, v16\n"
    "v_mfma_f32_16x16x32_bf16 v[24:27], a[200:203], a[168:171], v[24:27]\n"
    "v_mfma_f32_16x16x32_bf16 v[24:27], a[204:207], a[172:175], v[24:27]\n"
    "v_exp_f32 v17, v17\n"
    "v_mfma_f32_16x16x32_bf16 v[60:63], v[64:67], v[244:247], v[60:63]\n"
    "v_mfma_f32_16x16x32_bf16 v[28:31], a[204:207], a[188:191], v[44:47]\n"
    "v_cvt_pk_bf16_f32 v200, v16, v17\n"
    "s_waitcnt lgkmcnt(0)\n"
    "v_mfma_f32_16x16x32_bf16 v[28:31], a[200:203], a[184:187], v[28:31]\n"
    "v_exp_f32 v18, v18\n"
    "v_mfma_f32_16x16x32_bf16 v[28:31], a[196:199], a[180:183], v[28:31]\n"
    "ds_read_b128 a[200:203], v3 offset:22528\n"
    "ds_read_b128 a[204:207], v3 offset:23552\n"
    "v_mfma_f32_16x16x32_bf16 v[28:31], a[192:195], a[176:179], v[28:31]\n"
    "v_exp_f32 v19, v19\n"
    "v_mfma_f32_16x16x32_bf16 a[64:67], v[128:131], a[208:211], a[64:67]\n"
    "v_cvt_pk_bf16_f32 v201, v18, v19\n"
    "v_mfma_f32_16x16x32_bf16 a[64:67], v[132:135], a[212:215], a[64:67]\n"
    "v_exp_f32 v20, v20\n"
    "v_mfma_f32_16x16x32_bf16 a[64:67], v[136:139], a[216:219], a[64:67]\n"
    "ds_read_b128 a[192:195], v3 offset:20480\n"
    "ds_read_b128 a[196:199], v3 offset:21504\n"
    "v_mfma_f32_16x16x32_bf16 a[64:67], v[140:143], a[220:223], a[64:67]\n"
    "v_exp_f32 v21, v21\n"
    "v_mfma_f32_16x16x32_bf16 a[68:71], v[156:159], a[220:223], a[68:71]\n"
    "v_cvt_pk_bf16_f32 v216, v20, v21\n"
    "v_mfma_f32_16x16x32_bf16 a[68:71], v[152:155], a[216:219], a[68:71]\n"
    "v_exp_f32 v22, v22\n"
    "v_mfma_f32_16x16x32_bf16 a[68:71], v[148:151], a[212:215], a[68:71]\n"
    "v_mfma_f32_16x16x32_bf16 a[68:71], v[144:147], a[208:211], a[68:71]\n"
    "v_exp_f32 v23, v23\n"
    "v_mfma_f32_16x16x32_bf16 a[72:75], v[160:163], a[208:211], a[72:75]\n"
    "v_cvt_pk_bf16_f32 v217, v22, v23\n"
    "v_mfma_f32_16x16x32_bf16 a[72:75], v[164:167], a[212:215], a[72:75]\n"
    "v_exp_f32 v24, v24\n"
    "v_mfma_f32_16x16x32_bf16 a[72:75], v[168:171], a[216:219], a[72:75]\n"
    "v_mfma_f32_16x16x32_bf16 a[72:75], v[172:175], a[220:223], a[72:75]\n"
    "v_exp_f32 v25, v25\n"
    "v_mfma_f32_16x16x32_bf16 a[76:79], v[188:191], a[220:223], a[76:79]\n"
    "v_cvt_pk_bf16_f32 v232, v24, v25\n"
    "s_waitcnt lgkmcnt(0)\n"
    "v_mfma_f32_16x16x32_bf16 a[76:79], v[184:187], a[216:219], a[76:79]\n"
    "v_exp_f32 v26, v26\n"
    "v_mfma_f32_16x16x32_bf16 a[76:79], v[180:183], a[212:215], a[76:79]\n"
    "ds_read_b128 a[216:219], v3 offset:55296\n"
    "ds_read_b128 a[220:223], v3 offset:56320\n"
    "v_mfma_f32_16x16x32_bf16 a[76:79], v[176:179], a[208:211], a[76:79]\n"
    "v_exp_f32 v27, v27\n"
    "v_mfma_f32_16x16x32_bf16 v[16:19], a[192:195], a[128:131], v[32:35]\n"
    "v_cvt_pk_bf16_f32 v233, v26, v27\n"
    "v_mfma_f32_16x16x32_bf16 v[16:19], a[196:199], a[132:135], v[16:19]\n"
    "v_exp_f32 v28, v28\n"
    "v_mfma_f32_16x16x32_bf16 v[16:19], a[200:203], a[136:139], v[16:19]\n"
    "ds_read_b128 a[208:211], v3 offset:53248\n"
    "ds_read_b128 a[212:215], v3 offset:54272\n"
    "v_mfma_f32_16x16x32_bf16 v[16:19], a[204:207], a[140:143], v[16:19]\n"
    "v_exp_f32 v29, v29\n"
    "v_mfma_f32_16x16x32_bf16 v[20:23], a[204:207], a[156:159], v[36:39]\n"
    "v_cvt_pk_bf16_f32 v248, v28, v29\n"
    "v_mfma_f32_16x16x32_bf16 v[20:23], a[200:203], a[152:155], v[20:23]\n"
    "v_exp_f32 v30, v30\n"
    "v_mfma_f32_16x16x32_bf16 v[20:23], a[196:199], a[148:151], v[20:23]\n"
    "v_mfma_f32_16x16x32_bf16 v[20:23], a[192:195], a[144:147], v[20:23]\n"
    "v_exp_f32 v31, v31\n"
    "v_mfma_f32_16x16x32_bf16 v[24:27], a[192:195], a[160:163], v[40:43]\n"
    "v_cvt_pk_bf16_f32 v249, v30, v31\n"
    "v_mfma_f32_16x16x32_bf16 v[24:27], a[196:199], a[164:167], v[24:27]\n"
    "v_exp_f32 v16, v16\n"
    "v_mfma_f32_16x16x32_bf16 v[24:27], a[200:203], a[168:171], v[24:27]\n"
    "v_mfma_f32_16x16x32_bf16 v[24:27], a[204:207], a[172:175], v[24:27]\n"
    "v_exp_f32 v17, v17\n"
    "v_mfma_f32_16x16x32_bf16 v[28:31], a[204:207], a[188:191], v[44:47]\n"
    "v_cvt_pk_bf16_f32 v202, v16, v17\n"
    "s_waitcnt lgkmcnt(0)\n"
    "v_mfma_f32_16x16x32_bf16 v[28:31], a[200:203], a[184:187], v[28:31]\n"
    "v_exp_f32 v18, v18\n"
    "v_mfma_f32_16x16x32_bf16 v[28:31], a[196:199], a[180:183], v[28:31]\n"
    "ds_read_b128 a[200:203], v3 offset:26624\n"
    "ds_read_b128 a[204:207], v3 offset:27648\n"
    "v_mfma_f32_16x16x32_bf16 v[28:31], a[192:195], a[176:179], v[28:31]\n"
    "v_exp_f32 v19, v19\n"
    "v_mfma_f32_16x16x32_bf16 a[80:83], v[128:131], a[208:211], a[80:83]\n"
    "v_cvt_pk_bf16_f32 v203, v18, v19\n"
    "v_mfma_f32_16x16x32_bf16 a[80:83], v[132:135], a[212:215], a[80:83]\n"
    "v_exp_f32 v20, v20\n"
    "v_mfma_f32_16x16x32_bf16 a[80:83], v[136:139], a[216:219], a[80:83]\n"
    "ds_read_b128 a[192:195], v3 offset:24576\n"
    "ds_read_b128 a[196:199], v3 offset:25600\n"
    "v_mfma_f32_16x16x32_bf16 a[80:83], v[140:143], a[220:223], a[80:83]\n"
    "v_exp_f32 v21, v21\n"
    "v_mfma_f32_16x16x32_bf16 v[48:51], v[64:67], v[200:203], v[48:51]\n"
    "v_mfma_f32_16x16x32_bf16 a[84:87], v[156:159], a[220:223], a[84:87]\n"
    "v_cvt_pk_bf16_f32 v218, v20, v21\n"
    "v_mfma_f32_16x16x32_bf16 a[84:87], v[152:155], a[216:219], a[84:87]\n"
    "v_exp_f32 v22, v22\n"
    "v_mfma_f32_16x16x32_bf16 a[84:87], v[148:151], a[212:215], a[84:87]\n"
    "v_mfma_f32_16x16x32_bf16 a[84:87], v[144:147], a[208:211], a[84:87]\n"
    "v_exp_f32 v23, v23\n"
    "v_mfma_f32_16x16x32_bf16 a[88:91], v[160:163], a[208:211], a[88:91]\n"
    "v_cvt_pk_bf16_f32 v219, v22, v23\n"
    "v_mfma_f32_16x16x32_bf16 a[88:91], v[164:167], a[212:215], a[88:91]\n"
    "v_exp_f32 v24, v24\n"
    "v_mfma_f32_16x16x32_bf16 a[88:91], v[168:171], a[216:219], a[88:91]\n"
    "v_mfma_f32_16x16x32_bf16 a[88:91], v[172:175], a[220:223], a[88:91]\n"
    "v_exp_f32 v25, v25\n"
    "v_mfma_f32_16x16x32_bf16 v[52:55], v[64:67], v[216:219], v[52:55]\n"
    "v_mfma_f32_16x16x32_bf16 a[92:95], v[188:191], a[220:223], a[92:95]\n"
    "v_cvt_pk_bf16_f32 v234, v24, v25\n"
    "s_waitcnt lgkmcnt(0)\n"
    "v_mfma_f32_16x16x32_bf16 a[92:95], v[184:187], a[216:219], a[92:95]\n"
    "v_exp_f32 v26, v26\n"
    "v_mfma_f32_16x16x32_bf16 a[92:95], v[180:183], a[212:215], a[92:95]\n"
    "ds_read_b128 a[216:219], v3 offset:59392\n"
    "ds_read_b128 a[220:223], v3 offset:60416\n"
    "v_mfma_f32_16x16x32_bf16 a[92:95], v[176:179], a[208:211], a[92:95]\n"
    "v_exp_f32 v27, v27\n"
    "v_mfma_f32_16x16x32_bf16 v[16:19], a[192:195], a[128:131], v[32:35]\n"
    "v_cvt_pk_bf16_f32 v235, v26, v27\n"
    "v_mfma_f32_16x16x32_bf16 v[16:19], a[196:199], a[132:135], v[16:19]\n"
    "v_exp_f32 v28, v28\n"
    "v_mfma_f32_16x16x32_bf16 v[16:19], a[200:203], a[136:139], v[16:19]\n"
    "ds_read_b128 a[208:211], v3 offset:57344\n"
    "ds_read_b128 a[212:215], v3 offset:58368\n"
    "v_mfma_f32_16x16x32_bf16 v[16:19], a[204:207], a[140:143], v[16:19]\n"
    "v_exp_f32 v29, v29\n"
    "v_mfma_f32_16x16x32_bf16 v[56:59], v[64:67], v[232:235], v[56:59]\n"
    "v_mfma_f32_16x16x32_bf16 v[20:23], a[204:207], a[156:159], v[36:39]\n"
    "v_cvt_pk_bf16_f32 v250, v28, v29\n"
    "v_mfma_f32_16x16x32_bf16 v[20:23], a[200:203], a[152:155], v[20:23]\n"
    "v_exp_f32 v30, v30\n"
    "v_mfma_f32_16x16x32_bf16 v[20:23], a[196:199], a[148:151], v[20:23]\n"
    "v_mfma_f32_16x16x32_bf16 v[20:23], a[192:195], a[144:147], v[20:23]\n"
    "v_exp_f32 v31, v31\n"
    "v_mfma_f32_16x16x32_bf16 v[24:27], a[192:195], a[160:163], v[40:43]\n"
    "v_cvt_pk_bf16_f32 v251, v30, v31\n"
    "v_mfma_f32_16x16x32_bf16 v[24:27], a[196:199], a[164:167], v[24:27]\n"
    "v_exp_f32 v16, v16\n"
    "v_mfma_f32_16x16x32_bf16 v[24:27], a[200:203], a[168:171], v[24:27]\n"
    "v_mfma_f32_16x16x32_bf16 v[24:27], a[204:207], a[172:175], v[24:27]\n"
    "v_exp_f32 v17, v17\n"
    "v_mfma_f32_16x16x32_bf16 v[60:63], v[64:67], v[248:251], v[60:63]\n"
    "v_mfma_f32_16x16x32_bf16 v[28:31], a[204:207], a[188:191], v[44:47]\n"
    "v_cvt_pk_bf16_f32 v204, v16, v17\n"
    "s_waitcnt lgkmcnt(0)\n"
    "v_mfma_f32_16x16x32_bf16 v[28:31], a[200:203], a[184:187], v[28:31]\n"
    "v_exp_f32 v18, v18\n"
    "v_mfma_f32_16x16x32_bf16 v[28:31], a[196:199], a[180:183], v[28:31]\n"
    "ds_read_b128 a[200:203], v3 offset:30720\n"
    "ds_read_b128 a[204:207], v3 offset:31744\n"
    "v_mfma_f32_16x16x32_bf16 v[28:31], a[192:195], a[176:179], v[28:31]\n"
    "v_exp_f32 v19, v19\n"
    "v_mfma_f32_16x16x32_bf16 a[96:99], v[128:131], a[208:211], a[96:99]\n"
    "v_cvt_pk_bf16_f32 v205, v18, v19\n"
    "v_mfma_f32_16x16x32_bf16 a[96:99], v[132:135], a[212:215], a[96:99]\n"
    "v_exp_f32 v20, v20\n"
    "v_mfma_f32_16x16x32_bf16 a[96:99], v[136:139], a[216:219], a[96:99]\n"
    "ds_read_b128 a[192:195], v3 offset:28672\n"
    "ds_read_b128 a[196:199], v3 offset:29696\n"
    "v_mfma_f32_16x16x32_bf16 a[96:99], v[140:143], a[220:223], a[96:99]\n"
    "v_exp_f32 v21, v21\n"
    "v_mfma_f32_16x16x32_bf16 a[100:103], v[156:159], a[220:223], a[100:103]\n"
    "v_cvt_pk_bf16_f32 v220, v20, v21\n"
    "v_mfma_f32_16x16x32_bf16 a[100:103], v[152:155], a[216:219], a[100:103]\n"
    "v_exp_f32 v22, v22\n"
    "v_mfma_f32_16x16x32_bf16 a[100:103], v[148:151], a[212:215], a[100:103]\n"
    "v_mfma_f32_16x16x32_bf16 a[100:103], v[144:147], a[208:211], a[100:103]\n"
    "v_exp_f32 v23, v23\n"
    "v_mfma_f32_16x16x32_bf16 a[104:107], v[160:163], a[208:211], a[104:107]\n"
    "v_cvt_pk_bf16_f32 v221, v22, v23\n"
    "v_mfma_f32_16x16x32_bf16 a[104:107], v[164:167], a[212:215], a[104:107]\n"
    "v_exp_f32 v24, v24\n"
    "v_mfma_f32_16x16x32_bf16 a[104:107], v[168:171], a[216:219], a[104:107]\n"
    "v_mfma_f32_16x16x32_bf16 a[104:107], v[172:175], a[220:223], a[104:107]\n"
    "v_exp_f32 v25, v25\n"
    "v_mfma_f32_16x16x32_bf16 a[108:111], v[188:191], a[220:223], a[108:111]\n"
    "v_cvt_pk_bf16_f32 v236, v24, v25\n"
    "s_waitcnt lgkmcnt(0)\n"
    "v_mfma_f32_16x16x32_bf16 a[108:111], v[184:187], a[216:219], a[108:111]\n"
    "v_exp_f32 v26, v26\n"
    "v_mfma_f32_16x16x32_bf16 a[108:111], v[180:183], a[212:215], a[108:111]\n"
    "ds_read_b128 a[216:219], v3 offset:63488\n"
    "ds_read_b128 a[220:223], v3 offset:64512\n"
    "v_mfma_f32_16x16x32_bf16 a[108:111], v[176:179], a[208:211], a[108:111]\n"
    "v_exp_f32 v27, v27\n"
    "v_mfma_f32_16x16x32_bf16 v[16:19], a[192:195], a[128:131], v[32:35]\n"
    "v_cvt_pk_bf16_f32 v237, v26, v27\n"
    "v_mfma_f32_16x16x32_bf16 v[16:19], a[196:199], a[132:135], v[16:19]\n"
    "v_exp_f32 v28, v28\n"
    "v_mfma_f32_16x16x32_bf16 v[16:19], a[200:203], a[136:139], v[16:19]\n"
    "ds_read_b128 a[208:211], v3 offset:61440\n"
    "ds_read_b128 a[212:215], v3 offset:62464\n"
    "v_mfma_f32_16x16x32_bf16 v[16:19], a[204:207], a[140:143], v[16:19]\n"
    "v_exp_f32 v29, v29\n"
    "v_mfma_f32_16x16x32_bf16 v[20:23], a[204:207], a[156:159], v[36:39]\n"
    "v_cvt_pk_bf16_f32 v252, v28, v29\n"
    "v_mfma_f32_16x16x32_bf16 v[20:23], a[200:203], a[152:155], v[20:23]\n"
    "v_exp_f32 v30, v30\n"
    "v_mfma_f32_16x16x32_bf16 v[20:23], a[196:199], a[148:151], v[20:23]\n"
    "v_mfma_f32_16x16x32_bf16 v[20:23], a[192:195], a[144:147], v[20:23]\n"
    "v_exp_f32 v31, v31\n"
    "v_mfma_f32_16x16x32_bf16 v[24:27], a[192:195], a[160:163], v[40:43]\n"
    "v_cvt_pk_bf16_f32 v253, v30, v31\n"
    "v_mfma_f32_16x16x32_bf16 v[24:27], a[196:199], a[164:167], v[24:27]\n"
    "v_exp_f32 v16, v16\n"
    "v_mfma_f32_16x16x32_bf16 v[24:27], a[200:203], a[168:171], v[24:27]\n"
    "v_mfma_f32_16x16x32_bf16 v[24:27], a[204:207], a[172:175], v[24:27]\n"
    "v_exp_f32 v17, v17\n"
    "v_mfma_f32_16x16x32_bf16 v[28:31], a[204:207], a[188:191], v[44:47]\n"
    "v_cvt_pk_bf16_f32 v206, v16, v17\n"
    "s_waitcnt vmcnt(1) lgkmcnt(0)\n"
    "v_mfma_f32_16x16x32_bf16 v[28:31], a[200:203], a[184:187], v[28:31]\n"
    "v_exp_f32 v18, v18\n"
    "s_barrier\n"
    "v_mfma_f32_16x16x32_bf16 v[28:31], a[196:199], a[180:183], v[28:31]\n"
    "ds_read_b128 a[200:203], v2 offset:2048\n"
    "ds_read_b128 a[204:207], v2 offset:3072\n"
    "v_mfma_f32_16x16x32_bf16 v[28:31], a[192:195], a[176:179], v[28:31]\n"
    "v_exp_f32 v19, v19\n"
    "v_mfma_f32_16x16x32_bf16 a[112:115], v[128:131], a[208:211], a[112:115]\n"
    "v_cvt_pk_bf16_f32 v207, v18, v19\n"
    "s_add_u32 s48, s48, 0x10000\n"
    "v_mfma_f32_16x16x32_bf16 a[112:115], v[132:135], a[212:215], a[112:115]\n"
    "v_exp_f32 v20, v20\n"
    "v_mfma_f32_16x16x32_bf16 a[112:115], v[136:139], a[216:219], a[112:115]\n"
    "ds_read_b128 a[192:195], v2 offset:0\n"
    "ds_read_b128 a[196:199], v2 offset:1024\n"
    "v_mfma_f32_16x16x32_bf16 a[112:115], v[140:143], a[220:223], a[112:115]\n"
    "v_exp_f32 v21, v21\n"
    "v_mfma_f32_16x16x32_bf16 v[48:51], v[64:67], v[204:207], v[48:51]\n"
    "v_cmp_gt_u32 vcc, 32, v96\n"
    "v_mfma_f32_16x16x32_bf16 a[116:119], v[156:159], a[220:223], a[116:119]\n"
    "v_cvt_pk_bf16_f32 v222, v20, v21\n"
    "s_add_u32 s97, s97, 128\n"
    "v_mfma_f32_16x16x32_bf16 a[116:119], v[152:155], a[216:219], a[116:119]\n"
    "v_exp_f32 v22, v22\n"
    "v_mfma_f32_16x16x32_bf16 a[116:119], v[148:151], a[212:215], a[116:119]\n"
    "v_mfma_f32_16x16x32_bf16 a[116:119], v[144:147], a[208:211], a[116:119]\n"
    "v_exp_f32 v23, v23\n"
    "v_mfma_f32_16x16x32_bf16 a[120:123], v[160:163], a[208:211], a[120:123]\n"
    "v_cvt_pk_bf16_f32 v223, v22, v23\n"
    "s_mov_b32 m0, s53\n"
    "v_mfma_f32_16x16x32_bf16 a[120:123], v[164:167], a[212:215], a[120:123]\n"
    "v_exp_f32 v24, v24\n"
    "v_mfma_f32_16x16x32_bf16 a[120:123], v[168:171], a[216:219], a[120:123]\n"
    "s_cbranch_vccnz L_spin_C\n"
    "L_spinret_C:\n"
    "v_mfma_f32_16x16x32_bf16 a[120:123], v[172:175], a[220:223], a[120:123]\n"
    "v_exp_f32 v25, v25\n"
    "v_mfma_f32_16x16x32_bf16 v[52:55], v[64:67], v[220:223], v[52:55]\n"
    "s_add_u32 s96, s96, 128\n"
    "s_min_u32 s96, s96, 0x2b00\n"
    "v_mfma_f32_16x16x32_bf16 a[124:127], v[188:191], a[220:223], a[124:127]\n"
    "v_cvt_pk_bf16_f32 v238, v24, v25\n"
    "s_waitcnt lgkmcnt(0)\n"
    "v_mfma_f32_16x16x32_bf16 a[124:127], v[184:187], a[216:219], a[124:127]\n"
    "v_exp_f32 v26, v26\n"
    "v_mfma_f32_16x16x32_bf16 a[124:127], v[180:183], a[212:215], a[124:127]\n"
    "ds_read_b128 a[216:219], v2 offset:34816\n"
    "ds_read_b128 a[220:223], v2 offset:35840\n"
    "v_mfma_f32_16x16x32_bf16 a[124:127], v[176:179], a[208:211], a[124:127]\n"
    "v_exp_f32 v27, v27\n"
    "v_mfma_f32_16x16x32_bf16 v[16:19], a[192:195], a[128:131], v[32:35]\n"
    "v_cvt_pk_bf16_f32 v239, v26, v27\n"
    "v_mfma_f32_16x16x32_bf16 v[16:19], a[196:199], a[132:135], v[16:19]\n"
    "v_exp_f32 v28, v28\n"
    "v_mfma_f32_16x16x32_bf16 v[16:19], a[200:203], a[136:139], v[16:19]\n"
    "ds_read_b128 a[208:211], v2 offset:32768\n"
    "ds_read_b128 a[212:215], v2 offset:33792\n"
    "v_mfma_f32_16x16x32_bf16 v[16:19], a[204:207], a[140:143], v[16:19]\n"
    "v_exp_f32 v29, v29\n"
    "v_mfma_f32_16x16x32_bf16 v[56:59], v[64:67], v[236:239], v[56:59]\n"
    "buffer_load_dwordx4 v4, s[40:43], s48 offen offset:0 sc1 lds\n"
    "v_mfma_f32_16x16x32_bf16 v[20:23], a[204:207], a[156:159], v[36:39]\n"
    "v_cvt_pk_bf16_f32 v254, v28, v29\n"
    "v_cmp_nge_f32 vcc, s54, v48\n"
    "v_mfma_f32_16x16x32_bf16 v[20:23], a[200:203], a[152:155], v[20:23]\n"
    "v_exp_f32 v30, v30\n"
    "v_mfma_f32_16x16x32_bf16 v[20:23], a[196:199], a[148:151], v[20:23]\n"
    "s_cbranch_vccnz L_rare_D0\n"
    "L_ret_D0:\n"
    "v_add_f32 v68, v48, v68\n"
    "v_mfma_f32_16x16x32_bf16 v[20:23], a[192:195], a[144:147], v[20:23]\n"
    "v_exp_f32 v31, v31\n"
    "v_mfma_f32_16x16x32_bf16 v[24:27], a[192:195], a[160:163], v[40:43]\n"
    "v_cvt_pk_bf16_f32 v255, v30, v31\n"
    "v_cmp_nge_f32 vcc, s54, v52\n"
    "v_mfma_f32_16x16x32_bf16 v[24:27], a[196:199], a[164:167], v[24:27]\n"
    "v_exp_f32 v16, v16\n"
    "v_mfma_f32_16x16x32_bf16 v[24:27], a[200:203], a[168:171], v[24:27]\n"
    "s_cbranch_vccnz L_rare_D1\n"
    "L_ret_D1:\n"
    "v_add_f32 v69, v52, v69\n"
    "v_mfma_f32_16x16x32_bf16 v[24:27], a[204:207], a[172:175], v[24:27]\n"
    "v_exp_f32 v17, v17\n"
    "v_mfma_f32_16x16x32_bf16 v[60:63], v[64:67], v[252:255], v[60:63]\n"
    "buffer_load_dwordx4 v4, s[40:43], s48 offen offset:1024 sc1 lds\n"
    "v_mfma_f32_16x16x32_bf16 v[28:31], a[204:207], a[188:191], v[44:47]\n"
    "v_cvt_pk_bf16_f32 v128, v16, v17\n"
    "s_waitcnt lgkmcnt(0)\n"
    "v_mfma_f32_16x16x32_bf16 v[28:31], a[200:203], a[184:187], v[28:31]\n"
    "v_exp_f32 v18, v18\n"
    "v_mfma_f32_16x16x32_bf16 v[28:31], a[196:199], a[180:183], v[28:31]\n"
    "ds_read_b128 a[200:203], v2 offset:6144\n"
    "ds_read_b128 a[204:207], v2 offset:7168\n"
    "v_mfma_f32_16x16x32_bf16 v[28:31], a[192:195], a[176:179], v[28:31]\n"
    "v_exp_f32 v19, v19\n"
    "v_mfma_f32_16x16x32_bf16 a[0:3], v[192:195], a[208:211], a[0:3]\n"
    "v_cvt_pk_bf16_f32 v129, v18, v19\n"
    "v_mfma_f32_16x16x32_bf16 a[0:3], v[196:199], a[212:215], a[0:3]\n"
    "v_exp_f32 v20, v20\n"
    "v_mfma_f32_16x16x32_bf16 a[0:3], v[200:203], a[216:219], a[0:3]\n"
    "ds_read_b128 a[192:195], v2 offset:4096\n"
    "ds_read_b128 a[196:199], v2 offset:5120\n"
    "v_mfma_f32_16x16x32_bf16 a[0:3], v[204:207], a[220:223], a[0:3]\n"
    "v_exp_f32 v21, v21\n"
    "v_mfma_f32_16x16x32_bf16 a[4:7], v[220:223], a[220:223], a[4:7]\n"
    "v_cvt_pk_bf16_f32 v144, v20, v21\n"
    "v_cmp_nge_f32 vcc, s54, v56\n"
    "v_mfma_f32_16x16x32_bf16 a[4:7], v[216:219], a[216:219], a[4:7]\n"
    "v_exp_f32 v22, v22\n"
    "v_mfma_f32_16x16x32_bf16 a[4:7], v[212:215], a[212:215], a[4:7]\n"
    "s_cbranch_vccnz L_rare_D2\n"
    "L_ret_D2:\n"
    "v_add_f32 v70, v56, v70\n"
    "v_mfma_f32_16x16x32_bf16 a[4:7], v[208:211], a[208:211], a[4:7]\n"
    "v_exp_f32 v23, v23\n"
    "v_mfma_f32_16x16x32_bf16 a[8:11], v[224:227], a[208:211], a[8:11]\n"
    "v_cvt_pk_bf16_f32 v145, v22, v23\n"
    "v_cmp_nge_f32 vcc, s54, v60\n"
    "v_mfma_f32_16x16x32_bf16 a[8:11], v[228:231], a[212:215], a[8:11]\n"
    "v_exp_f32 v24, v24\n"
    "v_mfma_f32_16x16x32_bf16 a[8:11], v[232:235], a[216:219], a[8:11]\n"
    "s_cbranch_vccnz L_rare_D3\n"
    "L_ret_D3:\n"
    "v_add_f32 v71, v60, v71\n"
    "v_mfma_f32_16x16x32_bf16 a[8:11], v[236:239], a[220:223], a[8:11]\n"
    "v_exp_f32 v25, v25\n"
    "v_mfma_f32_16x16x32_bf16 a[12:15], v[252:255], a[220:223], a[12:15]\n"
    "v_cvt_pk_bf16_f32 v160, v24, v25\n"
    "s_waitcnt lgkmcnt(0)\n"
    "v_mfma_f32_16x16x32_bf16 a[12:15], v[248:251], a[216:219], a[12:15]\n"
    "v_exp_f32 v26, v26\n"
    "v_mfma_f32_16x16x32_bf16 a[12:15], v[244:247], a[212:215], a[12:15]\n"
    "ds_read_b128 a[216:219], v2 offset:38912\n"
    "ds_read_b128 a[220:223], v2 offset:39936\n"
    "v_mfma_f32_16x16x32_bf16 a[12:15], v[240:243], a[208:211], a[12:15]\n"
    "v_exp_f32 v27, v27\n"
    "v_mfma_f32_16x16x32_bf16 v[16:19], a[192:195], a[128:131], v[32:35]\n"
    "v_cvt_pk_bf16_f32 v161, v26, v27\n"
    "v_mfma_f32_16x16x32_bf16 v[16:19], a[196:199], a[132:135], v[16:19]\n"
    "v_exp_f32 v28, v28\n"
    "v_mfma_f32_16x16x32_bf16 v[16:19], a[200:203], a[136:139], v[16:19]\n"
    "ds_read_b128 a[208:211], v2 offset:36864\n"
    "ds_read_b128 a[212:215], v2 offset:37888\n"
    "v_mfma_f32_16x16x32_bf16 v[16:19], a[204:207], a[140:143], v[16:19]\n"
    "v_exp_f32 v29, v29\n"
    "v_mfma_f32_16x16x32_bf16 v[20:23], a[204:207], a[156:159], v[36:39]\n"
    "v_cvt_pk_bf16_f32 v176, v28, v29\n"
    "v_mfma_f32_16x16x32_bf16 v[20:23], a[200:203], a[152:155], v[20:23]\n"
    "v_exp_f32 v30, v30\n"
    "v_mfma_f32_16x16x32_bf16 v[20:23], a[196:199], a[148:151], v[20:23]\n"
    "buffer_load_dwordx4 v4, s[40:43], s48 offen offset:2048 sc1 lds\n"
    "v_mfma_f32_16x16x32_bf16 v[20:23], a[192:195], a[144:147], v[20:23]\n"
    "v_exp_f32 v31, v31\n"
    "v_mfma_f32_16x16x32_bf16 v[24:27], a[192:195], a[160:163], v[40:43]\n"
    "v_cvt_pk_bf16_f32 v177, v30, v31\n"
    "v_mfma_f32_16x16x32_bf16 v[24:27], a[196:199], a[164:167], v[24:27]\n"
    "v_exp_f32 v16, v16\n"
    "v_mfma_f32_16x16x32_bf16 v[24:27], a[200:203], a[168:171], v[24:27]\n"
    "buffer_load_dwordx4 v4, s[40:43], s48 offen offset:3072 sc1 lds\n"
    "s_add_u32 m0, m0, 0x1000\n"
    "v_mfma_f32_16x16x32_bf16 v[24:27], a[204:207], a[172:175], v[24:27]\n"
    "v_exp_f32 v17, v17\n"
    "v_mfma_f32_16x16x32_bf16 v[28:31], a[204:207], a[188:191], v[44:47]\n"
    "v_cvt_pk_bf16_f32 v130, v16, v17\n"
    "s_waitcnt lgkmcnt(0)\n"
    "v_mfma_f32_16x16x32_bf16 v[28:31], a[200:203], a[184:187], v[28:31]\n"
    "v_exp_f32 v18, v18\n"
    "v_mfma_f32_16x16x32_bf16 v[28:31], a[196:199], a[180:183], v[28:31]\n"
    "ds_read_b128 a[200:203], v2 offset:10240\n"
    "ds_read_b128 a[204:207], v2 offset:11264\n"
    "v_mfma_f32_16x16x32_bf16 v[28:31], a[192:195], a[176:179], v[28:31]\n"
    "v_exp_f32 v19, v19\n"
    "v_mfma_f32_16x16x32_bf16 a[16:19], v[192:195], a[208:211], a[16:19]\n"
    "v_cvt_pk_bf16_f32 v131, v18, v19\n"
    "v_mfma_f32_16x16x32_bf16 a[16:19], v[196:199], a[212:215], a[16:19]\n"
    "v_exp_f32 v20, v20\n"
    "v_mfma_f32_16x16x32_bf16 a[16:19], v[200:203], a[216:219], a[16:19]\n"
    "ds_read_b128 a[192:195], v2 offset:8192\n"
    "ds_read_b128 a[196:199], v2 offset:9216\n"
    "v_mfma_f32_16x16x32_bf16 a[16:19], v[204:207], a[220:223], a[16:19]\n"
    "v_exp_f32 v21, v21\n"
    "v_mfma_f32_16x16x32_bf16 v[48:51], v[64:67], v[128:131], 0\n"
    "buffer_load_dwordx4 v5, s[40:43], s48 offen offset:0 sc1 lds\n"
    "v_mfma_f32_16x16x32_bf16 a[20:23], v[220:223], a[220:223], a[20:23]\n"
    "v_cvt_pk_bf16_f32 v146, v20, v21\n"
    "v_mfma_f32_16x16x32_bf16 a[20:23], v[216:219], a[216:219], a[20:23]\n"
    "v_exp_f32 v22, v22\n"
    "v_mfma_f32_16x16x32_bf16 a[20:23], v[212:215], a[212:215], a[20:23]\n"
    "buffer_load_dwordx4 v5, s[40:43], s48 offen offset:1024 sc1 lds\n"
    "v_mfma_f32_16x16x32_bf16 a[20:23], v[208:211], a[208:211], a[20:23]\n"
    "v_exp_f32 v23, v23\n"
    "v_mfma_f32_16x16x32_bf16 a[24:27], v[224:227], a[208:211], a[24:27]\n"
    "v_cvt_pk_bf16_f32 v147, v22, v23\n"
    "v_mfma_f32_16x16x32_bf16 a[24:27], v[228:231], a[212:215], a[24:27]\n"
    "v_exp_f32 v24, v24\n"
    "v_mfma_f32_16x16x32_bf16 a[24:27], v[232:235], a[216:219], a[24:27]\n"
    "buffer_load_dwordx4 v5, s[40:43], s48 offen offset:2048 sc1 lds\n"
    "v_mfma_f32_16x16x32_bf16 a[24:27], v[236:239], a[220:223], a[24:27]\n"
    "v_exp_f32 v25, v25\n"
    "v_mfma_f32_16x16x32_bf16 v[52:55], v[64:67], v[144:147], 0\n"
    "buffer_load_dwordx4 v5, s[40:43], s48 offen offset:3072 sc1 lds\n"
    "s_add_u32 m0, m0, 0x1000\n"
    "v_mfma_f32_16x16x32_bf16 a[28:31], v[252:255], a[220:223], a[28:31]\n"
    "v_cvt_pk_bf16_f32 v162, v24, v25\n"
    "s_waitcnt lgkmcnt(0)\n"
    "v_mfma_f32_16x16x32_bf16 a[28:31], v[248:251], a[216:219], a[28:31]\n"
    "v_exp_f32 v26, v26\n"
    "v_mfma_f32_16x16x32_bf16 a[28:31], v[244:247], a[212:215], a[28:31]\n"
    "ds_read_b128 a[216:219], v2 offset:43008\n"
    "ds_read_b128 a[220:223], v2 offset:44032\n"
    "v_mfma_f32_16x16x32_bf16 a[28:31], v[240:243], a[208:211], a[28:31]\n"
    "v_exp_f32 v27, v27\n"
    "v_mfma_f32_16x16x32_bf16 v[16:19], a[192:195], a[128:131], v[32:35]\n"
    "v_cvt_pk_bf16_f32 v163, v26, v27\n"
    "v_mfma_f32_16x16x32_bf16 v[16:19], a[196:199], a[132:135], v[16:19]\n"
    "v_exp_f32 v28, v28\n"
    "v_mfma_f32_16x16x32_bf16 v[16:19], a[200:203], a[136:139], v[16:19]\n"
    "ds_read_b128 a[208:211], v2 offset:40960\n"
    "ds_read_b128 a[212:215], v2 offset:41984\n"
    "v_mfma_f32_16x16x32_bf16 v[16:19], a[204:207], a[140:143], v[16:19]\n"
    "v_exp_f32 v29, v29\n"
    "v_mfma_f32_16x16x32_bf16 v[56:59], v[64:67], v[160:163], 0\n"
    "v_mfma_f32_16x16x32_bf16 v[20:23], a[204:207], a[156:159], v[36:39]\n"
    "v_cvt_pk_bf16_f32 v178, v28, v29\n"
    "v_mfma_f32_16x16x32_bf16 v[20:23], a[200:203], a[152:155], v[20:23]\n"
    "v_exp_f32 v30, v30\n"
    "v_mfma_f32_16x16x32_bf16 v[20:23], a[196:199], a[148:151], v[20:23]\n"
    "buffer_load_dwordx4 v6, s[40:43], s48 offen offset:0 sc1 lds\n"
    "v_mfma_f32_16x16x32_bf16 v[20:23], a[192:195], a[144:147], v[20:23]\n"
    "v_exp_f32 v31, v31\n"
    "v_mfma_f32_16x16x32_bf16 v[24:27], a[192:195], a[160:163], v[40:43]\n"
    "v_cvt_pk_bf16_f32 v179, v30, v31\n"
    "v_mfma_f32_16x16x32_bf16 v[24:27], a[196:199], a[164:167], v[24:27]\n"
    "v_exp_f32 v16, v16\n"
    "v_mfma_f32_16x16x32_bf16 v[24:27], a[200:203], a[168:171], v[24:27]\n"
    "buffer_load_dwordx4 v6, s[40:43], s48 offen offset:1024 sc1 lds\n"
    "v_mfma_f32_16x16x32_bf16 v[24:27], a[204:207], a[172:175], v[24:27]\n"
    "v_exp_f32 v17, v17\n"
    "v_mfma_f32_16x16x32_bf16 v[60:63], v[64:67], v[176:179], 0\n"
    "v_mfma_f32_16x16x32_bf16 v[28:31], a[204:207], a[188:191], v[44:47]\n"
    "v_cvt_pk_bf16_f32 v132, v16, v17\n"
    "s_waitcnt lgkmcnt(0)\n"
    "v_mfma_f32_16x16x32_bf16 v[28:31], a[200:203], a[184:187], v[28:31]\n"
    "v_exp_f32 v18, v18\n"
    "v_mfma_f32_16x16x32_bf16 v[28:31], a[196:199], a[180:183], v[28:31]\n"
    "ds_read_b128 a[200:203], v2 offset:14336\n"
    "ds_read_b128 a[204:207], v2 offset:15360\n"
    "v_mfma_f32_16x16x32_bf16 v[28:31], a[192:195], a[176:179], v[28:31]\n"
    "v_exp_f32 v19, v19\n"
    "v_mfma_f32_16x16x32_bf16 a[32:35], v[192:195], a[208:211], a[32:35]\n"
    "v_cvt_pk_bf16_f32 v133, v18, v19\n"
    "v_mfma_f32_16x16x32_bf16 a[32:35], v[196:199], a[212:215], a[32:35]\n"
    "v_exp_f32 v20, v20\n"
    "v_mfma_f32_16x16x32_bf16 a[32:35], v[200:203], a[216:219], a[32:35]\n"
    "ds_read_b128 a[192:195], v2 offset:12288\n"
    "ds_read_b128 a[196:199], v2 offset:13312\n"
    "v_mfma_f32_16x16x32_bf16 a[32:35], v[204:207], a[220:223], a[32:35]\n"
    "v_exp_f32 v21, v21\n"
    "v_mfma_f32_16x16x32_bf16 a[36:39], v[220:223], a[220:223], a[36:39]\n"
    "v_cvt_pk_bf16_f32 v148, v20, v21\n"
    "v_mfma_f32_16x16x32_bf16 a[36:39], v[216:219], a[216:219], a[36:39]\n"
    "v_exp_f32 v22, v22\n"
    "v_mfma_f32_16x16x32_bf16 a[36:39], v[212:215], a[212:215], a[36:39]\n"
    "buffer_load_dwordx4 v6, s[40:43], s48 offen offset:2048 sc1 lds\n"
    "v_mfma_f32_16x16x32_bf16 a[36:39], v[208:211], a[208:211], a[36:39]\n"
    "v_exp_f32 v23, v23\n"
    "v_mfma_f32_16x16x32_bf16 a[40:43], v[224:227], a[208:211], a[40:43]\n"
    "v_cvt_pk_bf16_f32 v149, v22, v23\n"
    "v_mfma_f32_16x16x32_bf16 a[40:43], v[228:231], a[212:215], a[40:43]\n"
    "v_exp_f32 v24, v24\n"
    "v_mfma_f32_16x16x32_bf16 a[40:43], v[232:235], a[216:219], a[40:43]\n"
    "buffer_load_dwordx4 v6, s[40:43], s48 offen offset:3072 sc1 lds\n"
    "s_add_u32 m0, m0, 0x1000\n"
    "v_mfma_f32_16x16x32_bf16 a[40:43], v[236:239], a[220:223], a[40:43]\n"
    "v_exp_f32 v25, v25\n"
    "v_mfma_f32_16x16x32_bf16 a[44:47], v[252:255], a[220:223], a[44:47]\n"
    "v_cvt_pk_bf16_f32 v164, v24, v25\n"
    "s_waitcnt lgkmcnt(0)\n"
    "v_mfma_f32_16x16x32_bf16 a[44:47], v[248:251], a[216:219], a[44:47]\n"
    "v_exp_f32 v26, v26\n"
    "v_mfma_f32_16x16x32_bf16 a[44:47], v[244:247], a[212:215], a[44:47]\n"
    "ds_read_b128 a[216:219], v2 offset:47104\n"
    "ds_read_b128 a[220:223], v2 offset:48128\n"
    "v_mfma_f32_16x16x32_bf16 a[44:47], v[240:243], a[208:211], a[44:47]\n"
    "v_exp_f32 v27, v27\n"
    "v_mfma_f32_16x16x32_bf16 v[16:19], a[192:195], a[128:131], v[32:35]\n"
    "v_cvt_pk_bf16_f32 v165, v26, v27\n"
    "v_mfma_f32_16x16x32_bf16 v[16:19], a[196:199], a[132:135], v[16:19]\n"
    "v_exp_f32 v28, v28\n"
    "v_mfma_f32_16x16x32_bf16 v[16:19], a[200:203], a[136:139], v[16:19]\n"
    "ds_read_b128 a[208:211], v2 offset:45056\n"
    "ds_read_b128 a[212:215], v2 offset:46080\n"
    "v_mfma_f32_16x16x32_bf16 v[16:19], a[204:207], a[140:143], v[16:19]\n"
    "v_exp_f32 v29, v29\n"
    "v_mfma_f32_16x16x32_bf16 v[20:23], a[204:207], a[156:159], v[36:39]\n"
    "v_cvt_pk_bf16_f32 v180, v28, v29\n"
    "v_mfma_f32_16x16x32_bf16 v[20:23], a[200:203], a[152:155], v[20:23]\n"
    "v_exp_f32 v30, v30\n"
    "v_mfma_f32_16x16x32_bf16 v[20:23], a[196:199], a[148:151], v[20:23]\n"
    "buffer_load_dwordx4 v7, s[40:43], s48 offen offset:0 sc1 lds\n"
    "v_mfma_f32_16x16x32_bf16 v[20:23], a[192:195], a[144:147], v[20:23]\n"
    "v_exp_f32 v31, v31\n"
    "v_mfma_f32_16x16x32_bf16 v[24:27], a[192:195], a[160:163], v[40:43]\n"
    "v_cvt_pk_bf16_f32 v181, v30, v31\n"
    "v_mfma_f32_16x16x32_bf16 v[24:27], a[196:199], a[164:167], v[24:27]\n"
    "v_exp_f32 v16, v16\n"
    "v_mfma_f32_16x16x32_bf16 v[24:27], a[200:203], a[168:171], v[24:27]\n"
    "buffer_load_dwordx4 v7, s[40:43], s48 offen offset:1024 sc1 lds\n"
    "v_mfma_f32_16x16x32_bf16 v[24:27], a[204:207], a[172:175], v[24:27]\n"
    "v_exp_f32 v17, v17\n"
    "v_mfma_f32_16x16x32_bf16 v[28:31], a[204:207], a[188:191], v[44:47]\n"
    "v_cvt_pk_bf16_f32 v134, v16, v17\n"
    "s_waitcnt lgkmcnt(0)\n"
    "v_mfma_f32_16x16x32_bf16 v[28:31], a[200:203], a[184:187], v[28:31]\n"
    "v_exp_f32 v18, v18\n"
    "v_mfma_f32_16x16x32_bf16 v[28:31], a[196:199], a[180:183], v[28:31]\n"
    "ds_read_b128 a[200:203], v2 offset:18432\n"
    "ds_read_b128 a[204:207], v2 offset:19456\n"
    "v_mfma_f32_16x16x32_bf16 v[28:31], a[192:195], a[176:179], v[28:31]\n"
    "v_exp_f32 v19, v19\n"
    "v_mfma_f32_16x16x32_bf16 a[48:51], v[192:195], a[208:211], a[48:51]\n"
    "v_cvt_pk_bf16_f32 v135, v18, v19\n"
    "v_mfma_f32_16x16x32_bf16 a[48:51], v[196:199], a[212:215], a[48:51]\n"
    "v_exp_f32 v20, v20\n"
    "v_mfma_f32_16x16x32_bf16 a[48:51], v[200:203], a[216:219], a[48:51]\n"
    "ds_read_b128 a[192:195], v2 offset:16384\n"
    "ds_read_b128 a[196:199], v2 offset:17408\n"
    "v_mfma_f32_16x16x32_bf16 a[48:51], v[204:207], a[220:223], a[48:51]\n"
    "v_exp_f32 v21, v21\n"
    "v_mfma_f32_16x16x32_bf16 v[48:51], v[64:67], v[132:135], v[48:51]\n"
    "v_mfma_f32_16x16x32_bf16 a[52:55], v[220:223], a[220:223], a[52:55]\n"
    "v_cvt_pk_bf16_f32 v150, v20, v21\n"
    "v_mfma_f32_16x16x32_bf16 a[52:55], v[216:219], a[216:219], a[52:55]\n"
    "v_exp_f32 v22, v22\n"
    "v_mfma_f32_16x16x32_bf16 a[52:55], v[212:215], a[212:215], a[52:55]\n"
    "buffer_load_dwordx4 v7, s[40:43], s48 offen offset:2048 sc1 lds\n"
    "v_mfma_f32_16x16x32_bf16 a[52:55], v[208:211], a[208:211], a[52:55]\n"
    "v_exp_f32 v23, v23\n"
    "v_mfma_f32_16x16x32_bf16 a[56:59], v[224:227], a[208:211], a[56:59]\n"
    "v_cvt_pk_bf16_f32 v151, v22, v23\n"
    "v_mfma_f32_16x16x32_bf16 a[56:59], v[228:231], a[212:215], a[56:59]\n"
    "v_exp_f32 v24, v24\n"
    "v_mfma_f32_16x16x32_bf16 a[56:59], v[232:235], a[216:219], a[56:59]\n"
    "buffer_load_dwordx4 v7, s[40:43], s48 offen offset:3072 sc1 lds\n"
    "v_mfma_f32_16x16x32_bf16 a[56:59], v[236:239], a[220:223], a[56:59]\n"
    "v_exp_f32 v25, v25\n"
    "v_mfma_f32_16x16x32_bf16 v[52:55], v[64:67], v[148:151], v[52:55]\n"
    "v_mfma_f32_16x16x32_bf16 a[60:63], v[252:255], a[220:223], a[60:63]\n"
    "v_cvt_pk_bf16_f32 v166, v24, v25\n"
    "s_waitcnt lgkmcnt(0)\n"
    "v_mfma_f32_16x16x32_bf16 a[60:63], v[248:251], a[216:219], a[60:63]\n"
    "v_exp_f32 v26, v26\n"
    "v_mfma_f32_16x16x32_bf16 a[60:63], v[244:247], a[212:215], a[60:63]\n"
    "ds_read_b128 a[216:219], v2 offset:51200\n"
    "ds_read_b128 a[220:223], v2 offset:52224\n"
    "v_mfma_f32_16x16x32_bf16 a[60:63], v[240:243], a[208:211], a[60:63]\n"
    "v_exp_f32 v27, v27\n"
    "v_mfma_f32_16x16x32_bf16 v[16:19], a[192:195], a[128:131], v[32:35]\n"
    "v_cvt_pk_bf16_f32 v167, v26, v27\n"
    "v_mfma_f32_16x16x32_bf16 v[16:19], a[196:199], a[132:135], v[16:19]\n"
    "v_exp_f32 v28, v28\n"
    "v_mfma_f32_16x16x32_bf16 v[16:19], a[200:203], a[136:139], v[16:19]\n"
    "ds_read_b128 a[208:211], v2 offset:49152\n"
    "ds_read_b128 a[212:215], v2 offset:50176\n"
    "v_mfma_f32_16x16x32_bf16 v[16:19], a[204:207], a[140:143], v[16:19]\n"
    "v_exp_f32 v29, v29\n"
    "v_mfma_f32_16x16x32_bf16 v[56:59], v[64:67], v[164:167], v[56:59]\n"
    "buffer_load_dword v96, off, s[64:67], s96 offset:0 sc1\n"
    "buffer_atomic_add v78, v95, s[64:67], s97 offen\n"
    "v_mfma_f32_16x16x32_bf16 v[20:23], a[204:207], a[156:159], v[36:39]\n"
    "v_cvt_pk_bf16_f32 v182, v28, v29\n"
    "v_mfma_f32_16x16x32_bf16 v[20:23], a[200:203], a[152:155], v[20:23]\n"
    "v_exp_f32 v30, v30\n"
    "v_mfma_f32_16x16x32_bf16 v[20:23], a[196:199], a[148:151], v[20:23]\n"
    "v_mfma_f32_16x16x32_bf16 v[20:23], a[192:195], a[144:147], v[20:23]\n"
    "v_exp_f32 v31, v31\n"
    "v_mfma_f32_16x16x32_bf16 v[24:27], a[192:195], a[160:163], v[40:43]\n"
    "v_cvt_pk_bf16_f32 v183, v30, v31\n"
    "v_mfma_f32_16x16x32_bf16 v[24:27], a[196:199], a[164:167], v[24:27]\n"
    "v_exp_f32 v16, v16\n"
    "v_mfma_f32_16x16x32_bf16 v[24:27], a[200:203], a[168:171], v[24:27]\n"
    "v_mfma_f32_16x16x32_bf16 v[24:27], a[204:207], a[172:175], v[24:27]\n"
    "v_exp_f32 v17, v17\n"
    "v_mfma_f32_16x16x32_bf16 v[60:63], v[64:67], v[180:183], v[60:63]\n"
    "v_mfma_f32_16x16x32_bf16 v[28:31], a[204:207], a[188:191], v[44:47]\n"
    "v_cvt_pk_bf16_f32 v136, v16, v17\n"
    "s_waitcnt lgkmcnt(0)\n"
    "v_mfma_f32_16x16x32_bf16 v[28:31], a[200:203], a[184:187], v[28:31]\n"
    "v_exp_f32 v18, v18\n"
    "v_mfma_f32_16x16x32_bf16 v[28:31], a[196:199], a[180:183], v[28:31]\n"
    "ds_read_b128 a[200:203], v2 offset:22528\n"
    "ds_read_b128 a[204:207], v2 offset:23552\n"
    "v_mfma_f32_16x16x32_bf16 v[28:31], a[192:195], a[176:179], v[28:31]\n"
    "v_exp_f32 v19, v19\n"
    "v_mfma_f32_16x16x32_bf16 a[64:67], v[192:195], a[208:211], a[64:67]\n"
    "v_cvt_pk_bf16_f32 v137, v18, v19\n"
    "v_mfma_f32_16x16x32_bf16 a[64:67], v[196:199], a[212:215], a[64:67]\n"
    "v_exp_f32 v20, v20\n"
    "v_mfma_f32_16x16x32_bf16 a[64:67], v[200:203], a[216:219], a[64:67]\n"
    "ds_read_b128 a[192:195], v2 offset:20480\n"
    "ds_read_b128 a[196:199], v2 offset:21504\n"
    "v_mfma_f32_16x16x32_bf16 a[64:67], v[204:207], a[220:223], a[64:67]\n"
    "v_exp_f32 v21, v21\n"
    "v_mfma_f32_16x16x32_bf16 a[68:71], v[220:223], a[220:223], a[68:71]\n"
    "v_cvt_pk_bf16_f32 v152, v20, v21\n"
    "v_mfma_f32_16x16x32_bf16 a[68:71], v[216:219], a[216:219], a[68:71]\n"
    "v_exp_f32 v22, v22\n"
    "v_mfma_f32_16x16x32_bf16 a[68:71], v[212:215], a[212:215], a[68:71]\n"
    "v_mfma_f32_16x16x32_bf16 a[68:71], v[208:211], a[208:211], a[68:71]\n"
    "v_exp_f32 v23, v23\n"
    "v_mfma_f32_16x16x32_bf16 a[72:75], v[224:227], a[208:211], a[72:75]\n"
    "v_cvt_pk_bf16_f32 v153, v22, v23\n"
    "v_mfma_f32_16x16x32_bf16 a[72:75], v[228:231], a[212:215], a[72:75]\n"
    "v_exp_f32 v24, v24\n"
    "v_mfma_f32_16x16x32_bf16 a[72:75], v[232:235], a[216:219], a[72:75]\n"
    "v_mfma_f32_16x16x32_bf16 a[72:75], v[236:239], a[220:223], a[72:75]\n"
    "v_exp_f32 v25, v25\n"
    "v_mfma_f32_16x16x32_bf16 a[76:79], v[252:255], a[220:223], a[76:79]\n"
    "v_cvt_pk_bf16_f32 v168, v24, v25\n"
    "s_waitcnt lgkmcnt(0)\n"
    "v_mfma_f32_16x16x32_bf16 a[76:79], v[248:251], a[216:219], a[76:79]\n"
    "v_exp_f32 v26, v26\n"
    "v_mfma_f32_16x16x32_bf16 a[76:79], v[244:247], a[212:215], a[76:79]\n"
    "ds_read_b128 a[216:219], v2 offset:55296\n"
    "ds_read_b128 a[220:223], v2 offset:56320\n"
    "v_mfma_f32_16x16x32_bf16 a[76:79], v[240:243], a[208:211], a[76:79]\n"
    "v_exp_f32 v27, v27\n"
    "v_mfma_f32_16x16x32_bf16 v[16:19], a[192:195], a[128:131], v[32:35]\n"
    "v_cvt_pk_bf16_f32 v169, v26, v27\n"
    "v_mfma_f32_16x16x32_bf16 v[16:19], a[196:199], a[132:135], v[16:19]\n"
    "v_exp_f32 v28, v28\n"
    "v_mfma_f32_16x16x32_bf16 v[16:19], a[200:203], a[136:139], v[16:19]\n"
    "ds_read_b128 a[208:211], v2 offset:53248\n"
    "ds_read_b128 a[212:215], v2 offset:54272\n"
    "v_mfma_f32_16x16x32_bf16 v[16:19], a[204:207], a[140:143], v[16:19]\n"
    "v_exp_f32 v29, v29\n"
    "v_mfma_f32_16x16x32_bf16 v[20:23], a[204:207], a[156:159], v[36:39]\n"
    "v_cvt_pk_bf16_f32 v184, v28, v29\n"
    "v_mfma_f32_16x16x32_bf16 v[20:23], a[200:203], a[152:155], v[20:23]\n"
    "v_exp_f32 v30, v30\n"
    "v_mfma_f32_16x16x32_bf16 v[20:23], a[196:199], a[148:151], v[20:23]\n"
    "v_mfma_f32_16x16x32_bf16 v[20:23], a[192:195], a[144:147], v[20:23]\n"
    "v_exp_f32 v31, v31\n"
    "v_mfma_f32_16x16x32_bf16 v[24:27], a[192:195], a[160:163], v[40:43]\n"
    "v_cvt_pk_bf16_f32 v185, v30, v31\n"
    "v_mfma_f32_16x16x32_bf16 v[24:27], a[196:199], a[164:167], v[24:27]\n"
    "v_exp_f32 v16, v16\n"
    "v_mfma_f32_16x16x32_bf16 v[24:27], a[200:203], a[168:171], v[24:27]\n"
    "v_mfma_f32_16x16x32_bf16 v[24:27], a[204:207], a[172:175], v[24:27]\n"
    "v_exp_f32 v17, v17\n"
    "v_mfma_f32_16x16x32_bf16 v[28:31], a[204:207], a[188:191], v[44:47]\n"
    "v_cvt_pk_bf16_f32 v138, v16, v17\n"
    "s_waitcnt lgkmcnt(0)\n"
    "v_mfma_f32_16x16x32_bf16 v[28:31], a[200:203], a[184:187], v[28:31]\n"
    "v_exp_f32 v18, v18\n"
    "v_mfma_f32_16x16x32_bf16 v[28:31], a[196:199], a[180:183], v[28:31]\n"
    "ds_read_b128 a[200:203], v2 offset:26624\n"
    "ds_read_b128 a[204:207], v2 offset:27648\n"
    "v_mfma_f32_16x16x32_bf16 v[28:31], a[192:195], a[176:179], v[28:31]\n"
    "v_exp_f32 v19, v19\n"
    "v_mfma_f32_16x16x32_bf16 a[80:83], v[192:195], a[208:211], a[80:83]\n"
    "v_cvt_pk_bf16_f32 v139, v18, v19\n"
    "v_mfma_f32_16x16x32_bf16 a[80:83], v[196:199], a[212:215], a[80:83]\n"
    "v_exp_f32 v20, v20\n"
    "v_mfma_f32_16x16x32_bf16 a[80:83], v[200:203], a[216:219], a[80:83]\n"
    "ds_read_b128 a[192:195], v2 offset:24576\n"
    "ds_read_b128 a[196:199], v2 offset:25600\n"
    "v_mfma_f32_16x16x32_bf16 a[80:83], v[204:207], a[220:223], a[80:83]\n"
    "v_exp_f32 v21, v21\n"
    "v_mfma_f32_16x16x32_bf16 v[48:51], v[64:67], v[136:139], v[48:51]\n"
    "v_mfma_f32_16x16x32_bf16 a[84:87], v[220:223], a[220:223], a[84:87]\n"
    "v_cvt_pk_bf16_f32 v154, v20, v21\n"
    "v_mfma_f32_16x16x32_bf16 a[84:87], v[216:219], a[216:219], a[84:87]\n"
    "v_exp_f32 v22, v22\n"
    "v_mfma_f32_16x16x32_bf16 a[84:87], v[212:215], a[212:215], a[84:87]\n"
    "v_mfma_f32_16x16x32_bf16 a[84:87], v[208:211], a[208:211], a[84:87]\n"
    "v_exp_f32 v23, v23\n"
    "v_mfma_f32_16x16x32_bf16 a[88:91], v[224:227], a[208:211], a[88:91]\n"
    "v_cvt_pk_bf16_f32 v155, v22, v23\n"
    "v_mfma_f32_16x16x32_bf16 a[88:91], v[228:231], a[212:215], a[88:91]\n"
    "v_exp_f32 v24, v24\n"
    "v_mfma_f32_16x16x32_bf16 a[88:91], v[232:235], a[216:219], a[88:91]\n"
    "v_mfma_f32_16x16x32_bf16 a[88:91], v[236:239], a[220:223], a[88:91]\n"
    "v_exp_f32 v25, v25\n"
    "v_mfma_f32_16x16x32_bf16 v[52:55], v[64:67], v[152:155], v[52:55]\n"
    "v_mfma_f32_16x16x32_bf16 a[92:95], v[252:255], a[220:223], a[92:95]\n"
    "v_cvt_pk_bf16_f32 v170, v24, v25\n"
    "s_waitcnt lgkmcnt(0)\n"
    "v_mfma_f32_16x16x32_bf16 a[92:95], v[248:251], a[216:219], a[92:95]\n"
    "v_exp_f32 v26, v26\n"
    "v_mfma_f32_16x16x32_bf16 a[92:95], v[244:247], a[212:215], a[92:95]\n"
    "ds_read_b128 a[216:219], v2 offset:59392\n"
    "ds_read_b128 a[220:223], v2 offset:60416\n"
    "v_mfma_f32_16x16x32_bf16 a[92:95], v[240:243], a[208:211], a[92:95]\n"
    "v_exp_f32 v27, v27\n"
    "v_mfma_f32_16x16x32_bf16 v[16:19], a[192:195], a[128:131], v[32:35]\n"
    "v_cvt_pk_bf16_f32 v171, v26, v27\n"
    "v_mfma_f32_16x16x32_bf16 v[16:19], a[196:199], a[132:135], v[16:19]\n"
    "v_exp_f32 v28, v28\n"
    "v_mfma_f32_16x16x32_bf16 v[16:19], a[200:203], a[136:139], v[16:19]\n"
    "ds_read_b128 a[208:211], v2 offset:57344\n"
    "ds_read_b128 a[212:215], v2 offset:58368\n"
    "v_mfma_f32_16x16x32_bf16 v[16:19], a[204:207], a[140:143], v[16:19]\n"
    "v_exp_f32 v29, v29\n"
    "v_mfma_f32_16x16x32_bf16 v[56:59], v[64:67], v[168:171], v[56:59]\n"
    "v_mfma_f32_16x16x32_bf16 v[20:23], a[204:207], a[156:159], v[36:39]\n"
    "v_cvt_pk_bf16_f32 v186, v28, v29\n"
    "v_mfma_f32_16x16x32_bf16 v[20:23], a[200:203], a[152:155], v[20:23]\n"
    "v_exp_f32 v30, v30\n"
    "v_mfma_f32_16x16x32_bf16 v[20:23], a[196:199], a[148:151], v[20:23]\n"
    "v_mfma_f32_16x16x32_bf16 v[20:23], a[192:195], a[144:147], v[20:23]\n"
    "v_exp_f32 v31, v31\n"
    "v_mfma_f32_16x16x32_bf16 v[24:27], a[192:195], a[160:163], v[40:43]\n"
    "v_cvt_pk_bf16_f32 v187, v30, v31\n"
    "v_mfma_f32_16x16x32_bf16 v[24:27], a[196:199], a[164:167], v[24:27]\n"
    "v_exp_f32 v16, v16\n"
    "v_mfma_f32_16x16x32_bf16 v[24:27], a[200:203], a[168:171], v[24:27]\n"
    "v_mfma_f32_16x16x32_bf16 v[24:27], a[204:207], a[172:175], v[24:27]\n"
    "v_exp_f32 v17, v17\n"
    "v_mfma_f32_16x16x32_bf16 v[60:63], v[64:67], v[184:187], v[60:63]\n"
    "v_mfma_f32_16x16x32_bf16 v[28:31], a[204:207], a[188:191], v[44:47]\n"
    "v_cvt_pk_bf16_f32 v140, v16, v17\n"
    "s_waitcnt lgkmcnt(0)\n"
    "v_mfma_f32_16x16x32_bf16 v[28:31], a[200:203], a[184:187], v[28:31]\n"
    "v_exp_f32 v18, v18\n"
    "v_mfma_f32_16x16x32_bf16 v[28:31], a[196:199], a[180:183], v[28:31]\n"
    "ds_read_b128 a[200:203], v2 offset:30720\n"
    "ds_read_b128 a[204:207], v2 offset:31744\n"
    "v_mfma_f32_16x16x32_bf16 v[28:31], a[192:195], a[176:179], v[28:31]\n"
    "v_exp_f32 v19, v19\n"
    "v_mfma_f32_16x16x32_bf16 a[96:99], v[192:195], a[208:211], a[96:99]\n"
    "v_cvt_pk_bf16_f32 v141, v18, v19\n"
    "v_mfma_f32_16x16x32_bf16 a[96:99], v[196:199], a[212:215], a[96:99]\n"
    "v_exp_f32 v20, v20\n"
    "v_mfma_f32_16x16x32_bf16 a[96:99], v[200:203], a[216:219], a[96:99]\n"
    "ds_read_b128 a[192:195], v2 offset:28672\n"
    "ds_read_b128 a[196:199], v2 offset:29696\n"
    "v_mfma_f32_16x16x32_bf16 a[96:99], v[204:207], a[220:223], a[96:99]\n"
    "v_exp_f32 v21, v21\n"
    "v_mfma_f32_16x16x32_bf16 a[100:103], v[220:223], a[220:223], a[100:103]\n"
    "v_cvt_pk_bf16_f32 v156, v20, v21\n"
    "v_mfma_f32_16x16x32_bf16 a[100:103], v[216:219], a[216:219], a[100:103]\n"
    "v_exp_f32 v22, v22\n"
    "v_mfma_f32_16x16x32_bf16 a[100:103], v[212:215], a[212:215], a[100:103]\n"
    "v_mfma_f32_16x16x32_bf16 a[100:103], v[208:211], a[208:211], a[100:103]\n"
    "v_exp_f32 v23, v23\n"
    "v_mfma_f32_16x16x32_bf16 a[104:107], v[224:227], a[208:211], a[104:107]\n"
    "v_cvt_pk_bf16_f32 v157, v22, v23\n"
    "v_mfma_f32_16x16x32_bf16 a[104:107], v[228:231], a[212:215], a[104:107]\n"
    "v_exp_f32 v24, v24\n"
    "v_mfma_f32_16x16x32_bf16 a[104:107], v[232:235], a[216:219], a[104:107]\n"
    "v_mfma_f32_16x16x32_bf16 a[104:107], v[236:239], a[220:223], a[104:107]\n"
    "v_exp_f32 v25, v25\n"
    "v_mfma_f32_16x16x32_bf16 a[108:111], v[252:255], a[220:223], a[108:111]\n"
    "v_cvt_pk_bf16_f32 v172, v24, v25\n"
    "s_waitcnt lgkmcnt(0)\n"
    "v_mfma_f32_16x16x32_bf16 a[108:111], v[248:251], a[216:219], a[108:111]\n"
    "v_exp_f32 v26, v26\n"
    "v_mfma_f32_16x16x32_bf16 a[108:111], v[244:247], a[212:215], a[108:111]\n"
    "ds_read_b128 a[216:219], v2 offset:63488\n"
    "ds_read_b128 a[220:223], v2 offset:64512\n"
    "v_mfma_f32_16x16x32_bf16 a[108:111], v[240:243], a[208:211], a[108:111]\n"
    "v_exp_f32 v27, v27\n"
    "v_mfma_f32_16x16x32_bf16 v[16:19], a[192:195], a[128:131], v[32:35]\n"
    "v_cvt_pk_bf16_f32 v173, v26, v27\n"
    "v_mfma_f32_16x16x32_bf16 v[16:19], a[196:199], a[132:135], v[16:19]\n"
    "v_exp_f32 v28, v28\n"
    "v_mfma_f32_16x16x32_bf16 v[16:19], a[200:203], a[136:139], v[16:19]\n"
    "ds_read_b128 a[208:211], v2 offset:61440\n"
    "ds_read_b128 a[212:215], v2 offset:62464\n"
    "v_mfma_f32_16x16x32_bf16 v[16:19], a[204:207], a[140:143], v[16:19]\n"
    "v_exp_f32 v29, v29\n"
    "v_mfma_f32_16x16x32_bf16 v[20:23], a[204:207], a[156:159], v[36:39]\n"
    "v_cvt_pk_bf16_f32 v188, v28, v29\n"
    "v_mfma_f32_16x16x32_bf16 v[20:23], a[200:203], a[152:155], v[20:23]\n"
    "v_exp_f32 v30, v30\n"
    "v_mfma_f32_16x16x32_bf16 v[20:23], a[196:199], a[148:151], v[20:23]\n"
    "v_mfma_f32_16x16x32_bf16 v[20:23], a[192:195], a[144:147], v[20:23]\n"
    "v_exp_f32 v31, v31\n"
    "v_mfma_f32_16x16x32_bf16 v[24:27], a[192:195], a[160:163], v[40:43]\n"
    "v_cvt_pk_bf16_f32 v189, v30, v31\n"
    "v_mfma_f32_16x16x32_bf16 v[24:27], a[196:199], a[164:167], v[24:27]\n"
    "v_exp_f32 v16, v16\n"
    "v_mfma_f32_16x16x32_bf16 v[24:27], a[200:203], a[168:171], v[24:27]\n"
    "v_mfma_f32_16x16x32_bf16 v[24:27], a[204:207], a[172:175], v[24:27]\n"
    "v_exp_f32 v17, v17\n"
    "v_mfma_f32_16x16x32_bf16 v[28:31], a[204:207], a[188:191], v[44:47]\n"
    "v_cvt_pk_bf16_f32 v142, v16, v17\n"
    "s_waitcnt vmcnt(1) lgkmcnt(0)\n"
    "v_mfma_f32_16x16x32_bf16 v[28:31], a[200:203], a[184:187], v[28:31]\n"
    "v_exp_f32 v18, v18\n"
    "s_barrier\n"
    "v_mfma_f32_16x16x32_bf16 v[28:31], a[196:199], a[180:183], v[28:31]\n"
    "ds_read_b128 a[200:203], v3 offset:2048\n"
    "ds_read_b128 a[204:207], v3 offset:3072\n"
    "v_mfma_f32_16x16x32_bf16 v[28:31], a[192:195], a[176:179], v[28:31]\n"
    "v_exp_f32 v19, v19\n"
    "v_mfma_f32_16x16x32_bf16 a[112:115], v[192:195], a[208:211], a[112:115]\n"
    "v_cvt_pk_bf16_f32 v143, v18, v19\n"
    "s_add_u32 s48, s48, 0x10000\n"
    "v_mfma_f32_16x16x32_bf16 a[112:115], v[196:199], a[212:215], a[112:115]\n"
    "v_exp_f32 v20, v20\n"
    "v_mfma_f32_16x16x32_bf16 a[112:115], v[200:203], a[216:219], a[112:115]\n"
    "ds_read_b128 a[192:195], v3 offset:0\n"
    "ds_read_b128 a[196:199], v3 offset:1024\n"
    "v_mfma_f32_16x16x32_bf16 a[112:115], v[204:207], a[220:223], a[112:115]\n"
    "v_exp_f32 v21, v21\n"
    "v_mfma_f32_16x16x32_bf16 v[48:51], v[64:67], v[140:143], v[48:51]\n"
    "v_cmp_gt_u32 vcc, 32, v96\n"
    "v_mfma_f32_16x16x32_bf16 a[116:119], v[220:223], a[220:223], a[116:119]\n"
    "v_cvt_pk_bf16_f32 v158, v20, v21\n"
    "s_add_u32 s97, s97, 128\n"
    "v_mfma_f32_16x16x32_bf16 a[116:119], v[216:219], a[216:219], a[116:119]\n"
    "v_exp_f32 v22, v22\n"
    "v_mfma_f32_16x16x32_bf16 a[116:119], v[212:215], a[212:215], a[116:119]\n"
    "v_mfma_f32_16x16x32_bf16 a[116:119], v[208:211], a[208:211], a[116:119]\n"
    "v_exp_f32 v23, v23\n"
    "v_mfma_f32_16x16x32_bf16 a[120:123], v[224:227], a[208:211], a[120:123]\n"
    "v_cvt_pk_bf16_f32 v159, v22, v23\n"
    "s_mov_b32 m0, s52\n"
    "v_mfma_f32_16x16x32_bf16 a[120:123], v[228:231], a[212:215], a[120:123]\n"
    "v_exp_f32 v24, v24\n"
    "v_mfma_f32_16x16x32_bf16 a[120:123], v[232:235], a[216:219], a[120:123]\n"
    "s_cbranch_vccnz L_spin_D\n"
    "L_spinret_D:\n"
    "v_mfma_f32_16x16x32_bf16 a[120:123], v[236:239], a[220:223], a[120:123]\n"
    "v_exp_f32 v25, v25\n"
    "v_mfma_f32_16x16x32_bf16 v[52:55], v[64:67], v[156:159], v[52:55]\n"
    "s_add_u32 s96, s96, 128\n"
    "s_min_u32 s96, s96, 0x2b00\n"
    "v_mfma_f32_16x16x32_bf16 a[124:127], v[252:255], a[220:223], a[124:127]\n"
    "v_cvt_pk_bf16_f32 v174, v24, v25\n"
    "s_waitcnt lgkmcnt(0)\n"
    "v_mfma_f32_16x16x32_bf16 a[124:127], v[248:251], a[216:219], a[124:127]\n"
    "v_exp_f32 v26, v26\n"
    "v_mfma_f32_16x16x32_bf16 a[124:127], v[244:247], a[212:215], a[124:127]\n"
    "ds_read_b128 a[216:219], v3 offset:34816\n"
    "ds_read_b128 a[220:223], v3 offset:35840\n"
    "v_mfma_f32_16x16x32_bf16 a[124:127], v[240:243], a[208:211], a[124:127]\n"
    "v_exp_f32 v27, v27\n"
    "s_cmp_lg_u32 s48, 0x100000\n"
    "s_cbranch_scc1 L_bodyC\n"
    "L_tail:\n"
    "v_mfma_f32_16x16x32_bf16 v[100:103], a[192:195], a[128:131], v[32:35]\n"
    "v_cvt_pk_bf16_f32 v175, v26, v27\n"
    "v_exp_f32 v28, v28\n"
    "v_mfma_f32_16x16x32_bf16 v[100:103], a[196:199], a[132:135], v[100:103]\n"
    "v_exp_f32 v29, v29\n"
    "v_exp_f32 v30, v30\n"
    "v_mfma_f32_16x16x32_bf16 v[100:103], a[200:203], a[136:139], v[100:103]\n"
    "v_cvt_pk_bf16_f32 v190, v28, v29\n"
    "v_exp_f32 v31, v31\n"
    "v_mfma_f32_16x16x32_bf16 v[100:103], a[204:207], a[140:143], v[100:103]\n"
    "v_mfma_f32_16x16x32_bf16 v[56:59], v[64:67], v[172:175], v[56:59]\n"
    "v_cvt_pk_bf16_f32 v191, v30, v31\n"
    "v_mfma_f32_16x16x32_bf16 v[104:107], a[192:195], a[144:147], v[36:39]\n"
    "s_waitcnt lgkmcnt(0)\n"
    "ds_read_b128 a[208:211], v3 offset:4096\n"
    "ds_read_b128 a[212:215], v3 offset:5120\n"
    "ds_read_b128 a[216:219], v3 offset:6144\n"
    "ds_read_b128 a[220:223], v3 offset:7168\n"
    "v_mfma_f32_16x16x32_bf16 v[104:107], a[196:199], a[148:151], v[104:107]\n"
    "v_mfma_f32_16x16x32_bf16 v[104:107], a[200:203], a[152:155], v[104:107]\n"
    "v_mfma_f32_16x16x32_bf16 v[104:107], a[204:207], a[156:159], v[104:107]\n"
    "v_mfma_f32_16x16x32_bf16 v[108:111], a[192:195], a[160:163], v[40:43]\n"
    "v_mfma_f32_16x16x32_bf16 v[108:111], a[196:199], a[164:167], v[108:111]\n"
    "s_lshl_b32 s60, %6, 13\n"
    "s_add_u32 s60, s60, 0x8000\n"
    "s_add_u32 m0, s60, %7\n"
    "s_add_u32 s60, s60, s48\n"
    "buffer_load_dwordx4 v76, s[40:43], s60 offen offset:0 sc1 lds\n"
    "v_mfma_f32_16x16x32_bf16 v[108:111], a[200:203], a[168:171], v[108:111]\n"
    "v_mfma_f32_16x16x32_bf16 v[108:111], a[204:207], a[172:175], v[108:111]\n"
    "v_mfma_f32_16x16x32_bf16 v[60:63], v[64:67], v[188:191], v[60:63]\n"
    "v_mfma_f32_16x16x32_bf16 v[112:115], a[192:195], a[176:179], v[44:47]\n"
    "v_mfma_f32_16x16x32_bf16 v[112:115], a[196:199], a[180:183], v[112:115]\n"
    "buffer_load_dwordx4 v76, s[40:43], s60 offen offset:1024 sc1 lds\n"
    "v_mfma_f32_16x16x32_bf16 v[112:115], a[200:203], a[184:187], v[112:115]\n"
    "v_mfma_f32_16x16x32_bf16 v[112:115], a[204:207], a[188:191], v[112:115]\n"
    "v_cmp_nge_f32 vcc, s54, v48\n"
    "s_cbranch_vccnz L_rare_E0\n"
    "L_ret_E0:\n"
    "v_add_f32 v68, v48, v68\n"
    "v_cmp_nge_f32 vcc, s54, v52\n"
    "s_cbranch_vccnz L_rare_E1\n"
    "L_ret_E1:\n"
    "v_add_f32 v69, v52, v69\n"
    "v_cmp_nge_f32 vcc, s54, v56\n"
    "s_cbranch_vccnz L_rare_E2\n"
    "L_ret_E2:\n"
    "v_add_f32 v70, v56, v70\n"
    "v_cmp_nge_f32 vcc, s54, v60\n"
    "s_cbranch_vccnz L_rare_E3\n"
    "L_ret_E3:\n"
    "v_add_f32 v71, v60, v71\n"
    "ds_read_b128 a[192:195], v3 offset:8192\n"
    "ds_read_b128 a[196:199], v3 offset:9216\n"
    "ds_read_b128 a[200:203], v3 offset:10240\n"
    "ds_read_b128 a[204:207], v3 offset:11264\n"
    "s_waitcnt lgkmcnt(4)\n"
    "v_mfma_f32_16x16x32_bf16 v[16:19], a[208:211], a[128:131], v[32:35]\n"
    "v_exp_f32 v100, v100\n"
    "v_mfma_f32_16x16x32_bf16 v[16:19], a[212:215], a[132:135], v[16:19]\n"
    "buffer_load_dwordx4 v76, s[40:43], s60 offen offset:2048 sc1 lds\n"
    "v_exp_f32 v101, v101\n"
    "v_exp_f32 v102, v102\n"
    "v_mfma_f32_16x16x32_bf16 v[16:19], a[216:219], a[136:139], v[16:19]\n"
    "v_cvt_pk_bf16_f32 v192, v100, v101\n"
    "v_exp_f32 v103, v103\n"
    "v_mfma_f32_16x16x32_bf16 v[16:19], a[220:223], a[140:143], v[16:19]\n"
    "v_cvt_pk_bf16_f32 v193, v102, v103\n"
    "v_exp_f32 v104, v104\n"
    "v_mfma_f32_16x16x32_bf16 v[20:23], a[208:211], a[144:147], v[36:39]\n"
    "v_exp_f32 v105, v105\n"
    "v_mfma_f32_16x16x32_bf16 v[20:23], a[212:215], a[148:151], v[20:23]\n"
    "buffer_load_dwordx4 v76, s[40:43], s60 offen offset:3072 sc1 lds\n"
    "v_exp_f32 v106, v106\n"
    "v_cvt_pk_bf16_f32 v208, v104, v105\n"
    "v_mfma_f32_16x16x32_bf16 v[20:23], a[216:219], a[152:155], v[20:23]\n"
    "v_exp_f32 v107, v107\n"
    "v_mfma_f32_16x16x32_bf16 v[20:23], a[220:223], a[156:159], v[20:23]\n"
    "v_cvt_pk_bf16_f32 v209, v106, v107\n"
    "v_exp_f32 v108, v108\n"
    "v_mfma_f32_16x16x32_bf16 v[24:27], a[208:211], a[160:163], v[40:43]\n"
    "v_exp_f32 v109, v109\n"
    "v_mfma_f32_16x16x32_bf16 v[24:27], a[212:215], a[164:167], v[24:27]\n"
    "s_add_u32 s60, s60, 0x1000\n"
    "s_add_u32 m0, m0, 0x1000\n"
    "s_nop 0\n"
    "buffer_load_dwordx4 v76, s[40:43], s60 offen offset:0 sc1 lds\n"
    "v_exp_f32 v110, v110\n"
    "v_cvt_pk_bf16_f32 v224, v108, v109\n"
    "v_mfma_f32_16x16x32_bf16 v[24:27], a[216:219], a[168:171], v[24:27]\n"
    "v_exp_f32 v111, v111\n"
    "v_mfma_f32_16x16x32_bf16 v[24:27], a[220:223], a[172:175], v[24:27]\n"
    "v_cvt_pk_bf16_f32 v225, v110, v111\n"
    "v_exp_f32 v112, v112\n"
    "v_mfma_f32_16x16x32_bf16 v[28:31], a[208:211], a[176:179], v[44:47]\n"
    "v_exp_f32 v113, v113\n"
    "v_mfma_f32_16x16x32_bf16 v[28:31], a[212:215], a[180:183], v[28:31]\n"
    "buffer_load_dwordx4 v76, s[40:43], s60 offen offset:1024 sc1 lds\n"
    "v_exp_f32 v114, v114\n"
    "v_cvt_pk_bf16_f32 v240, v112, v113\n"
    "v_mfma_f32_16x16x32_bf16 v[28:31], a[216:219], a[184:187], v[28:31]\n"
    "v_exp_f32 v115, v115\n"
    "v_mfma_f32_16x16x32_bf16 v[28:31], a[220:223], a[188:191], v[28:31]\n"
    "v_cvt_pk_bf16_f32 v241, v114, v115\n"
    "v_exp_f32 v16, v16\n"
    "ds_read_b128 a[208:211], v3 offset:12288\n"
    "ds_read_b128 a[212:215], v3 offset:13312\n"
    "ds_read_b128 a[216:219], v3 offset:14336\n"
    "ds_read_b128 a[220:223], v3 offset:15360\n"
    "s_waitcnt lgkmcnt(4)\n"
    "v_mfma_f32_16x16x32_bf16 v[100:103], a[192:195], a[128:131], v[32:35]\n"
    "v_exp_f32 v17, v17\n"
    "v_mfma_f32_16x16x32_bf16 v[100:103], a[196:199], a[132:135], v[100:103]\n"
    "buffer_load_dwordx4 v76, s[40:43], s60 offen offset:2048 sc1 lds\n"
    "v_exp_f32 v18, v18\n"
    "v_cvt_pk_bf16_f32 v194, v16, v17\n"
    "v_mfma_f32_16x16x32_bf16 v[100:103], a[200:203], a[136:139], v[100:103]\n"
    "v_exp_f32 v19, v19\n"
    "v_mfma_f32_16x16x32_bf16 v[100:103], a[204:207], a[140:143], v[100:103]\n"
    "v_cvt_pk_bf16_f32 v195, v18, v19\n"
    "v_exp_f32 v20, v20\n"
    "v_mfma_f32_16x16x32_bf16 v[104:107], a[192:195], a[144:147], v[36:39]\n"
    "v_exp_f32 v21, v21\n"
    "v_mfma_f32_16x16x32_bf16 v[104:107], a[196:199], a[148:151], v[104:107]\n"
    "buffer_load_dwordx4 v76, s[40:43], s60 offen offset:3072 sc1 lds\n"
    "v_exp_f32 v22, v22\n"
    "v_cvt_pk_bf16_f32 v210, v20, v21\n"
    "v_mfma_f32_16x16x32_bf16 v[104:107], a[200:203], a[152:155], v[104:107]\n"
    "v_exp_f32 v23, v23\n"
    "v_mfma_f32_16x16x32_bf16 v[104:107], a[204:207], a[156:159], v[104:107]\n"
    "v_cvt_pk_bf16_f32 v211, v22, v23\n"
    "v_exp_f32 v24, v24\n"
    "v_mfma_f32_16x16x32_bf16 v[108:111], a[192:195], a[160:163], v[40:43]\n"
    "v_exp_f32 v25, v25\n"
    "v_mfma_f32_16x16x32_bf16 v[108:111], a[196:199], a[164:167], v[108:111]\n"
    "v_exp_f32 v26, v26\n"
    "v_cvt_pk_bf16_f32 v226, v24, v25\n"
    "v_mfma_f32_16x16x32_bf16 v[108:111], a[200:203], a[168:171], v[108:111]\n"
    "v_exp_f32 v27, v27\n"
    "v_mfma_f32_16x16x32_bf16 v[108:111], a[204:207], a[172:175], v[108:111]\n"
    "v_cvt_pk_bf16_f32 v227, v26, v27\n"
    "v_exp_f32 v28, v28\n"
    "v_mfma_f32_16x16x32_bf16 v[112:115], a[192:195], a[176:179], v[44:47]\n"
    "v_exp_f32 v29, v29\n"
    "v_mfma_f32_16x16x32_bf16 v[112:115], a[196:199], a[180:183], v[112:115]\n"
    "v_exp_f32 v30, v30\n"
    "v_cvt_pk_bf16_f32 v242, v28, v29\n"
    "v_mfma_f32_16x16x32_bf16 v[112:115], a[200:203], a[184:187], v[112:115]\n"
    "v_exp_f32 v31, v31\n"
    "v_mfma_f32_16x16x32_bf16 v[112:115], a[204:207], a[188:191], v[112:115]\n"
    "v_cvt_pk_bf16_f32 v243, v30, v31\n"
    "v_mfma_f32_16x16x32_bf16 v[48:51], v[64:67], v[192:195], 0\n"
    "ds_read_b128 a[192:195], v3 offset:16384\n"
    "ds_read_b128 a[196:199], v3 offset:17408\n"
    "ds_read_b128 a[200:203], v3 offset:18432\n"
    "ds_read_b128 a[204:207], v3 offset:19456\n"
    "s_waitcnt lgkmcnt(4)\n"
    "v_mfma_f32_16x16x32_bf16 v[16:19], a[208:211], a[128:131], v[32:35]\n"
    "v_mfma_f32_16x16x32_bf16 v[52:55], v[64:67], v[208:211], 0\n"
    "v_mfma_f32_16x16x32_bf16 v[16:19], a[212:215], a[132:135], v[16:19]\n"
    "v_mfma_f32_16x16x32_bf16 v[56:59], v[64:67], v[224:227], 0\n"
    "v_mfma_f32_16x16x32_bf16 v[60:63], v[64:67], v[240:243], 0\n"
    "v_mfma_f32_16x16x32_bf16 v[16:19], a[216:219], a[136:139], v[16:19]\n"
    "v_exp_f32 v100, v100\n"
    "v_mfma_f32_16x16x32_bf16 v[16:19], a[220:223], a[140:143], v[16:19]\n"
    "v_exp_f32 v101, v101\n"
    "v_exp_f32 v102, v102\n"
    "v_mfma_f32_16x16x32_bf16 v[20:23], a[208:211], a[144:147], v[36:39]\n"
    "v_cvt_pk_bf16_f32 v196, v100, v101\n"
    "v_exp_f32 v103, v103\n"
    "v_mfma_f32_16x16x32_bf16 v[20:23], a[212:215], a[148:151], v[20:23]\n"
    "v_cvt_pk_bf16_f32 v197, v102, v103\n"
    "v_exp_f32 v104, v104\n"
    "v_mfma_f32_16x16x32_bf16 v[20:23], a[216:219], a[152:155], v[20:23]\n"
    "v_exp_f32 v105, v105\n"
    "v_mfma_f32_16x16x32_bf16 v[20:23], a[220:223], a[156:159], v[20:23]\n"
    "v_exp_f32 v106, v106\n"
    "v_cvt_pk_bf16_f32 v212, v104, v105\n"
    "v_mfma_f32_16x16x32_bf16 v[24:27], a[208:211], a[160:163], v[40:43]\n"
    "v_exp_f32 v107, v107\n"
    "v_mfma_f32_16x16x32_bf16 v[24:27], a[212:215], a[164:167], v[24:27]\n"
    "v_cvt_pk_bf16_f32 v213, v106, v107\n"
    "v_exp_f32 v108, v108\n"
    "v_mfma_f32_16x16x32_bf16 v[24:27], a[216:219], a[168:171], v[24:27]\n"
    "v_exp_f32 v109, v109\n"
    "v_mfma_f32_16x16x32_bf16 v[24:27], a[220:223], a[172:175], v[24:27]\n"
    "v_exp_f32 v110, v110\n"
    "v_cvt_pk_bf16_f32 v228, v108, v109\n"
    "v_mfma_f32_16x16x32_bf16 v[28:31], a[208:211], a[176:179], v[44:47]\n"
    "v_exp_f32 v111, v111\n"
    "v_mfma_f32_16x16x32_bf16 v[28:31], a[212:215], a[180:183], v[28:31]\n"
    "v_cvt_pk_bf16_f32 v229, v110, v111\n"
    "v_exp_f32 v112, v112\n"
    "v_mfma_f32_16x16x32_bf16 v[28:31], a[216:219], a[184:187], v[28:31]\n"
    "v_exp_f32 v113, v113\n"
    "v_mfma_f32_16x16x32_bf16 v[28:31], a[220:223], a[188:191], v[28:31]\n"
    "v_exp_f32 v114, v114\n"
    "v_cvt_pk_bf16_f32 v244, v112, v113\n"
    "ds_read_b128 a[208:211], v3 offset:20480\n"
    "ds_read_b128 a[212:215], v3 offset:21504\n"
    "ds_read_b128 a[216:219], v3 offset:22528\n"
    "ds_read_b128 a[220:223], v3 offset:23552\n"
    "s_waitcnt lgkmcnt(4)\n"
    "v_mfma_f32_16x16x32_bf16 v[100:103], a[192:195], a[128:131], v[32:35]\n"
    "v_exp_f32 v115, v115\n"
    "v_mfma_f32_16x16x32_bf16 v[100:103], a[196:199], a[132:135], v[100:103]\n"
    "v_cvt_pk_bf16_f32 v245, v114, v115\n"
    "v_exp_f32 v16, v16\n"
    "v_mfma_f32_16x16x32_bf16 v[100:103], a[200:203], a[136:139], v[100:103]\n"
    "v_exp_f32 v17, v17\n"
    "v_mfma_f32_16x16x32_bf16 v[100:103], a[204:207], a[140:143], v[100:103]\n"
    "v_exp_f32 v18, v18\n"
    "v_cvt_pk_bf16_f32 v198, v16, v17\n"
    "v_mfma_f32_16x16x32_bf16 v[104:107], a[192:195], a[144:147], v[36:39]\n"
    "v_exp_f32 v19, v19\n"
    "v_mfma_f32_16x16x32_bf16 v[104:107], a[196:199], a[148:151], v[104:107]\n"
    "v_cvt_pk_bf16_f32 v199, v18, v19\n"
    "v_exp_f32 v20, v20\n"
    "v_mfma_f32_16x16x32_bf16 v[104:107], a[200:203], a[152:155], v[104:107]\n"
    "v_exp_f32 v21, v21\n"
    "v_mfma_f32_16x16x32_bf16 v[104:107], a[204:207], a[156:159], v[104:107]\n"
    "v_exp_f32 v22, v22\n"
    "v_cvt_pk_bf16_f32 v214, v20, v21\n"
    "v_mfma_f32_16x16x32_bf16 v[108:111], a[192:195], a[160:163], v[40:43]\n"
    "v_exp_f32 v23, v23\n"
    "v_mfma_f32_16x16x32_bf16 v[108:111], a[196:199], a[164:167], v[108:111]\n"
    "v_cvt_pk_bf16_f32 v215, v22, v23\n"
    "v_exp_f32 v24, v24\n"
    "v_mfma_f32_16x16x32_bf16 v[108:111], a[200:203], a[168:171], v[108:111]\n"
    "v_exp_f32 v25, v25\n"
    "v_mfma_f32_16x16x32_bf16 v[108:111], a[204:207], a[172:175], v[108:111]\n"
    "v_exp_f32 v26, v26\n"
    "v_cvt_pk_bf16_f32 v230, v24, v25\n"
    "v_mfma_f32_16x16x32_bf16 v[112:115], a[192:195], a[176:179], v[44:47]\n"
    "v_exp_f32 v27, v27\n"
    "v_mfma_f32_16x16x32_bf16 v[112:115], a[196:199], a[180:183], v[112:115]\n"
    "v_cvt_pk_bf16_f32 v231, v26, v27\n"
    "v_exp_f32 v28, v28\n"
    "v_mfma_f32_16x16x32_bf16 v[112:115], a[200:203], a[184:187], v[112:115]\n"
    "v_exp_f32 v29, v29\n"
    "v_mfma_f32_16x16x32_bf16 v[112:115], a[204:207], a[188:191], v[112:115]\n"
    "v_exp_f32 v30, v30\n"
    "v_cvt_pk_bf16_f32 v246, v28, v29\n"
    "ds_read_b128 a[192:195], v3 offset:24576\n"
    "ds_read_b128 a[196:199], v3 offset:25600\n"
    "ds_read_b128 a[200:203], v3 offset:26624\n"
    "ds_read_b128 a[204:207], v3 offset:27648\n"
    "s_waitcnt lgkmcnt(4)\n"
    "v_mfma_f32_16x16x32_bf16 v[16:19], a[208:211], a[128:131], v[32:35]\n"
    "v_exp_f32 v31, v31\n"
    "v_mfma_f32_16x16x32_bf16 v[16:19], a[212:215], a[132:135], v[16:19]\n"
    "v_cvt_pk_bf16_f32 v247, v30, v31\n"
    "v_mfma_f32_16x16x32_bf16 v[48:51], v[64:67], v[196:199], v[48:51]\n"
    "v_mfma_f32_16x16x32_bf16 v[16:19], a[216:219], a[136:139], v[16:19]\n"
    "v_mfma_f32_16x16x32_bf16 v[52:55], v[64:67], v[212:215], v[52:55]\n"
    "v_mfma_f32_16x16x32_bf16 v[16:19], a[220:223], a[140:143], v[16:19]\n"
    "v_mfma_f32_16x16x32_bf16 v[56:59], v[64:67], v[228:231], v[56:59]\n"
    "v_mfma_f32_16x16x32_bf16 v[60:63], v[64:67], v[244:247], v[60:63]\n"
    "v_mfma_f32_16x16x32_bf16 v[20:23], a[208:211], a[144:147], v[36:39]\n"
    "v_exp_f32 v100, v100\n"
    "v_mfma_f32_16x16x32_bf16 v[20:23], a[212:215], a[148:151], v[20:23]\n"
    "v_exp_f32 v101, v101\n"
    "v_exp_f32 v102, v102\n"
    "v_mfma_f32_16x16x32_bf16 v[20:23], a[216:219], a[152:155], v[20:23]\n"
    "v_cvt_pk_bf16_f32 v200, v100, v101\n"
    "v_exp_f32 v103, v103\n"
    "v_mfma_f32_16x16x32_bf16 v[20:23], a[220:223], a[156:159], v[20:23]\n"
    "v_cvt_pk_bf16_f32 v201, v102, v103\n"
    "v_exp_f32 v104, v104\n"
    "v_mfma_f32_16x16x32_bf16 v[24:27], a[208:211], a[160:163], v[40:43]\n"
    "v_exp_f32 v105, v105\n"
    "v_mfma_f32_16x16x32_bf16 v[24:27], a[212:215], a[164:167], v[24:27]\n"
    "v_exp_f32 v106, v106\n"
    "v_cvt_pk_bf16_f32 v216, v104, v105\n"
    "v_mfma_f32_16x16x32_bf16 v[24:27], a[216:219], a[168:171], v[24:27]\n"
    "v_exp_f32 v107, v107\n"
    "v_mfma_f32_16x16x32_bf16 v[24:27], a[220:223], a[172:175], v[24:27]\n"
    "v_cvt_pk_bf16_f32 v217, v106, v107\n"
    "v_exp_f32 v108, v108\n"
    "v_mfma_f32_16x16x32_bf16 v[28:31], a[208:211], a[176:179], v[44:47]\n"
    "v_exp_f32 v109, v109\n"
    "v_mfma_f32_16x16x32_bf16 v[28:31], a[212:215], a[180:183], v[28:31]\n"
    "v_exp_f32 v110, v110\n"
    "v_cvt_pk_bf16_f32 v232, v108, v109\n"
    "v_mfma_f32_16x16x32_bf16 v[28:31], a[216:219], a[184:187], v[28:31]\n"
    "v_exp_f32 v111, v111\n"
    "v_mfma_f32_16x16x32_bf16 v[28:31], a[220:223], a[188:191], v[28:31]\n"
    "v_cvt_pk_bf16_f32 v233, v110, v111\n"
    "v_exp_f32 v112, v112\n"
    "ds_read_b128 a[208:211], v3 offset:28672\n"
    "ds_read_b128 a[212:215], v3 offset:29696\n"
    "ds_read_b128 a[216:219], v3 offset:30720\n"
    "ds_read_b128 a[220:223], v3 offset:31744\n"
    "s_waitcnt lgkmcnt(4)\n"
    "v_mfma_f32_16x16x32_bf16 v[100:103], a[192:195], a[128:131], v[32:35]\n"
    "v_exp_f32 v113, v113\n"
    "v_mfma_f32_16x16x32_bf16 v[100:103], a[196:199], a[132:135], v[100:103]\n"
    "v_exp_f32 v114, v114\n"
    "v_cvt_pk_bf16_f32 v248, v112, v113\n"
    "v_mfma_f32_16x16x32_bf16 v[100:103], a[200:203], a[136:139], v[100:103]\n"
    "v_exp_f32 v115, v115\n"
    "v_mfma_f32_16x16x32_bf16 v[100:103], a[204:207], a[140:143], v[100:103]\n"
    "v_cvt_pk_bf16_f32 v249, v114, v115\n"
    "v_exp_f32 v16, v16\n"
    "v_mfma_f32_16x16x32_bf16 v[104:107], a[192:195], a[144:147], v[36:39]\n"
    "v_exp_f32 v17, v17\n"
    "v_mfma_f32_16x16x32_bf16 v[104:107], a[196:199], a[148:151], v[104:107]\n"
    "v_exp_f32 v18, v18\n"
    "v_cvt_pk_bf16_f32 v202, v16, v17\n"
    "v_mfma_f32_16x16x32_bf16 v[104:107], a[200:203], a[152:155], v[104:107]\n"
    "v_exp_f32 v19, v19\n"
    "v_mfma_f32_16x16x32_bf16 v[104:107], a[204:207], a[156:159], v[104:107]\n"
    "v_cvt_pk_bf16_f32 v203, v18, v19\n"
    "v_exp_f32 v20, v20\n"
    "v_mfma_f32_16x16x32_bf16 v[108:111], a[192:195], a[160:163], v[40:43]\n"
    "v_exp_f32 v21, v21\n"
    "v_mfma_f32_16x16x32_bf16 v[108:111], a[196:199], a[164:167], v[108:111]\n"
    "v_exp_f32 v22, v22\n"
    "v_cvt_pk_bf16_f32 v218, v20, v21\n"
    "v_mfma_f32_16x16x32_bf16 v[108:111], a[200:203], a[168:171], v[108:111]\n"
    "v_exp_f32 v23, v23\n"
    "v_mfma_f32_16x16x32_bf16 v[108:111], a[204:207], a[172:175], v[108:111]\n"
    "v_cvt_pk_bf16_f32 v219, v22, v23\n"
    "v_exp_f32 v24, v24\n"
    "v_mfma_f32_16x16x32_bf16 v[112:115], a[192:195], a[176:179], v[44:47]\n"
    "v_exp_f32 v25, v25\n"
    "v_mfma_f32_16x16x32_bf16 v[112:115], a[196:199], a[180:183], v[112:115]\n"
    "v_exp_f32 v26, v26\n"
    "v_cvt_pk_bf16_f32 v234, v24, v25\n"
    "v_mfma_f32_16x16x32_bf16 v[112:115], a[200:203], a[184:187], v[112:115]\n"
    "v_exp_f32 v27, v27\n"
    "v_mfma_f32_16x16x32_bf16 v[112:115], a[204:207], a[188:191], v[112:115]\n"
    "v_cvt_pk_bf16_f32 v235, v26, v27\n"
    "v_exp_f32 v28, v28\n"
    "s_waitcnt lgkmcnt(0)\n"
    "v_mfma_f32_16x16x32_bf16 v[16:19], a[208:211], a[128:131], v[32:35]\n"
    "v_exp_f32 v29, v29\n"
    "v_mfma_f32_16x16x32_bf16 v[16:19], a[212:215], a[132:135], v[16:19]\n"
    "v_exp_f32 v30, v30\n"
    "v_cvt_pk_bf16_f32 v250, v28, v29\n"
    "v_mfma_f32_16x16x32_bf16 v[16:19], a[216:219], a[136:139], v[16:19]\n"
    "v_exp_f32 v31, v31\n"
    "v_mfma_f32_16x16x32_bf16 v[16:19], a[220:223], a[140:143], v[16:19]\n"
    "v_cvt_pk_bf16_f32 v251, v30, v31\n"
    "v_mfma_f32_16x16x32_bf16 v[48:51], v[64:67], v[200:203], v[48:51]\n"
    "v_mfma_f32_16x16x32_bf16 v[20:23], a[208:211], a[144:147], v[36:39]\n"
    "v_mfma_f32_16x16x32_bf16 v[52:55], v[64:67], v[216:219], v[52:55]\n"
    "v_mfma_f32_16x16x32_bf16 v[20:23], a[212:215], a[148:151], v[20:23]\n"
    "v_mfma_f32_16x16x32_bf16 v[56:59], v[64:67], v[232:235], v[56:59]\n"
    "v_mfma_f32_16x16x32_bf16 v[60:63], v[64:67], v[248:251], v[60:63]\n"
    "v_mfma_f32_16x16x32_bf16 v[20:23], a[216:219], a[152:155], v[20:23]\n"
    "v_exp_f32 v100, v100\n"
    "v_mfma_f32_16x16x32_bf16 v[20:23], a[220:223], a[156:159], v[20:23]\n"
    "v_exp_f32 v101, v101\n"
    "v_exp_f32 v102, v102\n"
    "v_mfma_f32_16x16x32_bf16 v[24:27], a[208:211], a[160:163], v[40:43]\n"
    "v_cvt_pk_bf16_f32 v204, v100, v101\n"
    "v_exp_f32 v103, v103\n"
    "v_mfma_f32_16x16x32_bf16 v[24:27], a[212:215], a[164:167], v[24:27]\n"
    "v_cvt_pk_bf16_f32 v205, v102, v103\n"
    "v_exp_f32 v104, v104\n"
    "v_mfma_f32_16x16x32_bf16 v[24:27], a[216:219], a[168:171], v[24:27]\n"
    "v_exp_f32 v105, v105\n"
    "v_mfma_f32_16x16x32_bf16 v[24:27], a[220:223], a[172:175], v[24:27]\n"
    "v_exp_f32 v106, v106\n"
    "v_cvt_pk_bf16_f32 v220, v104, v105\n"
    "v_mfma_f32_16x16x32_bf16 v[28:31], a[208:211], a[176:179], v[44:47]\n"
    "v_exp_f32 v107, v107\n"
    "v_mfma_f32_16x16x32_bf16 v[28:31], a[212:215], a[180:183], v[28:31]\n"
    "v_cvt_pk_bf16_f32 v221, v106, v107\n"
    "v_exp_f32 v108, v108\n"
    "v_mfma_f32_16x16x32_bf16 v[28:31], a[216:219], a[184:187], v[28:31]\n"
    "v_exp_f32 v109, v109\n"
    "v_mfma_f32_16x16x32_bf16 v[28:31], a[220:223], a[188:191], v[28:31]\n"
    "v_exp_f32 v110, v110\n"
    "v_cvt_pk_bf16_f32 v236, v108, v109\n"
    "s_waitcnt vmcnt(0)\n"
    "s_barrier\n"
    "ds_read_b128 a[192:195], v3 offset:32768\n"
    "ds_read_b128 a[196:199], v3 offset:33792\n"
    "ds_read_b128 a[200:203], v3 offset:34816\n"
    "ds_read_b128 a[204:207], v3 offset:35840\n"
    "ds_read_b128 a[208:211], v2 offset:32768\n"
    "ds_read_b128 a[212:215], v2 offset:33792\n"
    "ds_read_b128 a[216:219], v2 offset:34816\n"
    "ds_read_b128 a[220:223], v2 offset:35840\n"
    "v_exp_f32 v111, v111\n"
    "s_nop 0\n"
    "v_cvt_pk_bf16_f32 v237, v110, v111\n"
    "v_exp_f32 v112, v112\n"
    "v_exp_f32 v113, v113\n"
    "v_exp_f32 v114, v114\n"
    "v_cvt_pk_bf16_f32 v252, v112, v113\n"
    "v_exp_f32 v115, v115\n"
    "s_nop 0\n"
    "v_cvt_pk_bf16_f32 v253, v114, v115\n"
    "v_exp_f32 v16, v16\n"
    "v_exp_f32 v17, v17\n"
    "v_exp_f32 v18, v18\n"
    "v_cvt_pk_bf16_f32 v206, v16, v17\n"
    "v_exp_f32 v19, v19\n"
    "s_nop 0\n"
    "v_cvt_pk_bf16_f32 v207, v18, v19\n"
    "v_exp_f32 v20, v20\n"
    "v_exp_f32 v21, v21\n"
    "v_exp_f32 v22, v22\n"
    "v_cvt_pk_bf16_f32 v222, v20, v21\n"
    "v_exp_f32 v23, v23\n"
    "s_nop 0\n"
    "v_cvt_pk_bf16_f32 v223, v22, v23\n"
    "v_exp_f32 v24, v24\n"
    "v_exp_f32 v25, v25\n"
    "v_exp_f32 v26, v26\n"
    "v_cvt_pk_bf16_f32 v238, v24, v25\n"
    "v_exp_f32 v27, v27\n"
    "s_nop 0\n"
    "v_cvt_pk_bf16_f32 v239, v26, v27\n"
    "v_exp_f32 v28, v28\n"
    "v_exp_f32 v29, v29\n"
    "v_exp_f32 v30, v30\n"
    "v_cvt_pk_bf16_f32 v254, v28, v29\n"
    "v_exp_f32 v31, v31\n"
    "s_nop 0\n"
    "v_cvt_pk_bf16_f32 v255, v30, v31\n"
    "v_mfma_f32_16x16x32_bf16 v[48:51], v[64:67], v[204:207], v[48:51]\n"
    "v_mfma_f32_16x16x32_bf16 v[52:55], v[64:67], v[220:223], v[52:55]\n"
    "v_mfma_f32_16x16x32_bf16 v[56:59], v[64:67], v[236:239], v[56:59]\n"
    "v_mfma_f32_16x16x32_bf16 v[60:63], v[64:67], v[252:255], v[60:63]\n"
    "s_nop 4\n"
    "v_cmp_nge_f32 vcc, s54, v48\n"
    "s_cbranch_vccnz L_rare_F0\n"
    "L_ret_F0:\n"
    "v_add_f32 v68, v48, v68\n"
    "v_cmp_nge_f32 vcc, s54, v52\n"
    "s_cbranch_vccnz L_rare_F1\n"
    "L_ret_F1:\n"
    "v_add_f32 v69, v52, v69\n"
    "v_cmp_nge_f32 vcc, s54, v56\n"
    "s_cbranch_vccnz L_rare_F2\n"
    "L_ret_F2:\n"
    "v_add_f32 v70, v56, v70\n"
    "v_cmp_nge_f32 vcc, s54, v60\n"
    "s_cbranch_vccnz L_rare_F3\n"
    "L_ret_F3:\n"
    "v_add_f32 v71, v60, v71\n"
    "v_rcp_f32 v72, v68\n"
    "v_rcp_f32 v73, v69\n"
    "v_rcp_f32 v74, v70\n"
    "v_rcp_f32 v75, v71\n"
    "s_waitcnt lgkmcnt(0)\n"
    "ds_bpermute_b32 v80, v99, v72 offset:0\n"
    "ds_bpermute_b32 v81, v99, v72 offset:4\n"
    "ds_bpermute_b32 v82, v99, v72 offset:8\n"
    "ds_bpermute_b32 v83, v99, v72 offset:12\n"
    "ds_bpermute_b32 v84, v99, v73 offset:0\n"
    "ds_bpermute_b32 v85, v99, v73 offset:4\n"
    "ds_bpermute_b32 v86, v99, v73 offset:8\n"
    "ds_bpermute_b32 v87, v99, v73 offset:12\n"
    "ds_bpermute_b32 v88, v99, v74 offset:0\n"
    "ds_bpermute_b32 v89, v99, v74 offset:4\n"
    "ds_bpermute_b32 v90, v99, v74 offset:8\n"
    "ds_bpermute_b32 v91, v99, v74 offset:12\n"
    "v_and_b32 v8, 3, v0\n"
    "v_lshrrev_b32 v9, 4, v0\n"
    "v_lshl_add_u32 v8, v9, 2, v8\n"
    "v_bfe_u32 v9, v0, 2, 2\n"
    "v_lshlrev_b32 v9, 4, v9\n"
    "v_lshl_add_u32 v96, v8, 8, v9\n"
    "s_mov_b32 s68, 0x55555555\n"
    "s_mov_b32 s69, 0x55555555\n"
    "s_mov_b32 s70, 0xaaaaaaaa\n"
    "s_mov_b32 s71, 0xaaaaaaaa\n"
    "s_mov_b32 s72, 0x33333333\n"
    "s_mov_b32 s73, 0x33333333\n"
    "s_mov_b32 s74, 0xcccccccc\n"
    "s_mov_b32 s75, 0xcccccccc\n"
    "s_waitcnt lgkmcnt(0)\n"
    "ds_bpermute_b32 v92, v99, v75 offset:0\n"
    "ds_bpermute_b32 v93, v99, v75 offset:4\n"
    "ds_bpermute_b32 v94, v99, v75 offset:8\n"
    "ds_bpermute_b32 v95, v99, v75 offset:12\n"
    "ds_read_b128 a[224:227], v3 offset:36864\n"
    "ds_read_b128 a[228:231], v3 offset:37888\n"
    "ds_read_b128 a[232:235], v3 offset:38912\n"
    "ds_read_b128 a[236:239], v3 offset:39936\n"
    "ds_read_b128 a[240:243], v2 offset:36864\n"
    "ds_read_b128 a[244:247], v2 offset:37888\n"
    "ds_read_b128 a[248:251], v2 offset:38912\n"
    "ds_read_b128 a[252:255], v2 offset:39936\n"
    "v_mfma_f32_16x16x32_bf16 a[0:3], v[128:131], a[192:195], a[0:3]\n"
    "v_mfma_f32_16x16x32_bf16 a[0:3], v[132:135], a[196:199], a[0:3]\n"
    "v_mfma_f32_16x16x32_bf16 a[0:3], v[136:139], a[200:203], a[0:3]\n"
    "v_mfma_f32_16x16x32_bf16 a[0:3], v[140:143], a[204:207], a[0:3]\n"
    "v_mfma_f32_16x16x32_bf16 a[0:3], v[192:195], a[208:211], a[0:3]\n"
    "v_mfma_f32_16x16x32_bf16 a[0:3], v[196:199], a[212:215], a[0:3]\n"
    "v_mfma_f32_16x16x32_bf16 a[0:3], v[200:203], a[216:219], a[0:3]\n"
    "v_mfma_f32_16x16x32_bf16 a[0:3], v[204:207], a[220:223], a[0:3]\n"
    "s_waitcnt lgkmcnt(4)\n"
    "v_mfma_f32_16x16x32_bf16 a[16:19], v[128:131], a[224:227], a[16:19]\n"
    "v_mfma_f32_16x16x32_bf16 a[16:19], v[132:135], a[228:231], a[16:19]\n"
    "v_mfma_f32_16x16x32_bf16 a[16:19], v[136:139], a[232:235], a[16:19]\n"
    "v_mfma_f32_16x16x32_bf16 a[16:19], v[140:143], a[236:239], a[16:19]\n"
    "s_nop 2\n"
    "v_accvgpr_read_b32 v16, a0\n"
    "s_waitcnt lgkmcnt(0)\n"
    "v_mfma_f32_16x16x32_bf16 a[16:19], v[192:195], a[240:243], a[16:19]\n"
    "v_accvgpr_read_b32 v17, a1\n"
    "v_accvgpr_read_b32 v18, a2\n"
    "v_mfma_f32_16x16x32_bf16 a[16:19], v[196:199], a[244:247], a[16:19]\n"
    "v_accvgpr_read_b32 v19, a3\n"
    "v_mul_f32 v16, v80, v16\n"
    "v_mfma_f32_16x16x32_bf16 a[16:19], v[200:203], a[248:251], a[16:19]\n"
    "v_mul_f32 v17, v81, v17\n"
    "v_mul_f32 v18, v82, v18\n"
    "v_mfma_f32_16x16x32_bf16 a[16:19], v[204:207], a[252:255], a[16:19]\n"
    "v_mul_f32 v19, v83, v19\n"
    "v_mfma_f32_16x16x32_bf16 a[4:7], v[144:147], a[192:195], a[4:7]\n"
    "v_mfma_f32_16x16x32_bf16 a[4:7], v[148:151], a[196:199], a[4:7]\n"
    "v_mfma_f32_16x16x32_bf16 a[4:7], v[152:155], a[200:203], a[4:7]\n"
    "v_mfma_f32_16x16x32_bf16 a[4:7], v[156:159], a[204:207], a[4:7]\n"
    "s_nop 2\n"
    "v_accvgpr_read_b32 v20, a16\n"
    "v_accvgpr_read_b32 v21, a17\n"
    "v_mfma_f32_16x16x32_bf16 a[4:7], v[208:211], a[208:211], a[4:7]\n"
    "v_accvgpr_read_b32 v22, a18\n"
    "v_accvgpr_read_b32 v23, a19\n"
    "v_mfma_f32_16x16x32_bf16 a[4:7], v[212:215], a[212:215], a[4:7]\n"
    "v_mul_f32 v20, v80, v20\n"
    "v_mul_f32 v21, v81, v21\n"
    "v_mfma_f32_16x16x32_bf16 a[4:7], v[216:219], a[216:219], a[4:7]\n"
    "v_mul_f32 v22, v82, v22\n"
    "v_mul_f32 v23, v83, v23\n"
    "v_mfma_f32_16x16x32_bf16 a[4:7], v[220:223], a[220:223], a[4:7]\n"
    "v_cvt_pk_bf16_f32 v32, v16, v20\n"
    "v_cvt_pk_bf16_f32 v33, v17, v21\n"
    "v_mfma_f32_16x16x32_bf16 a[20:23], v[144:147], a[224:227], a[20:23]\n"
    "v_cvt_pk_bf16_f32 v34, v18, v22\n"
    "v_cvt_pk_bf16_f32 v35, v19, v23\n"
    "v_mfma_f32_16x16x32_bf16 a[20:23], v[148:151], a[228:231], a[20:23]\n"
    "s_mov_b64 vcc, s[68:69]\n"
    "v_cndmask_b32_dpp v40, v33, v32, vcc quad_perm:[1,0,3,2] row_mask:0xf bank_mask:0xf\n"
    "v_mfma_f32_16x16x32_bf16 a[20:23], v[152:155], a[232:235], a[20:23]\n"
    "v_cndmask_b32_dpp v42, v35, v34, vcc quad_perm:[1,0,3,2] row_mask:0xf bank_mask:0xf\n"
    "s_mov_b64 vcc, s[70:71]\n"
    "v_mfma_f32_16x16x32_bf16 a[20:23], v[156:159], a[236:239], a[20:23]\n"
    "v_cndmask_b32_dpp v41, v32, v33, vcc quad_perm:[1,0,3,2] row_mask:0xf bank_mask:0xf\n"
    "v_cndmask_b32_dpp v43, v34, v35, vcc quad_perm:[1,0,3,2] row_mask:0xf bank_mask:0xf\n"
    "v_mfma_f32_16x16x32_bf16 a[20:23], v[208:211], a[240:243], a[20:23]\n"
    "s_mov_b64 vcc, s[72:73]\n"
    "v_cndmask_b32_dpp v48, v42, v40, vcc quad_perm:[2,3,0,1] row_mask:0xf bank_mask:0xf\n"
    "v_mfma_f32_16x16x32_bf16 a[20:23], v[212:215], a[244:247], a[20:23]\n"
    "v_cndmask_b32_dpp v49, v43, v41, vcc quad_perm:[2,3,0,1] row_mask:0xf bank_mask:0xf\n"
    "s_mov_b64 vcc, s[74:75]\n"
    "v_mfma_f32_16x16x32_bf16 a[20:23], v[216:219], a[248:251], a[20:23]\n"
    "v_cndmask_b32_dpp v50, v40, v42, vcc quad_perm:[2,3,0,1] row_mask:0xf bank_mask:0xf\n"
    "v_cndmask_b32_dpp v51, v41, v43, vcc quad_perm:[2,3,0,1] row_mask:0xf bank_mask:0xf\n"
    "v_mfma_f32_16x16x32_bf16 a[20:23], v[220:223], a[252:255], a[20:23]\n"
    "buffer_store_dwordx4 v[48:51], v96, s[44:47], 0 offen offset:0 sc1\n"
    "v_accvgpr_read_b32 v24, a4\n"
    "v_accvgpr_read_b32 v25, a5\n"
    "v_mfma_f32_16x16x32_bf16 a[8:11], v[160:163], a[192:195], a[8:11]\n"
    "v_accvgpr_read_b32 v26, a6\n"
    "v_accvgpr_read_b32 v27, a7\n"
    "v_mul_f32 v24, v84, v24\n"
    "v_mfma_f32_16x16x32_bf16 a[8:11], v[164:167], a[196:199], a[8:11]\n"
    "v_mul_f32 v25, v85, v25\n"
    "v_mul_f32 v26, v86, v26\n"
    "v_mfma_f32_16x16x32_bf16 a[8:11], v[168:171], a[200:203], a[8:11]\n"
    "v_mul_f32 v27, v87, v27\n"
    "v_mfma_f32_16x16x32_bf16 a[8:11], v[172:175], a[204:207], a[8:11]\n"
    "v_accvgpr_read_b32 v28, a20\n"
    "v_accvgpr_read_b32 v29, a21\n"
    "v_mfma_f32_16x16x32_bf16 a[8:11], v[224:227], a[208:211], a[8:11]\n"
    "v_accvgpr_read_b32 v30, a22\n"
    "v_accvgpr_read_b32 v31, a23\n"
    "v_mfma_f32_16x16x32_bf16 a[8:11], v[228:231], a[212:215], a[8:11]\n"
    "v_mul_f32 v28, v84, v28\n"
    "v_mul_f32 v29, v85, v29\n"
    "v_mfma_f32_16x16x32_bf16 a[8:11], v[232:235], a[216:219], a[8:11]\n"
    "v_mul_f32 v30, v86, v30\n"
    "v_mul_f32 v31, v87, v31\n"
    "v_mfma_f32_16x16x32_bf16 a[8:11], v[236:239], a[220:223], a[8:11]\n"
    "v_cvt_pk_bf16_f32 v36, v24, v28\n"
    "v_cvt_pk_bf16_f32 v37, v25, v29\n"
    "v_mfma_f32_16x16x32_bf16 a[24:27], v[160:163], a[224:227], a[24:27]\n"
    "v_cvt_pk_bf16_f32 v38, v26, v30\n"
    "v_cvt_pk_bf16_f32 v39, v27, v31\n"
    "v_mfma_f32_16x16x32_bf16 a[24:27], v[164:167], a[228:231], a[24:27]\n"
    "s_mov_b64 vcc, s[68:69]\n"
    "v_cndmask_b32_dpp v44, v37, v36, vcc quad_perm:[1,0,3,2] row_mask:0xf bank_mask:0xf\n"
    "v_mfma_f32_16x16x32_bf16 a[24:27], v[168:171], a[232:235], a[24:27]\n"
    "v_cndmask_b32_dpp v46, v39, v38, vcc quad_perm:[1,0,3,2] row_mask:0xf bank_mask:0xf\n"
    "s_mov_b64 vcc, s[70:71]\n"
    "v_mfma_f32_16x16x32_bf16 a[24:27], v[172:175], a[236:239], a[24:27]\n"
    "v_cndmask_b32_dpp v45, v36, v37, vcc quad_perm:[1,0,3,2] row_mask:0xf bank_mask:0xf\n"
    "v_cndmask_b32_dpp v47, v38, v39, vcc quad_perm:[1,0,3,2] row_mask:0xf bank_mask:0xf\n"
    "v_mfma_f32_16x16x32_bf16 a[24:27], v[224:227], a[240:243], a[24:27]\n"
    "s_mov_b64 vcc, s[72:73]\n"
    "v_cndmask_b32_dpp v52, v46, v44, vcc quad_perm:[2,3,0,1] row_mask:0xf bank_mask:0xf\n"
    "v_mfma_f32_16x16x32_bf16 a[24:27], v[228:231], a[244:247], a[24:27]\n"
    "v_cndmask_b32_dpp v53, v47, v45, vcc quad_perm:[2,3,0,1] row_mask:0xf bank_mask:0xf\n"
    "s_mov_b64 vcc, s[74:75]\n"
    "v_mfma_f32_16x16x32_bf16 a[24:27], v[232:235], a[248:251], a[24:27]\n"
    "v_cndmask_b32_dpp v54, v44, v46, vcc quad_perm:[2,3,0,1] row_mask:0xf bank_mask:0xf\n"
    "v_cndmask_b32_dpp v55, v45, v47, vcc quad_perm:[2,3,0,1] row_mask:0xf bank_mask:0xf\n"
    "v_mfma_f32_16x16x32_bf16 a[24:27], v[236:239], a[252:255], a[24:27]\n"
    "buffer_store_dwordx4 v[52:55], v96, s[44:47], s49 offen offset:0 sc1\n"
    "v_accvgpr_read_b32 v16, a8\n"
    "v_accvgpr_read_b32 v17, a9\n"
    "v_mfma_f32_16x16x32_bf16 a[12:15], v[176:179], a[192:195], a[12:15]\n"
    "ds_read_b128 a[192:195], v3 offset:40960\n"
    "v_accvgpr_read_b32 v18, a10\n"
    "v_accvgpr_read_b32 v19, a11\n"
    "v_mfma_f32_16x16x32_bf16 a[12:15], v[180:183], a[196:199], a[12:15]\n"
    "ds_read_b128 a[196:199], v3 offset:41984\n"
    "v_mul_f32 v16, v88, v16\n"
    "v_mfma_f32_16x16x32_bf16 a[12:15], v[184:187], a[200:203], a[12:15]\n"
    "ds_read_b128 a[200:203], v3 offset:43008\n"
    "v_mul_f32 v17, v89, v17\n"
    "v_mfma_f32_16x16x32_bf16 a[12:15], v[188:191], a[204:207], a[12:15]\n"
    "ds_read_b128 a[204:207], v3 offset:44032\n"
    "v_mul_f32 v18, v90, v18\n"
    "v_mfma_f32_16x16x32_bf16 a[12:15], v[240:243], a[208:211], a[12:15]\n"
    "ds_read_b128 a[208:211], v2 offset:40960\n"
    "v_mul_f32 v19, v91, v19\n"
    "v_mfma_f32_16x16x32_bf16 a[12:15], v[244:247], a[212:215], a[12:15]\n"
    "ds_read_b128 a[212:215], v2 offset:41984\n"
    "v_accvgpr_read_b32 v20, a24\n"
    "v_mfma_f32_16x16x32_bf16 a[12:15], v[248:251], a[216:219], a[12:15]\n"
    "ds_read_b128 a[216:219], v2 offset:43008\n"
    "v_accvgpr_read_b32 v21, a25\n"
    "v_mfma_f32_16x16x32_bf16 a[12:15], v[252:255], a[220:223], a[12:15]\n"
    "ds_read_b128 a[220:223], v2 offset:44032\n"
    "v_accvgpr_read_b32 v22, a26\n"
    "v_accvgpr_read_b32 v23, a27\n"
    "v_mfma_f32_16x16x32_bf16 a[28:31], v[176:179], a[224:227], a[28:31]\n"
    "ds_read_b128 a[224:227], v3 offset:45056\n"
    "v_mul_f32 v20, v88, v20\n"
    "v_mfma_f32_16x16x32_bf16 a[28:31], v[180:183], a[228:231], a[28:31]\n"
    "ds_read_b128 a[228:231], v3 offset:46080\n"
    "v_mul_f32 v21, v89, v21\n"
    "v_mfma_f32_16x16x32_bf16 a[28:31], v[184:187], a[232:235], a[28:31]\n"
    "ds_read_b128 a[232:235], v3 offset:47104\n"
    "v_mul_f32 v22, v90, v22\n"
    "v_mfma_f32_16x16x32_bf16 a[28:31], v[188:191], a[236:239], a[28:31]\n"
    "ds_read_b128 a[236:239], v3 offset:48128\n"
    "v_mul_f32 v23, v91, v23\n"
    "v_mfma_f32_16x16x32_bf16 a[28:31], v[240:243], a[240:243], a[28:31]\n"
    "ds_read_b128 a[240:243], v2 offset:45056\n"
    "v_cvt_pk_bf16_f32 v32, v16, v20\n"
    "v_mfma_f32_16x16x32_bf16 a[28:31], v[244:247], a[244:247], a[28:31]\n"
    "ds_read_b128 a[244:247], v2 offset:46080\n"
    "v_cvt_pk_bf16_f32 v33, v17, v21\n"
    "v_mfma_f32_16x16x32_bf16 a[28:31], v[248:251], a[248:251], a[28:31]\n"
    "s_waitcnt lgkmcnt(12)\n"
    "ds_read_b128 a[248:251], v2 offset:47104\n"
    "v_mfma_f32_16x16x32_bf16 a[28:31], v[252:255], a[252:255], a[28:31]\n"
    "ds_read_b128 a[252:255], v2 offset:48128\n"
    "v_cvt_pk_bf16_f32 v34, v18, v22\n"
    "s_waitcnt lgkmcnt(12)\n"
    "v_mfma_f32_16x16x32_bf16 a[32:35], v[128:131], a[192:195], a[32:35]\n"
    "v_cvt_pk_bf16_f32 v35, v19, v23\n"
    "s_mov_b64 vcc, s[68:69]\n"
    "v_cndmask_b32_dpp v40, v33, v32, vcc quad_perm:[1,0,3,2] row_mask:0xf bank_mask:0xf\n"
    "v_mfma_f32_16x16x32_bf16 a[32:35], v[132:135], a[196:199], a[32:35]\n"
    "v_cndmask_b32_dpp v42, v35, v34, vcc quad_perm:[1,0,3,2] row_mask:0xf bank_mask:0xf\n"
    "s_mov_b64 vcc, s[70:71]\n"
    "v_cndmask_b32_dpp v41, v32, v33, vcc quad_perm:[1,0,3,2] row_mask:0xf bank_mask:0xf\n"
    "v_mfma_f32_16x16x32_bf16 a[32:35], v[136:139], a[200:203], a[32:35]\n"
    "v_cndmask_b32_dpp v43, v34, v35, vcc quad_perm:[1,0,3,2] row_mask:0xf bank_mask:0xf\n"
    "s_mov_b64 vcc, s[72:73]\n"
    "v_cndmask_b32_dpp v56, v42, v40, vcc quad_perm:[2,3,0,1] row_mask:0xf bank_mask:0xf\n"
    "v_mfma_f32_16x16x32_bf16 a[32:35], v[140:143], a[204:207], a[32:35]\n"
    "v_cndmask_b32_dpp v57, v43, v41, vcc quad_perm:[2,3,0,1] row_mask:0xf bank_mask:0xf\n"
    "s_mov_b64 vcc, s[74:75]\n"
    "s_waitcnt lgkmcnt(8)\n"
    "v_mfma_f32_16x16x32_bf16 a[32:35], v[192:195], a[208:211], a[32:35]\n"
    "v_cndmask_b32_dpp v58, v40, v42, vcc quad_perm:[2,3,0,1] row_mask:0xf bank_mask:0xf\n"
    "v_cndmask_b32_dpp v59, v41, v43, vcc quad_perm:[2,3,0,1] row_mask:0xf bank_mask:0xf\n"
    "v_mfma_f32_16x16x32_bf16 a[32:35], v[196:199], a[212:215], a[32:35]\n"
    "buffer_store_dwordx4 v[56:59], v96, s[44:47], s50 offen offset:0 sc1\n"
    "v_accvgpr_read_b32 v24, a12\n"
    "v_accvgpr_read_b32 v25, a13\n"
    "v_mfma_f32_16x16x32_bf16 a[32:35], v[200:203], a[216:219], a[32:35]\n"
    "v_accvgpr_read_b32 v26, a14\n"
    "v_accvgpr_read_b32 v27, a15\n"
    "v_mul_f32 v24, v92, v24\n"
    "v_mfma_f32_16x16x32_bf16 a[32:35], v[204:207], a[220:223], a[32:35]\n"
    "v_mul_f32 v25, v93, v25\n"
    "v_mul_f32 v26, v94, v26\n"
    "s_waitcnt lgkmcnt(4)\n"
    "v_mfma_f32_16x16x32_bf16 a[48:51], v[128:131], a[224:227], a[48:51]\n"
    "v_mul_f32 v27, v95, v27\n"
    "v_accvgpr_read_b32 v28, a28\n"
    "v_accvgpr_read_b32 v29, a29\n"
    "v_mfma_f32_16x16x32_bf16 a[48:51], v[132:135], a[228:231], a[48:51]\n"
    "v_accvgpr_read_b32 v30, a30\n"
    "v_accvgpr_read_b32 v31, a31\n"
    "v_mul_f32 v28, v92, v28\n"
    "v_mfma_f32_16x16x32_bf16 a[48:51], v[136:139], a[232:235], a[48:51]\n"
    "v_mul_f32 v29, v93, v29\n"
    "v_mul_f32 v30, v94, v30\n"
    "v_mfma_f32_16x16x32_bf16 a[48:51], v[140:143], a[236:239], a[48:51]\n"
    "v_mul_f32 v31, v95, v31\n"
    "s_waitcnt lgkmcnt(0)\n"
    "v_mfma_f32_16x16x32_bf16 a[48:51], v[192:195], a[240:243], a[48:51]\n"
    "v_cvt_pk_bf16_f32 v36, v24, v28\n"
    "v_cvt_pk_bf16_f32 v37, v25, v29\n"
    "v_mfma_f32_16x16x32_bf16 a[48:51], v[196:199], a[244:247], a[48:51]\n"
    "v_cvt_pk_bf16_f32 v38, v26, v30\n"
    "v_cvt_pk_bf16_f32 v39, v27, v31\n"
    "v_mfma_f32_16x16x32_bf16 a[48:51], v[200:203], a[248:251], a[48:51]\n"
    "s_mov_b64 vcc, s[68:69]\n"
    "v_cndmask_b32_dpp v44, v37, v36, vcc quad_perm:[1,0,3,2] row_mask:0xf bank_mask:0xf\n"
    "v_mfma_f32_16x16x32_bf16 a[48:51], v[204:207], a[252:255], a[48:51]\n"
    "v_cndmask_b32_dpp v46, v39, v38, vcc quad_perm:[1,0,3,2] row_mask:0xf bank_mask:0xf\n"
    "s_mov_b64 vcc, s[70:71]\n"
    "v_cndmask_b32_dpp v45, v36, v37, vcc quad_perm:[1,0,3,2] row_mask:0xf bank_mask:0xf\n"
    "v_mfma_f32_16x16x32_bf16 a[36:39], v[144:147], a[192:195], a[36:39]\n"
    "v_cndmask_b32_dpp v47, v38, v39, vcc quad_perm:[1,0,3,2] row_mask:0xf bank_mask:0xf\n"
    "s_mov_b64 vcc, s[72:73]\n"
    "v_cndmask_b32_dpp v60, v46, v44, vcc quad_perm:[2,3,0,1] row_mask:0xf bank_mask:0xf\n"
    "v_mfma_f32_16x16x32_bf16 a[36:39], v[148:151], a[196:199], a[36:39]\n"
    "v_cndmask_b32_dpp v61, v47, v45, vcc quad_perm:[2,3,0,1] row_mask:0xf bank_mask:0xf\n"
    "s_mov_b64 vcc, s[74:75]\n"
    "v_cndmask_b32_dpp v62, v44, v46, vcc quad_perm:[2,3,0,1] row_mask:0xf bank_mask:0xf\n"
    "v_mfma_f32_16x16x32_bf16 a[36:39], v[152:155], a[200:203], a[36:39]\n"
    "v_cndmask_b32_dpp v63, v45, v47, vcc quad_perm:[2,3,0,1] row_mask:0xf bank_mask:0xf\n"
    "v_mfma_f32_16x16x32_bf16 a[36:39], v[156:159], a[204:207], a[36:39]\n"
    "buffer_store_dwordx4 v[60:63], v96, s[44:47], s51 offen offset:0 sc1\n"
    "v_accvgpr_read_b32 v16, a32\n"
    "v_accvgpr_read_b32 v17, a33\n"
    "v_mfma_f32_16x16x32_bf16 a[36:39], v[208:211], a[208:211], a[36:39]\n"
    "v_accvgpr_read_b32 v18, a34\n"
    "v_accvgpr_read_b32 v19, a35\n"
    "v_mul_f32 v16, v80, v16\n"
    "v_mfma_f32_16x16x32_bf16 a[36:39], v[212:215], a[212:215], a[36:39]\n"
    "v_mul_f32 v17, v81, v17\n"
    "v_mul_f32 v18, v82, v18\n"
    "v_mfma_f32_16x16x32_bf16 a[36:39], v[216:219], a[216:219], a[36:39]\n"
    "v_mul_f32 v19, v83, v19\n"
    "v_accvgpr_read_b32 v20, a48\n"
    "v_mfma_f32_16x16x32_bf16 a[36:39], v[220:223], a[220:223], a[36:39]\n"
    "v_accvgpr_read_b32 v21, a49\n"
    "v_accvgpr_read_b32 v22, a50\n"
    "v_accvgpr_read_b32 v23, a51\n"
    "v_mfma_f32_16x16x32_bf16 a[52:55], v[144:147], a[224:227], a[52:55]\n"
    "v_mul_f32 v20, v80, v20\n"
    "v_mul_f32 v21, v81, v21\n"
    "v_mfma_f32_16x16x32_bf16 a[52:55], v[148:151], a[228:231], a[52:55]\n"
    "v_mul_f32 v22, v82, v22\n"
    "v_mul_f32 v23, v83, v23\n"
    "v_mfma_f32_16x16x32_bf16 a[52:55], v[152:155], a[232:235], a[52:55]\n"
    "v_cvt_pk_bf16_f32 v32, v16, v20\n"
    "v_cvt_pk_bf16_f32 v33, v17, v21\n"
    "v_mfma_f32_16x16x32_bf16 a[52:55], v[156:159], a[236:239], a[52:55]\n"
    "v_cvt_pk_bf16_f32 v34, v18, v22\n"
    "v_cvt_pk_bf16_f32 v35, v19, v23\n"
    "v_mfma_f32_16x16x32_bf16 a[52:55], v[208:211], a[240:243], a[52:55]\n"
    "s_mov_b64 vcc, s[68:69]\n"
    "v_cndmask_b32_dpp v40, v33, v32, vcc quad_perm:[1,0,3,2] row_mask:0xf bank_mask:0xf\n"
    "v_mfma_f32_16x16x32_bf16 a[52:55], v[212:215], a[244:247], a[52:55]\n"
    "v_cndmask_b32_dpp v42, v35, v34, vcc quad_perm:[1,0,3,2] row_mask:0xf bank_mask:0xf\n"
    "s_mov_b64 vcc, s[70:71]\n"
    "v_mfma_f32_16x16x32_bf16 a[52:55], v[216:219], a[248:251], a[52:55]\n"
    "v_cndmask_b32_dpp v41, v32, v33, vcc quad_perm:[1,0,3,2] row_mask:0xf bank_mask:0xf\n"
    "v_cndmask_b32_dpp v43, v34, v35, vcc quad_perm:[1,0,3,2] row_mask:0xf bank_mask:0xf\n"
    "v_mfma_f32_16x16x32_bf16 a[52:55], v[220:223], a[252:255], a[52:55]\n"
    "s_mov_b64 vcc, s[72:73]\n"
    "v_cndmask_b32_dpp v48, v42, v40, vcc quad_perm:[2,3,0,1] row_mask:0xf bank_mask:0xf\n"
    "v_cndmask_b32_dpp v49, v43, v41, vcc quad_perm:[2,3,0,1] row_mask:0xf bank_mask:0xf\n"
    "v_mfma_f32_16x16x32_bf16 a[40:43], v[160:163], a[192:195], a[40:43]\n"
    "s_mov_b64 vcc, s[74:75]\n"
    "v_cndmask_b32_dpp v50, v40, v42, vcc quad_perm:[2,3,0,1] row_mask:0xf bank_mask:0xf\n"
    "v_cndmask_b32_dpp v51, v41, v43, vcc quad_perm:[2,3,0,1] row_mask:0xf bank_mask:0xf\n"
    "v_mfma_f32_16x16x32_bf16 a[40:43], v[164:167], a[196:199], a[40:43]\n"
    "buffer_store_dwordx4 v[48:51], v96, s[44:47], 0 offen offset:64 sc1\n"
    "v_accvgpr_read_b32 v24, a36\n"
    "v_accvgpr_read_b32 v25, a37\n"
    "v_mfma_f32_16x16x32_bf16 a[40:43], v[168:171], a[200:203], a[40:43]\n"
    "v_accvgpr_read_b32 v26, a38\n"
    "v_accvgpr_read_b32 v27, a39\n"
    "v_mul_f32 v24, v84, v24\n"
    "v_mfma_f32_16x16x32_bf16 a[40:43], v[172:175], a[204:207], a[40:43]\n"
    "v_mul_f32 v25, v85, v25\n"
    "v_mul_f32 v26, v86, v26\n"
    "v_mfma_f32_16x16x32_bf16 a[40:43], v[224:227], a[208:211], a[40:43]\n"
    "v_mul_f32 v27, v87, v27\n"
    "v_accvgpr_read_b32 v28, a52\n"
    "v_mfma_f32_16x16x32_bf16 a[40:43], v[228:231], a[212:215], a[40:43]\n"
    "v_accvgpr_read_b32 v29, a53\n"
    "v_accvgpr_read_b32 v30, a54\n"
    "v_mfma_f32_16x16x32_bf16 a[40:43], v[232:235], a[216:219], a[40:43]\n"
    "v_accvgpr_read_b32 v31, a55\n"
    "v_mul_f32 v28, v84, v28\n"
    "v_mfma_f32_16x16x32_bf16 a[40:43], v[236:239], a[220:223], a[40:43]\n"
    "v_mul_f32 v29, v85, v29\n"
    "v_mul_f32 v30, v86, v30\n"
    "v_mfma_f32_16x16x32_bf16 a[56:59], v[160:163], a[224:227], a[56:59]\n"
    "v_mul_f32 v31, v87, v31\n"
    "v_cvt_pk_bf16_f32 v36, v24, v28\n"
    "v_mfma_f32_16x16x32_bf16 a[56:59], v[164:167], a[228:231], a[56:59]\n"
    "v_cvt_pk_bf16_f32 v37, v25, v29\n"
    "v_cvt_pk_bf16_f32 v38, v26, v30\n"
    "v_mfma_f32_16x16x32_bf16 a[56:59], v[168:171], a[232:235], a[56:59]\n"
    "v_cvt_pk_bf16_f32 v39, v27, v31\n"
    "s_mov_b64 vcc, s[68:69]\n"
    "v_mfma_f32_16x16x32_bf16 a[56:59], v[172:175], a[236:239], a[56:59]\n"
    "v_cndmask_b32_dpp v44, v37, v36, vcc quad_perm:[1,0,3,2] row_mask:0xf bank_mask:0xf\n"
    "v_cndmask_b32_dpp v46, v39, v38, vcc quad_perm:[1,0,3,2] row_mask:0xf bank_mask:0xf\n"
    "v_mfma_f32_16x16x32_bf16 a[56:59], v[224:227], a[240:243], a[56:59]\n"
    "s_mov_b64 vcc, s[70:71]\n"
    "v_cndmask_b32_dpp v45, v36, v37, vcc quad_perm:[1,0,3,2] row_mask:0xf bank_mask:0xf\n"
    "v_mfma_f32_16x16x32_bf16 a[56:59], v[228:231], a[244:247], a[56:59]\n"
    "v_cndmask_b32_dpp v47, v38, v39, vcc quad_perm:[1,0,3,2] row_mask:0xf bank_mask:0xf\n"
    "s_mov_b64 vcc, s[72:73]\n"
    "v_mfma_f32_16x16x32_bf16 a[56:59], v[232:235], a[248:251], a[56:59]\n"
    "v_cndmask_b32_dpp v52, v46, v44, vcc quad_perm:[2,3,0,1] row_mask:0xf bank_mask:0xf\n"
    "v_cndmask_b32_dpp v53, v47, v45, vcc quad_perm:[2,3,0,1] row_mask:0xf bank_mask:0xf\n"
    "v_mfma_f32_16x16x32_bf16 a[56:59], v[236:239], a[252:255], a[56:59]\n"
    "s_mov_b64 vcc, s[74:75]\n"
    "v_cndmask_b32_dpp v54, v44, v46, vcc quad_perm:[2,3,0,1] row_mask:0xf bank_mask:0xf\n"
    "v_cndmask_b32_dpp v55, v45, v47, vcc quad_perm:[2,3,0,1] row_mask:0xf bank_mask:0xf\n"
    "v_mfma_f32_16x16x32_bf16 a[44:47], v[176:179], a[192:195], a[44:47]\n"
    "ds_read_b128 a[192:195], v3 offset:49152\n"
    "v_mfma_f32_16x16x32_bf16 a[44:47], v[180:183], a[196:199], a[44:47]\n"
    "ds_read_b128 a[196:199], v3 offset:50176\n"
    "v_mfma_f32_16x16x32_bf16 a[44:47], v[184:187], a[200:203], a[44:47]\n"
    "ds_read_b128 a[200:203], v3 offset:51200\n"
    "v_mfma_f32_16x16x32_bf16 a[44:47], v[188:191], a[204:207], a[44:47]\n"
    "ds_read_b128 a[204:207], v3 offset:52224\n"
    "v_mfma_f32_16x16x32_bf16 a[44:47], v[240:243], a[208:211], a[44:47]\n"
    "ds_read_b128 a[208:211], v2 offset:49152\n"
    "v_mfma_f32_16x16x32_bf16 a[44:47], v[244:247], a[212:215], a[44:47]\n"
    "ds_read_b128 a[212:215], v2 offset:50176\n"
    "v_mfma_f32_16x16x32_bf16 a[44:47], v[248:251], a[216:219], a[44:47]\n"
    "ds_read_b128 a[216:219], v2 offset:51200\n"
    "v_mfma_f32_16x16x32_bf16 a[44:47], v[252:255], a[220:223], a[44:47]\n"
    "ds_read_b128 a[220:223], v2 offset:52224\n"
    "v_mfma_f32_16x16x32_bf16 a[60:63], v[176:179], a[224:227], a[60:63]\n"
    "ds_read_b128 a[224:227], v3 offset:53248\n"
    "v_mfma_f32_16x16x32_bf16 a[60:63], v[180:183], a[228:231], a[60:63]\n"
    "ds_read_b128 a[228:231], v3 offset:54272\n"
    "v_mfma_f32_16x16x32_bf16 a[60:63], v[184:187], a[232:235], a[60:63]\n"
    "ds_read_b128 a[232:235], v3 offset:55296\n"
    "v_mfma_f32_16x16x32_bf16 a[60:63], v[188:191], a[236:239], a[60:63]\n"
    "ds_read_b128 a[236:239], v3 offset:56320\n"
    "v_mfma_f32_16x16x32_bf16 a[60:63], v[240:243], a[240:243], a[60:63]\n"
    "ds_read_b128 a[240:243], v2 offset:53248\n"
    "v_mfma_f32_16x16x32_bf16 a[60:63], v[244:247], a[244:247], a[60:63]\n"
    "ds_read_b128 a[244:247], v2 offset:54272\n"
    "v_mfma_f32_16x16x32_bf16 a[60:63], v[248:251], a[248:251], a[60:63]\n"
    "s_waitcnt lgkmcnt(12)\n"
    "ds_read_b128 a[248:251], v2 offset:55296\n"
    "v_mfma_f32_16x16x32_bf16 a[60:63], v[252:255], a[252:255], a[60:63]\n"
    "ds_read_b128 a[252:255], v2 offset:56320\n"
    "s_waitcnt lgkmcnt(12)\n"
    "v_mfma_f32_16x16x32_bf16 a[64:67], v[128:131], a[192:195], a[64:67]\n"
    "buffer_store_dwordx4 v[52:55], v96, s[44:47], s49 offen offset:64 sc1\n"
    "v_accvgpr_read_b32 v16, a40\n"
    "v_accvgpr_read_b32 v17, a41\n"
    "v_mfma_f32_16x16x32_bf16 a[64:67], v[132:135], a[196:199], a[64:67]\n"
    "v_accvgpr_read_b32 v18, a42\n"
    "v_accvgpr_read_b32 v19, a43\n"
    "v_mul_f32 v16, v88, v16\n"
    "v_mfma_f32_16x16x32_bf16 a[64:67], v[136:139], a[200:203], a[64:67]\n"
    "v_mul_f32 v17, v89, v17\n"
    "v_mul_f32 v18, v90, v18\n"
    "v_mul_f32 v19, v91, v19\n"
    "v_mfma_f32_16x16x32_bf16 a[64:67], v[140:143], a[204:207], a[64:67]\n"
    "v_accvgpr_read_b32 v20, a56\n"
    "v_accvgpr_read_b32 v21, a57\n"
    "s_waitcnt lgkmcnt(8)\n"
    "v_mfma_f32_16x16x32_bf16 a[64:67], v[192:195], a[208:211], a[64:67]\n"
    "v_accvgpr_read_b32 v22, a58\n"
    "v_accvgpr_read_b32 v23, a59\n"
    "v_mul_f32 v20, v88, v20\n"
    "v_mfma_f32_16x16x32_bf16 a[64:67], v[196:199], a[212:215], a[64:67]\n"
    "v_mul_f32 v21, v89, v21\n"
    "v_mul_f32 v22, v90, v22\n"
    "v_mul_f32 v23, v91, v23\n"
    "v_mfma_f32_16x16x32_bf16 a[64:67], v[200:203], a[216:219], a[64:67]\n"
    "v_cvt_pk_bf16_f32 v32, v16, v20\n"
    "v_cvt_pk_bf16_f32 v33, v17, v21\n"
    "v_cvt_pk_bf16_f32 v34, v18, v22\n"
    "v_mfma_f32_16x16x32_bf16 a[64:67], v[204:207], a[220:223], a[64:67]\n"
    "v_cvt_pk_bf16_f32 v35, v19, v23\n"
    "s_mov_b64 vcc, s[68:69]\n"
    "s_waitcnt lgkmcnt(4)\n"
    "v_mfma_f32_16x16x32_bf16 a[80:83], v[128:131], a[224:227], a[80:83]\n"
    "v_cndmask_b32_dpp v40, v33, v32, vcc quad_perm:[1,0,3,2] row_mask:0xf bank_mask:0xf\n"
    "v_cndmask_b32_dpp v42, v35, v34, vcc quad_perm:[1,0,3,2] row_mask:0xf bank_mask:0xf\n"
    "s_mov_b64 vcc, s[70:71]\n"
    "v_mfma_f32_16x16x32_bf16 a[80:83], v[132:135], a[228:231], a[80:83]\n"
    "v_cndmask_b32_dpp v41, v32, v33, vcc quad_perm:[1,0,3,2] row_mask:0xf bank_mask:0xf\n"
    "v_cndmask_b32_dpp v43, v34, v35, vcc quad_perm:[1,0,3,2] row_mask:0xf bank_mask:0xf\n"
    "s_mov_b64 vcc, s[72:73]\n"
    "v_mfma_f32_16x16x32_bf16 a[80:83], v[136:139], a[232:235], a[80:83]\n"
    "v_cndmask_b32_dpp v56, v42, v40, vcc quad_perm:[2,3,0,1] row_mask:0xf bank_mask:0xf\n"
    "v_cndmask_b32_dpp v57, v43, v41, vcc quad_perm:[2,3,0,1] row_mask:0xf bank_mask:0xf\n"
    "s_mov_b64 vcc, s[74:75]\n"
    "v_mfma_f32_16x16x32_bf16 a[80:83], v[140:143], a[236:239], a[80:83]\n"
    "v_cndmask_b32_dpp v58, v40, v42, vcc quad_perm:[2,3,0,1] row_mask:0xf bank_mask:0xf\n"
    "v_cndmask_b32_dpp v59, v41, v43, vcc quad_perm:[2,3,0,1] row_mask:0xf bank_mask:0xf\n"
    "s_waitcnt lgkmcnt(0)\n"
    "v_mfma_f32_16x16x32_bf16 a[80:83], v[192:195], a[240:243], a[80:83]\n"
    "buffer_store_dwordx4 v[56:59], v96, s[44:47], s50 offen offset:64 sc1\n"
    "v_accvgpr_read_b32 v24, a44\n"
    "v_accvgpr_read_b32 v25, a45\n"
    "v_mfma_f32_16x16x32_bf16 a[80:83], v[196:199], a[244:247], a[80:83]\n"
    "v_accvgpr_read_b32 v26, a46\n"
    "v_accvgpr_read_b32 v27, a47\n"
    "v_mul_f32 v24, v92, v24\n"
    "v_mfma_f32_16x16x32_bf16 a[80:83], v[200:203], a[248:251], a[80:83]\n"
    "v_mul_f32 v25, v93, v25\n"
    "v_mul_f32 v26, v94, v26\n"
    "v_mul_f32 v27, v95, v27\n"
    "v_mfma_f32_16x16x32_bf16 a[80:83], v[204:207], a[252:255], a[80:83]\n"
    "v_accvgpr_read_b32 v28, a60\n"
    "v_accvgpr_read_b32 v29, a61\n"
    "v_accvgpr_read_b32 v30, a62\n"
    "v_mfma_f32_16x16x32_bf16 a[68:71], v[144:147], a[192:195], a[68:71]\n"
    "v_accvgpr_read_b32 v31, a63\n"
    "v_mul_f32 v28, v92, v28\n"
    "v_mul_f32 v29, v93, v29\n"
    "v_mfma_f32_16x16x32_bf16 a[68:71], v[148:151], a[196:199], a[68:71]\n"
    "v_mul_f32 v30, v94, v30\n"
    "v_mul_f32 v31, v95, v31\n"
    "v_cvt_pk_bf16_f32 v36, v24, v28\n"
    "v_mfma_f32_16x16x32_bf16 a[68:71], v[152:155], a[200:203], a[68:71]\n"
    "v_cvt_pk_bf16_f32 v37, v25, v29\n"
    "v_cvt_pk_bf16_f32 v38, v26, v30\n"
    "v_cvt_pk_bf16_f32 v39, v27, v31\n"
    "v_mfma_f32_16x16x32_bf16 a[68:71], v[156:159], a[204:207], a[68:71]\n"
    "s_mov_b64 vcc, s[68:69]\n"
    "v_cndmask_b32_dpp v44, v37, v36, vcc quad_perm:[1,0,3,2] row_mask:0xf bank_mask:0xf\n"
    "v_cndmask_b32_dpp v46, v39, v38, vcc quad_perm:[1,0,3,2] row_mask:0xf bank_mask:0xf\n"
    "v_mfma_f32_16x16x32_bf16 a[68:71], v[208:211], a[208:211], a[68:71]\n"
    "s_mov_b64 vcc, s[70:71]\n"
    "v_cndmask_b32_dpp v45, v36, v37, vcc quad_perm:[1,0,3,2] row_mask:0xf bank_mask:0xf\n"
    "v_cndmask_b32_dpp v47, v38, v39, vcc quad_perm:[1,0,3,2] row_mask:0xf bank_mask:0xf\n"
    "v_mfma_f32_16x16x32_bf16 a[68:71], v[212:215], a[212:215], a[68:71]\n"
    "s_mov_b64 vcc, s[72:73]\n"
    "v_cndmask_b32_dpp v60, v46, v44, vcc quad_perm:[2,3,0,1] row_mask:0xf bank_mask:0xf\n"
    "v_cndmask_b32_dpp v61, v47, v45, vcc quad_perm:[2,3,0,1] row_mask:0xf bank_mask:0xf\n"
    "v_mfma_f32_16x16x32_bf16 a[68:71], v[216:219], a[216:219], a[68:71]\n"
    "s_mov_b64 vcc, s[74:75]\n"
    "v_cndmask_b32_dpp v62, v44, v46, vcc quad_perm:[2,3,0,1] row_mask:0xf bank_mask:0xf\n"
    "v_cndmask_b32_dpp v63, v45, v47, vcc quad_perm:[2,3,0,1] row_mask:0xf bank_mask:0xf\n"
    "v_mfma_f32_16x16x32_bf16 a[68:71], v[220:223], a[220:223], a[68:71]\n"
    "buffer_store_dwordx4 v[60:63], v96, s[44:47], s51 offen offset:64 sc1\n"
    "v_accvgpr_read_b32 v16, a64\n"
    "v_accvgpr_read_b32 v17, a65\n"
    "v_mfma_f32_16x16x32_bf16 a[84:87], v[144:147], a[224:227], a[84:87]\n"
    "v_accvgpr_read_b32 v18, a66\n"
    "v_accvgpr_read_b32 v19, a67\n"
    "v_mul_f32 v16, v80, v16\n"
    "v_mfma_f32_16x16x32_bf16 a[84:87], v[148:151], a[228:231], a[84:87]\n"
    "v_mul_f32 v17, v81, v17\n"
    "v_mul_f32 v18, v82, v18\n"
    "v_mul_f32 v19, v83, v19\n"
    "v_mfma_f32_16x16x32_bf16 a[84:87], v[152:155], a[232:235], a[84:87]\n"
    "v_accvgpr_read_b32 v20, a80\n"
    "v_accvgpr_read_b32 v21, a81\n"
    "v_accvgpr_read_b32 v22, a82\n"
    "v_mfma_f32_16x16x32_bf16 a[84:87], v[156:159], a[236:239], a[84:87]\n"
    "v_accvgpr_read_b32 v23, a83\n"
    "v_mul_f32 v20, v80, v20\n"
    "v_mfma_f32_16x16x32_bf16 a[84:87], v[208:211], a[240:243], a[84:87]\n"
    "v_mul_f32 v21, v81, v21\n"
    "v_mul_f32 v22, v82, v22\n"
    "v_mfma_f32_16x16x32_bf16 a[84:87], v[212:215], a[244:247], a[84:87]\n"
    "v_mul_f32 v23, v83, v23\n"
    "v_cvt_pk_bf16_f32 v32, v16, v20\n"
    "v_mfma_f32_16x16x32_bf16 a[84:87], v[216:219], a[248:251], a[84:87]\n"
    "v_cvt_pk_bf16_f32 v33, v17, v21\n"
    "v_cvt_pk_bf16_f32 v34, v18, v22\n"
    "v_mfma_f32_16x16x32_bf16 a[84:87], v[220:223], a[252:255], a[84:87]\n"
    "v_cvt_pk_bf16_f32 v35, v19, v23\n"
    "s_mov_b64 vcc, s[68:69]\n"
    "v_cndmask_b32_dpp v40, v33, v32, vcc quad_perm:[1,0,3,2] row_mask:0xf bank_mask:0xf\n"
    "v_mfma_f32_16x16x32_bf16 a[72:75], v[160:163], a[192:195], a[72:75]\n"
    "v_cndmask_b32_dpp v42, v35, v34, vcc quad_perm:[1,0,3,2] row_mask:0xf bank_mask:0xf\n"
    "s_mov_b64 vcc, s[70:71]\n"
    "v_cndmask_b32_dpp v41, v32, v33, vcc quad_perm:[1,0,3,2] row_mask:0xf bank_mask:0xf\n"
    "v_mfma_f32_16x16x32_bf16 a[72:75], v[164:167], a[196:199], a[72:75]\n"
    "v_cndmask_b32_dpp v43, v34, v35, vcc quad_perm:[1,0,3,2] row_mask:0xf bank_mask:0xf\n"
    "s_mov_b64 vcc, s[72:73]\n"
    "v_cndmask_b32_dpp v48, v42, v40, vcc quad_perm:[2,3,0,1] row_mask:0xf bank_mask:0xf\n"
    "v_mfma_f32_16x16x32_bf16 a[72:75], v[168:171], a[200:203], a[72:75]\n"
    "v_cndmask_b32_dpp v49, v43, v41, vcc quad_perm:[2,3,0,1] row_mask:0xf bank_mask:0xf\n"
    "s_mov_b64 vcc, s[74:75]\n"
    "v_cndmask_b32_dpp v50, v40, v42, vcc quad_perm:[2,3,0,1] row_mask:0xf bank_mask:0xf\n"
    "v_mfma_f32_16x16x32_bf16 a[72:75], v[172:175], a[204:207], a[72:75]\n"
    "v_cndmask_b32_dpp v51, v41, v43, vcc quad_perm:[2,3,0,1] row_mask:0xf bank_mask:0xf\n"
    "v_mfma_f32_16x16x32_bf16 a[72:75], v[224:227], a[208:211], a[72:75]\n"
    "buffer_store_dwordx4 v[48:51], v96, s[44:47], 0 offen offset:128 sc1\n"
    "v_accvgpr_read_b32 v24, a68\n"
    "v_accvgpr_read_b32 v25, a69\n"
    "v_mfma_f32_16x16x32_bf16 a[72:75], v[228:231], a[212:215], a[72:75]\n"
    "v_accvgpr_read_b32 v26, a70\n"
    "v_accvgpr_read_b32 v27, a71\n"
    "v_mul_f32 v24, v84, v24\n"
    "v_mfma_f32_16x16x32_bf16 a[72:75], v[232:235], a[216:219], a[72:75]\n"
    "v_mul_f32 v25, v85, v25\n"
    "v_mul_f32 v26, v86, v26\n"
    "v_mfma_f32_16x16x32_bf16 a[72:75], v[236:239], a[220:223], a[72:75]\n"
    "v_mul_f32 v27, v87, v27\n"
    "v_accvgpr_read_b32 v28, a84\n"
    "v_accvgpr_read_b32 v29, a85\n"
    "v_mfma_f32_16x16x32_bf16 a[88:91], v[160:163], a[224:227], a[88:91]\n"
    "v_accvgpr_read_b32 v30, a86\n"
    "v_accvgpr_read_b32 v31, a87\n"
    "v_mul_f32 v28, v84, v28\n"
    "v_mfma_f32_16x16x32_bf16 a[88:91], v[164:167], a[228:231], a[88:91]\n"
    "v_mul_f32 v29, v85, v29\n"
    "v_mul_f32 v30, v86, v30\n"
    "v_mfma_f32_16x16x32_bf16 a[88:91], v[168:171], a[232:235], a[88:91]\n"
    "v_mul_f32 v31, v87, v31\n"
    "v_cvt_pk_bf16_f32 v36, v24, v28\n"
    "v_mfma_f32_16x16x32_bf16 a[88:91], v[172:175], a[236:239], a[88:91]\n"
    "v_cvt_pk_bf16_f32 v37, v25, v29\n"
    "v_cvt_pk_bf16_f32 v38, v26, v30\n"
    "v_mfma_f32_16x16x32_bf16 a[88:91], v[224:227], a[240:243], a[88:91]\n"
    "v_cvt_pk_bf16_f32 v39, v27, v31\n"
    "s_mov_b64 vcc, s[68:69]\n"
    "v_mfma_f32_16x16x32_bf16 a[88:91], v[228:231], a[244:247], a[88:91]\n"
    "v_cndmask_b32_dpp v44, v37, v36, vcc quad_perm:[1,0,3,2] row_mask:0xf bank_mask:0xf\n"
    "v_cndmask_b32_dpp v46, v39, v38, vcc quad_perm:[1,0,3,2] row_mask:0xf bank_mask:0xf\n"
    "v_mfma_f32_16x16x32_bf16 a[88:91], v[232:235], a[248:251], a[88:91]\n"
    "s_mov_b64 vcc, s[70:71]\n"
    "v_cndmask_b32_dpp v45, v36, v37, vcc quad_perm:[1,0,3,2] row_mask:0xf bank_mask:0xf\n"
    "v_mfma_f32_16x16x32_bf16 a[88:91], v[236:239], a[252:255], a[88:91]\n"
    "v_cndmask_b32_dpp v47, v38, v39, vcc quad_perm:[1,0,3,2] row_mask:0xf bank_mask:0xf\n"
    "s_mov_b64 vcc, s[72:73]\n"
    "v_cndmask_b32_dpp v52, v46, v44, vcc quad_perm:[2,3,0,1] row_mask:0xf bank_mask:0xf\n"
    "v_mfma_f32_16x16x32_bf16 a[76:79], v[176:179], a[192:195], a[76:79]\n"
    "ds_read_b128 a[192:195], v3 offset:57344\n"
    "v_cndmask_b32_dpp v53, v47, v45, vcc quad_perm:[2,3,0,1] row_mask:0xf bank_mask:0xf\n"
    "s_mov_b64 vcc, s[74:75]\n"
    "v_mfma_f32_16x16x32_bf16 a[76:79], v[180:183], a[196:199], a[76:79]\n"
    "ds_read_b128 a[196:199], v3 offset:58368\n"
    "v_cndmask_b32_dpp v54, v44, v46, vcc quad_perm:[2,3,0,1] row_mask:0xf bank_mask:0xf\n"
    "v_cndmask_b32_dpp v55, v45, v47, vcc quad_perm:[2,3,0,1] row_mask:0xf bank_mask:0xf\n"
    "v_mfma_f32_16x16x32_bf16 a[76:79], v[184:187], a[200:203], a[76:79]\n"
    "ds_read_b128 a[200:203], v3 offset:59392\n"
    "v_mfma_f32_16x16x32_bf16 a[76:79], v[188:191], a[204:207], a[76:79]\n"
    "ds_read_b128 a[204:207], v3 offset:60416\n"
    "v_mfma_f32_16x16x32_bf16 a[76:79], v[240:243], a[208:211], a[76:79]\n"
    "ds_read_b128 a[208:211], v2 offset:57344\n"
    "v_mfma_f32_16x16x32_bf16 a[76:79], v[244:247], a[212:215], a[76:79]\n"
    "ds_read_b128 a[212:215], v2 offset:58368\n"
    "v_mfma_f32_16x16x32_bf16 a[76:79], v[248:251], a[216:219], a[76:79]\n"
    "ds_read_b128 a[216:219], v2 offset:59392\n"
    "v_mfma_f32_16x16x32_bf16 a[76:79], v[252:255], a[220:223], a[76:79]\n"
    "ds_read_b128 a[220:223], v2 offset:60416\n"
    "v_mfma_f32_16x16x32_bf16 a[92:95], v[176:179], a[224:227], a[92:95]\n"
    "ds_read_b128 a[224:227], v3 offset:61440\n"
    "v_mfma_f32_16x16x32_bf16 a[92:95], v[180:183], a[228:231], a[92:95]\n"
    "ds_read_b128 a[228:231], v3 offset:62464\n"
    "v_mfma_f32_16x16x32_bf16 a[92:95], v[184:187], a[232:235], a[92:95]\n"
    "ds_read_b128 a[232:235], v3 offset:63488\n"
    "v_mfma_f32_16x16x32_bf16 a[92:95], v[188:191], a[236:239], a[92:95]\n"
    "ds_read_b128 a[236:239], v3 offset:64512\n"
    "v_mfma_f32_16x16x32_bf16 a[92:95], v[240:243], a[240:243], a[92:95]\n"
    "ds_read_b128 a[240:243], v2 offset:61440\n"
    "v_mfma_f32_16x16x32_bf16 a[92:95], v[244:247], a[244:247], a[92:95]\n"
    "ds_read_b128 a[244:247], v2 offset:62464\n"
    "v_mfma_f32_16x16x32_bf16 a[92:95], v[248:251], a[248:251], a[92:95]\n"
    "s_waitcnt lgkmcnt(12)\n"
    "ds_read_b128 a[248:251], v2 offset:63488\n"
    "v_mfma_f32_16x16x32_bf16 a[92:95], v[252:255], a[252:255], a[92:95]\n"
    "ds_read_b128 a[252:255], v2 offset:64512\n"
    "s_waitcnt lgkmcnt(12)\n"
    "v_mfma_f32_16x16x32_bf16 a[96:99], v[128:131], a[192:195], a[96:99]\n"
    "buffer_store_dwordx4 v[52:55], v96, s[44:47], s49 offen offset:128 sc1\n"
    "v_accvgpr_read_b32 v16, a72\n"
    "v_accvgpr_read_b32 v17, a73\n"
    "v_mfma_f32_16x16x32_bf16 a[96:99], v[132:135], a[196:199], a[96:99]\n"
    "v_accvgpr_read_b32 v18, a74\n"
    "v_accvgpr_read_b32 v19, a75\n"
    "v_mul_f32 v16, v88, v16\n"
    "v_mfma_f32_16x16x32_bf16 a[96:99], v[136:139], a[200:203], a[96:99]\n"
    "v_mul_f32 v17, v89, v17\n"
    "v_mul_f32 v18, v90, v18\n"
    "v_mul_f32 v19, v91, v19\n"
    "v_mfma_f32_16x16x32_bf16 a[96:99], v[140:143], a[204:207], a[96:99]\n"
    "v_accvgpr_read_b32 v20, a88\n"
    "v_accvgpr_read_b32 v21, a89\n"
    "s_waitcnt lgkmcnt(8)\n"
    "v_mfma_f32_16x16x32_bf16 a[96:99], v[192:195], a[208:211], a[96:99]\n"
    "v_accvgpr_read_b32 v22, a90\n"
    "v_accvgpr_read_b32 v23, a91\n"
    "v_mul_f32 v20, v88, v20\n"
    "v_mfma_f32_16x16x32_bf16 a[96:99], v[196:199], a[212:215], a[96:99]\n"
    "v_mul_f32 v21, v89, v21\n"
    "v_mul_f32 v22, v90, v22\n"
    "v_mul_f32 v23, v91, v23\n"
    "v_mfma_f32_16x16x32_bf16 a[96:99], v[200:203], a[216:219], a[96:99]\n"
    "v_cvt_pk_bf16_f32 v32, v16, v20\n"
    "v_cvt_pk_bf16_f32 v33, v17, v21\n"
    "v_cvt_pk_bf16_f32 v34, v18, v22\n"
    "v_mfma_f32_16x16x32_bf16 a[96:99], v[204:207], a[220:223], a[96:99]\n"
    "v_cvt_pk_bf16_f32 v35, v19, v23\n"
    "s_mov_b64 vcc, s[68:69]\n"
    "s_waitcnt lgkmcnt(4)\n"
    "v_mfma_f32_16x16x32_bf16 a[112:115], v[128:131], a[224:227], a[112:115]\n"
    "v_cndmask_b32_dpp v40, v33, v32, vcc quad_perm:[1,0,3,2] row_mask:0xf bank_mask:0xf\n"
    "v_cndmask_b32_dpp v42, v35, v34, vcc quad_perm:[1,0,3,2] row_mask:0xf bank_mask:0xf\n"
    "s_mov_b64 vcc, s[70:71]\n"
    "v_mfma_f32_16x16x32_bf16 a[112:115], v[132:135], a[228:231], a[112:115]\n"
    "v_cndmask_b32_dpp v41, v32, v33, vcc quad_perm:[1,0,3,2] row_mask:0xf bank_mask:0xf\n"
    "v_cndmask_b32_dpp v43, v34, v35, vcc quad_perm:[1,0,3,2] row_mask:0xf bank_mask:0xf\n"
    "s_mov_b64 vcc, s[72:73]\n"
    "v_mfma_f32_16x16x32_bf16 a[112:115], v[136:139], a[232:235], a[112:115]\n"
    "v_cndmask_b32_dpp v56, v42, v40, vcc quad_perm:[2,3,0,1] row_mask:0xf bank_mask:0xf\n"
    "v_cndmask_b32_dpp v57, v43, v41, vcc quad_perm:[2,3,0,1] row_mask:0xf bank_mask:0xf\n"
    "s_mov_b64 vcc, s[74:75]\n"
    "v_mfma_f32_16x16x32_bf16 a[112:115], v[140:143], a[236:239], a[112:115]\n"
    "v_cndmask_b32_dpp v58, v40, v42, vcc quad_perm:[2,3,0,1] row_mask:0xf bank_mask:0xf\n"
    "v_cndmask_b32_dpp v59, v41, v43, vcc quad_perm:[2,3,0,1] row_mask:0xf bank_mask:0xf\n"
    "s_waitcnt lgkmcnt(0)\n"
    "v_mfma_f32_16x16x32_bf16 a[112:115], v[192:195], a[240:243], a[112:115]\n"
    "buffer_store_dwordx4 v[56:59], v96, s[44:47], s50 offen offset:128 sc1\n"
    "v_accvgpr_read_b32 v24, a76\n"
    "v_accvgpr_read_b32 v25, a77\n"
    "v_mfma_f32_16x16x32_bf16 a[112:115], v[196:199], a[244:247], a[112:115]\n"
    "v_accvgpr_read_b32 v26, a78\n"
    "v_accvgpr_read_b32 v27, a79\n"
    "v_mul_f32 v24, v92, v24\n"
    "v_mfma_f32_16x16x32_bf16 a[112:115], v[200:203], a[248:251], a[112:115]\n"
    "v_mul_f32 v25, v93, v25\n"
    "v_mul_f32 v26, v94, v26\n"
    "v_mul_f32 v27, v95, v27\n"
    "v_mfma_f32_16x16x32_bf16 a[112:115], v[204:207], a[252:255], a[112:115]\n"
    "v_accvgpr_read_b32 v28, a92\n"
    "v_accvgpr_read_b32 v29, a93\n"
    "v_accvgpr_read_b32 v30, a94\n"
    "v_mfma_f32_16x16x32_bf16 a[100:103], v[144:147], a[192:195], a[100:103]\n"
    "v_accvgpr_read_b32 v31, a95\n"
    "v_mul_f32 v28, v92, v28\n"
    "v_mul_f32 v29, v93, v29\n"
    "v_mfma_f32_16x16x32_bf16 a[100:103], v[148:151], a[196:199], a[100:103]\n"
    "v_mul_f32 v30, v94, v30\n"
    "v_mul_f32 v31, v95, v31\n"
    "v_cvt_pk_bf16_f32 v36, v24, v28\n"
    "v_mfma_f32_16x16x32_bf16 a[100:103], v[152:155], a[200:203], a[100:103]\n"
    "v_cvt_pk_bf16_f32 v37, v25, v29\n"
    "v_cvt_pk_bf16_f32 v38, v26, v30\n"
    "v_cvt_pk_bf16_f32 v39, v27, v31\n"
    "v_mfma_f32_16x16x32_bf16 a[100:103], v[156:159], a[204:207], a[100:103]\n"
    "s_mov_b64 vcc, s[68:69]\n"
    "v_cndmask_b32_dpp v44, v37, v36, vcc quad_perm:[1,0,3,2] row_mask:0xf bank_mask:0xf\n"
    "v_cndmask_b32_dpp v46, v39, v38, vcc quad_perm:[1,0,3,2] row_mask:0xf bank_mask:0xf\n"
    "v_mfma_f32_16x16x32_bf16 a[100:103], v[208:211], a[208:211], a[100:103]\n"
    "s_mov_b64 vcc, s[70:71]\n"
    "v_cndmask_b32_dpp v45, v36, v37, vcc quad_perm:[1,0,3,2] row_mask:0xf bank_mask:0xf\n"
    "v_cndmask_b32_dpp v47, v38, v39, vcc quad_perm:[1,0,3,2] row_mask:0xf bank_mask:0xf\n"
    "v_mfma_f32_16x16x32_bf16 a[100:103], v[212:215], a[212:215], a[100:103]\n"
    "s_mov_b64 vcc, s[72:73]\n"
    "v_cndmask_b32_dpp v60, v46, v44, vcc quad_perm:[2,3,0,1] row_mask:0xf bank_mask:0xf\n"
    "v_cndmask_b32_dpp v61, v47, v45, vcc quad_perm:[2,3,0,1] row_mask:0xf bank_mask:0xf\n"
    "v_mfma_f32_16x16x32_bf16 a[100:103], v[216:219], a[216:219], a[100:103]\n"
    "s_mov_b64 vcc, s[74:75]\n"
    "v_cndmask_b32_dpp v62, v44, v46, vcc quad_perm:[2,3,0,1] row_mask:0xf bank_mask:0xf\n"
    "v_cndmask_b32_dpp v63, v45, v47, vcc quad_perm:[2,3,0,1] row_mask:0xf bank_mask:0xf\n"
    "v_mfma_f32_16x16x32_bf16 a[100:103], v[220:223], a[220:223], a[100:103]\n"
    "buffer_store_dwordx4 v[60:63], v96, s[44:47], s51 offen offset:128 sc1\n"
    "v_accvgpr_read_b32 v16, a96\n"
    "v_accvgpr_read_b32 v17, a97\n"
    "v_mfma_f32_16x16x32_bf16 a[116:119], v[144:147], a[224:227], a[116:119]\n"
    "v_accvgpr_read_b32 v18, a98\n"
    "v_accvgpr_read_b32 v19, a99\n"
    "v_mul_f32 v16, v80, v16\n"
    "v_mfma_f32_16x16x32_bf16 a[116:119], v[148:151], a[228:231], a[116:119]\n"
    "v_mul_f32 v17, v81, v17\n"
    "v_mul_f32 v18, v82, v18\n"
    "v_mul_f32 v19, v83, v19\n"
    "v_mfma_f32_16x16x32_bf16 a[116:119], v[152:155], a[232:235], a[116:119]\n"
    "v_accvgpr_read_b32 v20, a112\n"
    "v_accvgpr_read_b32 v21, a113\n"
    "v_accvgpr_read_b32 v22, a114\n"
    "v_mfma_f32_16x16x32_bf16 a[116:119], v[156:159], a[236:239], a[116:119]\n"
    "v_accvgpr_read_b32 v23, a115\n"
    "v_mul_f32 v20, v80, v20\n"
    "v_mfma_f32_16x16x32_bf16 a[116:119], v[208:211], a[240:243], a[116:119]\n"
    "v_mul_f32 v21, v81, v21\n"
    "v_mul_f32 v22, v82, v22\n"
    "v_mfma_f32_16x16x32_bf16 a[116:119], v[212:215], a[244:247], a[116:119]\n"
    "v_mul_f32 v23, v83, v23\n"
    "v_cvt_pk_bf16_f32 v32, v16, v20\n"
    "v_mfma_f32_16x16x32_bf16 a[116:119], v[216:219], a[248:251], a[116:119]\n"
    "v_cvt_pk_bf16_f32 v33, v17, v21\n"
    "v_cvt_pk_bf16_f32 v34, v18, v22\n"
    "v_mfma_f32_16x16x32_bf16 a[116:119], v[220:223], a[252:255], a[116:119]\n"
    "v_cvt_pk_bf16_f32 v35, v19, v23\n"
    "s_mov_b64 vcc, s[68:69]\n"
    "v_cndmask_b32_dpp v40, v33, v32, vcc quad_perm:[1,0,3,2] row_mask:0xf bank_mask:0xf\n"
    "v_mfma_f32_16x16x32_bf16 a[104:107], v[160:163], a[192:195], a[104:107]\n"
    "v_cndmask_b32_dpp v42, v35, v34, vcc quad_perm:[1,0,3,2] row_mask:0xf bank_mask:0xf\n"
    "s_mov_b64 vcc, s[70:71]\n"
    "v_cndmask_b32_dpp v41, v32, v33, vcc quad_perm:[1,0,3,2] row_mask:0xf bank_mask:0xf\n"
    "v_mfma_f32_16x16x32_bf16 a[104:107], v[164:167], a[196:199], a[104:107]\n"
    "v_cndmask_b32_dpp v43, v34, v35, vcc quad_perm:[1,0,3,2] row_mask:0xf bank_mask:0xf\n"
    "s_mov_b64 vcc, s[72:73]\n"
    "v_cndmask_b32_dpp v48, v42, v40, vcc quad_perm:[2,3,0,1] row_mask:0xf bank_mask:0xf\n"
    "v_mfma_f32_16x16x32_bf16 a[104:107], v[168:171], a[200:203], a[104:107]\n"
    "v_cndmask_b32_dpp v49, v43, v41, vcc quad_perm:[2,3,0,1] row_mask:0xf bank_mask:0xf\n"
    "s_mov_b64 vcc, s[74:75]\n"
    "v_cndmask_b32_dpp v50, v40, v42, vcc quad_perm:[2,3,0,1] row_mask:0xf bank_mask:0xf\n"
    "v_mfma_f32_16x16x32_bf16 a[104:107], v[172:175], a[204:207], a[104:107]\n"
    "v_cndmask_b32_dpp v51, v41, v43, vcc quad_perm:[2,3,0,1] row_mask:0xf bank_mask:0xf\n"
    "v_mfma_f32_16x16x32_bf16 a[104:107], v[224:227], a[208:211], a[104:107]\n"
    "buffer_store_dwordx4 v[48:51], v96, s[44:47], 0 offen offset:192 sc1\n"
    "v_accvgpr_read_b32 v24, a100\n"
    "v_accvgpr_read_b32 v25, a101\n"
    "v_mfma_f32_16x16x32_bf16 a[104:107], v[228:231], a[212:215], a[104:107]\n"
    "v_accvgpr_read_b32 v26, a102\n"
    "v_accvgpr_read_b32 v27, a103\n"
    "v_mul_f32 v24, v84, v24\n"
    "v_mfma_f32_16x16x32_bf16 a[104:107], v[232:235], a[216:219], a[104:107]\n"
    "v_mul_f32 v25, v85, v25\n"
    "v_mul_f32 v26, v86, v26\n"
    "v_mfma_f32_16x16x32_bf16 a[104:107], v[236:239], a[220:223], a[104:107]\n"
    "v_mul_f32 v27, v87, v27\n"
    "v_accvgpr_read_b32 v28, a116\n"
    "v_accvgpr_read_b32 v29, a117\n"
    "v_mfma_f32_16x16x32_bf16 a[120:123], v[160:163], a[224:227], a[120:123]\n"
    "v_accvgpr_read_b32 v30, a118\n"
    "v_accvgpr_read_b32 v31, a119\n"
    "v_mul_f32 v28, v84, v28\n"
    "v_mfma_f32_16x16x32_bf16 a[120:123], v[164:167], a[228:231], a[120:123]\n"
    "v_mul_f32 v29, v85, v29\n"
    "v_mul_f32 v30, v86, v30\n"
    "v_mfma_f32_16x16x32_bf16 a[120:123], v[168:171], a[232:235], a[120:123]\n"
    "v_mul_f32 v31, v87, v31\n"
    "v_cvt_pk_bf16_f32 v36, v24, v28\n"
    "v_mfma_f32_16x16x32_bf16 a[120:123], v[172:175], a[236:239], a[120:123]\n"
    "v_cvt_pk_bf16_f32 v37, v25, v29\n"
    "v_cvt_pk_bf16_f32 v38, v26, v30\n"
    "v_mfma_f32_16x16x32_bf16 a[120:123], v[224:227], a[240:243], a[120:123]\n"
    "v_cvt_pk_bf16_f32 v39, v27, v31\n"
    "s_mov_b64 vcc, s[68:69]\n"
    "v_mfma_f32_16x16x32_bf16 a[120:123], v[228:231], a[244:247], a[120:123]\n"
    "v_cndmask_b32_dpp v44, v37, v36, vcc quad_perm:[1,0,3,2] row_mask:0xf bank_mask:0xf\n"
    "v_cndmask_b32_dpp v46, v39, v38, vcc quad_perm:[1,0,3,2] row_mask:0xf bank_mask:0xf\n"
    "v_mfma_f32_16x16x32_bf16 a[120:123], v[232:235], a[248:251], a[120:123]\n"
    "s_mov_b64 vcc, s[70:71]\n"
    "v_cndmask_b32_dpp v45, v36, v37, vcc quad_perm:[1,0,3,2] row_mask:0xf bank_mask:0xf\n"
    "v_mfma_f32_16x16x32_bf16 a[120:123], v[236:239], a[252:255], a[120:123]\n"
    "v_cndmask_b32_dpp v47, v38, v39, vcc quad_perm:[1,0,3,2] row_mask:0xf bank_mask:0xf\n"
    "s_mov_b64 vcc, s[72:73]\n"
    "v_cndmask_b32_dpp v52, v46, v44, vcc quad_perm:[2,3,0,1] row_mask:0xf bank_mask:0xf\n"
    "v_mfma_f32_16x16x32_bf16 a[108:111], v[176:179], a[192:195], a[108:111]\n"
    "v_cndmask_b32_dpp v53, v47, v45, vcc quad_perm:[2,3,0,1] row_mask:0xf bank_mask:0xf\n"
    "s_mov_b64 vcc, s[74:75]\n"
    "v_cndmask_b32_dpp v54, v44, v46, vcc quad_perm:[2,3,0,1] row_mask:0xf bank_mask:0xf\n"
    "v_mfma_f32_16x16x32_bf16 a[108:111], v[180:183], a[196:199], a[108:111]\n"
    "v_cndmask_b32_dpp v55, v45, v47, vcc quad_perm:[2,3,0,1] row_mask:0xf bank_mask:0xf\n"
    "v_mfma_f32_16x16x32_bf16 a[108:111], v[184:187], a[200:203], a[108:111]\n"
    "buffer_store_dwordx4 v[52:55], v96, s[44:47], s49 offen offset:192 sc1\n"
    "v_accvgpr_read_b32 v16, a104\n"
    "v_accvgpr_read_b32 v17, a105\n"
    "v_mfma_f32_16x16x32_bf16 a[108:111], v[188:191], a[204:207], a[108:111]\n"
    "v_accvgpr_read_b32 v18, a106\n"
    "v_accvgpr_read_b32 v19, a107\n"
    "v_mul_f32 v16, v88, v16\n"
    "v_mfma_f32_16x16x32_bf16 a[108:111], v[240:243], a[208:211], a[108:111]\n"
    "v_mul_f32 v17, v89, v17\n"
    "v_mul_f32 v18, v90, v18\n"
    "v_mfma_f32_16x16x32_bf16 a[108:111], v[244:247], a[212:215], a[108:111]\n"
    "v_mul_f32 v19, v91, v19\n"
    "v_accvgpr_read_b32 v20, a120\n"
    "v_mfma_f32_16x16x32_bf16 a[108:111], v[248:251], a[216:219], a[108:111]\n"
    "v_accvgpr_read_b32 v21, a121\n"
    "v_accvgpr_read_b32 v22, a122\n"
    "v_mfma_f32_16x16x32_bf16 a[108:111], v[252:255], a[220:223], a[108:111]\n"
    "v_accvgpr_read_b32 v23, a123\n"
    "v_mul_f32 v20, v88, v20\n"
    "v_mfma_f32_16x16x32_bf16 a[124:127], v[176:179], a[224:227], a[124:127]\n"
    "v_mul_f32 v21, v89, v21\n"
    "v_mul_f32 v22, v90, v22\n"
    "v_mfma_f32_16x16x32_bf16 a[124:127], v[180:183], a[228:231], a[124:127]\n"
    "v_mul_f32 v23, v91, v23\n"
    "v_cvt_pk_bf16_f32 v32, v16, v20\n"
    "v_mfma_f32_16x16x32_bf16 a[124:127], v[184:187], a[232:235], a[124:127]\n"
    "v_cvt_pk_bf16_f32 v33, v17, v21\n"
    "v_cvt_pk_bf16_f32 v34, v18, v22\n"
    "v_mfma_f32_16x16x32_bf16 a[124:127], v[188:191], a[236:239], a[124:127]\n"
    "v_cvt_pk_bf16_f32 v35, v19, v23\n"
    "s_mov_b64 vcc, s[68:69]\n"
    "v_mfma_f32_16x16x32_bf16 a[124:127], v[240:243], a[240:243], a[124:127]\n"
    "v_cndmask_b32_dpp v40, v33, v32, vcc quad_perm:[1,0,3,2] row_mask:0xf bank_mask:0xf\n"
    "v_cndmask_b32_dpp v42, v35, v34, vcc quad_perm:[1,0,3,2] row_mask:0xf bank_mask:0xf\n"
    "v_mfma_f32_16x16x32_bf16 a[124:127], v[244:247], a[244:247], a[124:127]\n"
    "s_mov_b64 vcc, s[70:71]\n"
    "v_cndmask_b32_dpp v41, v32, v33, vcc quad_perm:[1,0,3,2] row_mask:0xf bank_mask:0xf\n"
    "v_mfma_f32_16x16x32_bf16 a[124:127], v[248:251], a[248:251], a[124:127]\n"
    "v_cndmask_b32_dpp v43, v34, v35, vcc quad_perm:[1,0,3,2] row_mask:0xf bank_mask:0xf\n"
    "s_mov_b64 vcc, s[72:73]\n"
    "v_mfma_f32_16x16x32_bf16 a[124:127], v[252:255], a[252:255], a[124:127]\n"
    "v_cndmask_b32_dpp v56, v42, v40, vcc quad_perm:[2,3,0,1] row_mask:0xf bank_mask:0xf\n"
    "v_cndmask_b32_dpp v57, v43, v41, vcc quad_perm:[2,3,0,1] row_mask:0xf bank_mask:0xf\n"
    "s_mov_b64 vcc, s[74:75]\n"
    "v_cndmask_b32_dpp v58, v40, v42, vcc quad_perm:[2,3,0,1] row_mask:0xf bank_mask:0xf\n"
    "v_cndmask_b32_dpp v59, v41, v43, vcc quad_perm:[2,3,0,1] row_mask:0xf bank_mask:0xf\n"
    "buffer_store_dwordx4 v[56:59], v96, s[44:47], s50 offen offset:192 sc1\n"
    "v_accvgpr_read_b32 v24, a108\n"
    "v_accvgpr_read_b32 v25, a109\n"
    "v_accvgpr_read_b32 v26, a110\n"
    "v_accvgpr_read_b32 v27, a111\n"
    "v_mul_f32 v24, v92, v24\n"
    "v_mul_f32 v25, v93, v25\n"
    "v_mul_f32 v26, v94, v26\n"
    "v_mul_f32 v27, v95, v27\n"
    "v_accvgpr_read_b32 v28, a124\n"
    "v_accvgpr_read_b32 v29, a125\n"
    "v_accvgpr_read_b32 v30, a126\n"
    "v_accvgpr_read_b32 v31, a127\n"
    "v_mul_f32 v28, v92, v28\n"
    "v_mul_f32 v29, v93, v29\n"
    "v_mul_f32 v30, v94, v30\n"
    "v_mul_f32 v31, v95, v31\n"
    "v_cvt_pk_bf16_f32 v36, v24, v28\n"
    "v_cvt_pk_bf16_f32 v37, v25, v29\n"
    "v_cvt_pk_bf16_f32 v38, v26, v30\n"
    "v_cvt_pk_bf16_f32 v39, v27, v31\n"
    "s_mov_b64 vcc, s[68:69]\n"
    "v_cndmask_b32_dpp v44, v37, v36, vcc quad_perm:[1,0,3,2] row_mask:0xf bank_mask:0xf\n"
    "v_cndmask_b32_dpp v46, v39, v38, vcc quad_perm:[1,0,3,2] row_mask:0xf bank_mask:0xf\n"
    "s_mov_b64 vcc, s[70:71]\n"
    "v_cndmask_b32_dpp v45, v36, v37, vcc quad_perm:[1,0,3,2] row_mask:0xf bank_mask:0xf\n"
    "v_cndmask_b32_dpp v47, v38, v39, vcc quad_perm:[1,0,3,2] row_mask:0xf bank_mask:0xf\n"
    "s_mov_b64 vcc, s[72:73]\n"
    "v_cndmask_b32_dpp v60, v46, v44, vcc quad_perm:[2,3,0,1] row_mask:0xf bank_mask:0xf\n"
    "v_cndmask_b32_dpp v61, v47, v45, vcc quad_perm:[2,3,0,1] row_mask:0xf bank_mask:0xf\n"
    "s_mov_b64 vcc, s[74:75]\n"
    "v_cndmask_b32_dpp v62, v44, v46, vcc quad_perm:[2,3,0,1] row_mask:0xf bank_mask:0xf\n"
    "v_cndmask_b32_dpp v63, v45, v47, vcc quad_perm:[2,3,0,1] row_mask:0xf bank_mask:0xf\n"
    "buffer_store_dwordx4 v[60:63], v96, s[44:47], s51 offen offset:192 sc1\n"
    "s_branch L_end\n"
    "L_rare_A0:\n"
    "s_nop 7\n"
    "s_nop 7\n"
    "s_sub_u32 s56, s48, 0x20000\n"
    "s_add_u32 s57, s56, 0x0\n"
    "s_add_u32 s58, s56, 0x1000\n"
    "buffer_load_dwordx4 a[224:227], v76, s[40:43], s57 offen offset:0 sc1\n"
    "buffer_load_dwordx4 a[228:231], v76, s[40:43], s57 offen offset:1024 sc1\n"
    "buffer_load_dwordx4 a[232:235], v76, s[40:43], s57 offen offset:2048 sc1\n"
    "buffer_load_dwordx4 a[236:239], v76, s[40:43], s57 offen offset:3072 sc1\n"
    "buffer_load_dwordx4 a[240:243], v76, s[40:43], s58 offen offset:0 sc1\n"
    "buffer_load_dwordx4 a[244:247], v76, s[40:43], s58 offen offset:1024 sc1\n"
    "buffer_load_dwordx4 a[248:251], v76, s[40:43], s58 offen offset:2048 sc1\n"
    "buffer_load_dwordx4 a[252:255], v76, s[40:43], s58 offen offset:3072 sc1\n"
    "s_waitcnt vmcnt(0)\n"
    "v_mfma_f32_16x16x32_bf16 v[80:83], a[224:227], a[128:131], v[32:35]\n"
    "v_mfma_f32_16x16x32_bf16 v[80:83], a[228:231], a[132:135], v[80:83]\n"
    "v_mfma_f32_16x16x32_bf16 v[80:83], a[232:235], a[136:139], v[80:83]\n"
    "v_mfma_f32_16x16x32_bf16 v[80:83], a[236:239], a[140:143], v[80:83]\n"
    "v_mfma_f32_16x16x32_bf16 v[84:87], a[240:243], a[128:131], v[32:35]\n"
    "v_mfma_f32_16x16x32_bf16 v[84:87], a[244:247], a[132:135], v[84:87]\n"
    "v_mfma_f32_16x16x32_bf16 v[84:87], a[248:251], a[136:139], v[84:87]\n"
    "v_mfma_f32_16x16x32_bf16 v[84:87], a[252:255], a[140:143], v[84:87]\n"
    "s_nop 3\n"
    "v_max_f32 v9, v80, v80\n"
    "v_max3_f32 v9, v9, v80, v81\n"
    "v_max3_f32 v9, v9, v82, v83\n"
    "s_nop 0\n"
    "v_max3_f32 v9, v9, v84, v85\n"
    "v_max3_f32 v9, v9, v86, v87\n"
    "s_add_u32 s57, s56, 0x2000\n"
    "s_add_u32 s58, s56, 0x3000\n"
    "buffer_load_dwordx4 a[224:227], v76, s[40:43], s57 offen offset:0 sc1\n"
    "buffer_load_dwordx4 a[228:231], v76, s[40:43], s57 offen offset:1024 sc1\n"
    "buffer_load_dwordx4 a[232:235], v76, s[40:43], s57 offen offset:2048 sc1\n"
    "buffer_load_dwordx4 a[236:239], v76, s[40:43], s57 offen offset:3072 sc1\n"
    "buffer_load_dwordx4 a[240:243], v76, s[40:43], s58 offen offset:0 sc1\n"
    "buffer_load_dwordx4 a[244:247], v76, s[40:43], s58 offen offset:1024 sc1\n"
    "buffer_load_dwordx4 a[248:251], v76, s[40:43], s58 offen offset:2048 sc1\n"
    "buffer_load_dwordx4 a[252:255], v76, s[40:43], s58 offen offset:3072 sc1\n"
    "s_waitcnt vmcnt(0)\n"
    "v_mfma_f32_16x16x32_bf16 v[80:83], a[224:227], a[128:131], v[32:35]\n"
    "v_mfma_f32_16x16x32_bf16 v[80:83], a[228:231], a[132:135], v[80:83]\n"
    "v_mfma_f32_16x16x32_bf16 v[80:83], a[232:235], a[136:139], v[80:83]\n"
    "v_mfma_f32_16x16x32_bf16 v[80:83], a[236:239], a[140:143], v[80:83]\n"
    "v_mfma_f32_16x16x32_bf16 v[84:87], a[240:243], a[128:131], v[32:35]\n"
    "v_mfma_f32_16x16x32_bf16 v[84:87], a[244:247], a[132:135], v[84:87]\n"
    "v_mfma_f32_16x16x32_bf16 v[84:87], a[248:251], a[136:139], v[84:87]\n"
    "v_mfma_f32_16x16x32_bf16 v[84:87], a[252:255], a[140:143], v[84:87]\n"
    "s_nop 3\n"
    "v_max3_f32 v9, v9, v80, v81\n"
    "v_max3_f32 v9, v9, v82, v83\n"
    "s_nop 1\n"
    "v_max3_f32 v9, v9, v84, v85\n"
    "v_max3_f32 v9, v9, v86, v87\n"
    "s_add_u32 s57, s56, 0x4000\n"
    "s_add_u32 s58, s56, 0x5000\n"
    "buffer_load_dwordx4 a[224:227], v76, s[40:43], s57 offen offset:0 sc1\n"
    "buffer_load_dwordx4 a[228:231], v76, s[40:43], s57 offen offset:1024 sc1\n"
    "buffer_load_dwordx4 a[232:235], v76, s[40:43], s57 offen offset:2048 sc1\n"
    "buffer_load_dwordx4 a[236:239], v76, s[40:43], s57 offen offset:3072 sc1\n"
    "buffer_load_dwordx4 a[240:243], v76, s[40:43], s58 offen offset:0 sc1\n"
    "buffer_load_dwordx4 a[244:247], v76, s[40:43], s58 offen offset:1024 sc1\n"
    "buffer_load_dwordx4 a[248:251], v76, s[40:43], s58 offen offset:2048 sc1\n"
    "buffer_load_dwordx4 a[252:255], v76, s[40:43], s58 offen offset:3072 sc1\n"
    "s_waitcnt vmcnt(0)\n"
    "v_mfma_f32_16x16x32_bf16 v[80:83], a[224:227], a[128:131], v[32:35]\n"
    "v_mfma_f32_16x16x32_bf16 v[80:83], a[228:231], a[132:135], v[80:83]\n"
    "v_mfma_f32_16x16x32_bf16 v[80:83], a[232:235], a[136:139], v[80:83]\n"
    "v_mfma_f32_16x16x32_bf16 v[80:83], a[236:239], a[140:143], v[80:83]\n"
    "v_mfma_f32_16x16x32_bf16 v[84:87], a[240:243], a[128:131], v[32:35]\n"
    "v_mfma_f32_16x16x32_bf16 v[84:87], a[244:247], a[132:135], v[84:87]\n"
    "v_mfma_f32_16x16x32_bf16 v[84:87], a[248:251], a[136:139], v[84:87]\n"
    "v_mfma_f32_16x16x32_bf16 v[84:87], a[252:255], a[140:143], v[84:87]\n"
    "s_nop 3\n"
    "v_max3_f32 v9, v9, v80, v81\n"
    "v_max3_f32 v9, v9, v82, v83\n"
    "s_nop 1\n"
    "v_max3_f32 v9, v9, v84, v85\n"
    "v_max3_f32 v9, v9, v86, v87\n"
    "s_add_u32 s57, s56, 0x6000\n"
    "s_add_u32 s58, s56, 0x7000\n"
    "buffer_load_dwordx4 a[224:227], v76, s[40:43], s57 offen offset:0 sc1\n"
    "buffer_load_dwordx4 a[228:231], v76, s[40:43], s57 offen offset:1024 sc1\n"
    "buffer_load_dwordx4 a[232:235], v76, s[40:43], s57 offen offset:2048 sc1\n"
    "buffer_load_dwordx4 a[236:239], v76, s[40:43], s57 offen offset:3072 sc1\n"
    "buffer_load_dwordx4 a[240:243], v76, s[40:43], s58 offen offset:0 sc1\n"
    "buffer_load_dwordx4 a[244:247], v76, s[40:43], s58 offen offset:1024 sc1\n"
    "buffer_load_dwordx4 a[248:251], v76, s[40:43], s58 offen offset:2048 sc1\n"
    "buffer_load_dwordx4 a[252:255], v76, s[40:43], s58 offen offset:3072 sc1\n"
    "s_waitcnt vmcnt(0)\n"
    "v_mfma_f32_16x16x32_bf16 v[80:83], a[224:227], a[128:131], v[32:35]\n"
    "v_mfma_f32_16x16x32_bf16 v[80:83], a[228:231], a[132:135], v[80:83]\n"
    "v_mfma_f32_16x16x32_bf16 v[80:83], a[232:235], a[136:139], v[80:83]\n"
    "v_mfma_f32_16x16x32_bf16 v[80:83], a[236:239], a[140:143], v[80:83]\n"
    "v_mfma_f32_16x16x32_bf16 v[84:87], a[240:243], a[128:131], v[32:35]\n"
    "v_mfma_f32_16x16x32_bf16 v[84:87], a[244:247], a[132:135], v[84:87]\n"
    "v_mfma_f32_16x16x32_bf16 v[84:87], a[248:251], a[136:139], v[84:87]\n"
    "v_mfma_f32_16x16x32_bf16 v[84:87], a[252:255], a[140:143], v[84:87]\n"
    "s_nop 3\n"
    "v_max3_f32 v9, v9, v80, v81\n"
    "v_max3_f32 v9, v9, v82, v83\n"
    "s_nop 1\n"
    "v_max3_f32 v9, v9, v84, v85\n"
    "v_max3_f32 v9, v9, v86, v87\n"
    "v_mov_b32 v10, v9\n"
    "s_nop 1\n"
    "v_permlane16_swap_b32 v10, v9\n"
    "v_max_f32 v9, v10, v9\n"
    "v_mov_b32 v10, v9\n"
    "s_nop 1\n"
    "v_permlane32_swap_b32 v10, v9\n"
    "v_max_f32 v9, v10, v9\n"
    "v_max_f32 v9, 0, v9\n"
    "v_sub_f32 v32, v32, v9\n"
    "v_sub_f32 v33, v33, v9\n"
    "v_sub_f32 v34, v34, v9\n"
    "v_sub_f32 v35, v35, v9\n"
    "s_add_u32 s57, s56, 0x0\n"
    "s_add_u32 s58, s56, 0x1000\n"
    "buffer_load_dwordx4 a[224:227], v76, s[40:43], s57 offen offset:0 sc1\n"
    "buffer_load_dwordx4 a[228:231], v76, s[40:43], s57 offen offset:1024 sc1\n"
    "buffer_load_dwordx4 a[232:235], v76, s[40:43], s57 offen offset:2048 sc1\n"
    "buffer_load_dwordx4 a[236:239], v76, s[40:43], s57 offen offset:3072 sc1\n"
    "buffer_load_dwordx4 a[240:243], v76, s[40:43], s58 offen offset:0 sc1\n"
    "buffer_load_dwordx4 a[244:247], v76, s[40:43], s58 offen offset:1024 sc1\n"
    "buffer_load_dwordx4 a[248:251], v76, s[40:43], s58 offen offset:2048 sc1\n"
    "buffer_load_dwordx4 a[252:255], v76, s[40:43], s58 offen offset:3072 sc1\n"
    "s_waitcnt vmcnt(0)\n"
    "v_mfma_f32_16x16x32_bf16 v[80:83], a[224:227], a[128:131], v[32:35]\n"
    "v_mfma_f32_16x16x32_bf16 v[80:83], a[228:231], a[132:135], v[80:83]\n"
    "v_mfma_f32_16x16x32_bf16 v[80:83], a[232:235], a[136:139], v[80:83]\n"
    "v_mfma_f32_16x16x32_bf16 v[80:83], a[236:239], a[140:143], v[80:83]\n"
    "v_mfma_f32_16x16x32_bf16 v[84:87], a[240:243], a[128:131], v[32:35]\n"
    "v_mfma_f32_16x16x32_bf16 v[84:87], a[244:247], a[132:135], v[84:87]\n"
    "v_mfma_f32_16x16x32_bf16 v[84:87], a[248:251], a[136:139], v[84:87]\n"
    "v_mfma_f32_16x16x32_bf16 v[84:87], a[252:255], a[140:143], v[84:87]\n"
    "s_nop 3\n"
    "v_exp_f32 v80, v80\n"
    "v_exp_f32 v81, v81\n"
    "v_exp_f32 v82, v82\n"
    "v_exp_f32 v83, v83\n"
    "v_exp_f32 v84, v84\n"
    "v_exp_f32 v85, v85\n"
    "v_exp_f32 v86, v86\n"
    "v_exp_f32 v87, v87\n"
    "v_cvt_pk_bf16_f32 v128, v80, v81\n"
    "v_cvt_pk_bf16_f32 v129, v82, v83\n"
    "v_cvt_pk_bf16_f32 v130, v84, v85\n"
    "v_cvt_pk_bf16_f32 v131, v86, v87\n"
    "s_add_u32 s57, s56, 0x2000\n"
    "s_add_u32 s58, s56, 0x3000\n"
    "buffer_load_dwordx4 a[224:227], v76, s[40:43], s57 offen offset:0 sc1\n"
    "buffer_load_dwordx4 a[228:231], v76, s[40:43], s57 offen offset:1024 sc1\n"
    "buffer_load_dwordx4 a[232:235], v76, s[40:43], s57 offen offset:2048 sc1\n"
    "buffer_load_dwordx4 a[236:239], v76, s[40:43], s57 offen offset:3072 sc1\n"
    "buffer_load_dwordx4 a[240:243], v76, s[40:43], s58 offen offset:0 sc1\n"
    "buffer_load_dwordx4 a[244:247], v76, s[40:43], s58 offen offset:1024 sc1\n"
    "buffer_load_dwordx4 a[248:251], v76, s[40:43], s58 offen offset:2048 sc1\n"
    "buffer_load_dwordx4 a[252:255], v76, s[40:43], s58 offen offset:3072 sc1\n"
    "s_waitcnt vmcnt(0)\n"
    "v_mfma_f32_16x16x32_bf16 v[80:83], a[224:227], a[128:131], v[32:35]\n"
    "v_mfma_f32_16x16x32_bf16 v[80:83], a[228:231], a[132:135], v[80:83]\n"
    "v_mfma_f32_16x16x32_bf16 v[80:83], a[232:235], a[136:139], v[80:83]\n"
    "v_mfma_f32_16x16x32_bf16 v[80:83], a[236:239], a[140:143], v[80:83]\n"
    "v_mfma_f32_16x16x32_bf16 v[84:87], a[240:243], a[128:131], v[32:35]\n"
    "v_mfma_f32_16x16x32_bf16 v[84:87], a[244:247], a[132:135], v[84:87]\n"
    "v_mfma_f32_16x16x32_bf16 v[84:87], a[248:251], a[136:139], v[84:87]\n"
    "v_mfma_f32_16x16x32_bf16 v[84:87], a[252:255], a[140:143], v[84:87]\n"
    "s_nop 3\n"
    "v_exp_f32 v80, v80\n"
    "v_exp_f32 v81, v81\n"
    "v_exp_f32 v82, v82\n"
    "v_exp_f32 v83, v83\n"
    "v_exp_f32 v84, v84\n"
    "v_exp_f32 v85, v85\n"
    "v_exp_f32 v86, v86\n"
    "v_exp_f32 v87, v87\n"
    "v_cvt_pk_bf16_f32 v132, v80, v81\n"
    "v_cvt_pk_bf16_f32 v133, v82, v83\n"
    "v_cvt_pk_bf16_f32 v134, v84, v85\n"
    "v_cvt_pk_bf16_f32 v135, v86, v87\n"
    "s_add_u32 s57, s56, 0x4000\n"
    "s_add_u32 s58, s56, 0x5000\n"
    "buffer_load_dwordx4 a[224:227], v76, s[40:43], s57 offen offset:0 sc1\n"
    "buffer_load_dwordx4 a[228:231], v76, s[40:43], s57 offen offset:1024 sc1\n"
    "buffer_load_dwordx4 a[232:235], v76, s[40:43], s57 offen offset:2048 sc1\n"
    "buffer_load_dwordx4 a[236:239], v76, s[40:43], s57 offen offset:3072 sc1\n"
    "buffer_load_dwordx4 a[240:243], v76, s[40:43], s58 offen offset:0 sc1\n"
    "buffer_load_dwordx4 a[244:247], v76, s[40:43], s58 offen offset:1024 sc1\n"
    "buffer_load_dwordx4 a[248:251], v76, s[40:43], s58 offen offset:2048 sc1\n"
    "buffer_load_dwordx4 a[252:255], v76, s[40:43], s58 offen offset:3072 sc1\n"
    "s_waitcnt vmcnt(0)\n"
    "v_mfma_f32_16x16x32_bf16 v[80:83], a[224:227], a[128:131], v[32:35]\n"
    "v_mfma_f32_16x16x32_bf16 v[80:83], a[228:231], a[132:135], v[80:83]\n"
    "v_mfma_f32_16x16x32_bf16 v[80:83], a[232:235], a[136:139], v[80:83]\n"
    "v_mfma_f32_16x16x32_bf16 v[80:83], a[236:239], a[140:143], v[80:83]\n"
    "v_mfma_f32_16x16x32_bf16 v[84:87], a[240:243], a[128:131], v[32:35]\n"
    "v_mfma_f32_16x16x32_bf16 v[84:87], a[244:247], a[132:135], v[84:87]\n"
    "v_mfma_f32_16x16x32_bf16 v[84:87], a[248:251], a[136:139], v[84:87]\n"
    "v_mfma_f32_16x16x32_bf16 v[84:87], a[252:255], a[140:143], v[84:87]\n"
    "s_nop 3\n"
    "v_exp_f32 v80, v80\n"
    "v_exp_f32 v81, v81\n"
    "v_exp_f32 v82, v82\n"
    "v_exp_f32 v83, v83\n"
    "v_exp_f32 v84, v84\n"
    "v_exp_f32 v85, v85\n"
    "v_exp_f32 v86, v86\n"
    "v_exp_f32 v87, v87\n"
    "v_cvt_pk_bf16_f32 v136, v80, v81\n"
    "v_cvt_pk_bf16_f32 v137, v82, v83\n"
    "v_cvt_pk_bf16_f32 v138, v84, v85\n"
    "v_cvt_pk_bf16_f32 v139, v86, v87\n"
    "s_add_u32 s57, s56, 0x6000\n"
    "s_add_u32 s58, s56, 0x7000\n"
    "buffer_load_dwordx4 a[224:227], v76, s[40:43], s57 offen offset:0 sc1\n"
    "buffer_load_dwordx4 a[228:231], v76, s[40:43], s57 offen offset:1024 sc1\n"
    "buffer_load_dwordx4 a[232:235], v76, s[40:43], s57 offen offset:2048 sc1\n"
    "buffer_load_dwordx4 a[236:239], v76, s[40:43], s57 offen offset:3072 sc1\n"
    "buffer_load_dwordx4 a[240:243], v76, s[40:43], s58 offen offset:0 sc1\n"
    "buffer_load_dwordx4 a[244:247], v76, s[40:43], s58 offen offset:1024 sc1\n"
    "buffer_load_dwordx4 a[248:251], v76, s[40:43], s58 offen offset:2048 sc1\n"
    "buffer_load_dwordx4 a[252:255], v76, s[40:43], s58 offen offset:3072 sc1\n"
    "s_waitcnt vmcnt(0)\n"
    "v_mfma_f32_16x16x32_bf16 v[80:83], a[224:227], a[128:131], v[32:35]\n"
    "v_mfma_f32_16x16x32_bf16 v[80:83], a[228:231], a[132:135], v[80:83]\n"
    "v_mfma_f32_16x16x32_bf16 v[80:83], a[232:235], a[136:139], v[80:83]\n"
    "v_mfma_f32_16x16x32_bf16 v[80:83], a[236:239], a[140:143], v[80:83]\n"
    "v_mfma_f32_16x16x32_bf16 v[84:87], a[240:243], a[128:131], v[32:35]\n"
    "v_mfma_f32_16x16x32_bf16 v[84:87], a[244:247], a[132:135], v[84:87]\n"
    "v_mfma_f32_16x16x32_bf16 v[84:87], a[248:251], a[136:139], v[84:87]\n"
    "v_mfma_f32_16x16x32_bf16 v[84:87], a[252:255], a[140:143], v[84:87]\n"
    "s_nop 3\n"
    "v_exp_f32 v80, v80\n"
    "v_exp_f32 v81, v81\n"
    "v_exp_f32 v82, v82\n"
    "v_exp_f32 v83, v83\n"
    "v_exp_f32 v84, v84\n"
    "v_exp_f32 v85, v85\n"
    "v_exp_f32 v86, v86\n"
    "v_exp_f32 v87, v87\n"
    "v_cvt_pk_bf16_f32 v140, v80, v81\n"
    "v_cvt_pk_bf16_f32 v141, v82, v83\n"
    "v_cvt_pk_bf16_f32 v142, v84, v85\n"
    "v_cvt_pk_bf16_f32 v143, v86, v87\n"
    "v_mfma_f32_16x16x32_bf16 v[48:51], v[64:67], v[128:131], 0\n"
    "v_mfma_f32_16x16x32_bf16 v[48:51], v[64:67], v[132:135], v[48:51]\n"
    "v_mfma_f32_16x16x32_bf16 v[48:51], v[64:67], v[136:139], v[48:51]\n"
    "v_mfma_f32_16x16x32_bf16 v[48:51], v[64:67], v[140:143], v[48:51]\n"
    "v_sub_f32 v16, v16, v9\n"
    "v_sub_f32 v17, v17, v9\n"
    "v_sub_f32 v18, v18, v9\n"
    "v_sub_f32 v19, v19, v9\n"
    "v_sub_f32 v11, 0, v9\n"
    "v_exp_f32 v11, v11\n"
    "s_nop 0\n"
    "v_mul_f32 v68, v11, v68\n"
    "ds_bpermute_b32 v12, v99, v11 offset:0\n"
    "ds_bpermute_b32 v13, v99, v11 offset:4\n"
    "ds_bpermute_b32 v14, v99, v11 offset:8\n"
    "ds_bpermute_b32 v15, v99, v11 offset:12\n"
    "s_waitcnt lgkmcnt(0)\n"
    "v_accvgpr_read_b32 v80, a0\n"
    "v_accvgpr_read_b32 v81, a1\n"
    "v_accvgpr_read_b32 v82, a2\n"
    "v_accvgpr_read_b32 v83, a3\n"
    "v_accvgpr_read_b32 v84, a16\n"
    "v_accvgpr_read_b32 v85, a17\n"
    "v_accvgpr_read_b32 v86, a18\n"
    "v_accvgpr_read_b32 v87, a19\n"
    "v_mul_f32 v80, v12, v80\n"
    "v_mul_f32 v81, v13, v81\n"
    "v_mul_f32 v82, v14, v82\n"
    "v_mul_f32 v83, v15, v83\n"
    "v_mul_f32 v84, v12, v84\n"
    "v_mul_f32 v85, v13, v85\n"
    "v_mul_f32 v86, v14, v86\n"
    "v_mul_f32 v87, v15, v87\n"
    "v_accvgpr_write_b32 a0, v80\n"
    "v_accvgpr_write_b32 a1, v81\n"
    "v_accvgpr_write_b32 a2, v82\n"
    "v_accvgpr_write_b32 a3, v83\n"
    "v_accvgpr_write_b32 a16, v84\n"
    "v_accvgpr_write_b32 a17, v85\n"
    "v_accvgpr_write_b32 a18, v86\n"
    "v_accvgpr_write_b32 a19, v87\n"
    "v_accvgpr_read_b32 v80, a32\n"
    "v_accvgpr_read_b32 v81, a33\n"
    "v_accvgpr_read_b32 v82, a34\n"
    "v_accvgpr_read_b32 v83, a35\n"
    "v_accvgpr_read_b32 v84, a48\n"
    "v_accvgpr_read_b32 v85, a49\n"
    "v_accvgpr_read_b32 v86, a50\n"
    "v_accvgpr_read_b32 v87, a51\n"
    "v_mul_f32 v80, v12, v80\n"
    "v_mul_f32 v81, v13, v81\n"
    "v_mul_f32 v82, v14, v82\n"
    "v_mul_f32 v83, v15, v83\n"
    "v_mul_f32 v84, v12, v84\n"
    "v_mul_f32 v85, v13, v85\n"
    "v_mul_f32 v86, v14, v86\n"
    "v_mul_f32 v87, v15, v87\n"
    "v_accvgpr_write_b32 a32, v80\n"
    "v_accvgpr_write_b32 a33, v81\n"
    "v_accvgpr_write_b32 a34, v82\n"
    "v_accvgpr_write_b32 a35, v83\n"
    "v_accvgpr_write_b32 a48, v84\n"
    "v_accvgpr_write_b32 a49, v85\n"
    "v_accvgpr_write_b32 a50, v86\n"
    "v_accvgpr_write_b32 a51, v87\n"
    "v_accvgpr_read_b32 v80, a64\n"
    "v_accvgpr_read_b32 v81, a65\n"
    "v_accvgpr_read_b32 v82, a66\n"
    "v_accvgpr_read_b32 v83, a67\n"
    "v_accvgpr_read_b32 v84, a80\n"
    "v_accvgpr_read_b32 v85, a81\n"
    "v_accvgpr_read_b32 v86, a82\n"
    "v_accvgpr_read_b32 v87, a83\n"
    "v_mul_f32 v80, v12, v80\n"
    "v_mul_f32 v81, v13, v81\n"
    "v_mul_f32 v82, v14, v82\n"
    "v_mul_f32 v83, v15, v83\n"
    "v_mul_f32 v84, v12, v84\n"
    "v_mul_f32 v85, v13, v85\n"
    "v_mul_f32 v86, v14, v86\n"
    "v_mul_f32 v87, v15, v87\n"
    "v_accvgpr_write_b32 a64, v80\n"
    "v_accvgpr_write_b32 a65, v81\n"
    "v_accvgpr_write_b32 a66, v82\n"
    "v_accvgpr_write_b32 a67, v83\n"
    "v_accvgpr_write_b32 a80, v84\n"
    "v_accvgpr_write_b32 a81, v85\n"
    "v_accvgpr_write_b32 a82, v86\n"
    "v_accvgpr_write_b32 a83, v87\n"
    "v_accvgpr_read_b32 v80, a96\n"
    "v_accvgpr_read_b32 v81, a97\n"
    "v_accvgpr_read_b32 v82, a98\n"
    "v_accvgpr_read_b32 v83, a99\n"
    "v_accvgpr_read_b32 v84, a112\n"
    "v_accvgpr_read_b32 v85, a113\n"
    "v_accvgpr_read_b32 v86, a114\n"
    "v_accvgpr_read_b32 v87, a115\n"
    "v_mul_f32 v80, v12, v80\n"
    "v_mul_f32 v81, v13, v81\n"
    "v_mul_f32 v82, v14, v82\n"
    "v_mul_f32 v83, v15, v83\n"
    "v_mul_f32 v84, v12, v84\n"
    "v_mul_f32 v85, v13, v85\n"
    "v_mul_f32 v86, v14, v86\n"
    "v_mul_f32 v87, v15, v87\n"
    "v_accvgpr_write_b32 a96, v80\n"
    "v_accvgpr_write_b32 a97, v81\n"
    "v_accvgpr_write_b32 a98, v82\n"
    "v_accvgpr_write_b32 a99, v83\n"
    "v_accvgpr_write_b32 a112, v84\n"
    "v_accvgpr_write_b32 a113, v85\n"
    "v_accvgpr_write_b32 a114, v86\n"
    "v_accvgpr_write_b32 a115, v87\n"
    "s_nop 7\n"
    "s_nop 7\n"
    "s_branch L_ret_A0\n"
    "L_rare_A1:\n"
    "s_nop 7\n"
    "s_nop 7\n"
    "s_sub_u32 s56, s48, 0x20000\n"
    "s_add_u32 s57, s56, 0x0\n"
    "s_add_u32 s58, s56, 0x1000\n"
    "buffer_load_dwordx4 a[224:227], v76, s[40:43], s57 offen offset:0 sc1\n"
    "buffer_load_dwordx4 a[228:231], v76, s[40:43], s57 offen offset:1024 sc1\n"
    "buffer_load_dwordx4 a[232:235], v76, s[40:43], s57 offen offset:2048 sc1\n"
    "buffer_load_dwordx4 a[236:239], v76, s[40:43], s57 offen offset:3072 sc1\n"
    "buffer_load_dwordx4 a[240:243], v76, s[40:43], s58 offen offset:0 sc1\n"
    "buffer_load_dwordx4 a[244:247], v76, s[40:43], s58 offen offset:1024 sc1\n"
    "buffer_load_dwordx4 a[248:251], v76, s[40:43], s58 offen offset:2048 sc1\n"
    "buffer_load_dwordx4 a[252:255], v76, s[40:43], s58 offen offset:3072 sc1\n"
    "s_waitcnt vmcnt(0)\n"
    "v_mfma_f32_16x16x32_bf16 v[80:83], a[224:227], a[144:147], v[36:39]\n"
    "v_mfma_f32_16x16x32_bf16 v[80:83], a[228:231], a[148:151], v[80:83]\n"
    "v_mfma_f32_16x16x32_bf16 v[80:83], a[232:235], a[152:155], v[80:83]\n"
    "v_mfma_f32_16x16x32_bf16 v[80:83], a[236:239], a[156:159], v[80:83]\n"
    "v_mfma_f32_16x16x32_bf16 v[84:87], a[240:243], a[144:147], v[36:39]\n"
    "v_mfma_f32_16x16x32_bf16 v[84:87], a[244:247], a[148:151], v[84:87]\n"
    "v_mfma_f32_16x16x32_bf16 v[84:87], a[248:251], a[152:155], v[84:87]\n"
    "v_mfma_f32_16x16x32_bf16 v[84:87], a[252:255], a[156:159], v[84:87]\n"
    "s_nop 3\n"
    "v_max_f32 v9, v80, v80\n"
    "v_max3_f32 v9, v9, v80, v81\n"
    "v_max3_f32 v9, v9, v82, v83\n"
    "s_nop 0\n"
    "v_max3_f32 v9, v9, v84, v85\n"
    "v_max3_f32 v9, v9, v86, v87\n"
    "s_add_u32 s57, s56, 0x2000\n"
    "s_add_u32 s58, s56, 0x3000\n"
    "buffer_load_dwordx4 a[224:227], v76, s[40:43], s57 offen offset:0 sc1\n"
    "buffer_load_dwordx4 a[228:231], v76, s[40:43], s57 offen offset:1024 sc1\n"
    "buffer_load_dwordx4 a[232:235], v76, s[40:43], s57 offen offset:2048 sc1\n"
    "buffer_load_dwordx4 a[236:239], v76, s[40:43], s57 offen offset:3072 sc1\n"
    "buffer_load_dwordx4 a[240:243], v76, s[40:43], s58 offen offset:0 sc1\n"
    "buffer_load_dwordx4 a[244:247], v76, s[40:43], s58 offen offset:1024 sc1\n"
    "buffer_load_dwordx4 a[248:251], v76, s[40:43], s58 offen offset:2048 sc1\n"
    "buffer_load_dwordx4 a[252:255], v76, s[40:43], s58 offen offset:3072 sc1\n"
    "s_waitcnt vmcnt(0)\n"
    "v_mfma_f32_16x16x32_bf16 v[80:83], a[224:227], a[144:147], v[36:39]\n"
    "v_mfma_f32_16x16x32_bf16 v[80:83], a[228:231], a[148:151], v[80:83]\n"
    "v_mfma_f32_16x16x32_bf16 v[80:83], a[232:235], a[152:155], v[80:83]\n"
    "v_mfma_f32_16x16x32_bf16 v[80:83], a[236:239], a[156:159], v[80:83]\n"
    "v_mfma_f32_16x16x32_bf16 v[84:87], a[240:243], a[144:147], v[36:39]\n"
    "v_mfma_f32_16x16x32_bf16 v[84:87], a[244:247], a[148:151], v[84:87]\n"
    "v_mfma_f32_16x16x32_bf16 v[84:87], a[248:251], a[152:155], v[84:87]\n"
    "v_mfma_f32_16x16x32_bf16 v[84:87], a[252:255], a[156:159], v[84:87]\n"
    "s_nop 3\n"
    "v_max3_f32 v9, v9, v80, v81\n"
    "v_max3_f32 v9, v9, v82, v83\n"
    "s_nop 1\n"
    "v_max3_f32 v9, v9, v84, v85\n"
    "v_max3_f32 v9, v9, v86, v87\n"
    "s_add_u32 s57, s56, 0x4000\n"
    "s_add_u32 s58, s56, 0x5000\n"
    "buffer_load_dwordx4 a[224:227], v76, s[40:43], s57 offen offset:0 sc1\n"
    "buffer_load_dwordx4 a[228:231], v76, s[40:43], s57 offen offset:1024 sc1\n"
    "buffer_load_dwordx4 a[232:235], v76, s[40:43], s57 offen offset:2048 sc1\n"
    "buffer_load_dwordx4 a[236:239], v76, s[40:43], s57 offen offset:3072 sc1\n"
    "buffer_load_dwordx4 a[240:243], v76, s[40:43], s58 offen offset:0 sc1\n"
    "buffer_load_dwordx4 a[244:247], v76, s[40:43], s58 offen offset:1024 sc1\n"
    "buffer_load_dwordx4 a[248:251], v76, s[40:43], s58 offen offset:2048 sc1\n"
    "buffer_load_dwordx4 a[252:255], v76, s[40:43], s58 offen offset:3072 sc1\n"
    "s_waitcnt vmcnt(0)\n"
    "v_mfma_f32_16x16x32_bf16 v[80:83], a[224:227], a[144:147], v[36:39]\n"
    "v_mfma_f32_16x16x32_bf16 v[80:83], a[228:231], a[148:151], v[80:83]\n"
    "v_mfma_f32_16x16x32_bf16 v[80:83], a[232:235], a[152:155], v[80:83]\n"
    "v_mfma_f32_16x16x32_bf16 v[80:83], a[236:239], a[156:159], v[80:83]\n"
    "v_mfma_f32_16x16x32_bf16 v[84:87], a[240:243], a[144:147], v[36:39]\n"
    "v_mfma_f32_16x16x32_bf16 v[84:87], a[244:247], a[148:151], v[84:87]\n"
    "v_mfma_f32_16x16x32_bf16 v[84:87], a[248:251], a[152:155], v[84:87]\n"
    "v_mfma_f32_16x16x32_bf16 v[84:87], a[252:255], a[156:159], v[84:87]\n"
    "s_nop 3\n"
    "v_max3_f32 v9, v9, v80, v81\n"
    "v_max3_f32 v9, v9, v82, v83\n"
    "s_nop 1\n"
    "v_max3_f32 v9, v9, v84, v85\n"
    "v_max3_f32 v9, v9, v86, v87\n"
    "s_add_u32 s57, s56, 0x6000\n"
    "s_add_u32 s58, s56, 0x7000\n"
    "buffer_load_dwordx4 a[224:227], v76, s[40:43], s57 offen offset:0 sc1\n"
    "buffer_load_dwordx4 a[228:231], v76, s[40:43], s57 offen offset:1024 sc1\n"
    "buffer_load_dwordx4 a[232:235], v76, s[40:43], s57 offen offset:2048 sc1\n"
    "buffer_load_dwordx4 a[236:239], v76, s[40:43], s57 offen offset:3072 sc1\n"
    "buffer_load_dwordx4 a[240:243], v76, s[40:43], s58 offen offset:0 sc1\n"
    "buffer_load_dwordx4 a[244:247], v76, s[40:43], s58 offen offset:1024 sc1\n"
    "buffer_load_dwordx4 a[248:251], v76, s[40:43], s58 offen offset:2048 sc1\n"
    "buffer_load_dwordx4 a[252:255], v76, s[40:43], s58 offen offset:3072 sc1\n"
    "s_waitcnt vmcnt(0)\n"
    "v_mfma_f32_16x16x32_bf16 v[80:83], a[224:227], a[144:147], v[36:39]\n"
    "v_mfma_f32_16x16x32_bf16 v[80:83], a[228:231], a[148:151], v[80:83]\n"
    "v_mfma_f32_16x16x32_bf16 v[80:83], a[232:235], a[152:155], v[80:83]\n"
    "v_mfma_f32_16x16x32_bf16 v[80:83], a[236:239], a[156:159], v[80:83]\n"
    "v_mfma_f32_16x16x32_bf16 v[84:87], a[240:243], a[144:147], v[36:39]\n"
    "v_mfma_f32_16x16x32_bf16 v[84:87], a[244:247], a[148:151], v[84:87]\n"
    "v_mfma_f32_16x16x32_bf16 v[84:87], a[248:251], a[152:155], v[84:87]\n"
    "v_mfma_f32_16x16x32_bf16 v[84:87], a[252:255], a[156:159], v[84:87]\n"
    "s_nop 3\n"
    "v_max3_f32 v9, v9, v80, v81\n"
    "v_max3_f32 v9, v9, v82, v83\n"
    "s_nop 1\n"
    "v_max3_f32 v9, v9, v84, v85\n"
    "v_max3_f32 v9, v9, v86, v87\n"
    "v_mov_b32 v10, v9\n"
    "s_nop 1\n"
    "v_permlane16_swap_b32 v10, v9\n"
    "v_max_f32 v9, v10, v9\n"
    "v_mov_b32 v10, v9\n"
    "s_nop 1\n"
    "v_permlane32_swap_b32 v10, v9\n"
    "v_max_f32 v9, v10, v9\n"
    "v_max_f32 v9, 0, v9\n"
    "v_sub_f32 v36, v36, v9\n"
    "v_sub_f32 v37, v37, v9\n"
    "v_sub_f32 v38, v38, v9\n"
    "v_sub_f32 v39, v39, v9\n"
    "s_add_u32 s57, s56, 0x0\n"
    "s_add_u32 s58, s56, 0x1000\n"
    "buffer_load_dwordx4 a[224:227], v76, s[40:43], s57 offen offset:0 sc1\n"
    "buffer_load_dwordx4 a[228:231], v76, s[40:43], s57 offen offset:1024 sc1\n"
    "buffer_load_dwordx4 a[232:235], v76, s[40:43], s57 offen offset:2048 sc1\n"
    "buffer_load_dwordx4 a[236:239], v76, s[40:43], s57 offen offset:3072 sc1\n"
    "buffer_load_dwordx4 a[240:243], v76, s[40:43], s58 offen offset:0 sc1\n"
    "buffer_load_dwordx4 a[244:247], v76, s[40:43], s58 offen offset:1024 sc1\n"
    "buffer_load_dwordx4 a[248:251], v76, s[40:43], s58 offen offset:2048 sc1\n"
    "buffer_load_dwordx4 a[252:255], v76, s[40:43], s58 offen offset:3072 sc1\n"
    "s_waitcnt vmcnt(0)\n"
    "v_mfma_f32_16x16x32_bf16 v[80:83], a[224:227], a[144:147], v[36:39]\n"
    "v_mfma_f32_16x16x32_bf16 v[80:83], a[228:231], a[148:151], v[80:83]\n"
    "v_mfma_f32_16x16x32_bf16 v[80:83], a[232:235], a[152:155], v[80:83]\n"
    "v_mfma_f32_16x16x32_bf16 v[80:83], a[236:239], a[156:159], v[80:83]\n"
    "v_mfma_f32_16x16x32_bf16 v[84:87], a[240:243], a[144:147], v[36:39]\n"
    "v_mfma_f32_16x16x32_bf16 v[84:87], a[244:247], a[148:151], v[84:87]\n"
    "v_mfma_f32_16x16x32_bf16 v[84:87], a[248:251], a[152:155], v[84:87]\n"
    "v_mfma_f32_16x16x32_bf16 v[84:87], a[252:255], a[156:159], v[84:87]\n"
    "s_nop 3\n"
    "v_exp_f32 v80, v80\n"
    "v_exp_f32 v81, v81\n"
    "v_exp_f32 v82, v82\n"
    "v_exp_f32 v83, v83\n"
    "v_exp_f32 v84, v84\n"
    "v_exp_f32 v85, v85\n"
    "v_exp_f32 v86, v86\n"
    "v_exp_f32 v87, v87\n"
    "v_cvt_pk_bf16_f32 v144, v80, v81\n"
    "v_cvt_pk_bf16_f32 v145, v82, v83\n"
    "v_cvt_pk_bf16_f32 v146, v84, v85\n"
    "v_cvt_pk_bf16_f32 v147, v86, v87\n"
    "s_add_u32 s57, s56, 0x2000\n"
    "s_add_u32 s58, s56, 0x3000\n"
    "buffer_load_dwordx4 a[224:227], v76, s[40:43], s57 offen offset:0 sc1\n"
    "buffer_load_dwordx4 a[228:231], v76, s[40:43], s57 offen offset:1024 sc1\n"
    "buffer_load_dwordx4 a[232:235], v76, s[40:43], s57 offen offset:2048 sc1\n"
    "buffer_load_dwordx4 a[236:239], v76, s[40:43], s57 offen offset:3072 sc1\n"
    "buffer_load_dwordx4 a[240:243], v76, s[40:43], s58 offen offset:0 sc1\n"
    "buffer_load_dwordx4 a[244:247], v76, s[40:43], s58 offen offset:1024 sc1\n"
    "buffer_load_dwordx4 a[248:251], v76, s[40:43], s58 offen offset:2048 sc1\n"
    "buffer_load_dwordx4 a[252:255], v76, s[40:43], s58 offen offset:3072 sc1\n"
    "s_waitcnt vmcnt(0)\n"
    "v_mfma_f32_16x16x32_bf16 v[80:83], a[224:227], a[144:147], v[36:39]\n"
    "v_mfma_f32_16x16x32_bf16 v[80:83], a[228:231], a[148:151], v[80:83]\n"
    "v_mfma_f32_16x16x32_bf16 v[80:83], a[232:235], a[152:155], v[80:83]\n"
    "v_mfma_f32_16x16x32_bf16 v[80:83], a[236:239], a[156:159], v[80:83]\n"
    "v_mfma_f32_16x16x32_bf16 v[84:87], a[240:243], a[144:147], v[36:39]\n"
    "v_mfma_f32_16x16x32_bf16 v[84:87], a[244:247], a[148:151], v[84:87]\n"
    "v_mfma_f32_16x16x32_bf16 v[84:87], a[248:251], a[152:155], v[84:87]\n"
    "v_mfma_f32_16x16x32_bf16 v[84:87], a[252:255], a[156:159], v[84:87]\n"
    "s_nop 3\n"
    "v_exp_f32 v80, v80\n"
    "v_exp_f32 v81, v81\n"
    "v_exp_f32 v82, v82\n"
    "v_exp_f32 v83, v83\n"
    "v_exp_f32 v84, v84\n"
    "v_exp_f32 v85, v85\n"
    "v_exp_f32 v86, v86\n"
    "v_exp_f32 v87, v87\n"
    "v_cvt_pk_bf16_f32 v148, v80, v81\n"
    "v_cvt_pk_bf16_f32 v149, v82, v83\n"
    "v_cvt_pk_bf16_f32 v150, v84, v85\n"
    "v_cvt_pk_bf16_f32 v151, v86, v87\n"
    "s_add_u32 s57, s56, 0x4000\n"
    "s_add_u32 s58, s56, 0x5000\n"
    "buffer_load_dwordx4 a[224:227], v76, s[40:43], s57 offen offset:0 sc1\n"
    "buffer_load_dwordx4 a[228:231], v76, s[40:43], s57 offen offset:1024 sc1\n"
    "buffer_load_dwordx4 a[232:235], v76, s[40:43], s57 offen offset:2048 sc1\n"
    "buffer_load_dwordx4 a[236:239], v76, s[40:43], s57 offen offset:3072 sc1\n"
    "buffer_load_dwordx4 a[240:243], v76, s[40:43], s58 offen offset:0 sc1\n"
    "buffer_load_dwordx4 a[244:247], v76, s[40:43], s58 offen offset:1024 sc1\n"
    "buffer_load_dwordx4 a[248:251], v76, s[40:43], s58 offen offset:2048 sc1\n"
    "buffer_load_dwordx4 a[252:255], v76, s[40:43], s58 offen offset:3072 sc1\n"
    "s_waitcnt vmcnt(0)\n"
    "v_mfma_f32_16x16x32_bf16 v[80:83], a[224:227], a[144:147], v[36:39]\n"
    "v_mfma_f32_16x16x32_bf16 v[80:83], a[228:231], a[148:151], v[80:83]\n"
    "v_mfma_f32_16x16x32_bf16 v[80:83], a[232:235], a[152:155], v[80:83]\n"
    "v_mfma_f32_16x16x32_bf16 v[80:83], a[236:239], a[156:159], v[80:83]\n"
    "v_mfma_f32_16x16x32_bf16 v[84:87], a[240:243], a[144:147], v[36:39]\n"
    "v_mfma_f32_16x16x32_bf16 v[84:87], a[244:247], a[148:151], v[84:87]\n"
    "v_mfma_f32_16x16x32_bf16 v[84:87], a[248:251], a[152:155], v[84:87]\n"
    "v_mfma_f32_16x16x32_bf16 v[84:87], a[252:255], a[156:159], v[84:87]\n"
    "s_nop 3\n"
    "v_exp_f32 v80, v80\n"
    "v_exp_f32 v81, v81\n"
    "v_exp_f32 v82, v82\n"
    "v_exp_f32 v83, v83\n"
    "v_exp_f32 v84, v84\n"
    "v_exp_f32 v85, v85\n"
    "v_exp_f32 v86, v86\n"
    "v_exp_f32 v87, v87\n"
    "v_cvt_pk_bf16_f32 v152, v80, v81\n"
    "v_cvt_pk_bf16_f32 v153, v82, v83\n"
    "v_cvt_pk_bf16_f32 v154, v84, v85\n"
    "v_cvt_pk_bf16_f32 v155, v86, v87\n"
    "s_add_u32 s57, s56, 0x6000\n"
    "s_add_u32 s58, s56, 0x7000\n"
    "buffer_load_dwordx4 a[224:227], v76, s[40:43], s57 offen offset:0 sc1\n"
    "buffer_load_dwordx4 a[228:231], v76, s[40:43], s57 offen offset:1024 sc1\n"
    "buffer_load_dwordx4 a[232:235], v76, s[40:43], s57 offen offset:2048 sc1\n"
    "buffer_load_dwordx4 a[236:239], v76, s[40:43], s57 offen offset:3072 sc1\n"
    "buffer_load_dwordx4 a[240:243], v76, s[40:43], s58 offen offset:0 sc1\n"
    "buffer_load_dwordx4 a[244:247], v76, s[40:43], s58 offen offset:1024 sc1\n"
    "buffer_load_dwordx4 a[248:251], v76, s[40:43], s58 offen offset:2048 sc1\n"
    "buffer_load_dwordx4 a[252:255], v76, s[40:43], s58 offen offset:3072 sc1\n"
    "s_waitcnt vmcnt(0)\n"
    "v_mfma_f32_16x16x32_bf16 v[80:83], a[224:227], a[144:147], v[36:39]\n"
    "v_mfma_f32_16x16x32_bf16 v[80:83], a[228:231], a[148:151], v[80:83]\n"
    "v_mfma_f32_16x16x32_bf16 v[80:83], a[232:235], a[152:155], v[80:83]\n"
    "v_mfma_f32_16x16x32_bf16 v[80:83], a[236:239], a[156:159], v[80:83]\n"
    "v_mfma_f32_16x16x32_bf16 v[84:87], a[240:243], a[144:147], v[36:39]\n"
    "v_mfma_f32_16x16x32_bf16 v[84:87], a[244:247], a[148:151], v[84:87]\n"
    "v_mfma_f32_16x16x32_bf16 v[84:87], a[248:251], a[152:155], v[84:87]\n"
    "v_mfma_f32_16x16x32_bf16 v[84:87], a[252:255], a[156:159], v[84:87]\n"
    "s_nop 3\n"
    "v_exp_f32 v80, v80\n"
    "v_exp_f32 v81, v81\n"
    "v_exp_f32 v82, v82\n"
    "v_exp_f32 v83, v83\n"
    "v_exp_f32 v84, v84\n"
    "v_exp_f32 v85, v85\n"
    "v_exp_f32 v86, v86\n"
    "v_exp_f32 v87, v87\n"
    "v_cvt_pk_bf16_f32 v156, v80, v81\n"
    "v_cvt_pk_bf16_f32 v157, v82, v83\n"
    "v_cvt_pk_bf16_f32 v158, v84, v85\n"
    "v_cvt_pk_bf16_f32 v159, v86, v87\n"
    "v_mfma_f32_16x16x32_bf16 v[52:55], v[64:67], v[144:147], 0\n"
    "v_mfma_f32_16x16x32_bf16 v[52:55], v[64:67], v[148:151], v[52:55]\n"
    "v_mfma_f32_16x16x32_bf16 v[52:55], v[64:67], v[152:155], v[52:55]\n"
    "v_mfma_f32_16x16x32_bf16 v[52:55], v[64:67], v[156:159], v[52:55]\n"
    "v_sub_f32 v20, v20, v9\n"
    "v_sub_f32 v21, v21, v9\n"
    "v_sub_f32 v22, v22, v9\n"
    "v_sub_f32 v23, v23, v9\n"
    "v_sub_f32 v11, 0, v9\n"
    "v_exp_f32 v11, v11\n"
    "s_nop 0\n"
    "v_mul_f32 v69, v11, v69\n"
    "ds_bpermute_b32 v12, v99, v11 offset:0\n"
    "ds_bpermute_b32 v13, v99, v11 offset:4\n"
    "ds_bpermute_b32 v14, v99, v11 offset:8\n"
    "ds_bpermute_b32 v15, v99, v11 offset:12\n"
    "s_waitcnt lgkmcnt(0)\n"
    "v_accvgpr_read_b32 v80, a4\n"
    "v_accvgpr_read_b32 v81, a5\n"
    "v_accvgpr_read_b32 v82, a6\n"
    "v_accvgpr_read_b32 v83, a7\n"
    "v_accvgpr_read_b32 v84, a20\n"
    "v_accvgpr_read_b32 v85, a21\n"
    "v_accvgpr_read_b32 v86, a22\n"
    "v_accvgpr_read_b32 v87, a23\n"
    "v_mul_f32 v80, v12, v80\n"
    "v_mul_f32 v81, v13, v81\n"
    "v_mul_f32 v82, v14, v82\n"
    "v_mul_f32 v83, v15, v83\n"
    "v_mul_f32 v84, v12, v84\n"
    "v_mul_f32 v85, v13, v85\n"
    "v_mul_f32 v86, v14, v86\n"
    "v_mul_f32 v87, v15, v87\n"
    "v_accvgpr_write_b32 a4, v80\n"
    "v_accvgpr_write_b32 a5, v81\n"
    "v_accvgpr_write_b32 a6, v82\n"
    "v_accvgpr_write_b32 a7, v83\n"
    "v_accvgpr_write_b32 a20, v84\n"
    "v_accvgpr_write_b32 a21, v85\n"
    "v_accvgpr_write_b32 a22, v86\n"
    "v_accvgpr_write_b32 a23, v87\n"
    "v_accvgpr_read_b32 v80, a36\n"
    "v_accvgpr_read_b32 v81, a37\n"
    "v_accvgpr_read_b32 v82, a38\n"
    "v_accvgpr_read_b32 v83, a39\n"
    "v_accvgpr_read_b32 v84, a52\n"
    "v_accvgpr_read_b32 v85, a53\n"
    "v_accvgpr_read_b32 v86, a54\n"
    "v_accvgpr_read_b32 v87, a55\n"
    "v_mul_f32 v80, v12, v80\n"
    "v_mul_f32 v81, v13, v81\n"
    "v_mul_f32 v82, v14, v82\n"
    "v_mul_f32 v83, v15, v83\n"
    "v_mul_f32 v84, v12, v84\n"
    "v_mul_f32 v85, v13, v85\n"
    "v_mul_f32 v86, v14, v86\n"
    "v_mul_f32 v87, v15, v87\n"
    "v_accvgpr_write_b32 a36, v80\n"
    "v_accvgpr_write_b32 a37, v81\n"
    "v_accvgpr_write_b32 a38, v82\n"
    "v_accvgpr_write_b32 a39, v83\n"
    "v_accvgpr_write_b32 a52, v84\n"
    "v_accvgpr_write_b32 a53, v85\n"
    "v_accvgpr_write_b32 a54, v86\n"
    "v_accvgpr_write_b32 a55, v87\n"
    "v_accvgpr_read_b32 v80, a68\n"
    "v_accvgpr_read_b32 v81, a69\n"
    "v_accvgpr_read_b32 v82, a70\n"
    "v_accvgpr_read_b32 v83, a71\n"
    "v_accvgpr_read_b32 v84, a84\n"
    "v_accvgpr_read_b32 v85, a85\n"
    "v_accvgpr_read_b32 v86, a86\n"
    "v_accvgpr_read_b32 v87, a87\n"
    "v_mul_f32 v80, v12, v80\n"
    "v_mul_f32 v81, v13, v81\n"
    "v_mul_f32 v82, v14, v82\n"
    "v_mul_f32 v83, v15, v83\n"
    "v_mul_f32 v84, v12, v84\n"
    "v_mul_f32 v85, v13, v85\n"
    "v_mul_f32 v86, v14, v86\n"
    "v_mul_f32 v87, v15, v87\n"
    "v_accvgpr_write_b32 a68, v80\n"
    "v_accvgpr_write_b32 a69, v81\n"
    "v_accvgpr_write_b32 a70, v82\n"
    "v_accvgpr_write_b32 a71, v83\n"
    "v_accvgpr_write_b32 a84, v84\n"
    "v_accvgpr_write_b32 a85, v85\n"
    "v_accvgpr_write_b32 a86, v86\n"
    "v_accvgpr_write_b32 a87, v87\n"
    "v_accvgpr_read_b32 v80, a100\n"
    "v_accvgpr_read_b32 v81, a101\n"
    "v_accvgpr_read_b32 v82, a102\n"
    "v_accvgpr_read_b32 v83, a103\n"
    "v_accvgpr_read_b32 v84, a116\n"
    "v_accvgpr_read_b32 v85, a117\n"
    "v_accvgpr_read_b32 v86, a118\n"
    "v_accvgpr_read_b32 v87, a119\n"
    "v_mul_f32 v80, v12, v80\n"
    "v_mul_f32 v81, v13, v81\n"
    "v_mul_f32 v82, v14, v82\n"
    "v_mul_f32 v83, v15, v83\n"
    "v_mul_f32 v84, v12, v84\n"
    "v_mul_f32 v85, v13, v85\n"
    "v_mul_f32 v86, v14, v86\n"
    "v_mul_f32 v87, v15, v87\n"
    "v_accvgpr_write_b32 a100, v80\n"
    "v_accvgpr_write_b32 a101, v81\n"
    "v_accvgpr_write_b32 a102, v82\n"
    "v_accvgpr_write_b32 a103, v83\n"
    "v_accvgpr_write_b32 a116, v84\n"
    "v_accvgpr_write_b32 a117, v85\n"
    "v_accvgpr_write_b32 a118, v86\n"
    "v_accvgpr_write_b32 a119, v87\n"
    "s_nop 7\n"
    "s_nop 7\n"
    "s_branch L_ret_A1\n"
    "L_rare_A2:\n"
    "s_nop 7\n"
    "s_nop 7\n"
    "s_sub_u32 s56, s48, 0x20000\n"
    "s_add_u32 s57, s56, 0x0\n"
    "s_add_u32 s58, s56, 0x1000\n"
    "buffer_load_dwordx4 a[224:227], v76, s[40:43], s57 offen offset:0 sc1\n"
    "buffer_load_dwordx4 a[228:231], v76, s[40:43], s57 offen offset:1024 sc1\n"
    "buffer_load_dwordx4 a[232:235], v76, s[40:43], s57 offen offset:2048 sc1\n"
    "buffer_load_dwordx4 a[236:239], v76, s[40:43], s57 offen offset:3072 sc1\n"
    "buffer_load_dwordx4 a[240:243], v76, s[40:43], s58 offen offset:0 sc1\n"
    "buffer_load_dwordx4 a[244:247], v76, s[40:43], s58 offen offset:1024 sc1\n"
    "buffer_load_dwordx4 a[248:251], v76, s[40:43], s58 offen offset:2048 sc1\n"
    "buffer_load_dwordx4 a[252:255], v76, s[40:43], s58 offen offset:3072 sc1\n"
    "s_waitcnt vmcnt(0)\n"
    "v_mfma_f32_16x16x32_bf16 v[80:83], a[224:227], a[160:163], v[40:43]\n"
    "v_mfma_f32_16x16x32_bf16 v[80:83], a[228:231], a[164:167], v[80:83]\n"
    "v_mfma_f32_16x16x32_bf16 v[80:83], a[232:235], a[168:171], v[80:83]\n"
    "v_mfma_f32_16x16x32_bf16 v[80:83], a[236:239], a[172:175], v[80:83]\n"
    "v_mfma_f32_16x16x32_bf16 v[84:87], a[240:243], a[160:163], v[40:43]\n"
    "v_mfma_f32_16x16x32_bf16 v[84:87], a[244:247], a[164:167], v[84:87]\n"
    "v_mfma_f32_16x16x32_bf16 v[84:87], a[248:251], a[168:171], v[84:87]\n"
    "v_mfma_f32_16x16x32_bf16 v[84:87], a[252:255], a[172:175], v[84:87]\n"
    "s_nop 3\n"
    "v_max_f32 v9, v80, v80\n"
    "v_max3_f32 v9, v9, v80, v81\n"
    "v_max3_f32 v9, v9, v82, v83\n"
    "s_nop 0\n"
    "v_max3_f32 v9, v9, v84, v85\n"
    "v_max3_f32 v9, v9, v86, v87\n"
    "s_add_u32 s57, s56, 0x2000\n"
    "s_add_u32 s58, s56, 0x3000\n"
    "buffer_load_dwordx4 a[224:227], v76, s[40:43], s57 offen offset:0 sc1\n"
    "buffer_load_dwordx4 a[228:231], v76, s[40:43], s57 offen offset:1024 sc1\n"
    "buffer_load_dwordx4 a[232:235], v76, s[40:43], s57 offen offset:2048 sc1\n"
    "buffer_load_dwordx4 a[236:239], v76, s[40:43], s57 offen offset:3072 sc1\n"
    "buffer_load_dwordx4 a[240:243], v76, s[40:43], s58 offen offset:0 sc1\n"
    "buffer_load_dwordx4 a[244:247], v76, s[40:43], s58 offen offset:1024 sc1\n"
    "buffer_load_dwordx4 a[248:251], v76, s[40:43], s58 offen offset:2048 sc1\n"
    "buffer_load_dwordx4 a[252:255], v76, s[40:43], s58 offen offset:3072 sc1\n"
    "s_waitcnt vmcnt(0)\n"
    "v_mfma_f32_16x16x32_bf16 v[80:83], a[224:227], a[160:163], v[40:43]\n"
    "v_mfma_f32_16x16x32_bf16 v[80:83], a[228:231], a[164:167], v[80:83]\n"
    "v_mfma_f32_16x16x32_bf16 v[80:83], a[232:235], a[168:171], v[80:83]\n"
    "v_mfma_f32_16x16x32_bf16 v[80:83], a[236:239], a[172:175], v[80:83]\n"
    "v_mfma_f32_16x16x32_bf16 v[84:87], a[240:243], a[160:163], v[40:43]\n"
    "v_mfma_f32_16x16x32_bf16 v[84:87], a[244:247], a[164:167], v[84:87]\n"
    "v_mfma_f32_16x16x32_bf16 v[84:87], a[248:251], a[168:171], v[84:87]\n"
    "v_mfma_f32_16x16x32_bf16 v[84:87], a[252:255], a[172:175], v[84:87]\n"
    "s_nop 3\n"
    "v_max3_f32 v9, v9, v80, v81\n"
    "v_max3_f32 v9, v9, v82, v83\n"
    "s_nop 1\n"
    "v_max3_f32 v9, v9, v84, v85\n"
    "v_max3_f32 v9, v9, v86, v87\n"
    "s_add_u32 s57, s56, 0x4000\n"
    "s_add_u32 s58, s56, 0x5000\n"
    "buffer_load_dwordx4 a[224:227], v76, s[40:43], s57 offen offset:0 sc1\n"
    "buffer_load_dwordx4 a[228:231], v76, s[40:43], s57 offen offset:1024 sc1\n"
    "buffer_load_dwordx4 a[232:235], v76, s[40:43], s57 offen offset:2048 sc1\n"
    "buffer_load_dwordx4 a[236:239], v76, s[40:43], s57 offen offset:3072 sc1\n"
    "buffer_load_dwordx4 a[240:243], v76, s[40:43], s58 offen offset:0 sc1\n"
    "buffer_load_dwordx4 a[244:247], v76, s[40:43], s58 offen offset:1024 sc1\n"
    "buffer_load_dwordx4 a[248:251], v76, s[40:43], s58 offen offset:2048 sc1\n"
    "buffer_load_dwordx4 a[252:255], v76, s[40:43], s58 offen offset:3072 sc1\n"
    "s_waitcnt vmcnt(0)\n"
    "v_mfma_f32_16x16x32_bf16 v[80:83], a[224:227], a[160:163], v[40:43]\n"
    "v_mfma_f32_16x16x32_bf16 v[80:83], a[228:231], a[164:167], v[80:83]\n"
    "v_mfma_f32_16x16x32_bf16 v[80:83], a[232:235], a[168:171], v[80:83]\n"
    "v_mfma_f32_16x16x32_bf16 v[80:83], a[236:239], a[172:175], v[80:83]\n"
    "v_mfma_f32_16x16x32_bf16 v[84:87], a[240:243], a[160:163], v[40:43]\n"
    "v_mfma_f32_16x16x32_bf16 v[84:87], a[244:247], a[164:167], v[84:87]\n"
    "v_mfma_f32_16x16x32_bf16 v[84:87], a[248:251], a[168:171], v[84:87]\n"
    "v_mfma_f32_16x16x32_bf16 v[84:87], a[252:255], a[172:175], v[84:87]\n"
    "s_nop 3\n"
    "v_max3_f32 v9, v9, v80, v81\n"
    "v_max3_f32 v9, v9, v82, v83\n"
    "s_nop 1\n"
    "v_max3_f32 v9, v9, v84, v85\n"
    "v_max3_f32 v9, v9, v86, v87\n"
    "s_add_u32 s57, s56, 0x6000\n"
    "s_add_u32 s58, s56, 0x7000\n"
    "buffer_load_dwordx4 a[224:227], v76, s[40:43], s57 offen offset:0 sc1\n"
    "buffer_load_dwordx4 a[228:231], v76, s[40:43], s57 offen offset:1024 sc1\n"
    "buffer_load_dwordx4 a[232:235], v76, s[40:43], s57 offen offset:2048 sc1\n"
    "buffer_load_dwordx4 a[236:239], v76, s[40:43], s57 offen offset:3072 sc1\n"
    "buffer_load_dwordx4 a[240:243], v76, s[40:43], s58 offen offset:0 sc1\n"
    "buffer_load_dwordx4 a[244:247], v76, s[40:43], s58 offen offset:1024 sc1\n"
    "buffer_load_dwordx4 a[248:251], v76, s[40:43], s58 offen offset:2048 sc1\n"
    "buffer_load_dwordx4 a[252:255], v76, s[40:43], s58 offen offset:3072 sc1\n"
    "s_waitcnt vmcnt(0)\n"
    "v_mfma_f32_16x16x32_bf16 v[80:83], a[224:227], a[160:163], v[40:43]\n"
    "v_mfma_f32_16x16x32_bf16 v[80:83], a[228:231], a[164:167], v[80:83]\n"
    "v_mfma_f32_16x16x32_bf16 v[80:83], a[232:235], a[168:171], v[80:83]\n"
    "v_mfma_f32_16x16x32_bf16 v[80:83], a[236:239], a[172:175], v[80:83]\n"
    "v_mfma_f32_16x16x32_bf16 v[84:87], a[240:243], a[160:163], v[40:43]\n"
    "v_mfma_f32_16x16x32_bf16 v[84:87], a[244:247], a[164:167], v[84:87]\n"
    "v_mfma_f32_16x16x32_bf16 v[84:87], a[248:251], a[168:171], v[84:87]\n"
    "v_mfma_f32_16x16x32_bf16 v[84:87], a[252:255], a[172:175], v[84:87]\n"
    "s_nop 3\n"
    "v_max3_f32 v9, v9, v80, v81\n"
    "v_max3_f32 v9, v9, v82, v83\n"
    "s_nop 1\n"
    "v_max3_f32 v9, v9, v84, v85\n"
    "v_max3_f32 v9, v9, v86, v87\n"
    "v_mov_b32 v10, v9\n"
    "s_nop 1\n"
    "v_permlane16_swap_b32 v10, v9\n"
    "v_max_f32 v9, v10, v9\n"
    "v_mov_b32 v10, v9\n"
    "s_nop 1\n"
    "v_permlane32_swap_b32 v10, v9\n"
    "v_max_f32 v9, v10, v9\n"
    "v_max_f32 v9, 0, v9\n"
    "v_sub_f32 v40, v40, v9\n"
    "v_sub_f32 v41, v41, v9\n"
    "v_sub_f32 v42, v42, v9\n"
    "v_sub_f32 v43, v43, v9\n"
    "s_add_u32 s57, s56, 0x0\n"
    "s_add_u32 s58, s56, 0x1000\n"
    "buffer_load_dwordx4 a[224:227], v76, s[40:43], s57 offen offset:0 sc1\n"
    "buffer_load_dwordx4 a[228:231], v76, s[40:43], s57 offen offset:1024 sc1\n"
    "buffer_load_dwordx4 a[232:235], v76, s[40:43], s57 offen offset:2048 sc1\n"
    "buffer_load_dwordx4 a[236:239], v76, s[40:43], s57 offen offset:3072 sc1\n"
    "buffer_load_dwordx4 a[240:243], v76, s[40:43], s58 offen offset:0 sc1\n"
    "buffer_load_dwordx4 a[244:247], v76, s[40:43], s58 offen offset:1024 sc1\n"
    "buffer_load_dwordx4 a[248:251], v76, s[40:43], s58 offen offset:2048 sc1\n"
    "buffer_load_dwordx4 a[252:255], v76, s[40:43], s58 offen offset:3072 sc1\n"
    "s_waitcnt vmcnt(0)\n"
    "v_mfma_f32_16x16x32_bf16 v[80:83], a[224:227], a[160:163], v[40:43]\n"
    "v_mfma_f32_16x16x32_bf16 v[80:83], a[228:231], a[164:167], v[80:83]\n"
    "v_mfma_f32_16x16x32_bf16 v[80:83], a[232:235], a[168:171], v[80:83]\n"
    "v_mfma_f32_16x16x32_bf16 v[80:83], a[236:239], a[172:175], v[80:83]\n"
    "v_mfma_f32_16x16x32_bf16 v[84:87], a[240:243], a[160:163], v[40:43]\n"
    "v_mfma_f32_16x16x32_bf16 v[84:87], a[244:247], a[164:167], v[84:87]\n"
    "v_mfma_f32_16x16x32_bf16 v[84:87], a[248:251], a[168:171], v[84:87]\n"
    "v_mfma_f32_16x16x32_bf16 v[84:87], a[252:255], a[172:175], v[84:87]\n"
    "s_nop 3\n"
    "v_exp_f32 v80, v80\n"
    "v_exp_f32 v81, v81\n"
    "v_exp_f32 v82, v82\n"
    "v_exp_f32 v83, v83\n"
    "v_exp_f32 v84, v84\n"
    "v_exp_f32 v85, v85\n"
    "v_exp_f32 v86, v86\n"
    "v_exp_f32 v87, v87\n"
    "v_cvt_pk_bf16_f32 v160, v80, v81\n"
    "v_cvt_pk_bf16_f32 v161, v82, v83\n"
    "v_cvt_pk_bf16_f32 v162, v84, v85\n"
    "v_cvt_pk_bf16_f32 v163, v86, v87\n"
    "s_add_u32 s57, s56, 0x2000\n"
    "s_add_u32 s58, s56, 0x3000\n"
    "buffer_load_dwordx4 a[224:227], v76, s[40:43], s57 offen offset:0 sc1\n"
    "buffer_load_dwordx4 a[228:231], v76, s[40:43], s57 offen offset:1024 sc1\n"
    "buffer_load_dwordx4 a[232:235], v76, s[40:43], s57 offen offset:2048 sc1\n"
    "buffer_load_dwordx4 a[236:239], v76, s[40:43], s57 offen offset:3072 sc1\n"
    "buffer_load_dwordx4 a[240:243], v76, s[40:43], s58 offen offset:0 sc1\n"
    "buffer_load_dwordx4 a[244:247], v76, s[40:43], s58 offen offset:1024 sc1\n"
    "buffer_load_dwordx4 a[248:251], v76, s[40:43], s58 offen offset:2048 sc1\n"
    "buffer_load_dwordx4 a[252:255], v76, s[40:43], s58 offen offset:3072 sc1\n"
    "s_waitcnt vmcnt(0)\n"
    "v_mfma_f32_16x16x32_bf16 v[80:83], a[224:227], a[160:163], v[40:43]\n"
    "v_mfma_f32_16x16x32_bf16 v[80:83], a[228:231], a[164:167], v[80:83]\n"
    "v_mfma_f32_16x16x32_bf16 v[80:83], a[232:235], a[168:171], v[80:83]\n"
    "v_mfma_f32_16x16x32_bf16 v[80:83], a[236:239], a[172:175], v[80:83]\n"
    "v_mfma_f32_16x16x32_bf16 v[84:87], a[240:243], a[160:163], v[40:43]\n"
    "v_mfma_f32_16x16x32_bf16 v[84:87], a[244:247], a[164:167], v[84:87]\n"
    "v_mfma_f32_16x16x32_bf16 v[84:87], a[248:251], a[168:171], v[84:87]\n"
    "v_mfma_f32_16x16x32_bf16 v[84:87], a[252:255], a[172:175], v[84:87]\n"
    "s_nop 3\n"
    "v_exp_f32 v80, v80\n"
    "v_exp_f32 v81, v81\n"
    "v_exp_f32 v82, v82\n"
    "v_exp_f32 v83, v83\n"
    "v_exp_f32 v84, v84\n"
    "v_exp_f32 v85, v85\n"
    "v_exp_f32 v86, v86\n"
    "v_exp_f32 v87, v87\n"
    "v_cvt_pk_bf16_f32 v164, v80, v81\n"
    "v_cvt_pk_bf16_f32 v165, v82, v83\n"
    "v_cvt_pk_bf16_f32 v166, v84, v85\n"
    "v_cvt_pk_bf16_f32 v167, v86, v87\n"
    "s_add_u32 s57, s56, 0x4000\n"
    "s_add_u32 s58, s56, 0x5000\n"
    "buffer_load_dwordx4 a[224:227], v76, s[40:43], s57 offen offset:0 sc1\n"
    "buffer_load_dwordx4 a[228:231], v76, s[40:43], s57 offen offset:1024 sc1\n"
    "buffer_load_dwordx4 a[232:235], v76, s[40:43], s57 offen offset:2048 sc1\n"
    "buffer_load_dwordx4 a[236:239], v76, s[40:43], s57 offen offset:3072 sc1\n"
    "buffer_load_dwordx4 a[240:243], v76, s[40:43], s58 offen offset:0 sc1\n"
    "buffer_load_dwordx4 a[244:247], v76, s[40:43], s58 offen offset:1024 sc1\n"
    "buffer_load_dwordx4 a[248:251], v76, s[40:43], s58 offen offset:2048 sc1\n"
    "buffer_load_dwordx4 a[252:255], v76, s[40:43], s58 offen offset:3072 sc1\n"
    "s_waitcnt vmcnt(0)\n"
    "v_mfma_f32_16x16x32_bf16 v[80:83], a[224:227], a[160:163], v[40:43]\n"
    "v_mfma_f32_16x16x32_bf16 v[80:83], a[228:231], a[164:167], v[80:83]\n"
    "v_mfma_f32_16x16x32_bf16 v[80:83], a[232:235], a[168:171], v[80:83]\n"
    "v_mfma_f32_16x16x32_bf16 v[80:83], a[236:239], a[172:175], v[80:83]\n"
    "v_mfma_f32_16x16x32_bf16 v[84:87], a[240:243], a[160:163], v[40:43]\n"
    "v_mfma_f32_16x16x32_bf16 v[84:87], a[244:247], a[164:167], v[84:87]\n"
    "v_mfma_f32_16x16x32_bf16 v[84:87], a[248:251], a[168:171], v[84:87]\n"
    "v_mfma_f32_16x16x32_bf16 v[84:87], a[252:255], a[172:175], v[84:87]\n"
    "s_nop 3\n"
    "v_exp_f32 v80, v80\n"
    "v_exp_f32 v81, v81\n"
    "v_exp_f32 v82, v82\n"
    "v_exp_f32 v83, v83\n"
    "v_exp_f32 v84, v84\n"
    "v_exp_f32 v85, v85\n"
    "v_exp_f32 v86, v86\n"
    "v_exp_f32 v87, v87\n"
    "v_cvt_pk_bf16_f32 v168, v80, v81\n"
    "v_cvt_pk_bf16_f32 v169, v82, v83\n"
    "v_cvt_pk_bf16_f32 v170, v84, v85\n"
    "v_cvt_pk_bf16_f32 v171, v86, v87\n"
    "s_add_u32 s57, s56, 0x6000\n"
    "s_add_u32 s58, s56, 0x7000\n"
    "buffer_load_dwordx4 a[224:227], v76, s[40:43], s57 offen offset:0 sc1\n"
    "buffer_load_dwordx4 a[228:231], v76, s[40:43], s57 offen offset:1024 sc1\n"
    "buffer_load_dwordx4 a[232:235], v76, s[40:43], s57 offen offset:2048 sc1\n"
    "buffer_load_dwordx4 a[236:239], v76, s[40:43], s57 offen offset:3072 sc1\n"
    "buffer_load_dwordx4 a[240:243], v76, s[40:43], s58 offen offset:0 sc1\n"
    "buffer_load_dwordx4 a[244:247], v76, s[40:43], s58 offen offset:1024 sc1\n"
    "buffer_load_dwordx4 a[248:251], v76, s[40:43], s58 offen offset:2048 sc1\n"
    "buffer_load_dwordx4 a[252:255], v76, s[40:43], s58 offen offset:3072 sc1\n"
    "s_waitcnt vmcnt(0)\n"
    "v_mfma_f32_16x16x32_bf16 v[80:83], a[224:227], a[160:163], v[40:43]\n"
    "v_mfma_f32_16x16x32_bf16 v[80:83], a[228:231], a[164:167], v[80:83]\n"
    "v_mfma_f32_16x16x32_bf16 v[80:83], a[232:235], a[168:171], v[80:83]\n"
    "v_mfma_f32_16x16x32_bf16 v[80:83], a[236:239], a[172:175], v[80:83]\n"
    "v_mfma_f32_16x16x32_bf16 v[84:87], a[240:243], a[160:163], v[40:43]\n"
    "v_mfma_f32_16x16x32_bf16 v[84:87], a[244:247], a[164:167], v[84:87]\n"
    "v_mfma_f32_16x16x32_bf16 v[84:87], a[248:251], a[168:171], v[84:87]\n"
    "v_mfma_f32_16x16x32_bf16 v[84:87], a[252:255], a[172:175], v[84:87]\n"
    "s_nop 3\n"
    "v_exp_f32 v80, v80\n"
    "v_exp_f32 v81, v81\n"
    "v_exp_f32 v82, v82\n"
    "v_exp_f32 v83, v83\n"
    "v_exp_f32 v84, v84\n"
    "v_exp_f32 v85, v85\n"
    "v_exp_f32 v86, v86\n"
    "v_exp_f32 v87, v87\n"
    "v_cvt_pk_bf16_f32 v172, v80, v81\n"
    "v_cvt_pk_bf16_f32 v173, v82, v83\n"
    "v_cvt_pk_bf16_f32 v174, v84, v85\n"
    "v_cvt_pk_bf16_f32 v175, v86, v87\n"
    "v_mfma_f32_16x16x32_bf16 v[56:59], v[64:67], v[160:163], 0\n"
    "v_mfma_f32_16x16x32_bf16 v[56:59], v[64:67], v[164:167], v[56:59]\n"
    "v_mfma_f32_16x16x32_bf16 v[56:59], v[64:67], v[168:171], v[56:59]\n"
    "v_mfma_f32_16x16x32_bf16 v[56:59], v[64:67], v[172:175], v[56:59]\n"
    "v_sub_f32 v24, v24, v9\n"
    "v_sub_f32 v25, v25, v9\n"
    "v_sub_f32 v26, v26, v9\n"
    "v_sub_f32 v27, v27, v9\n"
    "v_sub_f32 v11, 0, v9\n"
    "v_exp_f32 v11, v11\n"
    "s_nop 0\n"
    "v_mul_f32 v70, v11, v70\n"
    "ds_bpermute_b32 v12, v99, v11 offset:0\n"
    "ds_bpermute_b32 v13, v99, v11 offset:4\n"
    "ds_bpermute_b32 v14, v99, v11 offset:8\n"
    "ds_bpermute_b32 v15, v99, v11 offset:12\n"
    "s_waitcnt lgkmcnt(0)\n"
    "v_accvgpr_read_b32 v80, a8\n"
    "v_accvgpr_read_b32 v81, a9\n"
    "v_accvgpr_read_b32 v82, a10\n"
    "v_accvgpr_read_b32 v83, a11\n"
    "v_accvgpr_read_b32 v84, a24\n"
    "v_accvgpr_read_b32 v85, a25\n"
    "v_accvgpr_read_b32 v86, a26\n"
    "v_accvgpr_read_b32 v87, a27\n"
    "v_mul_f32 v80, v12, v80\n"
    "v_mul_f32 v81, v13, v81\n"
    "v_mul_f32 v82, v14, v82\n"
    "v_mul_f32 v83, v15, v83\n"
    "v_mul_f32 v84, v12, v84\n"
    "v_mul_f32 v85, v13, v85\n"
    "v_mul_f32 v86, v14, v86\n"
    "v_mul_f32 v87, v15, v87\n"
    "v_accvgpr_write_b32 a8, v80\n"
    "v_accvgpr_write_b32 a9, v81\n"
    "v_accvgpr_write_b32 a10, v82\n"
    "v_accvgpr_write_b32 a11, v83\n"
    "v_accvgpr_write_b32 a24, v84\n"
    "v_accvgpr_write_b32 a25, v85\n"
    "v_accvgpr_write_b32 a26, v86\n"
    "v_accvgpr_write_b32 a27, v87\n"
    "v_accvgpr_read_b32 v80, a40\n"
    "v_accvgpr_read_b32 v81, a41\n"
    "v_accvgpr_read_b32 v82, a42\n"
    "v_accvgpr_read_b32 v83, a43\n"
    "v_accvgpr_read_b32 v84, a56\n"
    "v_accvgpr_read_b32 v85, a57\n"
    "v_accvgpr_read_b32 v86, a58\n"
    "v_accvgpr_read_b32 v87, a59\n"
    "v_mul_f32 v80, v12, v80\n"
    "v_mul_f32 v81, v13, v81\n"
    "v_mul_f32 v82, v14, v82\n"
    "v_mul_f32 v83, v15, v83\n"
    "v_mul_f32 v84, v12, v84\n"
    "v_mul_f32 v85, v13, v85\n"
    "v_mul_f32 v86, v14, v86\n"
    "v_mul_f32 v87, v15, v87\n"
    "v_accvgpr_write_b32 a40, v80\n"
    "v_accvgpr_write_b32 a41, v81\n"
    "v_accvgpr_write_b32 a42, v82\n"
    "v_accvgpr_write_b32 a43, v83\n"
    "v_accvgpr_write_b32 a56, v84\n"
    "v_accvgpr_write_b32 a57, v85\n"
    "v_accvgpr_write_b32 a58, v86\n"
    "v_accvgpr_write_b32 a59, v87\n"
    "v_accvgpr_read_b32 v80, a72\n"
    "v_accvgpr_read_b32 v81, a73\n"
    "v_accvgpr_read_b32 v82, a74\n"
    "v_accvgpr_read_b32 v83, a75\n"
    "v_accvgpr_read_b32 v84, a88\n"
    "v_accvgpr_read_b32 v85, a89\n"
    "v_accvgpr_read_b32 v86, a90\n"
    "v_accvgpr_read_b32 v87, a91\n"
    "v_mul_f32 v80, v12, v80\n"
    "v_mul_f32 v81, v13, v81\n"
    "v_mul_f32 v82, v14, v82\n"
    "v_mul_f32 v83, v15, v83\n"
    "v_mul_f32 v84, v12, v84\n"
    "v_mul_f32 v85, v13, v85\n"
    "v_mul_f32 v86, v14, v86\n"
    "v_mul_f32 v87, v15, v87\n"
    "v_accvgpr_write_b32 a72, v80\n"
    "v_accvgpr_write_b32 a73, v81\n"
    "v_accvgpr_write_b32 a74, v82\n"
    "v_accvgpr_write_b32 a75, v83\n"
    "v_accvgpr_write_b32 a88, v84\n"
    "v_accvgpr_write_b32 a89, v85\n"
    "v_accvgpr_write_b32 a90, v86\n"
    "v_accvgpr_write_b32 a91, v87\n"
    "v_accvgpr_read_b32 v80, a104\n"
    "v_accvgpr_read_b32 v81, a105\n"
    "v_accvgpr_read_b32 v82, a106\n"
    "v_accvgpr_read_b32 v83, a107\n"
    "v_accvgpr_read_b32 v84, a120\n"
    "v_accvgpr_read_b32 v85, a121\n"
    "v_accvgpr_read_b32 v86, a122\n"
    "v_accvgpr_read_b32 v87, a123\n"
    "v_mul_f32 v80, v12, v80\n"
    "v_mul_f32 v81, v13, v81\n"
    "v_mul_f32 v82, v14, v82\n"
    "v_mul_f32 v83, v15, v83\n"
    "v_mul_f32 v84, v12, v84\n"
    "v_mul_f32 v85, v13, v85\n"
    "v_mul_f32 v86, v14, v86\n"
    "v_mul_f32 v87, v15, v87\n"
    "v_accvgpr_write_b32 a104, v80\n"
    "v_accvgpr_write_b32 a105, v81\n"
    "v_accvgpr_write_b32 a106, v82\n"
    "v_accvgpr_write_b32 a107, v83\n"
    "v_accvgpr_write_b32 a120, v84\n"
    "v_accvgpr_write_b32 a121, v85\n"
    "v_accvgpr_write_b32 a122, v86\n"
    "v_accvgpr_write_b32 a123, v87\n"
    "s_nop 7\n"
    "s_nop 7\n"
    "s_branch L_ret_A2\n"
    "L_rare_A3:\n"
    "s_nop 7\n"
    "s_nop 7\n"
    "s_sub_u32 s56, s48, 0x20000\n"
    "s_add_u32 s57, s56, 0x0\n"
    "s_add_u32 s58, s56, 0x1000\n"
    "buffer_load_dwordx4 a[224:227], v76, s[40:43], s57 offen offset:0 sc1\n"
    "buffer_load_dwordx4 a[228:231], v76, s[40:43], s57 offen offset:1024 sc1\n"
    "buffer_load_dwordx4 a[232:235], v76, s[40:43], s57 offen offset:2048 sc1\n"
    "buffer_load_dwordx4 a[236:239], v76, s[40:43], s57 offen offset:3072 sc1\n"
    "buffer_load_dwordx4 a[240:243], v76, s[40:43], s58 offen offset:0 sc1\n"
    "buffer_load_dwordx4 a[244:247], v76, s[40:43], s58 offen offset:1024 sc1\n"
    "buffer_load_dwordx4 a[248:251], v76, s[40:43], s58 offen offset:2048 sc1\n"
    "buffer_load_dwordx4 a[252:255], v76, s[40:43], s58 offen offset:3072 sc1\n"
    "s_waitcnt vmcnt(0)\n"
    "v_mfma_f32_16x16x32_bf16 v[80:83], a[224:227], a[176:179], v[44:47]\n"
    "v_mfma_f32_16x16x32_bf16 v[80:83], a[228:231], a[180:183], v[80:83]\n"
    "v_mfma_f32_16x16x32_bf16 v[80:83], a[232:235], a[184:187], v[80:83]\n"
    "v_mfma_f32_16x16x32_bf16 v[80:83], a[236:239], a[188:191], v[80:83]\n"
    "v_mfma_f32_16x16x32_bf16 v[84:87], a[240:243], a[176:179], v[44:47]\n"
    "v_mfma_f32_16x16x32_bf16 v[84:87], a[244:247], a[180:183], v[84:87]\n"
    "v_mfma_f32_16x16x32_bf16 v[84:87], a[248:251], a[184:187], v[84:87]\n"
    "v_mfma_f32_16x16x32_bf16 v[84:87], a[252:255], a[188:191], v[84:87]\n"
    "s_nop 3\n"
    "v_max_f32 v9, v80, v80\n"
    "v_max3_f32 v9, v9, v80, v81\n"
    "v_max3_f32 v9, v9, v82, v83\n"
    "s_nop 0\n"
    "v_max3_f32 v9, v9, v84, v85\n"
    "v_max3_f32 v9, v9, v86, v87\n"
    "s_add_u32 s57, s56, 0x2000\n"
    "s_add_u32 s58, s56, 0x3000\n"
    "buffer_load_dwordx4 a[224:227], v76, s[40:43], s57 offen offset:0 sc1\n"
    "buffer_load_dwordx4 a[228:231], v76, s[40:43], s57 offen offset:1024 sc1\n"
    "buffer_load_dwordx4 a[232:235], v76, s[40:43], s57 offen offset:2048 sc1\n"
    "buffer_load_dwordx4 a[236:239], v76, s[40:43], s57 offen offset:3072 sc1\n"
    "buffer_load_dwordx4 a[240:243], v76, s[40:43], s58 offen offset:0 sc1\n"
    "buffer_load_dwordx4 a[244:247], v76, s[40:43], s58 offen offset:1024 sc1\n"
    "buffer_load_dwordx4 a[248:251], v76, s[40:43], s58 offen offset:2048 sc1\n"
    "buffer_load_dwordx4 a[252:255], v76, s[40:43], s58 offen offset:3072 sc1\n"
    "s_waitcnt vmcnt(0)\n"
    "v_mfma_f32_16x16x32_bf16 v[80:83], a[224:227], a[176:179], v[44:47]\n"
    "v_mfma_f32_16x16x32_bf16 v[80:83], a[228:231], a[180:183], v[80:83]\n"
    "v_mfma_f32_16x16x32_bf16 v[80:83], a[232:235], a[184:187], v[80:83]\n"
    "v_mfma_f32_16x16x32_bf16 v[80:83], a[236:239], a[188:191], v[80:83]\n"
    "v_mfma_f32_16x16x32_bf16 v[84:87], a[240:243], a[176:179], v[44:47]\n"
    "v_mfma_f32_16x16x32_bf16 v[84:87], a[244:247], a[180:183], v[84:87]\n"
    "v_mfma_f32_16x16x32_bf16 v[84:87], a[248:251], a[184:187], v[84:87]\n"
    "v_mfma_f32_16x16x32_bf16 v[84:87], a[252:255], a[188:191], v[84:87]\n"
    "s_nop 3\n"
    "v_max3_f32 v9, v9, v80, v81\n"
    "v_max3_f32 v9, v9, v82, v83\n"
    "s_nop 1\n"
    "v_max3_f32 v9, v9, v84, v85\n"
    "v_max3_f32 v9, v9, v86, v87\n"
    "s_add_u32 s57, s56, 0x4000\n"
    "s_add_u32 s58, s56, 0x5000\n"
    "buffer_load_dwordx4 a[224:227], v76, s[40:43], s57 offen offset:0 sc1\n"
    "buffer_load_dwordx4 a[228:231], v76, s[40:43], s57 offen offset:1024 sc1\n"
    "buffer_load_dwordx4 a[232:235], v76, s[40:43], s57 offen offset:2048 sc1\n"
    "buffer_load_dwordx4 a[236:239], v76, s[40:43], s57 offen offset:3072 sc1\n"
    "buffer_load_dwordx4 a[240:243], v76, s[40:43], s58 offen offset:0 sc1\n"
    "buffer_load_dwordx4 a[244:247], v76, s[40:43], s58 offen offset:1024 sc1\n"
    "buffer_load_dwordx4 a[248:251], v76, s[40:43], s58 offen offset:2048 sc1\n"
    "buffer_load_dwordx4 a[252:255], v76, s[40:43], s58 offen offset:3072 sc1\n"
    "s_waitcnt vmcnt(0)\n"
    "v_mfma_f32_16x16x32_bf16 v[80:83], a[224:227], a[176:179], v[44:47]\n"
    "v_mfma_f32_16x16x32_bf16 v[80:83], a[228:231], a[180:183], v[80:83]\n"
    "v_mfma_f32_16x16x32_bf16 v[80:83], a[232:235], a[184:187], v[80:83]\n"
    "v_mfma_f32_16x16x32_bf16 v[80:83], a[236:239], a[188:191], v[80:83]\n"
    "v_mfma_f32_16x16x32_bf16 v[84:87], a[240:243], a[176:179], v[44:47]\n"
    "v_mfma_f32_16x16x32_bf16 v[84:87], a[244:247], a[180:183], v[84:87]\n"
    "v_mfma_f32_16x16x32_bf16 v[84:87], a[248:251], a[184:187], v[84:87]\n"
    "v_mfma_f32_16x16x32_bf16 v[84:87], a[252:255], a[188:191], v[84:87]\n"
    "s_nop 3\n"
    "v_max3_f32 v9, v9, v80, v81\n"
    "v_max3_f32 v9, v9, v82, v83\n"
    "s_nop 1\n"
    "v_max3_f32 v9, v9, v84, v85\n"
    "v_max3_f32 v9, v9, v86, v87\n"
    "s_add_u32 s57, s56, 0x6000\n"
    "s_add_u32 s58, s56, 0x7000\n"
    "buffer_load_dwordx4 a[224:227], v76, s[40:43], s57 offen offset:0 sc1\n"
    "buffer_load_dwordx4 a[228:231], v76, s[40:43], s57 offen offset:1024 sc1\n"
    "buffer_load_dwordx4 a[232:235], v76, s[40:43], s57 offen offset:2048 sc1\n"
    "buffer_load_dwordx4 a[236:239], v76, s[40:43], s57 offen offset:3072 sc1\n"
    "buffer_load_dwordx4 a[240:243], v76, s[40:43], s58 offen offset:0 sc1\n"
    "buffer_load_dwordx4 a[244:247], v76, s[40:43], s58 offen offset:1024 sc1\n"
    "buffer_load_dwordx4 a[248:251], v76, s[40:43], s58 offen offset:2048 sc1\n"
    "buffer_load_dwordx4 a[252:255], v76, s[40:43], s58 offen offset:3072 sc1\n"
    "s_waitcnt vmcnt(0)\n"
    "v_mfma_f32_16x16x32_bf16 v[80:83], a[224:227], a[176:179], v[44:47]\n"
    "v_mfma_f32_16x16x32_bf16 v[80:83], a[228:231], a[180:183], v[80:83]\n"
    "v_mfma_f32_16x16x32_bf16 v[80:83], a[232:235], a[184:187], v[80:83]\n"
    "v_mfma_f32_16x16x32_bf16 v[80:83], a[236:239], a[188:191], v[80:83]\n"
    "v_mfma_f32_16x16x32_bf16 v[84:87], a[240:243], a[176:179], v[44:47]\n"
    "v_mfma_f32_16x16x32_bf16 v[84:87], a[244:247], a[180:183], v[84:87]\n"
    "v_mfma_f32_16x16x32_bf16 v[84:87], a[248:251], a[184:187], v[84:87]\n"
    "v_mfma_f32_16x16x32_bf16 v[84:87], a[252:255], a[188:191], v[84:87]\n"
    "s_nop 3\n"
    "v_max3_f32 v9, v9, v80, v81\n"
    "v_max3_f32 v9, v9, v82, v83\n"
    "s_nop 1\n"
    "v_max3_f32 v9, v9, v84, v85\n"
    "v_max3_f32 v9, v9, v86, v87\n"
    "v_mov_b32 v10, v9\n"
    "s_nop 1\n"
    "v_permlane16_swap_b32 v10, v9\n"
    "v_max_f32 v9, v10, v9\n"
    "v_mov_b32 v10, v9\n"
    "s_nop 1\n"
    "v_permlane32_swap_b32 v10, v9\n"
    "v_max_f32 v9, v10, v9\n"
    "v_max_f32 v9, 0, v9\n"
    "v_sub_f32 v44, v44, v9\n"
    "v_sub_f32 v45, v45, v9\n"
    "v_sub_f32 v46, v46, v9\n"
    "v_sub_f32 v47, v47, v9\n"
    "s_add_u32 s57, s56, 0x0\n"
    "s_add_u32 s58, s56, 0x1000\n"
    "buffer_load_dwordx4 a[224:227], v76, s[40:43], s57 offen offset:0 sc1\n"
    "buffer_load_dwordx4 a[228:231], v76, s[40:43], s57 offen offset:1024 sc1\n"
    "buffer_load_dwordx4 a[232:235], v76, s[40:43], s57 offen offset:2048 sc1\n"
    "buffer_load_dwordx4 a[236:239], v76, s[40:43], s57 offen offset:3072 sc1\n"
    "buffer_load_dwordx4 a[240:243], v76, s[40:43], s58 offen offset:0 sc1\n"
    "buffer_load_dwordx4 a[244:247], v76, s[40:43], s58 offen offset:1024 sc1\n"
    "buffer_load_dwordx4 a[248:251], v76, s[40:43], s58 offen offset:2048 sc1\n"
    "buffer_load_dwordx4 a[252:255], v76, s[40:43], s58 offen offset:3072 sc1\n"
    "s_waitcnt vmcnt(0)\n"
    "v_mfma_f32_16x16x32_bf16 v[80:83], a[224:227], a[176:179], v[44:47]\n"
    "v_mfma_f32_16x16x32_bf16 v[80:83], a[228:231], a[180:183], v[80:83]\n"
    "v_mfma_f32_16x16x32_bf16 v[80:83], a[232:235], a[184:187], v[80:83]\n"
    "v_mfma_f32_16x16x32_bf16 v[80:83], a[236:239], a[188:191], v[80:83]\n"
    "v_mfma_f32_16x16x32_bf16 v[84:87], a[240:243], a[176:179], v[44:47]\n"
    "v_mfma_f32_16x16x32_bf16 v[84:87], a[244:247], a[180:183], v[84:87]\n"
    "v_mfma_f32_16x16x32_bf16 v[84:87], a[248:251], a[184:187], v[84:87]\n"
    "v_mfma_f32_16x16x32_bf16 v[84:87], a[252:255], a[188:191], v[84:87]\n"
    "s_nop 3\n"
    "v_exp_f32 v80, v80\n"
    "v_exp_f32 v81, v81\n"
    "v_exp_f32 v82, v82\n"
    "v_exp_f32 v83, v83\n"
    "v_exp_f32 v84, v84\n"
    "v_exp_f32 v85, v85\n"
    "v_exp_f32 v86, v86\n"
    "v_exp_f32 v87, v87\n"
    "v_cvt_pk_bf16_f32 v176, v80, v81\n"
    "v_cvt_pk_bf16_f32 v177, v82, v83\n"
    "v_cvt_pk_bf16_f32 v178, v84, v85\n"
    "v_cvt_pk_bf16_f32 v179, v86, v87\n"
    "s_add_u32 s57, s56, 0x2000\n"
    "s_add_u32 s58, s56, 0x3000\n"
    "buffer_load_dwordx4 a[224:227], v76, s[40:43], s57 offen offset:0 sc1\n"
    "buffer_load_dwordx4 a[228:231], v76, s[40:43], s57 offen offset:1024 sc1\n"
    "buffer_load_dwordx4 a[232:235], v76, s[40:43], s57 offen offset:2048 sc1\n"
    "buffer_load_dwordx4 a[236:239], v76, s[40:43], s57 offen offset:3072 sc1\n"
    "buffer_load_dwordx4 a[240:243], v76, s[40:43], s58 offen offset:0 sc1\n"
    "buffer_load_dwordx4 a[244:247], v76, s[40:43], s58 offen offset:1024 sc1\n"
    "buffer_load_dwordx4 a[248:251], v76, s[40:43], s58 offen offset:2048 sc1\n"
    "buffer_load_dwordx4 a[252:255], v76, s[40:43], s58 offen offset:3072 sc1\n"
    "s_waitcnt vmcnt(0)\n"
    "v_mfma_f32_16x16x32_bf16 v[80:83], a[224:227], a[176:179], v[44:47]\n"
    "v_mfma_f32_16x16x32_bf16 v[80:83], a[228:231], a[180:183], v[80:83]\n"
    "v_mfma_f32_16x16x32_bf16 v[80:83], a[232:235], a[184:187], v[80:83]\n"
    "v_mfma_f32_16x16x32_bf16 v[80:83], a[236:239], a[188:191], v[80:83]\n"
    "v_mfma_f32_16x16x32_bf16 v[84:87], a[240:243], a[176:179], v[44:47]\n"
    "v_mfma_f32_16x16x32_bf16 v[84:87], a[244:247], a[180:183], v[84:87]\n"
    "v_mfma_f32_16x16x32_bf16 v[84:87], a[248:251], a[184:187], v[84:87]\n"
    "v_mfma_f32_16x16x32_bf16 v[84:87], a[252:255], a[188:191], v[84:87]\n"
    "s_nop 3\n"
    "v_exp_f32 v80, v80\n"
    "v_exp_f32 v81, v81\n"
    "v_exp_f32 v82, v82\n"
    "v_exp_f32 v83, v83\n"
    "v_exp_f32 v84, v84\n"
    "v_exp_f32 v85, v85\n"
    "v_exp_f32 v86, v86\n"
    "v_exp_f32 v87, v87\n"
    "v_cvt_pk_bf16_f32 v180, v80, v81\n"
    "v_cvt_pk_bf16_f32 v181, v82, v83\n"
    "v_cvt_pk_bf16_f32 v182, v84, v85\n"
    "v_cvt_pk_bf16_f32 v183, v86, v87\n"
    "s_add_u32 s57, s56, 0x4000\n"
    "s_add_u32 s58, s56, 0x5000\n"
    "buffer_load_dwordx4 a[224:227], v76, s[40:43], s57 offen offset:0 sc1\n"
    "buffer_load_dwordx4 a[228:231], v76, s[40:43], s57 offen offset:1024 sc1\n"
    "buffer_load_dwordx4 a[232:235], v76, s[40:43], s57 offen offset:2048 sc1\n"
    "buffer_load_dwordx4 a[236:239], v76, s[40:43], s57 offen offset:3072 sc1\n"
    "buffer_load_dwordx4 a[240:243], v76, s[40:43], s58 offen offset:0 sc1\n"
    "buffer_load_dwordx4 a[244:247], v76, s[40:43], s58 offen offset:1024 sc1\n"
    "buffer_load_dwordx4 a[248:251], v76, s[40:43], s58 offen offset:2048 sc1\n"
    "buffer_load_dwordx4 a[252:255], v76, s[40:43], s58 offen offset:3072 sc1\n"
    "s_waitcnt vmcnt(0)\n"
    "v_mfma_f32_16x16x32_bf16 v[80:83], a[224:227], a[176:179], v[44:47]\n"
    "v_mfma_f32_16x16x32_bf16 v[80:83], a[228:231], a[180:183], v[80:83]\n"
    "v_mfma_f32_16x16x32_bf16 v[80:83], a[232:235], a[184:187], v[80:83]\n"
    "v_mfma_f32_16x16x32_bf16 v[80:83], a[236:239], a[188:191], v[80:83]\n"
    "v_mfma_f32_16x16x32_bf16 v[84:87], a[240:243], a[176:179], v[44:47]\n"
    "v_mfma_f32_16x16x32_bf16 v[84:87], a[244:247], a[180:183], v[84:87]\n"
    "v_mfma_f32_16x16x32_bf16 v[84:87], a[248:251], a[184:187], v[84:87]\n"
    "v_mfma_f32_16x16x32_bf16 v[84:87], a[252:255], a[188:191], v[84:87]\n"
    "s_nop 3\n"
    "v_exp_f32 v80, v80\n"
    "v_exp_f32 v81, v81\n"
    "v_exp_f32 v82, v82\n"
    "v_exp_f32 v83, v83\n"
    "v_exp_f32 v84, v84\n"
    "v_exp_f32 v85, v85\n"
    "v_exp_f32 v86, v86\n"
    "v_exp_f32 v87, v87\n"
    "v_cvt_pk_bf16_f32 v184, v80, v81\n"
    "v_cvt_pk_bf16_f32 v185, v82, v83\n"
    "v_cvt_pk_bf16_f32 v186, v84, v85\n"
    "v_cvt_pk_bf16_f32 v187, v86, v87\n"
    "s_add_u32 s57, s56, 0x6000\n"
    "s_add_u32 s58, s56, 0x7000\n"
    "buffer_load_dwordx4 a[224:227], v76, s[40:43], s57 offen offset:0 sc1\n"
    "buffer_load_dwordx4 a[228:231], v76, s[40:43], s57 offen offset:1024 sc1\n"
    "buffer_load_dwordx4 a[232:235], v76, s[40:43], s57 offen offset:2048 sc1\n"
    "buffer_load_dwordx4 a[236:239], v76, s[40:43], s57 offen offset:3072 sc1\n"
    "buffer_load_dwordx4 a[240:243], v76, s[40:43], s58 offen offset:0 sc1\n"
    "buffer_load_dwordx4 a[244:247], v76, s[40:43], s58 offen offset:1024 sc1\n"
    "buffer_load_dwordx4 a[248:251], v76, s[40:43], s58 offen offset:2048 sc1\n"
    "buffer_load_dwordx4 a[252:255], v76, s[40:43], s58 offen offset:3072 sc1\n"
    "s_waitcnt vmcnt(0)\n"
    "v_mfma_f32_16x16x32_bf16 v[80:83], a[224:227], a[176:179], v[44:47]\n"
    "v_mfma_f32_16x16x32_bf16 v[80:83], a[228:231], a[180:183], v[80:83]\n"
    "v_mfma_f32_16x16x32_bf16 v[80:83], a[232:235], a[184:187], v[80:83]\n"
    "v_mfma_f32_16x16x32_bf16 v[80:83], a[236:239], a[188:191], v[80:83]\n"
    "v_mfma_f32_16x16x32_bf16 v[84:87], a[240:243], a[176:179], v[44:47]\n"
    "v_mfma_f32_16x16x32_bf16 v[84:87], a[244:247], a[180:183], v[84:87]\n"
    "v_mfma_f32_16x16x32_bf16 v[84:87], a[248:251], a[184:187], v[84:87]\n"
    "v_mfma_f32_16x16x32_bf16 v[84:87], a[252:255], a[188:191], v[84:87]\n"
    "s_nop 3\n"
    "v_exp_f32 v80, v80\n"
    "v_exp_f32 v81, v81\n"
    "v_exp_f32 v82, v82\n"
    "v_exp_f32 v83, v83\n"
    "v_exp_f32 v84, v84\n"
    "v_exp_f32 v85, v85\n"
    "v_exp_f32 v86, v86\n"
    "v_exp_f32 v87, v87\n"
    "v_cvt_pk_bf16_f32 v188, v80, v81\n"
    "v_cvt_pk_bf16_f32 v189, v82, v83\n"
    "v_cvt_pk_bf16_f32 v190, v84, v85\n"
    "v_cvt_pk_bf16_f32 v191, v86, v87\n"
    "v_mfma_f32_16x16x32_bf16 v[60:63], v[64:67], v[176:179], 0\n"
    "v_mfma_f32_16x16x32_bf16 v[60:63], v[64:67], v[180:183], v[60:63]\n"
    "v_mfma_f32_16x16x32_bf16 v[60:63], v[64:67], v[184:187], v[60:63]\n"
    "v_mfma_f32_16x16x32_bf16 v[60:63], v[64:67], v[188:191], v[60:63]\n"
    "v_sub_f32 v28, v28, v9\n"
    "v_sub_f32 v29, v29, v9\n"
    "v_sub_f32 v30, v30, v9\n"
    "v_sub_f32 v31, v31, v9\n"
    "v_sub_f32 v11, 0, v9\n"
    "v_exp_f32 v11, v11\n"
    "s_nop 0\n"
    "v_mul_f32 v71, v11, v71\n"
    "ds_bpermute_b32 v12, v99, v11 offset:0\n"
    "ds_bpermute_b32 v13, v99, v11 offset:4\n"
    "ds_bpermute_b32 v14, v99, v11 offset:8\n"
    "ds_bpermute_b32 v15, v99, v11 offset:12\n"
    "s_waitcnt lgkmcnt(0)\n"
    "v_accvgpr_read_b32 v80, a12\n"
    "v_accvgpr_read_b32 v81, a13\n"
    "v_accvgpr_read_b32 v82, a14\n"
    "v_accvgpr_read_b32 v83, a15\n"
    "v_accvgpr_read_b32 v84, a28\n"
    "v_accvgpr_read_b32 v85, a29\n"
    "v_accvgpr_read_b32 v86, a30\n"
    "v_accvgpr_read_b32 v87, a31\n"
    "v_mul_f32 v80, v12, v80\n"
    "v_mul_f32 v81, v13, v81\n"
    "v_mul_f32 v82, v14, v82\n"
    "v_mul_f32 v83, v15, v83\n"
    "v_mul_f32 v84, v12, v84\n"
    "v_mul_f32 v85, v13, v85\n"
    "v_mul_f32 v86, v14, v86\n"
    "v_mul_f32 v87, v15, v87\n"
    "v_accvgpr_write_b32 a12, v80\n"
    "v_accvgpr_write_b32 a13, v81\n"
    "v_accvgpr_write_b32 a14, v82\n"
    "v_accvgpr_write_b32 a15, v83\n"
    "v_accvgpr_write_b32 a28, v84\n"
    "v_accvgpr_write_b32 a29, v85\n"
    "v_accvgpr_write_b32 a30, v86\n"
    "v_accvgpr_write_b32 a31, v87\n"
    "v_accvgpr_read_b32 v80, a44\n"
    "v_accvgpr_read_b32 v81, a45\n"
    "v_accvgpr_read_b32 v82, a46\n"
    "v_accvgpr_read_b32 v83, a47\n"
    "v_accvgpr_read_b32 v84, a60\n"
    "v_accvgpr_read_b32 v85, a61\n"
    "v_accvgpr_read_b32 v86, a62\n"
    "v_accvgpr_read_b32 v87, a63\n"
    "v_mul_f32 v80, v12, v80\n"
    "v_mul_f32 v81, v13, v81\n"
    "v_mul_f32 v82, v14, v82\n"
    "v_mul_f32 v83, v15, v83\n"
    "v_mul_f32 v84, v12, v84\n"
    "v_mul_f32 v85, v13, v85\n"
    "v_mul_f32 v86, v14, v86\n"
    "v_mul_f32 v87, v15, v87\n"
    "v_accvgpr_write_b32 a44, v80\n"
    "v_accvgpr_write_b32 a45, v81\n"
    "v_accvgpr_write_b32 a46, v82\n"
    "v_accvgpr_write_b32 a47, v83\n"
    "v_accvgpr_write_b32 a60, v84\n"
    "v_accvgpr_write_b32 a61, v85\n"
    "v_accvgpr_write_b32 a62, v86\n"
    "v_accvgpr_write_b32 a63, v87\n"
    "v_accvgpr_read_b32 v80, a76\n"
    "v_accvgpr_read_b32 v81, a77\n"
    "v_accvgpr_read_b32 v82, a78\n"
    "v_accvgpr_read_b32 v83, a79\n"
    "v_accvgpr_read_b32 v84, a92\n"
    "v_accvgpr_read_b32 v85, a93\n"
    "v_accvgpr_read_b32 v86, a94\n"
    "v_accvgpr_read_b32 v87, a95\n"
    "v_mul_f32 v80, v12, v80\n"
    "v_mul_f32 v81, v13, v81\n"
    "v_mul_f32 v82, v14, v82\n"
    "v_mul_f32 v83, v15, v83\n"
    "v_mul_f32 v84, v12, v84\n"
    "v_mul_f32 v85, v13, v85\n"
    "v_mul_f32 v86, v14, v86\n"
    "v_mul_f32 v87, v15, v87\n"
    "v_accvgpr_write_b32 a76, v80\n"
    "v_accvgpr_write_b32 a77, v81\n"
    "v_accvgpr_write_b32 a78, v82\n"
    "v_accvgpr_write_b32 a79, v83\n"
    "v_accvgpr_write_b32 a92, v84\n"
    "v_accvgpr_write_b32 a93, v85\n"
    "v_accvgpr_write_b32 a94, v86\n"
    "v_accvgpr_write_b32 a95, v87\n"
    "v_accvgpr_read_b32 v80, a108\n"
    "v_accvgpr_read_b32 v81, a109\n"
    "v_accvgpr_read_b32 v82, a110\n"
    "v_accvgpr_read_b32 v83, a111\n"
    "v_accvgpr_read_b32 v84, a124\n"
    "v_accvgpr_read_b32 v85, a125\n"
    "v_accvgpr_read_b32 v86, a126\n"
    "v_accvgpr_read_b32 v87, a127\n"
    "v_mul_f32 v80, v12, v80\n"
    "v_mul_f32 v81, v13, v81\n"
    "v_mul_f32 v82, v14, v82\n"
    "v_mul_f32 v83, v15, v83\n"
    "v_mul_f32 v84, v12, v84\n"
    "v_mul_f32 v85, v13, v85\n"
    "v_mul_f32 v86, v14, v86\n"
    "v_mul_f32 v87, v15, v87\n"
    "v_accvgpr_write_b32 a108, v80\n"
    "v_accvgpr_write_b32 a109, v81\n"
    "v_accvgpr_write_b32 a110, v82\n"
    "v_accvgpr_write_b32 a111, v83\n"
    "v_accvgpr_write_b32 a124, v84\n"
    "v_accvgpr_write_b32 a125, v85\n"
    "v_accvgpr_write_b32 a126, v86\n"
    "v_accvgpr_write_b32 a127, v87\n"
    "s_nop 7\n"
    "s_nop 7\n"
    "s_branch L_ret_A3\n"
    "L_rare_B0:\n"
    "s_nop 7\n"
    "s_nop 7\n"
    "s_sub_u32 s56, s48, 0x20000\n"
    "s_add_u32 s57, s56, 0x0\n"
    "s_add_u32 s58, s56, 0x1000\n"
    "buffer_load_dwordx4 a[224:227], v76, s[40:43], s57 offen offset:0 sc1\n"
    "buffer_load_dwordx4 a[228:231], v76, s[40:43], s57 offen offset:1024 sc1\n"
    "buffer_load_dwordx4 a[232:235], v76, s[40:43], s57 offen offset:2048 sc1\n"
    "buffer_load_dwordx4 a[236:239], v76, s[40:43], s57 offen offset:3072 sc1\n"
    "buffer_load_dwordx4 a[240:243], v76, s[40:43], s58 offen offset:0 sc1\n"
    "buffer_load_dwordx4 a[244:247], v76, s[40:43], s58 offen offset:1024 sc1\n"
    "buffer_load_dwordx4 a[248:251], v76, s[40:43], s58 offen offset:2048 sc1\n"
    "buffer_load_dwordx4 a[252:255], v76, s[40:43], s58 offen offset:3072 sc1\n"
    "s_waitcnt vmcnt(0)\n"
    "v_mfma_f32_16x16x32_bf16 v[80:83], a[224:227], a[128:131], v[32:35]\n"
    "v_mfma_f32_16x16x32_bf16 v[80:83], a[228:231], a[132:135], v[80:83]\n"
    "v_mfma_f32_16x16x32_bf16 v[80:83], a[232:235], a[136:139], v[80:83]\n"
    "v_mfma_f32_16x16x32_bf16 v[80:83], a[236:239], a[140:143], v[80:83]\n"
    "v_mfma_f32_16x16x32_bf16 v[84:87], a[240:243], a[128:131], v[32:35]\n"
    "v_mfma_f32_16x16x32_bf16 v[84:87], a[244:247], a[132:135], v[84:87]\n"
    "v_mfma_f32_16x16x32_bf16 v[84:87], a[248:251], a[136:139], v[84:87]\n"
    "v_mfma_f32_16x16x32_bf16 v[84:87], a[252:255], a[140:143], v[84:87]\n"
    "s_nop 3\n"
    "v_max_f32 v9, v80, v80\n"
    "v_max3_f32 v9, v9, v80, v81\n"
    "v_max3_f32 v9, v9, v82, v83\n"
    "s_nop 0\n"
    "v_max3_f32 v9, v9, v84, v85\n"
    "v_max3_f32 v9, v9, v86, v87\n"
    "s_add_u32 s57, s56, 0x2000\n"
    "s_add_u32 s58, s56, 0x3000\n"
    "buffer_load_dwordx4 a[224:227], v76, s[40:43], s57 offen offset:0 sc1\n"
    "buffer_load_dwordx4 a[228:231], v76, s[40:43], s57 offen offset:1024 sc1\n"
    "buffer_load_dwordx4 a[232:235], v76, s[40:43], s57 offen offset:2048 sc1\n"
    "buffer_load_dwordx4 a[236:239], v76, s[40:43], s57 offen offset:3072 sc1\n"
    "buffer_load_dwordx4 a[240:243], v76, s[40:43], s58 offen offset:0 sc1\n"
    "buffer_load_dwordx4 a[244:247], v76, s[40:43], s58 offen offset:1024 sc1\n"
    "buffer_load_dwordx4 a[248:251], v76, s[40:43], s58 offen offset:2048 sc1\n"
    "buffer_load_dwordx4 a[252:255], v76, s[40:43], s58 offen offset:3072 sc1\n"
    "s_waitcnt vmcnt(0)\n"
    "v_mfma_f32_16x16x32_bf16 v[80:83], a[224:227], a[128:131], v[32:35]\n"
    "v_mfma_f32_16x16x32_bf16 v[80:83], a[228:231], a[132:135], v[80:83]\n"
    "v_mfma_f32_16x16x32_bf16 v[80:83], a[232:235], a[136:139], v[80:83]\n"
    "v_mfma_f32_16x16x32_bf16 v[80:83], a[236:239], a[140:143], v[80:83]\n"
    "v_mfma_f32_16x16x32_bf16 v[84:87], a[240:243], a[128:131], v[32:35]\n"
    "v_mfma_f32_16x16x32_bf16 v[84:87], a[244:247], a[132:135], v[84:87]\n"
    "v_mfma_f32_16x16x32_bf16 v[84:87], a[248:251], a[136:139], v[84:87]\n"
    "v_mfma_f32_16x16x32_bf16 v[84:87], a[252:255], a[140:143], v[84:87]\n"
    "s_nop 3\n"
    "v_max3_f32 v9, v9, v80, v81\n"
    "v_max3_f32 v9, v9, v82, v83\n"
    "s_nop 1\n"
    "v_max3_f32 v9, v9, v84, v85\n"
    "v_max3_f32 v9, v9, v86, v87\n"
    "s_add_u32 s57, s56, 0x4000\n"
    "s_add_u32 s58, s56, 0x5000\n"
    "buffer_load_dwordx4 a[224:227], v76, s[40:43], s57 offen offset:0 sc1\n"
    "buffer_load_dwordx4 a[228:231], v76, s[40:43], s57 offen offset:1024 sc1\n"
    "buffer_load_dwordx4 a[232:235], v76, s[40:43], s57 offen offset:2048 sc1\n"
    "buffer_load_dwordx4 a[236:239], v76, s[40:43], s57 offen offset:3072 sc1\n"
    "buffer_load_dwordx4 a[240:243], v76, s[40:43], s58 offen offset:0 sc1\n"
    "buffer_load_dwordx4 a[244:247], v76, s[40:43], s58 offen offset:1024 sc1\n"
    "buffer_load_dwordx4 a[248:251], v76, s[40:43], s58 offen offset:2048 sc1\n"
    "buffer_load_dwordx4 a[252:255], v76, s[40:43], s58 offen offset:3072 sc1\n"
    "s_waitcnt vmcnt(0)\n"
    "v_mfma_f32_16x16x32_bf16 v[80:83], a[224:227], a[128:131], v[32:35]\n"
    "v_mfma_f32_16x16x32_bf16 v[80:83], a[228:231], a[132:135], v[80:83]\n"
    "v_mfma_f32_16x16x32_bf16 v[80:83], a[232:235], a[136:139], v[80:83]\n"
    "v_mfma_f32_16x16x32_bf16 v[80:83], a[236:239], a[140:143], v[80:83]\n"
    "v_mfma_f32_16x16x32_bf16 v[84:87], a[240:243], a[128:131], v[32:35]\n"
    "v_mfma_f32_16x16x32_bf16 v[84:87], a[244:247], a[132:135], v[84:87]\n"
    "v_mfma_f32_16x16x32_bf16 v[84:87], a[248:251], a[136:139], v[84:87]\n"
    "v_mfma_f32_16x16x32_bf16 v[84:87], a[252:255], a[140:143], v[84:87]\n"
    "s_nop 3\n"
    "v_max3_f32 v9, v9, v80, v81\n"
    "v_max3_f32 v9, v9, v82, v83\n"
    "s_nop 1\n"
    "v_max3_f32 v9, v9, v84, v85\n"
    "v_max3_f32 v9, v9, v86, v87\n"
    "s_add_u32 s57, s56, 0x6000\n"
    "s_add_u32 s58, s56, 0x7000\n"
    "buffer_load_dwordx4 a[224:227], v76, s[40:43], s57 offen offset:0 sc1\n"
    "buffer_load_dwordx4 a[228:231], v76, s[40:43], s57 offen offset:1024 sc1\n"
    "buffer_load_dwordx4 a[232:235], v76, s[40:43], s57 offen offset:2048 sc1\n"
    "buffer_load_dwordx4 a[236:239], v76, s[40:43], s57 offen offset:3072 sc1\n"
    "buffer_load_dwordx4 a[240:243], v76, s[40:43], s58 offen offset:0 sc1\n"
    "buffer_load_dwordx4 a[244:247], v76, s[40:43], s58 offen offset:1024 sc1\n"
    "buffer_load_dwordx4 a[248:251], v76, s[40:43], s58 offen offset:2048 sc1\n"
    "buffer_load_dwordx4 a[252:255], v76, s[40:43], s58 offen offset:3072 sc1\n"
    "s_waitcnt vmcnt(0)\n"
    "v_mfma_f32_16x16x32_bf16 v[80:83], a[224:227], a[128:131], v[32:35]\n"
    "v_mfma_f32_16x16x32_bf16 v[80:83], a[228:231], a[132:135], v[80:83]\n"
    "v_mfma_f32_16x16x32_bf16 v[80:83], a[232:235], a[136:139], v[80:83]\n"
    "v_mfma_f32_16x16x32_bf16 v[80:83], a[236:239], a[140:143], v[80:83]\n"
    "v_mfma_f32_16x16x32_bf16 v[84:87], a[240:243], a[128:131], v[32:35]\n"
    "v_mfma_f32_16x16x32_bf16 v[84:87], a[244:247], a[132:135], v[84:87]\n"
    "v_mfma_f32_16x16x32_bf16 v[84:87], a[248:251], a[136:139], v[84:87]\n"
    "v_mfma_f32_16x16x32_bf16 v[84:87], a[252:255], a[140:143], v[84:87]\n"
    "s_nop 3\n"
    "v_max3_f32 v9, v9, v80, v81\n"
    "v_max3_f32 v9, v9, v82, v83\n"
    "s_nop 1\n"
    "v_max3_f32 v9, v9, v84, v85\n"
    "v_max3_f32 v9, v9, v86, v87\n"
    "v_mov_b32 v10, v9\n"
    "s_nop 1\n"
    "v_permlane16_swap_b32 v10, v9\n"
    "v_max_f32 v9, v10, v9\n"
    "v_mov_b32 v10, v9\n"
    "s_nop 1\n"
    "v_permlane32_swap_b32 v10, v9\n"
    "v_max_f32 v9, v10, v9\n"
    "v_max_f32 v9, 0, v9\n"
    "v_sub_f32 v32, v32, v9\n"
    "v_sub_f32 v33, v33, v9\n"
    "v_sub_f32 v34, v34, v9\n"
    "v_sub_f32 v35, v35, v9\n"
    "s_add_u32 s57, s56, 0x0\n"
    "s_add_u32 s58, s56, 0x1000\n"
    "buffer_load_dwordx4 a[224:227], v76, s[40:43], s57 offen offset:0 sc1\n"
    "buffer_load_dwordx4 a[228:231], v76, s[40:43], s57 offen offset:1024 sc1\n"
    "buffer_load_dwordx4 a[232:235], v76, s[40:43], s57 offen offset:2048 sc1\n"
    "buffer_load_dwordx4 a[236:239], v76, s[40:43], s57 offen offset:3072 sc1\n"
    "buffer_load_dwordx4 a[240:243], v76, s[40:43], s58 offen offset:0 sc1\n"
    "buffer_load_dwordx4 a[244:247], v76, s[40:43], s58 offen offset:1024 sc1\n"
    "buffer_load_dwordx4 a[248:251], v76, s[40:43], s58 offen offset:2048 sc1\n"
    "buffer_load_dwordx4 a[252:255], v76, s[40:43], s58 offen offset:3072 sc1\n"
    "s_waitcnt vmcnt(0)\n"
    "v_mfma_f32_16x16x32_bf16 v[80:83], a[224:227], a[128:131], v[32:35]\n"
    "v_mfma_f32_16x16x32_bf16 v[80:83], a[228:231], a[132:135], v[80:83]\n"
    "v_mfma_f32_16x16x32_bf16 v[80:83], a[232:235], a[136:139], v[80:83]\n"
    "v_mfma_f32_16x16x32_bf16 v[80:83], a[236:239], a[140:143], v[80:83]\n"
    "v_mfma_f32_16x16x32_bf16 v[84:87], a[240:243], a[128:131], v[32:35]\n"
    "v_mfma_f32_16x16x32_bf16 v[84:87], a[244:247], a[132:135], v[84:87]\n"
    "v_mfma_f32_16x16x32_bf16 v[84:87], a[248:251], a[136:139], v[84:87]\n"
    "v_mfma_f32_16x16x32_bf16 v[84:87], a[252:255], a[140:143], v[84:87]\n"
    "s_nop 3\n"
    "v_exp_f32 v80, v80\n"
    "v_exp_f32 v81, v81\n"
    "v_exp_f32 v82, v82\n"
    "v_exp_f32 v83, v83\n"
    "v_exp_f32 v84, v84\n"
    "v_exp_f32 v85, v85\n"
    "v_exp_f32 v86, v86\n"
    "v_exp_f32 v87, v87\n"
    "v_cvt_pk_bf16_f32 v192, v80, v81\n"
    "v_cvt_pk_bf16_f32 v193, v82, v83\n"
    "v_cvt_pk_bf16_f32 v194, v84, v85\n"
    "v_cvt_pk_bf16_f32 v195, v86, v87\n"
    "s_add_u32 s57, s56, 0x2000\n"
    "s_add_u32 s58, s56, 0x3000\n"
    "buffer_load_dwordx4 a[224:227], v76, s[40:43], s57 offen offset:0 sc1\n"
    "buffer_load_dwordx4 a[228:231], v76, s[40:43], s57 offen offset:1024 sc1\n"
    "buffer_load_dwordx4 a[232:235], v76, s[40:43], s57 offen offset:2048 sc1\n"
    "buffer_load_dwordx4 a[236:239], v76, s[40:43], s57 offen offset:3072 sc1\n"
    "buffer_load_dwordx4 a[240:243], v76, s[40:43], s58 offen offset:0 sc1\n"
    "buffer_load_dwordx4 a[244:247], v76, s[40:43], s58 offen offset:1024 sc1\n"
    "buffer_load_dwordx4 a[248:251], v76, s[40:43], s58 offen offset:2048 sc1\n"
    "buffer_load_dwordx4 a[252:255], v76, s[40:43], s58 offen offset:3072 sc1\n"
    "s_waitcnt vmcnt(0)\n"
    "v_mfma_f32_16x16x32_bf16 v[80:83], a[224:227], a[128:131], v[32:35]\n"
    "v_mfma_f32_16x16x32_bf16 v[80:83], a[228:231], a[132:135], v[80:83]\n"
    "v_mfma_f32_16x16x32_bf16 v[80:83], a[232:235], a[136:139], v[80:83]\n"
    "v_mfma_f32_16x16x32_bf16 v[80:83], a[236:239], a[140:143], v[80:83]\n"
    "v_mfma_f32_16x16x32_bf16 v[84:87], a[240:243], a[128:131], v[32:35]\n"
    "v_mfma_f32_16x16x32_bf16 v[84:87], a[244:247], a[132:135], v[84:87]\n"
    "v_mfma_f32_16x16x32_bf16 v[84:87], a[248:251], a[136:139], v[84:87]\n"
    "v_mfma_f32_16x16x32_bf16 v[84:87], a[252:255], a[140:143], v[84:87]\n"
    "s_nop 3\n"
    "v_exp_f32 v80, v80\n"
    "v_exp_f32 v81, v81\n"
    "v_exp_f32 v82, v82\n"
    "v_exp_f32 v83, v83\n"
    "v_exp_f32 v84, v84\n"
    "v_exp_f32 v85, v85\n"
    "v_exp_f32 v86, v86\n"
    "v_exp_f32 v87, v87\n"
    "v_cvt_pk_bf16_f32 v196, v80, v81\n"
    "v_cvt_pk_bf16_f32 v197, v82, v83\n"
    "v_cvt_pk_bf16_f32 v198, v84, v85\n"
    "v_cvt_pk_bf16_f32 v199, v86, v87\n"
    "s_add_u32 s57, s56, 0x4000\n"
    "s_add_u32 s58, s56, 0x5000\n"
    "buffer_load_dwordx4 a[224:227], v76, s[40:43], s57 offen offset:0 sc1\n"
    "buffer_load_dwordx4 a[228:231], v76, s[40:43], s57 offen offset:1024 sc1\n"
    "buffer_load_dwordx4 a[232:235], v76, s[40:43], s57 offen offset:2048 sc1\n"
    "buffer_load_dwordx4 a[236:239], v76, s[40:43], s57 offen offset:3072 sc1\n"
    "buffer_load_dwordx4 a[240:243], v76, s[40:43], s58 offen offset:0 sc1\n"
    "buffer_load_dwordx4 a[244:247], v76, s[40:43], s58 offen offset:1024 sc1\n"
    "buffer_load_dwordx4 a[248:251], v76, s[40:43], s58 offen offset:2048 sc1\n"
    "buffer_load_dwordx4 a[252:255], v76, s[40:43], s58 offen offset:3072 sc1\n"
    "s_waitcnt vmcnt(0)\n"
    "v_mfma_f32_16x16x32_bf16 v[80:83], a[224:227], a[128:131], v[32:35]\n"
    "v_mfma_f32_16x16x32_bf16 v[80:83], a[228:231], a[132:135], v[80:83]\n"
    "v_mfma_f32_16x16x32_bf16 v[80:83], a[232:235], a[136:139], v[80:83]\n"
    "v_mfma_f32_16x16x32_bf16 v[80:83], a[236:239], a[140:143], v[80:83]\n"
    "v_mfma_f32_16x16x32_bf16 v[84:87], a[240:243], a[128:131], v[32:35]\n"
    "v_mfma_f32_16x16x32_bf16 v[84:87], a[244:247], a[132:135], v[84:87]\n"
    "v_mfma_f32_16x16x32_bf16 v[84:87], a[248:251], a[136:139], v[84:87]\n"
    "v_mfma_f32_16x16x32_bf16 v[84:87], a[252:255], a[140:143], v[84:87]\n"
    "s_nop 3\n"
    "v_exp_f32 v80, v80\n"
    "v_exp_f32 v81, v81\n"
    "v_exp_f32 v82, v82\n"
    "v_exp_f32 v83, v83\n"
    "v_exp_f32 v84, v84\n"
    "v_exp_f32 v85, v85\n"
    "v_exp_f32 v86, v86\n"
    "v_exp_f32 v87, v87\n"
    "v_cvt_pk_bf16_f32 v200, v80, v81\n"
    "v_cvt_pk_bf16_f32 v201, v82, v83\n"
    "v_cvt_pk_bf16_f32 v202, v84, v85\n"
    "v_cvt_pk_bf16_f32 v203, v86, v87\n"
    "s_add_u32 s57, s56, 0x6000\n"
    "s_add_u32 s58, s56, 0x7000\n"
    "buffer_load_dwordx4 a[224:227], v76, s[40:43], s57 offen offset:0 sc1\n"
    "buffer_load_dwordx4 a[228:231], v76, s[40:43], s57 offen offset:1024 sc1\n"
    "buffer_load_dwordx4 a[232:235], v76, s[40:43], s57 offen offset:2048 sc1\n"
    "buffer_load_dwordx4 a[236:239], v76, s[40:43], s57 offen offset:3072 sc1\n"
    "buffer_load_dwordx4 a[240:243], v76, s[40:43], s58 offen offset:0 sc1\n"
    "buffer_load_dwordx4 a[244:247], v76, s[40:43], s58 offen offset:1024 sc1\n"
    "buffer_load_dwordx4 a[248:251], v76, s[40:43], s58 offen offset:2048 sc1\n"
    "buffer_load_dwordx4 a[252:255], v76, s[40:43], s58 offen offset:3072 sc1\n"
    "s_waitcnt vmcnt(0)\n"
    "v_mfma_f32_16x16x32_bf16 v[80:83], a[224:227], a[128:131], v[32:35]\n"
    "v_mfma_f32_16x16x32_bf16 v[80:83], a[228:231], a[132:135], v[80:83]\n"
    "v_mfma_f32_16x16x32_bf16 v[80:83], a[232:235], a[136:139], v[80:83]\n"
    "v_mfma_f32_16x16x32_bf16 v[80:83], a[236:239], a[140:143], v[80:83]\n"
    "v_mfma_f32_16x16x32_bf16 v[84:87], a[240:243], a[128:131], v[32:35]\n"
    "v_mfma_f32_16x16x32_bf16 v[84:87], a[244:247], a[132:135], v[84:87]\n"
    "v_mfma_f32_16x16x32_bf16 v[84:87], a[248:251], a[136:139], v[84:87]\n"
    "v_mfma_f32_16x16x32_bf16 v[84:87], a[252:255], a[140:143], v[84:87]\n"
    "s_nop 3\n"
    "v_exp_f32 v80, v80\n"
    "v_exp_f32 v81, v81\n"
    "v_exp_f32 v82, v82\n"
    "v_exp_f32 v83, v83\n"
    "v_exp_f32 v84, v84\n"
    "v_exp_f32 v85, v85\n"
    "v_exp_f32 v86, v86\n"
    "v_exp_f32 v87, v87\n"
    "v_cvt_pk_bf16_f32 v204, v80, v81\n"
    "v_cvt_pk_bf16_f32 v205, v82, v83\n"
    "v_cvt_pk_bf16_f32 v206, v84, v85\n"
    "v_cvt_pk_bf16_f32 v207, v86, v87\n"
    "v_mfma_f32_16x16x32_bf16 v[48:51], v[64:67], v[192:195], 0\n"
    "v_mfma_f32_16x16x32_bf16 v[48:51], v[64:67], v[196:199], v[48:51]\n"
    "v_mfma_f32_16x16x32_bf16 v[48:51], v[64:67], v[200:203], v[48:51]\n"
    "v_mfma_f32_16x16x32_bf16 v[48:51], v[64:67], v[204:207], v[48:51]\n"
    "v_sub_f32 v16, v16, v9\n"
    "v_sub_f32 v17, v17, v9\n"
    "v_sub_f32 v18, v18, v9\n"
    "v_sub_f32 v19, v19, v9\n"
    "v_sub_f32 v11, 0, v9\n"
    "v_exp_f32 v11, v11\n"
    "s_nop 0\n"
    "v_mul_f32 v68, v11, v68\n"
    "ds_bpermute_b32 v12, v99, v11 offset:0\n"
    "ds_bpermute_b32 v13, v99, v11 offset:4\n"
    "ds_bpermute_b32 v14, v99, v11 offset:8\n"
    "ds_bpermute_b32 v15, v99, v11 offset:12\n"
    "s_waitcnt lgkmcnt(0)\n"
    "v_accvgpr_read_b32 v80, a0\n"
    "v_accvgpr_read_b32 v81, a1\n"
    "v_accvgpr_read_b32 v82, a2\n"
    "v_accvgpr_read_b32 v83, a3\n"
    "v_accvgpr_read_b32 v84, a16\n"
    "v_accvgpr_read_b32 v85, a17\n"
    "v_accvgpr_read_b32 v86, a18\n"
    "v_accvgpr_read_b32 v87, a19\n"
    "v_mul_f32 v80, v12, v80\n"
    "v_mul_f32 v81, v13, v81\n"
    "v_mul_f32 v82, v14, v82\n"
    "v_mul_f32 v83, v15, v83\n"
    "v_mul_f32 v84, v12, v84\n"
    "v_mul_f32 v85, v13, v85\n"
    "v_mul_f32 v86, v14, v86\n"
    "v_mul_f32 v87, v15, v87\n"
    "v_accvgpr_write_b32 a0, v80\n"
    "v_accvgpr_write_b32 a1, v81\n"
    "v_accvgpr_write_b32 a2, v82\n"
    "v_accvgpr_write_b32 a3, v83\n"
    "v_accvgpr_write_b32 a16, v84\n"
    "v_accvgpr_write_b32 a17, v85\n"
    "v_accvgpr_write_b32 a18, v86\n"
    "v_accvgpr_write_b32 a19, v87\n"
    "v_accvgpr_read_b32 v80, a32\n"
    "v_accvgpr_read_b32 v81, a33\n"
    "v_accvgpr_read_b32 v82, a34\n"
    "v_accvgpr_read_b32 v83, a35\n"
    "v_accvgpr_read_b32 v84, a48\n"
    "v_accvgpr_read_b32 v85, a49\n"
    "v_accvgpr_read_b32 v86, a50\n"
    "v_accvgpr_read_b32 v87, a51\n"
    "v_mul_f32 v80, v12, v80\n"
    "v_mul_f32 v81, v13, v81\n"
    "v_mul_f32 v82, v14, v82\n"
    "v_mul_f32 v83, v15, v83\n"
    "v_mul_f32 v84, v12, v84\n"
    "v_mul_f32 v85, v13, v85\n"
    "v_mul_f32 v86, v14, v86\n"
    "v_mul_f32 v87, v15, v87\n"
    "v_accvgpr_write_b32 a32, v80\n"
    "v_accvgpr_write_b32 a33, v81\n"
    "v_accvgpr_write_b32 a34, v82\n"
    "v_accvgpr_write_b32 a35, v83\n"
    "v_accvgpr_write_b32 a48, v84\n"
    "v_accvgpr_write_b32 a49, v85\n"
    "v_accvgpr_write_b32 a50, v86\n"
    "v_accvgpr_write_b32 a51, v87\n"
    "v_accvgpr_read_b32 v80, a64\n"
    "v_accvgpr_read_b32 v81, a65\n"
    "v_accvgpr_read_b32 v82, a66\n"
    "v_accvgpr_read_b32 v83, a67\n"
    "v_accvgpr_read_b32 v84, a80\n"
    "v_accvgpr_read_b32 v85, a81\n"
    "v_accvgpr_read_b32 v86, a82\n"
    "v_accvgpr_read_b32 v87, a83\n"
    "v_mul_f32 v80, v12, v80\n"
    "v_mul_f32 v81, v13, v81\n"
    "v_mul_f32 v82, v14, v82\n"
    "v_mul_f32 v83, v15, v83\n"
    "v_mul_f32 v84, v12, v84\n"
    "v_mul_f32 v85, v13, v85\n"
    "v_mul_f32 v86, v14, v86\n"
    "v_mul_f32 v87, v15, v87\n"
    "v_accvgpr_write_b32 a64, v80\n"
    "v_accvgpr_write_b32 a65, v81\n"
    "v_accvgpr_write_b32 a66, v82\n"
    "v_accvgpr_write_b32 a67, v83\n"
    "v_accvgpr_write_b32 a80, v84\n"
    "v_accvgpr_write_b32 a81, v85\n"
    "v_accvgpr_write_b32 a82, v86\n"
    "v_accvgpr_write_b32 a83, v87\n"
    "v_accvgpr_read_b32 v80, a96\n"
    "v_accvgpr_read_b32 v81, a97\n"
    "v_accvgpr_read_b32 v82, a98\n"
    "v_accvgpr_read_b32 v83, a99\n"
    "v_accvgpr_read_b32 v84, a112\n"
    "v_accvgpr_read_b32 v85, a113\n"
    "v_accvgpr_read_b32 v86, a114\n"
    "v_accvgpr_read_b32 v87, a115\n"
    "v_mul_f32 v80, v12, v80\n"
    "v_mul_f32 v81, v13, v81\n"
    "v_mul_f32 v82, v14, v82\n"
    "v_mul_f32 v83, v15, v83\n"
    "v_mul_f32 v84, v12, v84\n"
    "v_mul_f32 v85, v13, v85\n"
    "v_mul_f32 v86, v14, v86\n"
    "v_mul_f32 v87, v15, v87\n"
    "v_accvgpr_write_b32 a96, v80\n"
    "v_accvgpr_write_b32 a97, v81\n"
    "v_accvgpr_write_b32 a98, v82\n"
    "v_accvgpr_write_b32 a99, v83\n"
    "v_accvgpr_write_b32 a112, v84\n"
    "v_accvgpr_write_b32 a113, v85\n"
    "v_accvgpr_write_b32 a114, v86\n"
    "v_accvgpr_write_b32 a115, v87\n"
    "s_nop 7\n"
    "s_nop 7\n"
    "s_branch L_ret_B0\n"
    "L_rare_B1:\n"
    "s_nop 7\n"
    "s_nop 7\n"
    "s_sub_u32 s56, s48, 0x20000\n"
    "s_add_u32 s57, s56, 0x0\n"
    "s_add_u32 s58, s56, 0x1000\n"
    "buffer_load_dwordx4 a[224:227], v76, s[40:43], s57 offen offset:0 sc1\n"
    "buffer_load_dwordx4 a[228:231], v76, s[40:43], s57 offen offset:1024 sc1\n"
    "buffer_load_dwordx4 a[232:235], v76, s[40:43], s57 offen offset:2048 sc1\n"
    "buffer_load_dwordx4 a[236:239], v76, s[40:43], s57 offen offset:3072 sc1\n"
    "buffer_load_dwordx4 a[240:243], v76, s[40:43], s58 offen offset:0 sc1\n"
    "buffer_load_dwordx4 a[244:247], v76, s[40:43], s58 offen offset:1024 sc1\n"
    "buffer_load_dwordx4 a[248:251], v76, s[40:43], s58 offen offset:2048 sc1\n"
    "buffer_load_dwordx4 a[252:255], v76, s[40:43], s58 offen offset:3072 sc1\n"
    "s_waitcnt vmcnt(0)\n"
    "v_mfma_f32_16x16x32_bf16 v[80:83], a[224:227], a[144:147], v[36:39]\n"
    "v_mfma_f32_16x16x32_bf16 v[80:83], a[228:231], a[148:151], v[80:83]\n"
    "v_mfma_f32_16x16x32_bf16 v[80:83], a[232:235], a[152:155], v[80:83]\n"
    "v_mfma_f32_16x16x32_bf16 v[80:83], a[236:239], a[156:159], v[80:83]\n"
    "v_mfma_f32_16x16x32_bf16 v[84:87], a[240:243], a[144:147], v[36:39]\n"
    "v_mfma_f32_16x16x32_bf16 v[84:87], a[244:247], a[148:151], v[84:87]\n"
    "v_mfma_f32_16x16x32_bf16 v[84:87], a[248:251], a[152:155], v[84:87]\n"
    "v_mfma_f32_16x16x32_bf16 v[84:87], a[252:255], a[156:159], v[84:87]\n"
    "s_nop 3\n"
    "v_max_f32 v9, v80, v80\n"
    "v_max3_f32 v9, v9, v80, v81\n"
    "v_max3_f32 v9, v9, v82, v83\n"
    "s_nop 0\n"
    "v_max3_f32 v9, v9, v84, v85\n"
    "v_max3_f32 v9, v9, v86, v87\n"
    "s_add_u32 s57, s56, 0x2000\n"
    "s_add_u32 s58, s56, 0x3000\n"
    "buffer_load_dwordx4 a[224:227], v76, s[40:43], s57 offen offset:0 sc1\n"
    "buffer_load_dwordx4 a[228:231], v76, s[40:43], s57 offen offset:1024 sc1\n"
    "buffer_load_dwordx4 a[232:235], v76, s[40:43], s57 offen offset:2048 sc1\n"
    "buffer_load_dwordx4 a[236:239], v76, s[40:43], s57 offen offset:3072 sc1\n"
    "buffer_load_dwordx4 a[240:243], v76, s[40:43], s58 offen offset:0 sc1\n"
    "buffer_load_dwordx4 a[244:247], v76, s[40:43], s58 offen offset:1024 sc1\n"
    "buffer_load_dwordx4 a[248:251], v76, s[40:43], s58 offen offset:2048 sc1\n"
    "buffer_load_dwordx4 a[252:255], v76, s[40:43], s58 offen offset:3072 sc1\n"
    "s_waitcnt vmcnt(0)\n"
    "v_mfma_f32_16x16x32_bf16 v[80:83], a[224:227], a[144:147], v[36:39]\n"
    "v_mfma_f32_16x16x32_bf16 v[80:83], a[228:231], a[148:151], v[80:83]\n"
    "v_mfma_f32_16x16x32_bf16 v[80:83], a[232:235], a[152:155], v[80:83]\n"
    "v_mfma_f32_16x16x32_bf16 v[80:83], a[236:239], a[156:159], v[80:83]\n"
    "v_mfma_f32_16x16x32_bf16 v[84:87], a[240:243], a[144:147], v[36:39]\n"
    "v_mfma_f32_16x16x32_bf16 v[84:87], a[244:247], a[148:151], v[84:87]\n"
    "v_mfma_f32_16x16x32_bf16 v[84:87], a[248:251], a[152:155], v[84:87]\n"
    "v_mfma_f32_16x16x32_bf16 v[84:87], a[252:255], a[156:159], v[84:87]\n"
    "s_nop 3\n"
    "v_max3_f32 v9, v9, v80, v81\n"
    "v_max3_f32 v9, v9, v82, v83\n"
    "s_nop 1\n"
    "v_max3_f32 v9, v9, v84, v85\n"
    "v_max3_f32 v9, v9, v86, v87\n"
    "s_add_u32 s57, s56, 0x4000\n"
    "s_add_u32 s58, s56, 0x5000\n"
    "buffer_load_dwordx4 a[224:227], v76, s[40:43], s57 offen offset:0 sc1\n"
    "buffer_load_dwordx4 a[228:231], v76, s[40:43], s57 offen offset:1024 sc1\n"
    "buffer_load_dwordx4 a[232:235], v76, s[40:43], s57 offen offset:2048 sc1\n"
    "buffer_load_dwordx4 a[236:239], v76, s[40:43], s57 offen offset:3072 sc1\n"
    "buffer_load_dwordx4 a[240:243], v76, s[40:43], s58 offen offset:0 sc1\n"
    "buffer_load_dwordx4 a[244:247], v76, s[40:43], s58 offen offset:1024 sc1\n"
    "buffer_load_dwordx4 a[248:251], v76, s[40:43], s58 offen offset:2048 sc1\n"
    "buffer_load_dwordx4 a[252:255], v76, s[40:43], s58 offen offset:3072 sc1\n"
    "s_waitcnt vmcnt(0)\n"
    "v_mfma_f32_16x16x32_bf16 v[80:83], a[224:227], a[144:147], v[36:39]\n"
    "v_mfma_f32_16x16x32_bf16 v[80:83], a[228:231], a[148:151], v[80:83]\n"
    "v_mfma_f32_16x16x32_bf16 v[80:83], a[232:235], a[152:155], v[80:83]\n"
    "v_mfma_f32_16x16x32_bf16 v[80:83], a[236:239], a[156:159], v[80:83]\n"
    "v_mfma_f32_16x16x32_bf16 v[84:87], a[240:243], a[144:147], v[36:39]\n"
    "v_mfma_f32_16x16x32_bf16 v[84:87], a[244:247], a[148:151], v[84:87]\n"
    "v_mfma_f32_16x16x32_bf16 v[84:87], a[248:251], a[152:155], v[84:87]\n"
    "v_mfma_f32_16x16x32_bf16 v[84:87], a[252:255], a[156:159], v[84:87]\n"
    "s_nop 3\n"
    "v_max3_f32 v9, v9, v80, v81\n"
    "v_max3_f32 v9, v9, v82, v83\n"
    "s_nop 1\n"
    "v_max3_f32 v9, v9, v84, v85\n"
    "v_max3_f32 v9, v9, v86, v87\n"
    "s_add_u32 s57, s56, 0x6000\n"
    "s_add_u32 s58, s56, 0x7000\n"
    "buffer_load_dwordx4 a[224:227], v76, s[40:43], s57 offen offset:0 sc1\n"
    "buffer_load_dwordx4 a[228:231], v76, s[40:43], s57 offen offset:1024 sc1\n"
    "buffer_load_dwordx4 a[232:235], v76, s[40:43], s57 offen offset:2048 sc1\n"
    "buffer_load_dwordx4 a[236:239], v76, s[40:43], s57 offen offset:3072 sc1\n"
    "buffer_load_dwordx4 a[240:243], v76, s[40:43], s58 offen offset:0 sc1\n"
    "buffer_load_dwordx4 a[244:247], v76, s[40:43], s58 offen offset:1024 sc1\n"
    "buffer_load_dwordx4 a[248:251], v76, s[40:43], s58 offen offset:2048 sc1\n"
    "buffer_load_dwordx4 a[252:255], v76, s[40:43], s58 offen offset:3072 sc1\n"
    "s_waitcnt vmcnt(0)\n"
    "v_mfma_f32_16x16x32_bf16 v[80:83], a[224:227], a[144:147], v[36:39]\n"
    "v_mfma_f32_16x16x32_bf16 v[80:83], a[228:231], a[148:151], v[80:83]\n"
    "v_mfma_f32_16x16x32_bf16 v[80:83], a[232:235], a[152:155], v[80:83]\n"
    "v_mfma_f32_16x16x32_bf16 v[80:83], a[236:239], a[156:159], v[80:83]\n"
    "v_mfma_f32_16x16x32_bf16 v[84:87], a[240:243], a[144:147], v[36:39]\n"
    "v_mfma_f32_16x16x32_bf16 v[84:87], a[244:247], a[148:151], v[84:87]\n"
    "v_mfma_f32_16x16x32_bf16 v[84:87], a[248:251], a[152:155], v[84:87]\n"
    "v_mfma_f32_16x16x32_bf16 v[84:87], a[252:255], a[156:159], v[84:87]\n"
    "s_nop 3\n"
    "v_max3_f32 v9, v9, v80, v81\n"
    "v_max3_f32 v9, v9, v82, v83\n"
    "s_nop 1\n"
    "v_max3_f32 v9, v9, v84, v85\n"
    "v_max3_f32 v9, v9, v86, v87\n"
    "v_mov_b32 v10, v9\n"
    "s_nop 1\n"
    "v_permlane16_swap_b32 v10, v9\n"
    "v_max_f32 v9, v10, v9\n"
    "v_mov_b32 v10, v9\n"
    "s_nop 1\n"
    "v_permlane32_swap_b32 v10, v9\n"
    "v_max_f32 v9, v10, v9\n"
    "v_max_f32 v9, 0, v9\n"
    "v_sub_f32 v36, v36, v9\n"
    "v_sub_f32 v37, v37, v9\n"
    "v_sub_f32 v38, v38, v9\n"
    "v_sub_f32 v39, v39, v9\n"
    "s_add_u32 s57, s56, 0x0\n"
    "s_add_u32 s58, s56, 0x1000\n"
    "buffer_load_dwordx4 a[224:227], v76, s[40:43], s57 offen offset:0 sc1\n"
    "buffer_load_dwordx4 a[228:231], v76, s[40:43], s57 offen offset:1024 sc1\n"
    "buffer_load_dwordx4 a[232:235], v76, s[40:43], s57 offen offset:2048 sc1\n"
    "buffer_load_dwordx4 a[236:239], v76, s[40:43], s57 offen offset:3072 sc1\n"
    "buffer_load_dwordx4 a[240:243], v76, s[40:43], s58 offen offset:0 sc1\n"
    "buffer_load_dwordx4 a[244:247], v76, s[40:43], s58 offen offset:1024 sc1\n"
    "buffer_load_dwordx4 a[248:251], v76, s[40:43], s58 offen offset:2048 sc1\n"
    "buffer_load_dwordx4 a[252:255], v76, s[40:43], s58 offen offset:3072 sc1\n"
    "s_waitcnt vmcnt(0)\n"
    "v_mfma_f32_16x16x32_bf16 v[80:83], a[224:227], a[144:147], v[36:39]\n"
    "v_mfma_f32_16x16x32_bf16 v[80:83], a[228:231], a[148:151], v[80:83]\n"
    "v_mfma_f32_16x16x32_bf16 v[80:83], a[232:235], a[152:155], v[80:83]\n"
    "v_mfma_f32_16x16x32_bf16 v[80:83], a[236:239], a[156:159], v[80:83]\n"
    "v_mfma_f32_16x16x32_bf16 v[84:87], a[240:243], a[144:147], v[36:39]\n"
    "v_mfma_f32_16x16x32_bf16 v[84:87], a[244:247], a[148:151], v[84:87]\n"
    "v_mfma_f32_16x16x32_bf16 v[84:87], a[248:251], a[152:155], v[84:87]\n"
    "v_mfma_f32_16x16x32_bf16 v[84:87], a[252:255], a[156:159], v[84:87]\n"
    "s_nop 3\n"
    "v_exp_f32 v80, v80\n"
    "v_exp_f32 v81, v81\n"
    "v_exp_f32 v82, v82\n"
    "v_exp_f32 v83, v83\n"
    "v_exp_f32 v84, v84\n"
    "v_exp_f32 v85, v85\n"
    "v_exp_f32 v86, v86\n"
    "v_exp_f32 v87, v87\n"
    "v_cvt_pk_bf16_f32 v208, v80, v81\n"
    "v_cvt_pk_bf16_f32 v209, v82, v83\n"
    "v_cvt_pk_bf16_f32 v210, v84, v85\n"
    "v_cvt_pk_bf16_f32 v211, v86, v87\n"
    "s_add_u32 s57, s56, 0x2000\n"
    "s_add_u32 s58, s56, 0x3000\n"
    "buffer_load_dwordx4 a[224:227], v76, s[40:43], s57 offen offset:0 sc1\n"
    "buffer_load_dwordx4 a[228:231], v76, s[40:43], s57 offen offset:1024 sc1\n"
    "buffer_load_dwordx4 a[232:235], v76, s[40:43], s57 offen offset:2048 sc1\n"
    "buffer_load_dwordx4 a[236:239], v76, s[40:43], s57 offen offset:3072 sc1\n"
    "buffer_load_dwordx4 a[240:243], v76, s[40:43], s58 offen offset:0 sc1\n"
    "buffer_load_dwordx4 a[244:247], v76, s[40:43], s58 offen offset:1024 sc1\n"
    "buffer_load_dwordx4 a[248:251], v76, s[40:43], s58 offen offset:2048 sc1\n"
    "buffer_load_dwordx4 a[252:255], v76, s[40:43], s58 offen offset:3072 sc1\n"
    "s_waitcnt vmcnt(0)\n"
    "v_mfma_f32_16x16x32_bf16 v[80:83], a[224:227], a[144:147], v[36:39]\n"
    "v_mfma_f32_16x16x32_bf16 v[80:83], a[228:231], a[148:151], v[80:83]\n"
    "v_mfma_f32_16x16x32_bf16 v[80:83], a[232:235], a[152:155], v[80:83]\n"
    "v_mfma_f32_16x16x32_bf16 v[80:83], a[236:239], a[156:159], v[80:83]\n"
    "v_mfma_f32_16x16x32_bf16 v[84:87], a[240:243], a[144:147], v[36:39]\n"
    "v_mfma_f32_16x16x32_bf16 v[84:87], a[244:247], a[148:151], v[84:87]\n"
    "v_mfma_f32_16x16x32_bf16 v[84:87], a[248:251], a[152:155], v[84:87]\n"
    "v_mfma_f32_16x16x32_bf16 v[84:87], a[252:255], a[156:159], v[84:87]\n"
    "s_nop 3\n"
    "v_exp_f32 v80, v80\n"
    "v_exp_f32 v81, v81\n"
    "v_exp_f32 v82, v82\n"
    "v_exp_f32 v83, v83\n"
    "v_exp_f32 v84, v84\n"
    "v_exp_f32 v85, v85\n"
    "v_exp_f32 v86, v86\n"
    "v_exp_f32 v87, v87\n"
    "v_cvt_pk_bf16_f32 v212, v80, v81\n"
    "v_cvt_pk_bf16_f32 v213, v82, v83\n"
    "v_cvt_pk_bf16_f32 v214, v84, v85\n"
    "v_cvt_pk_bf16_f32 v215, v86, v87\n"
    "s_add_u32 s57, s56, 0x4000\n"
    "s_add_u32 s58, s56, 0x5000\n"
    "buffer_load_dwordx4 a[224:227], v76, s[40:43], s57 offen offset:0 sc1\n"
    "buffer_load_dwordx4 a[228:231], v76, s[40:43], s57 offen offset:1024 sc1\n"
    "buffer_load_dwordx4 a[232:235], v76, s[40:43], s57 offen offset:2048 sc1\n"
    "buffer_load_dwordx4 a[236:239], v76, s[40:43], s57 offen offset:3072 sc1\n"
    "buffer_load_dwordx4 a[240:243], v76, s[40:43], s58 offen offset:0 sc1\n"
    "buffer_load_dwordx4 a[244:247], v76, s[40:43], s58 offen offset:1024 sc1\n"
    "buffer_load_dwordx4 a[248:251], v76, s[40:43], s58 offen offset:2048 sc1\n"
    "buffer_load_dwordx4 a[252:255], v76, s[40:43], s58 offen offset:3072 sc1\n"
    "s_waitcnt vmcnt(0)\n"
    "v_mfma_f32_16x16x32_bf16 v[80:83], a[224:227], a[144:147], v[36:39]\n"
    "v_mfma_f32_16x16x32_bf16 v[80:83], a[228:231], a[148:151], v[80:83]\n"
    "v_mfma_f32_16x16x32_bf16 v[80:83], a[232:235], a[152:155], v[80:83]\n"
    "v_mfma_f32_16x16x32_bf16 v[80:83], a[236:239], a[156:159], v[80:83]\n"
    "v_mfma_f32_16x16x32_bf16 v[84:87], a[240:243], a[144:147], v[36:39]\n"
    "v_mfma_f32_16x16x32_bf16 v[84:87], a[244:247], a[148:151], v[84:87]\n"
    "v_mfma_f32_16x16x32_bf16 v[84:87], a[248:251], a[152:155], v[84:87]\n"
    "v_mfma_f32_16x16x32_bf16 v[84:87], a[252:255], a[156:159], v[84:87]\n"
    "s_nop 3\n"
    "v_exp_f32 v80, v80\n"
    "v_exp_f32 v81, v81\n"
    "v_exp_f32 v82, v82\n"
    "v_exp_f32 v83, v83\n"
    "v_exp_f32 v84, v84\n"
    "v_exp_f32 v85, v85\n"
    "v_exp_f32 v86, v86\n"
    "v_exp_f32 v87, v87\n"
    "v_cvt_pk_bf16_f32 v216, v80, v81\n"
    "v_cvt_pk_bf16_f32 v217, v82, v83\n"
    "v_cvt_pk_bf16_f32 v218, v84, v85\n"
    "v_cvt_pk_bf16_f32 v219, v86, v87\n"
    "s_add_u32 s57, s56, 0x6000\n"
    "s_add_u32 s58, s56, 0x7000\n"
    "buffer_load_dwordx4 a[224:227], v76, s[40:43], s57 offen offset:0 sc1\n"
    "buffer_load_dwordx4 a[228:231], v76, s[40:43], s57 offen offset:1024 sc1\n"
    "buffer_load_dwordx4 a[232:235], v76, s[40:43], s57 offen offset:2048 sc1\n"
    "buffer_load_dwordx4 a[236:239], v76, s[40:43], s57 offen offset:3072 sc1\n"
    "buffer_load_dwordx4 a[240:243], v76, s[40:43], s58 offen offset:0 sc1\n"
    "buffer_load_dwordx4 a[244:247], v76, s[40:43], s58 offen offset:1024 sc1\n"
    "buffer_load_dwordx4 a[248:251], v76, s[40:43], s58 offen offset:2048 sc1\n"
    "buffer_load_dwordx4 a[252:255], v76, s[40:43], s58 offen offset:3072 sc1\n"
    "s_waitcnt vmcnt(0)\n"
    "v_mfma_f32_16x16x32_bf16 v[80:83], a[224:227], a[144:147], v[36:39]\n"
    "v_mfma_f32_16x16x32_bf16 v[80:83], a[228:231], a[148:151], v[80:83]\n"
    "v_mfma_f32_16x16x32_bf16 v[80:83], a[232:235], a[152:155], v[80:83]\n"
    "v_mfma_f32_16x16x32_bf16 v[80:83], a[236:239], a[156:159], v[80:83]\n"
    "v_mfma_f32_16x16x32_bf16 v[84:87], a[240:243], a[144:147], v[36:39]\n"
    "v_mfma_f32_16x16x32_bf16 v[84:87], a[244:247], a[148:151], v[84:87]\n"
    "v_mfma_f32_16x16x32_bf16 v[84:87], a[248:251], a[152:155], v[84:87]\n"
    "v_mfma_f32_16x16x32_bf16 v[84:87], a[252:255], a[156:159], v[84:87]\n"
    "s_nop 3\n"
    "v_exp_f32 v80, v80\n"
    "v_exp_f32 v81, v81\n"
    "v_exp_f32 v82, v82\n"
    "v_exp_f32 v83, v83\n"
    "v_exp_f32 v84, v84\n"
    "v_exp_f32 v85, v85\n"
    "v_exp_f32 v86, v86\n"
    "v_exp_f32 v87, v87\n"
    "v_cvt_pk_bf16_f32 v220, v80, v81\n"
    "v_cvt_pk_bf16_f32 v221, v82, v83\n"
    "v_cvt_pk_bf16_f32 v222, v84, v85\n"
    "v_cvt_pk_bf16_f32 v223, v86, v87\n"
    "v_mfma_f32_16x16x32_bf16 v[52:55], v[64:67], v[208:211], 0\n"
    "v_mfma_f32_16x16x32_bf16 v[52:55], v[64:67], v[212:215], v[52:55]\n"
    "v_mfma_f32_16x16x32_bf16 v[52:55], v[64:67], v[216:219], v[52:55]\n"
    "v_mfma_f32_16x16x32_bf16 v[52:55], v[64:67], v[220:223], v[52:55]\n"
    "v_sub_f32 v20, v20, v9\n"
    "v_sub_f32 v21, v21, v9\n"
    "v_sub_f32 v22, v22, v9\n"
    "v_sub_f32 v23, v23, v9\n"
    "v_sub_f32 v11, 0, v9\n"
    "v_exp_f32 v11, v11\n"
    "s_nop 0\n"
    "v_mul_f32 v69, v11, v69\n"
    "ds_bpermute_b32 v12, v99, v11 offset:0\n"
    "ds_bpermute_b32 v13, v99, v11 offset:4\n"
    "ds_bpermute_b32 v14, v99, v11 offset:8\n"
    "ds_bpermute_b32 v15, v99, v11 offset:12\n"
    "s_waitcnt lgkmcnt(0)\n"
    "v_accvgpr_read_b32 v80, a4\n"
    "v_accvgpr_read_b32 v81, a5\n"
    "v_accvgpr_read_b32 v82, a6\n"
    "v_accvgpr_read_b32 v83, a7\n"
    "v_accvgpr_read_b32 v84, a20\n"
    "v_accvgpr_read_b32 v85, a21\n"
    "v_accvgpr_read_b32 v86, a22\n"
    "v_accvgpr_read_b32 v87, a23\n"
    "v_mul_f32 v80, v12, v80\n"
    "v_mul_f32 v81, v13, v81\n"
    "v_mul_f32 v82, v14, v82\n"
    "v_mul_f32 v83, v15, v83\n"
    "v_mul_f32 v84, v12, v84\n"
    "v_mul_f32 v85, v13, v85\n"
    "v_mul_f32 v86, v14, v86\n"
    "v_mul_f32 v87, v15, v87\n"
    "v_accvgpr_write_b32 a4, v80\n"
    "v_accvgpr_write_b32 a5, v81\n"
    "v_accvgpr_write_b32 a6, v82\n"
    "v_accvgpr_write_b32 a7, v83\n"
    "v_accvgpr_write_b32 a20, v84\n"
    "v_accvgpr_write_b32 a21, v85\n"
    "v_accvgpr_write_b32 a22, v86\n"
    "v_accvgpr_write_b32 a23, v87\n"
    "v_accvgpr_read_b32 v80, a36\n"
    "v_accvgpr_read_b32 v81, a37\n"
    "v_accvgpr_read_b32 v82, a38\n"
    "v_accvgpr_read_b32 v83, a39\n"
    "v_accvgpr_read_b32 v84, a52\n"
    "v_accvgpr_read_b32 v85, a53\n"
    "v_accvgpr_read_b32 v86, a54\n"
    "v_accvgpr_read_b32 v87, a55\n"
    "v_mul_f32 v80, v12, v80\n"
    "v_mul_f32 v81, v13, v81\n"
    "v_mul_f32 v82, v14, v82\n"
    "v_mul_f32 v83, v15, v83\n"
    "v_mul_f32 v84, v12, v84\n"
    "v_mul_f32 v85, v13, v85\n"
    "v_mul_f32 v86, v14, v86\n"
    "v_mul_f32 v87, v15, v87\n"
    "v_accvgpr_write_b32 a36, v80\n"
    "v_accvgpr_write_b32 a37, v81\n"
    "v_accvgpr_write_b32 a38, v82\n"
    "v_accvgpr_write_b32 a39, v83\n"
    "v_accvgpr_write_b32 a52, v84\n"
    "v_accvgpr_write_b32 a53, v85\n"
    "v_accvgpr_write_b32 a54, v86\n"
    "v_accvgpr_write_b32 a55, v87\n"
    "v_accvgpr_read_b32 v80, a68\n"
    "v_accvgpr_read_b32 v81, a69\n"
    "v_accvgpr_read_b32 v82, a70\n"
    "v_accvgpr_read_b32 v83, a71\n"
    "v_accvgpr_read_b32 v84, a84\n"
    "v_accvgpr_read_b32 v85, a85\n"
    "v_accvgpr_read_b32 v86, a86\n"
    "v_accvgpr_read_b32 v87, a87\n"
    "v_mul_f32 v80, v12, v80\n"
    "v_mul_f32 v81, v13, v81\n"
    "v_mul_f32 v82, v14, v82\n"
    "v_mul_f32 v83, v15, v83\n"
    "v_mul_f32 v84, v12, v84\n"
    "v_mul_f32 v85, v13, v85\n"
    "v_mul_f32 v86, v14, v86\n"
    "v_mul_f32 v87, v15, v87\n"
    "v_accvgpr_write_b32 a68, v80\n"
    "v_accvgpr_write_b32 a69, v81\n"
    "v_accvgpr_write_b32 a70, v82\n"
    "v_accvgpr_write_b32 a71, v83\n"
    "v_accvgpr_write_b32 a84, v84\n"
    "v_accvgpr_write_b32 a85, v85\n"
    "v_accvgpr_write_b32 a86, v86\n"
    "v_accvgpr_write_b32 a87, v87\n"
    "v_accvgpr_read_b32 v80, a100\n"
    "v_accvgpr_read_b32 v81, a101\n"
    "v_accvgpr_read_b32 v82, a102\n"
    "v_accvgpr_read_b32 v83, a103\n"
    "v_accvgpr_read_b32 v84, a116\n"
    "v_accvgpr_read_b32 v85, a117\n"
    "v_accvgpr_read_b32 v86, a118\n"
    "v_accvgpr_read_b32 v87, a119\n"
    "v_mul_f32 v80, v12, v80\n"
    "v_mul_f32 v81, v13, v81\n"
    "v_mul_f32 v82, v14, v82\n"
    "v_mul_f32 v83, v15, v83\n"
    "v_mul_f32 v84, v12, v84\n"
    "v_mul_f32 v85, v13, v85\n"
    "v_mul_f32 v86, v14, v86\n"
    "v_mul_f32 v87, v15, v87\n"
    "v_accvgpr_write_b32 a100, v80\n"
    "v_accvgpr_write_b32 a101, v81\n"
    "v_accvgpr_write_b32 a102, v82\n"
    "v_accvgpr_write_b32 a103, v83\n"
    "v_accvgpr_write_b32 a116, v84\n"
    "v_accvgpr_write_b32 a117, v85\n"
    "v_accvgpr_write_b32 a118, v86\n"
    "v_accvgpr_write_b32 a119, v87\n"
    "s_nop 7\n"
    "s_nop 7\n"
    "s_branch L_ret_B1\n"
    "L_rare_B2:\n"
    "s_nop 7\n"
    "s_nop 7\n"
    "s_sub_u32 s56, s48, 0x20000\n"
    "s_add_u32 s57, s56, 0x0\n"
    "s_add_u32 s58, s56, 0x1000\n"
    "buffer_load_dwordx4 a[224:227], v76, s[40:43], s57 offen offset:0 sc1\n"
    "buffer_load_dwordx4 a[228:231], v76, s[40:43], s57 offen offset:1024 sc1\n"
    "buffer_load_dwordx4 a[232:235], v76, s[40:43], s57 offen offset:2048 sc1\n"
    "buffer_load_dwordx4 a[236:239], v76, s[40:43], s57 offen offset:3072 sc1\n"
    "buffer_load_dwordx4 a[240:243], v76, s[40:43], s58 offen offset:0 sc1\n"
    "buffer_load_dwordx4 a[244:247], v76, s[40:43], s58 offen offset:1024 sc1\n"
    "buffer_load_dwordx4 a[248:251], v76, s[40:43], s58 offen offset:2048 sc1\n"
    "buffer_load_dwordx4 a[252:255], v76, s[40:43], s58 offen offset:3072 sc1\n"
    "s_waitcnt vmcnt(0)\n"
    "v_mfma_f32_16x16x32_bf16 v[80:83], a[224:227], a[160:163], v[40:43]\n"
    "v_mfma_f32_16x16x32_bf16 v[80:83], a[228:231], a[164:167], v[80:83]\n"
    "v_mfma_f32_16x16x32_bf16 v[80:83], a[232:235], a[168:171], v[80:83]\n"
    "v_mfma_f32_16x16x32_bf16 v[80:83], a[236:239], a[172:175], v[80:83]\n"
    "v_mfma_f32_16x16x32_bf16 v[84:87], a[240:243], a[160:163], v[40:43]\n"
    "v_mfma_f32_16x16x32_bf16 v[84:87], a[244:247], a[164:167], v[84:87]\n"
    "v_mfma_f32_16x16x32_bf16 v[84:87], a[248:251], a[168:171], v[84:87]\n"
    "v_mfma_f32_16x16x32_bf16 v[84:87], a[252:255], a[172:175], v[84:87]\n"
    "s_nop 3\n"
    "v_max_f32 v9, v80, v80\n"
    "v_max3_f32 v9, v9, v80, v81\n"
    "v_max3_f32 v9, v9, v82, v83\n"
    "s_nop 0\n"
    "v_max3_f32 v9, v9, v84, v85\n"
    "v_max3_f32 v9, v9, v86, v87\n"
    "s_add_u32 s57, s56, 0x2000\n"
    "s_add_u32 s58, s56, 0x3000\n"
    "buffer_load_dwordx4 a[224:227], v76, s[40:43], s57 offen offset:0 sc1\n"
    "buffer_load_dwordx4 a[228:231], v76, s[40:43], s57 offen offset:1024 sc1\n"
    "buffer_load_dwordx4 a[232:235], v76, s[40:43], s57 offen offset:2048 sc1\n"
    "buffer_load_dwordx4 a[236:239], v76, s[40:43], s57 offen offset:3072 sc1\n"
    "buffer_load_dwordx4 a[240:243], v76, s[40:43], s58 offen offset:0 sc1\n"
    "buffer_load_dwordx4 a[244:247], v76, s[40:43], s58 offen offset:1024 sc1\n"
    "buffer_load_dwordx4 a[248:251], v76, s[40:43], s58 offen offset:2048 sc1\n"
    "buffer_load_dwordx4 a[252:255], v76, s[40:43], s58 offen offset:3072 sc1\n"
    "s_waitcnt vmcnt(0)\n"
    "v_mfma_f32_16x16x32_bf16 v[80:83], a[224:227], a[160:163], v[40:43]\n"
    "v_mfma_f32_16x16x32_bf16 v[80:83], a[228:231], a[164:167], v[80:83]\n"
    "v_mfma_f32_16x16x32_bf16 v[80:83], a[232:235], a[168:171], v[80:83]\n"
    "v_mfma_f32_16x16x32_bf16 v[80:83], a[236:239], a[172:175], v[80:83]\n"
    "v_mfma_f32_16x16x32_bf16 v[84:87], a[240:243], a[160:163], v[40:43]\n"
    "v_mfma_f32_16x16x32_bf16 v[84:87], a[244:247], a[164:167], v[84:87]\n"
    "v_mfma_f32_16x16x32_bf16 v[84:87], a[248:251], a[168:171], v[84:87]\n"
    "v_mfma_f32_16x16x32_bf16 v[84:87], a[252:255], a[172:175], v[84:87]\n"
    "s_nop 3\n"
    "v_max3_f32 v9, v9, v80, v81\n"
    "v_max3_f32 v9, v9, v82, v83\n"
    "s_nop 1\n"
    "v_max3_f32 v9, v9, v84, v85\n"
    "v_max3_f32 v9, v9, v86, v87\n"
    "s_add_u32 s57, s56, 0x4000\n"
    "s_add_u32 s58, s56, 0x5000\n"
    "buffer_load_dwordx4 a[224:227], v76, s[40:43], s57 offen offset:0 sc1\n"
    "buffer_load_dwordx4 a[228:231], v76, s[40:43], s57 offen offset:1024 sc1\n"
    "buffer_load_dwordx4 a[232:235], v76, s[40:43], s57 offen offset:2048 sc1\n"
    "buffer_load_dwordx4 a[236:239], v76, s[40:43], s57 offen offset:3072 sc1\n"
    "buffer_load_dwordx4 a[240:243], v76, s[40:43], s58 offen offset:0 sc1\n"
    "buffer_load_dwordx4 a[244:247], v76, s[40:43], s58 offen offset:1024 sc1\n"
    "buffer_load_dwordx4 a[248:251], v76, s[40:43], s58 offen offset:2048 sc1\n"
    "buffer_load_dwordx4 a[252:255], v76, s[40:43], s58 offen offset:3072 sc1\n"
    "s_waitcnt vmcnt(0)\n"
    "v_mfma_f32_16x16x32_bf16 v[80:83], a[224:227], a[160:163], v[40:43]\n"
    "v_mfma_f32_16x16x32_bf16 v[80:83], a[228:231], a[164:167], v[80:83]\n"
    "v_mfma_f32_16x16x32_bf16 v[80:83], a[232:235], a[168:171], v[80:83]\n"
    "v_mfma_f32_16x16x32_bf16 v[80:83], a[236:239], a[172:175], v[80:83]\n"
    "v_mfma_f32_16x16x32_bf16 v[84:87], a[240:243], a[160:163], v[40:43]\n"
    "v_mfma_f32_16x16x32_bf16 v[84:87], a[244:247], a[164:167], v[84:87]\n"
    "v_mfma_f32_16x16x32_bf16 v[84:87], a[248:251], a[168:171], v[84:87]\n"
    "v_mfma_f32_16x16x32_bf16 v[84:87], a[252:255], a[172:175], v[84:87]\n"
    "s_nop 3\n"
    "v_max3_f32 v9, v9, v80, v81\n"
    "v_max3_f32 v9, v9, v82, v83\n"
    "s_nop 1\n"
    "v_max3_f32 v9, v9, v84, v85\n"
    "v_max3_f32 v9, v9, v86, v87\n"
    "s_add_u32 s57, s56, 0x6000\n"
    "s_add_u32 s58, s56, 0x7000\n"
    "buffer_load_dwordx4 a[224:227], v76, s[40:43], s57 offen offset:0 sc1\n"
    "buffer_load_dwordx4 a[228:231], v76, s[40:43], s57 offen offset:1024 sc1\n"
    "buffer_load_dwordx4 a[232:235], v76, s[40:43], s57 offen offset:2048 sc1\n"
    "buffer_load_dwordx4 a[236:239], v76, s[40:43], s57 offen offset:3072 sc1\n"
    "buffer_load_dwordx4 a[240:243], v76, s[40:43], s58 offen offset:0 sc1\n"
    "buffer_load_dwordx4 a[244:247], v76, s[40:43], s58 offen offset:1024 sc1\n"
    "buffer_load_dwordx4 a[248:251], v76, s[40:43], s58 offen offset:2048 sc1\n"
    "buffer_load_dwordx4 a[252:255], v76, s[40:43], s58 offen offset:3072 sc1\n"
    "s_waitcnt vmcnt(0)\n"
    "v_mfma_f32_16x16x32_bf16 v[80:83], a[224:227], a[160:163], v[40:43]\n"
    "v_mfma_f32_16x16x32_bf16 v[80:83], a[228:231], a[164:167], v[80:83]\n"
    "v_mfma_f32_16x16x32_bf16 v[80:83], a[232:235], a[168:171], v[80:83]\n"
    "v_mfma_f32_16x16x32_bf16 v[80:83], a[236:239], a[172:175], v[80:83]\n"
    "v_mfma_f32_16x16x32_bf16 v[84:87], a[240:243], a[160:163], v[40:43]\n"
    "v_mfma_f32_16x16x32_bf16 v[84:87], a[244:247], a[164:167], v[84:87]\n"
    "v_mfma_f32_16x16x32_bf16 v[84:87], a[248:251], a[168:171], v[84:87]\n"
    "v_mfma_f32_16x16x32_bf16 v[84:87], a[252:255], a[172:175], v[84:87]\n"
    "s_nop 3\n"
    "v_max3_f32 v9, v9, v80, v81\n"
    "v_max3_f32 v9, v9, v82, v83\n"
    "s_nop 1\n"
    "v_max3_f32 v9, v9, v84, v85\n"
    "v_max3_f32 v9, v9, v86, v87\n"
    "v_mov_b32 v10, v9\n"
    "s_nop 1\n"
    "v_permlane16_swap_b32 v10, v9\n"
    "v_max_f32 v9, v10, v9\n"
    "v_mov_b32 v10, v9\n"
    "s_nop 1\n"
    "v_permlane32_swap_b32 v10, v9\n"
    "v_max_f32 v9, v10, v9\n"
    "v_max_f32 v9, 0, v9\n"
    "v_sub_f32 v40, v40, v9\n"
    "v_sub_f32 v41, v41, v9\n"
    "v_sub_f32 v42, v42, v9\n"
    "v_sub_f32 v43, v43, v9\n"
    "s_add_u32 s57, s56, 0x0\n"
    "s_add_u32 s58, s56, 0x1000\n"
    "buffer_load_dwordx4 a[224:227], v76, s[40:43], s57 offen offset:0 sc1\n"
    "buffer_load_dwordx4 a[228:231], v76, s[40:43], s57 offen offset:1024 sc1\n"
    "buffer_load_dwordx4 a[232:235], v76, s[40:43], s57 offen offset:2048 sc1\n"
    "buffer_load_dwordx4 a[236:239], v76, s[40:43], s57 offen offset:3072 sc1\n"
    "buffer_load_dwordx4 a[240:243], v76, s[40:43], s58 offen offset:0 sc1\n"
    "buffer_load_dwordx4 a[244:247], v76, s[40:43], s58 offen offset:1024 sc1\n"
    "buffer_load_dwordx4 a[248:251], v76, s[40:43], s58 offen offset:2048 sc1\n"
    "buffer_load_dwordx4 a[252:255], v76, s[40:43], s58 offen offset:3072 sc1\n"
    "s_waitcnt vmcnt(0)\n"
    "v_mfma_f32_16x16x32_bf16 v[80:83], a[224:227], a[160:163], v[40:43]\n"
    "v_mfma_f32_16x16x32_bf16 v[80:83], a[228:231], a[164:167], v[80:83]\n"
    "v_mfma_f32_16x16x32_bf16 v[80:83], a[232:235], a[168:171], v[80:83]\n"
    "v_mfma_f32_16x16x32_bf16 v[80:83], a[236:239], a[172:175], v[80:83]\n"
    "v_mfma_f32_16x16x32_bf16 v[84:87], a[240:243], a[160:163], v[40:43]\n"
    "v_mfma_f32_16x16x32_bf16 v[84:87], a[244:247], a[164:167], v[84:87]\n"
    "v_mfma_f32_16x16x32_bf16 v[84:87], a[248:251], a[168:171], v[84:87]\n"
    "v_mfma_f32_16x16x32_bf16 v[84:87], a[252:255], a[172:175], v[84:87]\n"
    "s_nop 3\n"
    "v_exp_f32 v80, v80\n"
    "v_exp_f32 v81, v81\n"
    "v_exp_f32 v82, v82\n"
    "v_exp_f32 v83, v83\n"
    "v_exp_f32 v84, v84\n"
    "v_exp_f32 v85, v85\n"
    "v_exp_f32 v86, v86\n"
    "v_exp_f32 v87, v87\n"
    "v_cvt_pk_bf16_f32 v224, v80, v81\n"
    "v_cvt_pk_bf16_f32 v225, v82, v83\n"
    "v_cvt_pk_bf16_f32 v226, v84, v85\n"
    "v_cvt_pk_bf16_f32 v227, v86, v87\n"
    "s_add_u32 s57, s56, 0x2000\n"
    "s_add_u32 s58, s56, 0x3000\n"
    "buffer_load_dwordx4 a[224:227], v76, s[40:43], s57 offen offset:0 sc1\n"
    "buffer_load_dwordx4 a[228:231], v76, s[40:43], s57 offen offset:1024 sc1\n"
    "buffer_load_dwordx4 a[232:235], v76, s[40:43], s57 offen offset:2048 sc1\n"
    "buffer_load_dwordx4 a[236:239], v76, s[40:43], s57 offen offset:3072 sc1\n"
    "buffer_load_dwordx4 a[240:243], v76, s[40:43], s58 offen offset:0 sc1\n"
    "buffer_load_dwordx4 a[244:247], v76, s[40:43], s58 offen offset:1024 sc1\n"
    "buffer_load_dwordx4 a[248:251], v76, s[40:43], s58 offen offset:2048 sc1\n"
    "buffer_load_dwordx4 a[252:255], v76, s[40:43], s58 offen offset:3072 sc1\n"
    "s_waitcnt vmcnt(0)\n"
    "v_mfma_f32_16x16x32_bf16 v[80:83], a[224:227], a[160:163], v[40:43]\n"
    "v_mfma_f32_16x16x32_bf16 v[80:83], a[228:231], a[164:167], v[80:83]\n"
    "v_mfma_f32_16x16x32_bf16 v[80:83], a[232:235], a[168:171], v[80:83]\n"
    "v_mfma_f32_16x16x32_bf16 v[80:83], a[236:239], a[172:175], v[80:83]\n"
    "v_mfma_f32_16x16x32_bf16 v[84:87], a[240:243], a[160:163], v[40:43]\n"
    "v_mfma_f32_16x16x32_bf16 v[84:87], a[244:247], a[164:167], v[84:87]\n"
    "v_mfma_f32_16x16x32_bf16 v[84:87], a[248:251], a[168:171], v[84:87]\n"
    "v_mfma_f32_16x16x32_bf16 v[84:87], a[252:255], a[172:175], v[84:87]\n"
    "s_nop 3\n"
    "v_exp_f32 v80, v80\n"
    "v_exp_f32 v81, v81\n"
    "v_exp_f32 v82, v82\n"
    "v_exp_f32 v83, v83\n"
    "v_exp_f32 v84, v84\n"
    "v_exp_f32 v85, v85\n"
    "v_exp_f32 v86, v86\n"
    "v_exp_f32 v87, v87\n"
    "v_cvt_pk_bf16_f32 v228, v80, v81\n"
    "v_cvt_pk_bf16_f32 v229, v82, v83\n"
    "v_cvt_pk_bf16_f32 v230, v84, v85\n"
    "v_cvt_pk_bf16_f32 v231, v86, v87\n"
    "s_add_u32 s57, s56, 0x4000\n"
    "s_add_u32 s58, s56, 0x5000\n"
    "buffer_load_dwordx4 a[224:227], v76, s[40:43], s57 offen offset:0 sc1\n"
    "buffer_load_dwordx4 a[228:231], v76, s[40:43], s57 offen offset:1024 sc1\n"
    "buffer_load_dwordx4 a[232:235], v76, s[40:43], s57 offen offset:2048 sc1\n"
    "buffer_load_dwordx4 a[236:239], v76, s[40:43], s57 offen offset:3072 sc1\n"
    "buffer_load_dwordx4 a[240:243], v76, s[40:43], s58 offen offset:0 sc1\n"
    "buffer_load_dwordx4 a[244:247], v76, s[40:43], s58 offen offset:1024 sc1\n"
    "buffer_load_dwordx4 a[248:251], v76, s[40:43], s58 offen offset:2048 sc1\n"
    "buffer_load_dwordx4 a[252:255], v76, s[40:43], s58 offen offset:3072 sc1\n"
    "s_waitcnt vmcnt(0)\n"
    "v_mfma_f32_16x16x32_bf16 v[80:83], a[224:227], a[160:163], v[40:43]\n"
    "v_mfma_f32_16x16x32_bf16 v[80:83], a[228:231], a[164:167], v[80:83]\n"
    "v_mfma_f32_16x16x32_bf16 v[80:83], a[232:235], a[168:171], v[80:83]\n"
    "v_mfma_f32_16x16x32_bf16 v[80:83], a[236:239], a[172:175], v[80:83]\n"
    "v_mfma_f32_16x16x32_bf16 v[84:87], a[240:243], a[160:163], v[40:43]\n"
    "v_mfma_f32_16x16x32_bf16 v[84:87], a[244:247], a[164:167], v[84:87]\n"
    "v_mfma_f32_16x16x32_bf16 v[84:87], a[248:251], a[168:171], v[84:87]\n"
    "v_mfma_f32_16x16x32_bf16 v[84:87], a[252:255], a[172:175], v[84:87]\n"
    "s_nop 3\n"
    "v_exp_f32 v80, v80\n"
    "v_exp_f32 v81, v81\n"
    "v_exp_f32 v82, v82\n"
    "v_exp_f32 v83, v83\n"
    "v_exp_f32 v84, v84\n"
    "v_exp_f32 v85, v85\n"
    "v_exp_f32 v86, v86\n"
    "v_exp_f32 v87, v87\n"
    "v_cvt_pk_bf16_f32 v232, v80, v81\n"
    "v_cvt_pk_bf16_f32 v233, v82, v83\n"
    "v_cvt_pk_bf16_f32 v234, v84, v85\n"
    "v_cvt_pk_bf16_f32 v235, v86, v87\n"
    "s_add_u32 s57, s56, 0x6000\n"
    "s_add_u32 s58, s56, 0x7000\n"
    "buffer_load_dwordx4 a[224:227], v76, s[40:43], s57 offen offset:0 sc1\n"
    "buffer_load_dwordx4 a[228:231], v76, s[40:43], s57 offen offset:1024 sc1\n"
    "buffer_load_dwordx4 a[232:235], v76, s[40:43], s57 offen offset:2048 sc1\n"
    "buffer_load_dwordx4 a[236:239], v76, s[40:43], s57 offen offset:3072 sc1\n"
    "buffer_load_dwordx4 a[240:243], v76, s[40:43], s58 offen offset:0 sc1\n"
    "buffer_load_dwordx4 a[244:247], v76, s[40:43], s58 offen offset:1024 sc1\n"
    "buffer_load_dwordx4 a[248:251], v76, s[40:43], s58 offen offset:2048 sc1\n"
    "buffer_load_dwordx4 a[252:255], v76, s[40:43], s58 offen offset:3072 sc1\n"
    "s_waitcnt vmcnt(0)\n"
    "v_mfma_f32_16x16x32_bf16 v[80:83], a[224:227], a[160:163], v[40:43]\n"
    "v_mfma_f32_16x16x32_bf16 v[80:83], a[228:231], a[164:167], v[80:83]\n"
    "v_mfma_f32_16x16x32_bf16 v[80:83], a[232:235], a[168:171], v[80:83]\n"
    "v_mfma_f32_16x16x32_bf16 v[80:83], a[236:239], a[172:175], v[80:83]\n"
    "v_mfma_f32_16x16x32_bf16 v[84:87], a[240:243], a[160:163], v[40:43]\n"
    "v_mfma_f32_16x16x32_bf16 v[84:87], a[244:247], a[164:167], v[84:87]\n"
    "v_mfma_f32_16x16x32_bf16 v[84:87], a[248:251], a[168:171], v[84:87]\n"
    "v_mfma_f32_16x16x32_bf16 v[84:87], a[252:255], a[172:175], v[84:87]\n"
    "s_nop 3\n"
    "v_exp_f32 v80, v80\n"
    "v_exp_f32 v81, v81\n"
    "v_exp_f32 v82, v82\n"
    "v_exp_f32 v83, v83\n"
    "v_exp_f32 v84, v84\n"
    "v_exp_f32 v85, v85\n"
    "v_exp_f32 v86, v86\n"
    "v_exp_f32 v87, v87\n"
    "v_cvt_pk_bf16_f32 v236, v80, v81\n"
    "v_cvt_pk_bf16_f32 v237, v82, v83\n"
    "v_cvt_pk_bf16_f32 v238, v84, v85\n"
    "v_cvt_pk_bf16_f32 v239, v86, v87\n"
    "v_mfma_f32_16x16x32_bf16 v[56:59], v[64:67], v[224:227], 0\n"
    "v_mfma_f32_16x16x32_bf16 v[56:59], v[64:67], v[228:231], v[56:59]\n"
    "v_mfma_f32_16x16x32_bf16 v[56:59], v[64:67], v[232:235], v[56:59]\n"
    "v_mfma_f32_16x16x32_bf16 v[56:59], v[64:67], v[236:239], v[56:59]\n"
    "v_sub_f32 v24, v24, v9\n"
    "v_sub_f32 v25, v25, v9\n"
    "v_sub_f32 v26, v26, v9\n"
    "v_sub_f32 v27, v27, v9\n"
    "v_sub_f32 v11, 0, v9\n"
    "v_exp_f32 v11, v11\n"
    "s_nop 0\n"
    "v_mul_f32 v70, v11, v70\n"
    "ds_bpermute_b32 v12, v99, v11 offset:0\n"
    "ds_bpermute_b32 v13, v99, v11 offset:4\n"
    "ds_bpermute_b32 v14, v99, v11 offset:8\n"
    "ds_bpermute_b32 v15, v99, v11 offset:12\n"
    "s_waitcnt lgkmcnt(0)\n"
    "v_accvgpr_read_b32 v80, a8\n"
    "v_accvgpr_read_b32 v81, a9\n"
    "v_accvgpr_read_b32 v82, a10\n"
    "v_accvgpr_read_b32 v83, a11\n"
    "v_accvgpr_read_b32 v84, a24\n"
    "v_accvgpr_read_b32 v85, a25\n"
    "v_accvgpr_read_b32 v86, a26\n"
    "v_accvgpr_read_b32 v87, a27\n"
    "v_mul_f32 v80, v12, v80\n"
    "v_mul_f32 v81, v13, v81\n"
    "v_mul_f32 v82, v14, v82\n"
    "v_mul_f32 v83, v15, v83\n"
    "v_mul_f32 v84, v12, v84\n"
    "v_mul_f32 v85, v13, v85\n"
    "v_mul_f32 v86, v14, v86\n"
    "v_mul_f32 v87, v15, v87\n"
    "v_accvgpr_write_b32 a8, v80\n"
    "v_accvgpr_write_b32 a9, v81\n"
    "v_accvgpr_write_b32 a10, v82\n"
    "v_accvgpr_write_b32 a11, v83\n"
    "v_accvgpr_write_b32 a24, v84\n"
    "v_accvgpr_write_b32 a25, v85\n"
    "v_accvgpr_write_b32 a26, v86\n"
    "v_accvgpr_write_b32 a27, v87\n"
    "v_accvgpr_read_b32 v80, a40\n"
    "v_accvgpr_read_b32 v81, a41\n"
    "v_accvgpr_read_b32 v82, a42\n"
    "v_accvgpr_read_b32 v83, a43\n"
    "v_accvgpr_read_b32 v84, a56\n"
    "v_accvgpr_read_b32 v85, a57\n"
    "v_accvgpr_read_b32 v86, a58\n"
    "v_accvgpr_read_b32 v87, a59\n"
    "v_mul_f32 v80, v12, v80\n"
    "v_mul_f32 v81, v13, v81\n"
    "v_mul_f32 v82, v14, v82\n"
    "v_mul_f32 v83, v15, v83\n"
    "v_mul_f32 v84, v12, v84\n"
    "v_mul_f32 v85, v13, v85\n"
    "v_mul_f32 v86, v14, v86\n"
    "v_mul_f32 v87, v15, v87\n"
    "v_accvgpr_write_b32 a40, v80\n"
    "v_accvgpr_write_b32 a41, v81\n"
    "v_accvgpr_write_b32 a42, v82\n"
    "v_accvgpr_write_b32 a43, v83\n"
    "v_accvgpr_write_b32 a56, v84\n"
    "v_accvgpr_write_b32 a57, v85\n"
    "v_accvgpr_write_b32 a58, v86\n"
    "v_accvgpr_write_b32 a59, v87\n"
    "v_accvgpr_read_b32 v80, a72\n"
    "v_accvgpr_read_b32 v81, a73\n"
    "v_accvgpr_read_b32 v82, a74\n"
    "v_accvgpr_read_b32 v83, a75\n"
    "v_accvgpr_read_b32 v84, a88\n"
    "v_accvgpr_read_b32 v85, a89\n"
    "v_accvgpr_read_b32 v86, a90\n"
    "v_accvgpr_read_b32 v87, a91\n"
    "v_mul_f32 v80, v12, v80\n"
    "v_mul_f32 v81, v13, v81\n"
    "v_mul_f32 v82, v14, v82\n"
    "v_mul_f32 v83, v15, v83\n"
    "v_mul_f32 v84, v12, v84\n"
    "v_mul_f32 v85, v13, v85\n"
    "v_mul_f32 v86, v14, v86\n"
    "v_mul_f32 v87, v15, v87\n"
    "v_accvgpr_write_b32 a72, v80\n"
    "v_accvgpr_write_b32 a73, v81\n"
    "v_accvgpr_write_b32 a74, v82\n"
    "v_accvgpr_write_b32 a75, v83\n"
    "v_accvgpr_write_b32 a88, v84\n"
    "v_accvgpr_write_b32 a89, v85\n"
    "v_accvgpr_write_b32 a90, v86\n"
    "v_accvgpr_write_b32 a91, v87\n"
    "v_accvgpr_read_b32 v80, a104\n"
    "v_accvgpr_read_b32 v81, a105\n"
    "v_accvgpr_read_b32 v82, a106\n"
    "v_accvgpr_read_b32 v83, a107\n"
    "v_accvgpr_read_b32 v84, a120\n"
    "v_accvgpr_read_b32 v85, a121\n"
    "v_accvgpr_read_b32 v86, a122\n"
    "v_accvgpr_read_b32 v87, a123\n"
    "v_mul_f32 v80, v12, v80\n"
    "v_mul_f32 v81, v13, v81\n"
    "v_mul_f32 v82, v14, v82\n"
    "v_mul_f32 v83, v15, v83\n"
    "v_mul_f32 v84, v12, v84\n"
    "v_mul_f32 v85, v13, v85\n"
    "v_mul_f32 v86, v14, v86\n"
    "v_mul_f32 v87, v15, v87\n"
    "v_accvgpr_write_b32 a104, v80\n"
    "v_accvgpr_write_b32 a105, v81\n"
    "v_accvgpr_write_b32 a106, v82\n"
    "v_accvgpr_write_b32 a107, v83\n"
    "v_accvgpr_write_b32 a120, v84\n"
    "v_accvgpr_write_b32 a121, v85\n"
    "v_accvgpr_write_b32 a122, v86\n"
    "v_accvgpr_write_b32 a123, v87\n"
    "s_nop 7\n"
    "s_nop 7\n"
    "s_branch L_ret_B2\n"
    "L_rare_B3:\n"
    "s_nop 7\n"
    "s_nop 7\n"
    "s_sub_u32 s56, s48, 0x20000\n"
    "s_add_u32 s57, s56, 0x0\n"
    "s_add_u32 s58, s56, 0x1000\n"
    "buffer_load_dwordx4 a[224:227], v76, s[40:43], s57 offen offset:0 sc1\n"
    "buffer_load_dwordx4 a[228:231], v76, s[40:43], s57 offen offset:1024 sc1\n"
    "buffer_load_dwordx4 a[232:235], v76, s[40:43], s57 offen offset:2048 sc1\n"
    "buffer_load_dwordx4 a[236:239], v76, s[40:43], s57 offen offset:3072 sc1\n"
    "buffer_load_dwordx4 a[240:243], v76, s[40:43], s58 offen offset:0 sc1\n"
    "buffer_load_dwordx4 a[244:247], v76, s[40:43], s58 offen offset:1024 sc1\n"
    "buffer_load_dwordx4 a[248:251], v76, s[40:43], s58 offen offset:2048 sc1\n"
    "buffer_load_dwordx4 a[252:255], v76, s[40:43], s58 offen offset:3072 sc1\n"
    "s_waitcnt vmcnt(0)\n"
    "v_mfma_f32_16x16x32_bf16 v[80:83], a[224:227], a[176:179], v[44:47]\n"
    "v_mfma_f32_16x16x32_bf16 v[80:83], a[228:231], a[180:183], v[80:83]\n"
    "v_mfma_f32_16x16x32_bf16 v[80:83], a[232:235], a[184:187], v[80:83]\n"
    "v_mfma_f32_16x16x32_bf16 v[80:83], a[236:239], a[188:191], v[80:83]\n"
    "v_mfma_f32_16x16x32_bf16 v[84:87], a[240:243], a[176:179], v[44:47]\n"
    "v_mfma_f32_16x16x32_bf16 v[84:87], a[244:247], a[180:183], v[84:87]\n"
    "v_mfma_f32_16x16x32_bf16 v[84:87], a[248:251], a[184:187], v[84:87]\n"
    "v_mfma_f32_16x16x32_bf16 v[84:87], a[252:255], a[188:191], v[84:87]\n"
    "s_nop 3\n"
    "v_max_f32 v9, v80, v80\n"
    "v_max3_f32 v9, v9, v80, v81\n"
    "v_max3_f32 v9, v9, v82, v83\n"
    "s_nop 0\n"
    "v_max3_f32 v9, v9, v84, v85\n"
    "v_max3_f32 v9, v9, v86, v87\n"
    "s_add_u32 s57, s56, 0x2000\n"
    "s_add_u32 s58, s56, 0x3000\n"
    "buffer_load_dwordx4 a[224:227], v76, s[40:43], s57 offen offset:0 sc1\n"
    "buffer_load_dwordx4 a[228:231], v76, s[40:43], s57 offen offset:1024 sc1\n"
    "buffer_load_dwordx4 a[232:235], v76, s[40:43], s57 offen offset:2048 sc1\n"
    "buffer_load_dwordx4 a[236:239], v76, s[40:43], s57 offen offset:3072 sc1\n"
    "buffer_load_dwordx4 a[240:243], v76, s[40:43], s58 offen offset:0 sc1\n"
    "buffer_load_dwordx4 a[244:247], v76, s[40:43], s58 offen offset:1024 sc1\n"
    "buffer_load_dwordx4 a[248:251], v76, s[40:43], s58 offen offset:2048 sc1\n"
    "buffer_load_dwordx4 a[252:255], v76, s[40:43], s58 offen offset:3072 sc1\n"
    "s_waitcnt vmcnt(0)\n"
    "v_mfma_f32_16x16x32_bf16 v[80:83], a[224:227], a[176:179], v[44:47]\n"
    "v_mfma_f32_16x16x32_bf16 v[80:83], a[228:231], a[180:183], v[80:83]\n"
    "v_mfma_f32_16x16x32_bf16 v[80:83], a[232:235], a[184:187], v[80:83]\n"
    "v_mfma_f32_16x16x32_bf16 v[80:83], a[236:239], a[188:191], v[80:83]\n"
    "v_mfma_f32_16x16x32_bf16 v[84:87], a[240:243], a[176:179], v[44:47]\n"
    "v_mfma_f32_16x16x32_bf16 v[84:87], a[244:247], a[180:183], v[84:87]\n"
    "v_mfma_f32_16x16x32_bf16 v[84:87], a[248:251], a[184:187], v[84:87]\n"
    "v_mfma_f32_16x16x32_bf16 v[84:87], a[252:255], a[188:191], v[84:87]\n"
    "s_nop 3\n"
    "v_max3_f32 v9, v9, v80, v81\n"
    "v_max3_f32 v9, v9, v82, v83\n"
    "s_nop 1\n"
    "v_max3_f32 v9, v9, v84, v85\n"
    "v_max3_f32 v9, v9, v86, v87\n"
    "s_add_u32 s57, s56, 0x4000\n"
    "s_add_u32 s58, s56, 0x5000\n"
    "buffer_load_dwordx4 a[224:227], v76, s[40:43], s57 offen offset:0 sc1\n"
    "buffer_load_dwordx4 a[228:231], v76, s[40:43], s57 offen offset:1024 sc1\n"
    "buffer_load_dwordx4 a[232:235], v76, s[40:43], s57 offen offset:2048 sc1\n"
    "buffer_load_dwordx4 a[236:239], v76, s[40:43], s57 offen offset:3072 sc1\n"
    "buffer_load_dwordx4 a[240:243], v76, s[40:43], s58 offen offset:0 sc1\n"
    "buffer_load_dwordx4 a[244:247], v76, s[40:43], s58 offen offset:1024 sc1\n"
    "buffer_load_dwordx4 a[248:251], v76, s[40:43], s58 offen offset:2048 sc1\n"
    "buffer_load_dwordx4 a[252:255], v76, s[40:43], s58 offen offset:3072 sc1\n"
    "s_waitcnt vmcnt(0)\n"
    "v_mfma_f32_16x16x32_bf16 v[80:83], a[224:227], a[176:179], v[44:47]\n"
    "v_mfma_f32_16x16x32_bf16 v[80:83], a[228:231], a[180:183], v[80:83]\n"
    "v_mfma_f32_16x16x32_bf16 v[80:83], a[232:235], a[184:187], v[80:83]\n"
    "v_mfma_f32_16x16x32_bf16 v[80:83], a[236:239], a[188:191], v[80:83]\n"
    "v_mfma_f32_16x16x32_bf16 v[84:87], a[240:243], a[176:179], v[44:47]\n"
    "v_mfma_f32_16x16x32_bf16 v[84:87], a[244:247], a[180:183], v[84:87]\n"
    "v_mfma_f32_16x16x32_bf16 v[84:87], a[248:251], a[184:187], v[84:87]\n"
    "v_mfma_f32_16x16x32_bf16 v[84:87], a[252:255], a[188:191], v[84:87]\n"
    "s_nop 3\n"
    "v_max3_f32 v9, v9, v80, v81\n"
    "v_max3_f32 v9, v9, v82, v83\n"
    "s_nop 1\n"
    "v_max3_f32 v9, v9, v84, v85\n"
    "v_max3_f32 v9, v9, v86, v87\n"
    "s_add_u32 s57, s56, 0x6000\n"
    "s_add_u32 s58, s56, 0x7000\n"
    "buffer_load_dwordx4 a[224:227], v76, s[40:43], s57 offen offset:0 sc1\n"
    "buffer_load_dwordx4 a[228:231], v76, s[40:43], s57 offen offset:1024 sc1\n"
    "buffer_load_dwordx4 a[232:235], v76, s[40:43], s57 offen offset:2048 sc1\n"
    "buffer_load_dwordx4 a[236:239], v76, s[40:43], s57 offen offset:3072 sc1\n"
    "buffer_load_dwordx4 a[240:243], v76, s[40:43], s58 offen offset:0 sc1\n"
    "buffer_load_dwordx4 a[244:247], v76, s[40:43], s58 offen offset:1024 sc1\n"
    "buffer_load_dwordx4 a[248:251], v76, s[40:43], s58 offen offset:2048 sc1\n"
    "buffer_load_dwordx4 a[252:255], v76, s[40:43], s58 offen offset:3072 sc1\n"
    "s_waitcnt vmcnt(0)\n"
    "v_mfma_f32_16x16x32_bf16 v[80:83], a[224:227], a[176:179], v[44:47]\n"
    "v_mfma_f32_16x16x32_bf16 v[80:83], a[228:231], a[180:183], v[80:83]\n"
    "v_mfma_f32_16x16x32_bf16 v[80:83], a[232:235], a[184:187], v[80:83]\n"
    "v_mfma_f32_16x16x32_bf16 v[80:83], a[236:239], a[188:191], v[80:83]\n"
    "v_mfma_f32_16x16x32_bf16 v[84:87], a[240:243], a[176:179], v[44:47]\n"
    "v_mfma_f32_16x16x32_bf16 v[84:87], a[244:247], a[180:183], v[84:87]\n"
    "v_mfma_f32_16x16x32_bf16 v[84:87], a[248:251], a[184:187], v[84:87]\n"
    "v_mfma_f32_16x16x32_bf16 v[84:87], a[252:255], a[188:191], v[84:87]\n"
    "s_nop 3\n"
    "v_max3_f32 v9, v9, v80, v81\n"
    "v_max3_f32 v9, v9, v82, v83\n"
    "s_nop 1\n"
    "v_max3_f32 v9, v9, v84, v85\n"
    "v_max3_f32 v9, v9, v86, v87\n"
    "v_mov_b32 v10, v9\n"
    "s_nop 1\n"
    "v_permlane16_swap_b32 v10, v9\n"
    "v_max_f32 v9, v10, v9\n"
    "v_mov_b32 v10, v9\n"
    "s_nop 1\n"
    "v_permlane32_swap_b32 v10, v9\n"
    "v_max_f32 v9, v10, v9\n"
    "v_max_f32 v9, 0, v9\n"
    "v_sub_f32 v44, v44, v9\n"
    "v_sub_f32 v45, v45, v9\n"
    "v_sub_f32 v46, v46, v9\n"
    "v_sub_f32 v47, v47, v9\n"
    "s_add_u32 s57, s56, 0x0\n"
    "s_add_u32 s58, s56, 0x1000\n"
    "buffer_load_dwordx4 a[224:227], v76, s[40:43], s57 offen offset:0 sc1\n"
    "buffer_load_dwordx4 a[228:231], v76, s[40:43], s57 offen offset:1024 sc1\n"
    "buffer_load_dwordx4 a[232:235], v76, s[40:43], s57 offen offset:2048 sc1\n"
    "buffer_load_dwordx4 a[236:239], v76, s[40:43], s57 offen offset:3072 sc1\n"
    "buffer_load_dwordx4 a[240:243], v76, s[40:43], s58 offen offset:0 sc1\n"
    "buffer_load_dwordx4 a[244:247], v76, s[40:43], s58 offen offset:1024 sc1\n"
    "buffer_load_dwordx4 a[248:251], v76, s[40:43], s58 offen offset:2048 sc1\n"
    "buffer_load_dwordx4 a[252:255], v76, s[40:43], s58 offen offset:3072 sc1\n"
    "s_waitcnt vmcnt(0)\n"
    "v_mfma_f32_16x16x32_bf16 v[80:83], a[224:227], a[176:179], v[44:47]\n"
    "v_mfma_f32_16x16x32_bf16 v[80:83], a[228:231], a[180:183], v[80:83]\n"
    "v_mfma_f32_16x16x32_bf16 v[80:83], a[232:235], a[184:187], v[80:83]\n"
    "v_mfma_f32_16x16x32_bf16 v[80:83], a[236:239], a[188:191], v[80:83]\n"
    "v_mfma_f32_16x16x32_bf16 v[84:87], a[240:243], a[176:179], v[44:47]\n"
    "v_mfma_f32_16x16x32_bf16 v[84:87], a[244:247], a[180:183], v[84:87]\n"
    "v_mfma_f32_16x16x32_bf16 v[84:87], a[248:251], a[184:187], v[84:87]\n"
    "v_mfma_f32_16x16x32_bf16 v[84:87], a[252:255], a[188:191], v[84:87]\n"
    "s_nop 3\n"
    "v_exp_f32 v80, v80\n"
    "v_exp_f32 v81, v81\n"
    "v_exp_f32 v82, v82\n"
    "v_exp_f32 v83, v83\n"
    "v_exp_f32 v84, v84\n"
    "v_exp_f32 v85, v85\n"
    "v_exp_f32 v86, v86\n"
    "v_exp_f32 v87, v87\n"
    "v_cvt_pk_bf16_f32 v240, v80, v81\n"
    "v_cvt_pk_bf16_f32 v241, v82, v83\n"
    "v_cvt_pk_bf16_f32 v242, v84, v85\n"
    "v_cvt_pk_bf16_f32 v243, v86, v87\n"
    "s_add_u32 s57, s56, 0x2000\n"
    "s_add_u32 s58, s56, 0x3000\n"
    "buffer_load_dwordx4 a[224:227], v76, s[40:43], s57 offen offset:0 sc1\n"
    "buffer_load_dwordx4 a[228:231], v76, s[40:43], s57 offen offset:1024 sc1\n"
    "buffer_load_dwordx4 a[232:235], v76, s[40:43], s57 offen offset:2048 sc1\n"
    "buffer_load_dwordx4 a[236:239], v76, s[40:43], s57 offen offset:3072 sc1\n"
    "buffer_load_dwordx4 a[240:243], v76, s[40:43], s58 offen offset:0 sc1\n"
    "buffer_load_dwordx4 a[244:247], v76, s[40:43], s58 offen offset:1024 sc1\n"
    "buffer_load_dwordx4 a[248:251], v76, s[40:43], s58 offen offset:2048 sc1\n"
    "buffer_load_dwordx4 a[252:255], v76, s[40:43], s58 offen offset:3072 sc1\n"
    "s_waitcnt vmcnt(0)\n"
    "v_mfma_f32_16x16x32_bf16 v[80:83], a[224:227], a[176:179], v[44:47]\n"
    "v_mfma_f32_16x16x32_bf16 v[80:83], a[228:231], a[180:183], v[80:83]\n"
    "v_mfma_f32_16x16x32_bf16 v[80:83], a[232:235], a[184:187], v[80:83]\n"
    "v_mfma_f32_16x16x32_bf16 v[80:83], a[236:239], a[188:191], v[80:83]\n"
    "v_mfma_f32_16x16x32_bf16 v[84:87], a[240:243], a[176:179], v[44:47]\n"
    "v_mfma_f32_16x16x32_bf16 v[84:87], a[244:247], a[180:183], v[84:87]\n"
    "v_mfma_f32_16x16x32_bf16 v[84:87], a[248:251], a[184:187], v[84:87]\n"
    "v_mfma_f32_16x16x32_bf16 v[84:87], a[252:255], a[188:191], v[84:87]\n"
    "s_nop 3\n"
    "v_exp_f32 v80, v80\n"
    "v_exp_f32 v81, v81\n"
    "v_exp_f32 v82, v82\n"
    "v_exp_f32 v83, v83\n"
    "v_exp_f32 v84, v84\n"
    "v_exp_f32 v85, v85\n"
    "v_exp_f32 v86, v86\n"
    "v_exp_f32 v87, v87\n"
    "v_cvt_pk_bf16_f32 v244, v80, v81\n"
    "v_cvt_pk_bf16_f32 v245, v82, v83\n"
    "v_cvt_pk_bf16_f32 v246, v84, v85\n"
    "v_cvt_pk_bf16_f32 v247, v86, v87\n"
    "s_add_u32 s57, s56, 0x4000\n"
    "s_add_u32 s58, s56, 0x5000\n"
    "buffer_load_dwordx4 a[224:227], v76, s[40:43], s57 offen offset:0 sc1\n"
    "buffer_load_dwordx4 a[228:231], v76, s[40:43], s57 offen offset:1024 sc1\n"
    "buffer_load_dwordx4 a[232:235], v76, s[40:43], s57 offen offset:2048 sc1\n"
    "buffer_load_dwordx4 a[236:239], v76, s[40:43], s57 offen offset:3072 sc1\n"
    "buffer_load_dwordx4 a[240:243], v76, s[40:43], s58 offen offset:0 sc1\n"
    "buffer_load_dwordx4 a[244:247], v76, s[40:43], s58 offen offset:1024 sc1\n"
    "buffer_load_dwordx4 a[248:251], v76, s[40:43], s58 offen offset:2048 sc1\n"
    "buffer_load_dwordx4 a[252:255], v76, s[40:43], s58 offen offset:3072 sc1\n"
    "s_waitcnt vmcnt(0)\n"
    "v_mfma_f32_16x16x32_bf16 v[80:83], a[224:227], a[176:179], v[44:47]\n"
    "v_mfma_f32_16x16x32_bf16 v[80:83], a[228:231], a[180:183], v[80:83]\n"
    "v_mfma_f32_16x16x32_bf16 v[80:83], a[232:235], a[184:187], v[80:83]\n"
    "v_mfma_f32_16x16x32_bf16 v[80:83], a[236:239], a[188:191], v[80:83]\n"
    "v_mfma_f32_16x16x32_bf16 v[84:87], a[240:243], a[176:179], v[44:47]\n"
    "v_mfma_f32_16x16x32_bf16 v[84:87], a[244:247], a[180:183], v[84:87]\n"
    "v_mfma_f32_16x16x32_bf16 v[84:87], a[248:251], a[184:187], v[84:87]\n"
    "v_mfma_f32_16x16x32_bf16 v[84:87], a[252:255], a[188:191], v[84:87]\n"
    "s_nop 3\n"
    "v_exp_f32 v80, v80\n"
    "v_exp_f32 v81, v81\n"
    "v_exp_f32 v82, v82\n"
    "v_exp_f32 v83, v83\n"
    "v_exp_f32 v84, v84\n"
    "v_exp_f32 v85, v85\n"
    "v_exp_f32 v86, v86\n"
    "v_exp_f32 v87, v87\n"
    "v_cvt_pk_bf16_f32 v248, v80, v81\n"
    "v_cvt_pk_bf16_f32 v249, v82, v83\n"
    "v_cvt_pk_bf16_f32 v250, v84, v85\n"
    "v_cvt_pk_bf16_f32 v251, v86, v87\n"
    "s_add_u32 s57, s56, 0x6000\n"
    "s_add_u32 s58, s56, 0x7000\n"
    "buffer_load_dwordx4 a[224:227], v76, s[40:43], s57 offen offset:0 sc1\n"
    "buffer_load_dwordx4 a[228:231], v76, s[40:43], s57 offen offset:1024 sc1\n"
    "buffer_load_dwordx4 a[232:235], v76, s[40:43], s57 offen offset:2048 sc1\n"
    "buffer_load_dwordx4 a[236:239], v76, s[40:43], s57 offen offset:3072 sc1\n"
    "buffer_load_dwordx4 a[240:243], v76, s[40:43], s58 offen offset:0 sc1\n"
    "buffer_load_dwordx4 a[244:247], v76, s[40:43], s58 offen offset:1024 sc1\n"
    "buffer_load_dwordx4 a[248:251], v76, s[40:43], s58 offen offset:2048 sc1\n"
    "buffer_load_dwordx4 a[252:255], v76, s[40:43], s58 offen offset:3072 sc1\n"
    "s_waitcnt vmcnt(0)\n"
    "v_mfma_f32_16x16x32_bf16 v[80:83], a[224:227], a[176:179], v[44:47]\n"
    "v_mfma_f32_16x16x32_bf16 v[80:83], a[228:231], a[180:183], v[80:83]\n"
    "v_mfma_f32_16x16x32_bf16 v[80:83], a[232:235], a[184:187], v[80:83]\n"
    "v_mfma_f32_16x16x32_bf16 v[80:83], a[236:239], a[188:191], v[80:83]\n"
    "v_mfma_f32_16x16x32_bf16 v[84:87], a[240:243], a[176:179], v[44:47]\n"
    "v_mfma_f32_16x16x32_bf16 v[84:87], a[244:247], a[180:183], v[84:87]\n"
    "v_mfma_f32_16x16x32_bf16 v[84:87], a[248:251], a[184:187], v[84:87]\n"
    "v_mfma_f32_16x16x32_bf16 v[84:87], a[252:255], a[188:191], v[84:87]\n"
    "s_nop 3\n"
    "v_exp_f32 v80, v80\n"
    "v_exp_f32 v81, v81\n"
    "v_exp_f32 v82, v82\n"
    "v_exp_f32 v83, v83\n"
    "v_exp_f32 v84, v84\n"
    "v_exp_f32 v85, v85\n"
    "v_exp_f32 v86, v86\n"
    "v_exp_f32 v87, v87\n"
    "v_cvt_pk_bf16_f32 v252, v80, v81\n"
    "v_cvt_pk_bf16_f32 v253, v82, v83\n"
    "v_cvt_pk_bf16_f32 v254, v84, v85\n"
    "v_cvt_pk_bf16_f32 v255, v86, v87\n"
    "v_mfma_f32_16x16x32_bf16 v[60:63], v[64:67], v[240:243], 0\n"
    "v_mfma_f32_16x16x32_bf16 v[60:63], v[64:67], v[244:247], v[60:63]\n"
    "v_mfma_f32_16x16x32_bf16 v[60:63], v[64:67], v[248:251], v[60:63]\n"
    "v_mfma_f32_16x16x32_bf16 v[60:63], v[64:67], v[252:255], v[60:63]\n"
    "v_sub_f32 v28, v28, v9\n"
    "v_sub_f32 v29, v29, v9\n"
    "v_sub_f32 v30, v30, v9\n"
    "v_sub_f32 v31, v31, v9\n"
    "v_sub_f32 v11, 0, v9\n"
    "v_exp_f32 v11, v11\n"
    "s_nop 0\n"
    "v_mul_f32 v71, v11, v71\n"
    "ds_bpermute_b32 v12, v99, v11 offset:0\n"
    "ds_bpermute_b32 v13, v99, v11 offset:4\n"
    "ds_bpermute_b32 v14, v99, v11 offset:8\n"
    "ds_bpermute_b32 v15, v99, v11 offset:12\n"
    "s_waitcnt lgkmcnt(0)\n"
    "v_accvgpr_read_b32 v80, a12\n"
    "v_accvgpr_read_b32 v81, a13\n"
    "v_accvgpr_read_b32 v82, a14\n"
    "v_accvgpr_read_b32 v83, a15\n"
    "v_accvgpr_read_b32 v84, a28\n"
    "v_accvgpr_read_b32 v85, a29\n"
    "v_accvgpr_read_b32 v86, a30\n"
    "v_accvgpr_read_b32 v87, a31\n"
    "v_mul_f32 v80, v12, v80\n"
    "v_mul_f32 v81, v13, v81\n"
    "v_mul_f32 v82, v14, v82\n"
    "v_mul_f32 v83, v15, v83\n"
    "v_mul_f32 v84, v12, v84\n"
    "v_mul_f32 v85, v13, v85\n"
    "v_mul_f32 v86, v14, v86\n"
    "v_mul_f32 v87, v15, v87\n"
    "v_accvgpr_write_b32 a12, v80\n"
    "v_accvgpr_write_b32 a13, v81\n"
    "v_accvgpr_write_b32 a14, v82\n"
    "v_accvgpr_write_b32 a15, v83\n"
    "v_accvgpr_write_b32 a28, v84\n"
    "v_accvgpr_write_b32 a29, v85\n"
    "v_accvgpr_write_b32 a30, v86\n"
    "v_accvgpr_write_b32 a31, v87\n"
    "v_accvgpr_read_b32 v80, a44\n"
    "v_accvgpr_read_b32 v81, a45\n"
    "v_accvgpr_read_b32 v82, a46\n"
    "v_accvgpr_read_b32 v83, a47\n"
    "v_accvgpr_read_b32 v84, a60\n"
    "v_accvgpr_read_b32 v85, a61\n"
    "v_accvgpr_read_b32 v86, a62\n"
    "v_accvgpr_read_b32 v87, a63\n"
    "v_mul_f32 v80, v12, v80\n"
    "v_mul_f32 v81, v13, v81\n"
    "v_mul_f32 v82, v14, v82\n"
    "v_mul_f32 v83, v15, v83\n"
    "v_mul_f32 v84, v12, v84\n"
    "v_mul_f32 v85, v13, v85\n"
    "v_mul_f32 v86, v14, v86\n"
    "v_mul_f32 v87, v15, v87\n"
    "v_accvgpr_write_b32 a44, v80\n"
    "v_accvgpr_write_b32 a45, v81\n"
    "v_accvgpr_write_b32 a46, v82\n"
    "v_accvgpr_write_b32 a47, v83\n"
    "v_accvgpr_write_b32 a60, v84\n"
    "v_accvgpr_write_b32 a61, v85\n"
    "v_accvgpr_write_b32 a62, v86\n"
    "v_accvgpr_write_b32 a63, v87\n"
    "v_accvgpr_read_b32 v80, a76\n"
    "v_accvgpr_read_b32 v81, a77\n"
    "v_accvgpr_read_b32 v82, a78\n"
    "v_accvgpr_read_b32 v83, a79\n"
    "v_accvgpr_read_b32 v84, a92\n"
    "v_accvgpr_read_b32 v85, a93\n"
    "v_accvgpr_read_b32 v86, a94\n"
    "v_accvgpr_read_b32 v87, a95\n"
    "v_mul_f32 v80, v12, v80\n"
    "v_mul_f32 v81, v13, v81\n"
    "v_mul_f32 v82, v14, v82\n"
    "v_mul_f32 v83, v15, v83\n"
    "v_mul_f32 v84, v12, v84\n"
    "v_mul_f32 v85, v13, v85\n"
    "v_mul_f32 v86, v14, v86\n"
    "v_mul_f32 v87, v15, v87\n"
    "v_accvgpr_write_b32 a76, v80\n"
    "v_accvgpr_write_b32 a77, v81\n"
    "v_accvgpr_write_b32 a78, v82\n"
    "v_accvgpr_write_b32 a79, v83\n"
    "v_accvgpr_write_b32 a92, v84\n"
    "v_accvgpr_write_b32 a93, v85\n"
    "v_accvgpr_write_b32 a94, v86\n"
    "v_accvgpr_write_b32 a95, v87\n"
    "v_accvgpr_read_b32 v80, a108\n"
    "v_accvgpr_read_b32 v81, a109\n"
    "v_accvgpr_read_b32 v82, a110\n"
    "v_accvgpr_read_b32 v83, a111\n"
    "v_accvgpr_read_b32 v84, a124\n"
    "v_accvgpr_read_b32 v85, a125\n"
    "v_accvgpr_read_b32 v86, a126\n"
    "v_accvgpr_read_b32 v87, a127\n"
    "v_mul_f32 v80, v12, v80\n"
    "v_mul_f32 v81, v13, v81\n"
    "v_mul_f32 v82, v14, v82\n"
    "v_mul_f32 v83, v15, v83\n"
    "v_mul_f32 v84, v12, v84\n"
    "v_mul_f32 v85, v13, v85\n"
    "v_mul_f32 v86, v14, v86\n"
    "v_mul_f32 v87, v15, v87\n"
    "v_accvgpr_write_b32 a108, v80\n"
    "v_accvgpr_write_b32 a109, v81\n"
    "v_accvgpr_write_b32 a110, v82\n"
    "v_accvgpr_write_b32 a111, v83\n"
    "v_accvgpr_write_b32 a124, v84\n"
    "v_accvgpr_write_b32 a125, v85\n"
    "v_accvgpr_write_b32 a126, v86\n"
    "v_accvgpr_write_b32 a127, v87\n"
    "s_nop 7\n"
    "s_nop 7\n"
    "s_branch L_ret_B3\n"
    "L_rare_C0:\n"
    "s_nop 7\n"
    "s_nop 7\n"
    "s_sub_u32 s56, s48, 0x20000\n"
    "s_add_u32 s57, s56, 0x0\n"
    "s_add_u32 s58, s56, 0x1000\n"
    "buffer_load_dwordx4 a[224:227], v76, s[40:43], s57 offen offset:0 sc1\n"
    "buffer_load_dwordx4 a[228:231], v76, s[40:43], s57 offen offset:1024 sc1\n"
    "buffer_load_dwordx4 a[232:235], v76, s[40:43], s57 offen offset:2048 sc1\n"
    "buffer_load_dwordx4 a[236:239], v76, s[40:43], s57 offen offset:3072 sc1\n"
    "buffer_load_dwordx4 a[240:243], v76, s[40:43], s58 offen offset:0 sc1\n"
    "buffer_load_dwordx4 a[244:247], v76, s[40:43], s58 offen offset:1024 sc1\n"
    "buffer_load_dwordx4 a[248:251], v76, s[40:43], s58 offen offset:2048 sc1\n"
    "buffer_load_dwordx4 a[252:255], v76, s[40:43], s58 offen offset:3072 sc1\n"
    "s_waitcnt vmcnt(0)\n"
    "v_mfma_f32_16x16x32_bf16 v[80:83], a[224:227], a[128:131], v[32:35]\n"
    "v_mfma_f32_16x16x32_bf16 v[80:83], a[228:231], a[132:135], v[80:83]\n"
    "v_mfma_f32_16x16x32_bf16 v[80:83], a[232:235], a[136:139], v[80:83]\n"
    "v_mfma_f32_16x16x32_bf16 v[80:83], a[236:239], a[140:143], v[80:83]\n"
    "v_mfma_f32_16x16x32_bf16 v[84:87], a[240:243], a[128:131], v[32:35]\n"
    "v_mfma_f32_16x16x32_bf16 v[84:87], a[244:247], a[132:135], v[84:87]\n"
    "v_mfma_f32_16x16x32_bf16 v[84:87], a[248:251], a[136:139], v[84:87]\n"
    "v_mfma_f32_16x16x32_bf16 v[84:87], a[252:255], a[140:143], v[84:87]\n"
    "s_nop 3\n"
    "v_max_f32 v9, v80, v80\n"
    "v_max3_f32 v9, v9, v80, v81\n"
    "v_max3_f32 v9, v9, v82, v83\n"
    "s_nop 0\n"
    "v_max3_f32 v9, v9, v84, v85\n"
    "v_max3_f32 v9, v9, v86, v87\n"
    "s_add_u32 s57, s56, 0x2000\n"
    "s_add_u32 s58, s56, 0x3000\n"
    "buffer_load_dwordx4 a[224:227], v76, s[40:43], s57 offen offset:0 sc1\n"
    "buffer_load_dwordx4 a[228:231], v76, s[40:43], s57 offen offset:1024 sc1\n"
    "buffer_load_dwordx4 a[232:235], v76, s[40:43], s57 offen offset:2048 sc1\n"
    "buffer_load_dwordx4 a[236:239], v76, s[40:43], s57 offen offset:3072 sc1\n"
    "buffer_load_dwordx4 a[240:243], v76, s[40:43], s58 offen offset:0 sc1\n"
    "buffer_load_dwordx4 a[244:247], v76, s[40:43], s58 offen offset:1024 sc1\n"
    "buffer_load_dwordx4 a[248:251], v76, s[40:43], s58 offen offset:2048 sc1\n"
    "buffer_load_dwordx4 a[252:255], v76, s[40:43], s58 offen offset:3072 sc1\n"
    "s_waitcnt vmcnt(0)\n"
    "v_mfma_f32_16x16x32_bf16 v[80:83], a[224:227], a[128:131], v[32:35]\n"
    "v_mfma_f32_16x16x32_bf16 v[80:83], a[228:231], a[132:135], v[80:83]\n"
    "v_mfma_f32_16x16x32_bf16 v[80:83], a[232:235], a[136:139], v[80:83]\n"
    "v_mfma_f32_16x16x32_bf16 v[80:83], a[236:239], a[140:143], v[80:83]\n"
    "v_mfma_f32_16x16x32_bf16 v[84:87], a[240:243], a[128:131], v[32:35]\n"
    "v_mfma_f32_16x16x32_bf16 v[84:87], a[244:247], a[132:135], v[84:87]\n"
    "v_mfma_f32_16x16x32_bf16 v[84:87], a[248:251], a[136:139], v[84:87]\n"
    "v_mfma_f32_16x16x32_bf16 v[84:87], a[252:255], a[140:143], v[84:87]\n"
    "s_nop 3\n"
    "v_max3_f32 v9, v9, v80, v81\n"
    "v_max3_f32 v9, v9, v82, v83\n"
    "s_nop 1\n"
    "v_max3_f32 v9, v9, v84, v85\n"
    "v_max3_f32 v9, v9, v86, v87\n"
    "s_add_u32 s57, s56, 0x4000\n"
    "s_add_u32 s58, s56, 0x5000\n"
    "buffer_load_dwordx4 a[224:227], v76, s[40:43], s57 offen offset:0 sc1\n"
    "buffer_load_dwordx4 a[228:231], v76, s[40:43], s57 offen offset:1024 sc1\n"
    "buffer_load_dwordx4 a[232:235], v76, s[40:43], s57 offen offset:2048 sc1\n"
    "buffer_load_dwordx4 a[236:239], v76, s[40:43], s57 offen offset:3072 sc1\n"
    "buffer_load_dwordx4 a[240:243], v76, s[40:43], s58 offen offset:0 sc1\n"
    "buffer_load_dwordx4 a[244:247], v76, s[40:43], s58 offen offset:1024 sc1\n"
    "buffer_load_dwordx4 a[248:251], v76, s[40:43], s58 offen offset:2048 sc1\n"
    "buffer_load_dwordx4 a[252:255], v76, s[40:43], s58 offen offset:3072 sc1\n"
    "s_waitcnt vmcnt(0)\n"
    "v_mfma_f32_16x16x32_bf16 v[80:83], a[224:227], a[128:131], v[32:35]\n"
    "v_mfma_f32_16x16x32_bf16 v[80:83], a[228:231], a[132:135], v[80:83]\n"
    "v_mfma_f32_16x16x32_bf16 v[80:83], a[232:235], a[136:139], v[80:83]\n"
    "v_mfma_f32_16x16x32_bf16 v[80:83], a[236:239], a[140:143], v[80:83]\n"
    "v_mfma_f32_16x16x32_bf16 v[84:87], a[240:243], a[128:131], v[32:35]\n"
    "v_mfma_f32_16x16x32_bf16 v[84:87], a[244:247], a[132:135], v[84:87]\n"
    "v_mfma_f32_16x16x32_bf16 v[84:87], a[248:251], a[136:139], v[84:87]\n"
    "v_mfma_f32_16x16x32_bf16 v[84:87], a[252:255], a[140:143], v[84:87]\n"
    "s_nop 3\n"
    "v_max3_f32 v9, v9, v80, v81\n"
    "v_max3_f32 v9, v9, v82, v83\n"
    "s_nop 1\n"
    "v_max3_f32 v9, v9, v84, v85\n"
    "v_max3_f32 v9, v9, v86, v87\n"
    "s_add_u32 s57, s56, 0x6000\n"
    "s_add_u32 s58, s56, 0x7000\n"
    "buffer_load_dwordx4 a[224:227], v76, s[40:43], s57 offen offset:0 sc1\n"
    "buffer_load_dwordx4 a[228:231], v76, s[40:43], s57 offen offset:1024 sc1\n"
    "buffer_load_dwordx4 a[232:235], v76, s[40:43], s57 offen offset:2048 sc1\n"
    "buffer_load_dwordx4 a[236:239], v76, s[40:43], s57 offen offset:3072 sc1\n"
    "buffer_load_dwordx4 a[240:243], v76, s[40:43], s58 offen offset:0 sc1\n"
    "buffer_load_dwordx4 a[244:247], v76, s[40:43], s58 offen offset:1024 sc1\n"
    "buffer_load_dwordx4 a[248:251], v76, s[40:43], s58 offen offset:2048 sc1\n"
    "buffer_load_dwordx4 a[252:255], v76, s[40:43], s58 offen offset:3072 sc1\n"
    "s_waitcnt vmcnt(0)\n"
    "v_mfma_f32_16x16x32_bf16 v[80:83], a[224:227], a[128:131], v[32:35]\n"
    "v_mfma_f32_16x16x32_bf16 v[80:83], a[228:231], a[132:135], v[80:83]\n"
    "v_mfma_f32_16x16x32_bf16 v[80:83], a[232:235], a[136:139], v[80:83]\n"
    "v_mfma_f32_16x16x32_bf16 v[80:83], a[236:239], a[140:143], v[80:83]\n"
    "v_mfma_f32_16x16x32_bf16 v[84:87], a[240:243], a[128:131], v[32:35]\n"
    "v_mfma_f32_16x16x32_bf16 v[84:87], a[244:247], a[132:135], v[84:87]\n"
    "v_mfma_f32_16x16x32_bf16 v[84:87], a[248:251], a[136:139], v[84:87]\n"
    "v_mfma_f32_16x16x32_bf16 v[84:87], a[252:255], a[140:143], v[84:87]\n"
    "s_nop 3\n"
    "v_max3_f32 v9, v9, v80, v81\n"
    "v_max3_f32 v9, v9, v82, v83\n"
    "s_nop 1\n"
    "v_max3_f32 v9, v9, v84, v85\n"
    "v_max3_f32 v9, v9, v86, v87\n"
    "v_mov_b32 v10, v9\n"
    "s_nop 1\n"
    "v_permlane16_swap_b32 v10, v9\n"
    "v_max_f32 v9, v10, v9\n"
    "v_mov_b32 v10, v9\n"
    "s_nop 1\n"
    "v_permlane32_swap_b32 v10, v9\n"
    "v_max_f32 v9, v10, v9\n"
    "v_max_f32 v9, 0, v9\n"
    "v_sub_f32 v32, v32, v9\n"
    "v_sub_f32 v33, v33, v9\n"
    "v_sub_f32 v34, v34, v9\n"
    "v_sub_f32 v35, v35, v9\n"
    "s_add_u32 s57, s56, 0x0\n"
    "s_add_u32 s58, s56, 0x1000\n"
    "buffer_load_dwordx4 a[224:227], v76, s[40:43], s57 offen offset:0 sc1\n"
    "buffer_load_dwordx4 a[228:231], v76, s[40:43], s57 offen offset:1024 sc1\n"
    "buffer_load_dwordx4 a[232:235], v76, s[40:43], s57 offen offset:2048 sc1\n"
    "buffer_load_dwordx4 a[236:239], v76, s[40:43], s57 offen offset:3072 sc1\n"
    "buffer_load_dwordx4 a[240:243], v76, s[40:43], s58 offen offset:0 sc1\n"
    "buffer_load_dwordx4 a[244:247], v76, s[40:43], s58 offen offset:1024 sc1\n"
    "buffer_load_dwordx4 a[248:251], v76, s[40:43], s58 offen offset:2048 sc1\n"
    "buffer_load_dwordx4 a[252:255], v76, s[40:43], s58 offen offset:3072 sc1\n"
    "s_waitcnt vmcnt(0)\n"
    "v_mfma_f32_16x16x32_bf16 v[80:83], a[224:227], a[128:131], v[32:35]\n"
    "v_mfma_f32_16x16x32_bf16 v[80:83], a[228:231], a[132:135], v[80:83]\n"
    "v_mfma_f32_16x16x32_bf16 v[80:83], a[232:235], a[136:139], v[80:83]\n"
    "v_mfma_f32_16x16x32_bf16 v[80:83], a[236:239], a[140:143], v[80:83]\n"
    "v_mfma_f32_16x16x32_bf16 v[84:87], a[240:243], a[128:131], v[32:35]\n"
    "v_mfma_f32_16x16x32_bf16 v[84:87], a[244:247], a[132:135], v[84:87]\n"
    "v_mfma_f32_16x16x32_bf16 v[84:87], a[248:251], a[136:139], v[84:87]\n"
    "v_mfma_f32_16x16x32_bf16 v[84:87], a[252:255], a[140:143], v[84:87]\n"
    "s_nop 3\n"
    "v_exp_f32 v80, v80\n"
    "v_exp_f32 v81, v81\n"
    "v_exp_f32 v82, v82\n"
    "v_exp_f32 v83, v83\n"
    "v_exp_f32 v84, v84\n"
    "v_exp_f32 v85, v85\n"
    "v_exp_f32 v86, v86\n"
    "v_exp_f32 v87, v87\n"
    "v_cvt_pk_bf16_f32 v128, v80, v81\n"
    "v_cvt_pk_bf16_f32 v129, v82, v83\n"
    "v_cvt_pk_bf16_f32 v130, v84, v85\n"
    "v_cvt_pk_bf16_f32 v131, v86, v87\n"
    "s_add_u32 s57, s56, 0x2000\n"
    "s_add_u32 s58, s56, 0x3000\n"
    "buffer_load_dwordx4 a[224:227], v76, s[40:43], s57 offen offset:0 sc1\n"
    "buffer_load_dwordx4 a[228:231], v76, s[40:43], s57 offen offset:1024 sc1\n"
    "buffer_load_dwordx4 a[232:235], v76, s[40:43], s57 offen offset:2048 sc1\n"
    "buffer_load_dwordx4 a[236:239], v76, s[40:43], s57 offen offset:3072 sc1\n"
    "buffer_load_dwordx4 a[240:243], v76, s[40:43], s58 offen offset:0 sc1\n"
    "buffer_load_dwordx4 a[244:247], v76, s[40:43], s58 offen offset:1024 sc1\n"
    "buffer_load_dwordx4 a[248:251], v76, s[40:43], s58 offen offset:2048 sc1\n"
    "buffer_load_dwordx4 a[252:255], v76, s[40:43], s58 offen offset:3072 sc1\n"
    "s_waitcnt vmcnt(0)\n"
    "v_mfma_f32_16x16x32_bf16 v[80:83], a[224:227], a[128:131], v[32:35]\n"
    "v_mfma_f32_16x16x32_bf16 v[80:83], a[228:231], a[132:135], v[80:83]\n"
    "v_mfma_f32_16x16x32_bf16 v[80:83], a[232:235], a[136:139], v[80:83]\n"
    "v_mfma_f32_16x16x32_bf16 v[80:83], a[236:239], a[140:143], v[80:83]\n"
    "v_mfma_f32_16x16x32_bf16 v[84:87], a[240:243], a[128:131], v[32:35]\n"
    "v_mfma_f32_16x16x32_bf16 v[84:87], a[244:247], a[132:135], v[84:87]\n"
    "v_mfma_f32_16x16x32_bf16 v[84:87], a[248:251], a[136:139], v[84:87]\n"
    "v_mfma_f32_16x16x32_bf16 v[84:87], a[252:255], a[140:143], v[84:87]\n"
    "s_nop 3\n"
    "v_exp_f32 v80, v80\n"
    "v_exp_f32 v81, v81\n"
    "v_exp_f32 v82, v82\n"
    "v_exp_f32 v83, v83\n"
    "v_exp_f32 v84, v84\n"
    "v_exp_f32 v85, v85\n"
    "v_exp_f32 v86, v86\n"
    "v_exp_f32 v87, v87\n"
    "v_cvt_pk_bf16_f32 v132, v80, v81\n"
    "v_cvt_pk_bf16_f32 v133, v82, v83\n"
    "v_cvt_pk_bf16_f32 v134, v84, v85\n"
    "v_cvt_pk_bf16_f32 v135, v86, v87\n"
    "s_add_u32 s57, s56, 0x4000\n"
    "s_add_u32 s58, s56, 0x5000\n"
    "buffer_load_dwordx4 a[224:227], v76, s[40:43], s57 offen offset:0 sc1\n"
    "buffer_load_dwordx4 a[228:231], v76, s[40:43], s57 offen offset:1024 sc1\n"
    "buffer_load_dwordx4 a[232:235], v76, s[40:43], s57 offen offset:2048 sc1\n"
    "buffer_load_dwordx4 a[236:239], v76, s[40:43], s57 offen offset:3072 sc1\n"
    "buffer_load_dwordx4 a[240:243], v76, s[40:43], s58 offen offset:0 sc1\n"
    "buffer_load_dwordx4 a[244:247], v76, s[40:43], s58 offen offset:1024 sc1\n"
    "buffer_load_dwordx4 a[248:251], v76, s[40:43], s58 offen offset:2048 sc1\n"
    "buffer_load_dwordx4 a[252:255], v76, s[40:43], s58 offen offset:3072 sc1\n"
    "s_waitcnt vmcnt(0)\n"
    "v_mfma_f32_16x16x32_bf16 v[80:83], a[224:227], a[128:131], v[32:35]\n"
    "v_mfma_f32_16x16x32_bf16 v[80:83], a[228:231], a[132:135], v[80:83]\n"
    "v_mfma_f32_16x16x32_bf16 v[80:83], a[232:235], a[136:139], v[80:83]\n"
    "v_mfma_f32_16x16x32_bf16 v[80:83], a[236:239], a[140:143], v[80:83]\n"
    "v_mfma_f32_16x16x32_bf16 v[84:87], a[240:243], a[128:131], v[32:35]\n"
    "v_mfma_f32_16x16x32_bf16 v[84:87], a[244:247], a[132:135], v[84:87]\n"
    "v_mfma_f32_16x16x32_bf16 v[84:87], a[248:251], a[136:139], v[84:87]\n"
    "v_mfma_f32_16x16x32_bf16 v[84:87], a[252:255], a[140:143], v[84:87]\n"
    "s_nop 3\n"
    "v_exp_f32 v80, v80\n"
    "v_exp_f32 v81, v81\n"
    "v_exp_f32 v82, v82\n"
    "v_exp_f32 v83, v83\n"
    "v_exp_f32 v84, v84\n"
    "v_exp_f32 v85, v85\n"
    "v_exp_f32 v86, v86\n"
    "v_exp_f32 v87, v87\n"
    "v_cvt_pk_bf16_f32 v136, v80, v81\n"
    "v_cvt_pk_bf16_f32 v137, v82, v83\n"
    "v_cvt_pk_bf16_f32 v138, v84, v85\n"
    "v_cvt_pk_bf16_f32 v139, v86, v87\n"
    "s_add_u32 s57, s56, 0x6000\n"
    "s_add_u32 s58, s56, 0x7000\n"
    "buffer_load_dwordx4 a[224:227], v76, s[40:43], s57 offen offset:0 sc1\n"
    "buffer_load_dwordx4 a[228:231], v76, s[40:43], s57 offen offset:1024 sc1\n"
    "buffer_load_dwordx4 a[232:235], v76, s[40:43], s57 offen offset:2048 sc1\n"
    "buffer_load_dwordx4 a[236:239], v76, s[40:43], s57 offen offset:3072 sc1\n"
    "buffer_load_dwordx4 a[240:243], v76, s[40:43], s58 offen offset:0 sc1\n"
    "buffer_load_dwordx4 a[244:247], v76, s[40:43], s58 offen offset:1024 sc1\n"
    "buffer_load_dwordx4 a[248:251], v76, s[40:43], s58 offen offset:2048 sc1\n"
    "buffer_load_dwordx4 a[252:255], v76, s[40:43], s58 offen offset:3072 sc1\n"
    "s_waitcnt vmcnt(0)\n"
    "v_mfma_f32_16x16x32_bf16 v[80:83], a[224:227], a[128:131], v[32:35]\n"
    "v_mfma_f32_16x16x32_bf16 v[80:83], a[228:231], a[132:135], v[80:83]\n"
    "v_mfma_f32_16x16x32_bf16 v[80:83], a[232:235], a[136:139], v[80:83]\n"
    "v_mfma_f32_16x16x32_bf16 v[80:83], a[236:239], a[140:143], v[80:83]\n"
    "v_mfma_f32_16x16x32_bf16 v[84:87], a[240:243], a[128:131], v[32:35]\n"
    "v_mfma_f32_16x16x32_bf16 v[84:87], a[244:247], a[132:135], v[84:87]\n"
    "v_mfma_f32_16x16x32_bf16 v[84:87], a[248:251], a[136:139], v[84:87]\n"
    "v_mfma_f32_16x16x32_bf16 v[84:87], a[252:255], a[140:143], v[84:87]\n"
    "s_nop 3\n"
    "v_exp_f32 v80, v80\n"
    "v_exp_f32 v81, v81\n"
    "v_exp_f32 v82, v82\n"
    "v_exp_f32 v83, v83\n"
    "v_exp_f32 v84, v84\n"
    "v_exp_f32 v85, v85\n"
    "v_exp_f32 v86, v86\n"
    "v_exp_f32 v87, v87\n"
    "v_cvt_pk_bf16_f32 v140, v80, v81\n"
    "v_cvt_pk_bf16_f32 v141, v82, v83\n"
    "v_cvt_pk_bf16_f32 v142, v84, v85\n"
    "v_cvt_pk_bf16_f32 v143, v86, v87\n"
    "v_mfma_f32_16x16x32_bf16 v[48:51], v[64:67], v[128:131], 0\n"
    "v_mfma_f32_16x16x32_bf16 v[48:51], v[64:67], v[132:135], v[48:51]\n"
    "v_mfma_f32_16x16x32_bf16 v[48:51], v[64:67], v[136:139], v[48:51]\n"
    "v_mfma_f32_16x16x32_bf16 v[48:51], v[64:67], v[140:143], v[48:51]\n"
    "v_sub_f32 v16, v16, v9\n"
    "v_sub_f32 v17, v17, v9\n"
    "v_sub_f32 v18, v18, v9\n"
    "v_sub_f32 v19, v19, v9\n"
    "v_sub_f32 v11, 0, v9\n"
    "v_exp_f32 v11, v11\n"
    "s_nop 0\n"
    "v_mul_f32 v68, v11, v68\n"
    "ds_bpermute_b32 v12, v99, v11 offset:0\n"
    "ds_bpermute_b32 v13, v99, v11 offset:4\n"
    "ds_bpermute_b32 v14, v99, v11 offset:8\n"
    "ds_bpermute_b32 v15, v99, v11 offset:12\n"
    "s_waitcnt lgkmcnt(0)\n"
    "v_accvgpr_read_b32 v80, a0\n"
    "v_accvgpr_read_b32 v81, a1\n"
    "v_accvgpr_read_b32 v82, a2\n"
    "v_accvgpr_read_b32 v83, a3\n"
    "v_accvgpr_read_b32 v84, a16\n"
    "v_accvgpr_read_b32 v85, a17\n"
    "v_accvgpr_read_b32 v86, a18\n"
    "v_accvgpr_read_b32 v87, a19\n"
    "v_mul_f32 v80, v12, v80\n"
    "v_mul_f32 v81, v13, v81\n"
    "v_mul_f32 v82, v14, v82\n"
    "v_mul_f32 v83, v15, v83\n"
    "v_mul_f32 v84, v12, v84\n"
    "v_mul_f32 v85, v13, v85\n"
    "v_mul_f32 v86, v14, v86\n"
    "v_mul_f32 v87, v15, v87\n"
    "v_accvgpr_write_b32 a0, v80\n"
    "v_accvgpr_write_b32 a1, v81\n"
    "v_accvgpr_write_b32 a2, v82\n"
    "v_accvgpr_write_b32 a3, v83\n"
    "v_accvgpr_write_b32 a16, v84\n"
    "v_accvgpr_write_b32 a17, v85\n"
    "v_accvgpr_write_b32 a18, v86\n"
    "v_accvgpr_write_b32 a19, v87\n"
    "v_accvgpr_read_b32 v80, a32\n"
    "v_accvgpr_read_b32 v81, a33\n"
    "v_accvgpr_read_b32 v82, a34\n"
    "v_accvgpr_read_b32 v83, a35\n"
    "v_accvgpr_read_b32 v84, a48\n"
    "v_accvgpr_read_b32 v85, a49\n"
    "v_accvgpr_read_b32 v86, a50\n"
    "v_accvgpr_read_b32 v87, a51\n"
    "v_mul_f32 v80, v12, v80\n"
    "v_mul_f32 v81, v13, v81\n"
    "v_mul_f32 v82, v14, v82\n"
    "v_mul_f32 v83, v15, v83\n"
    "v_mul_f32 v84, v12, v84\n"
    "v_mul_f32 v85, v13, v85\n"
    "v_mul_f32 v86, v14, v86\n"
    "v_mul_f32 v87, v15, v87\n"
    "v_accvgpr_write_b32 a32, v80\n"
    "v_accvgpr_write_b32 a33, v81\n"
    "v_accvgpr_write_b32 a34, v82\n"
    "v_accvgpr_write_b32 a35, v83\n"
    "v_accvgpr_write_b32 a48, v84\n"
    "v_accvgpr_write_b32 a49, v85\n"
    "v_accvgpr_write_b32 a50, v86\n"
    "v_accvgpr_write_b32 a51, v87\n"
    "v_accvgpr_read_b32 v80, a64\n"
    "v_accvgpr_read_b32 v81, a65\n"
    "v_accvgpr_read_b32 v82, a66\n"
    "v_accvgpr_read_b32 v83, a67\n"
    "v_accvgpr_read_b32 v84, a80\n"
    "v_accvgpr_read_b32 v85, a81\n"
    "v_accvgpr_read_b32 v86, a82\n"
    "v_accvgpr_read_b32 v87, a83\n"
    "v_mul_f32 v80, v12, v80\n"
    "v_mul_f32 v81, v13, v81\n"
    "v_mul_f32 v82, v14, v82\n"
    "v_mul_f32 v83, v15, v83\n"
    "v_mul_f32 v84, v12, v84\n"
    "v_mul_f32 v85, v13, v85\n"
    "v_mul_f32 v86, v14, v86\n"
    "v_mul_f32 v87, v15, v87\n"
    "v_accvgpr_write_b32 a64, v80\n"
    "v_accvgpr_write_b32 a65, v81\n"
    "v_accvgpr_write_b32 a66, v82\n"
    "v_accvgpr_write_b32 a67, v83\n"
    "v_accvgpr_write_b32 a80, v84\n"
    "v_accvgpr_write_b32 a81, v85\n"
    "v_accvgpr_write_b32 a82, v86\n"
    "v_accvgpr_write_b32 a83, v87\n"
    "v_accvgpr_read_b32 v80, a96\n"
    "v_accvgpr_read_b32 v81, a97\n"
    "v_accvgpr_read_b32 v82, a98\n"
    "v_accvgpr_read_b32 v83, a99\n"
    "v_accvgpr_read_b32 v84, a112\n"
    "v_accvgpr_read_b32 v85, a113\n"
    "v_accvgpr_read_b32 v86, a114\n"
    "v_accvgpr_read_b32 v87, a115\n"
    "v_mul_f32 v80, v12, v80\n"
    "v_mul_f32 v81, v13, v81\n"
    "v_mul_f32 v82, v14, v82\n"
    "v_mul_f32 v83, v15, v83\n"
    "v_mul_f32 v84, v12, v84\n"
    "v_mul_f32 v85, v13, v85\n"
    "v_mul_f32 v86, v14, v86\n"
    "v_mul_f32 v87, v15, v87\n"
    "v_accvgpr_write_b32 a96, v80\n"
    "v_accvgpr_write_b32 a97, v81\n"
    "v_accvgpr_write_b32 a98, v82\n"
    "v_accvgpr_write_b32 a99, v83\n"
    "v_accvgpr_write_b32 a112, v84\n"
    "v_accvgpr_write_b32 a113, v85\n"
    "v_accvgpr_write_b32 a114, v86\n"
    "v_accvgpr_write_b32 a115, v87\n"
    "s_nop 7\n"
    "s_nop 7\n"
    "s_branch L_ret_C0\n"
    "L_rare_C1:\n"
    "s_nop 7\n"
    "s_nop 7\n"
    "s_sub_u32 s56, s48, 0x20000\n"
    "s_add_u32 s57, s56, 0x0\n"
    "s_add_u32 s58, s56, 0x1000\n"
    "buffer_load_dwordx4 a[224:227], v76, s[40:43], s57 offen offset:0 sc1\n"
    "buffer_load_dwordx4 a[228:231], v76, s[40:43], s57 offen offset:1024 sc1\n"
    "buffer_load_dwordx4 a[232:235], v76, s[40:43], s57 offen offset:2048 sc1\n"
    "buffer_load_dwordx4 a[236:239], v76, s[40:43], s57 offen offset:3072 sc1\n"
    "buffer_load_dwordx4 a[240:243], v76, s[40:43], s58 offen offset:0 sc1\n"
    "buffer_load_dwordx4 a[244:247], v76, s[40:43], s58 offen offset:1024 sc1\n"
    "buffer_load_dwordx4 a[248:251], v76, s[40:43], s58 offen offset:2048 sc1\n"
    "buffer_load_dwordx4 a[252:255], v76, s[40:43], s58 offen offset:3072 sc1\n"
    "s_waitcnt vmcnt(0)\n"
    "v_mfma_f32_16x16x32_bf16 v[80:83], a[224:227], a[144:147], v[36:39]\n"
    "v_mfma_f32_16x16x32_bf16 v[80:83], a[228:231], a[148:151], v[80:83]\n"
    "v_mfma_f32_16x16x32_bf16 v[80:83], a[232:235], a[152:155], v[80:83]\n"
    "v_mfma_f32_16x16x32_bf16 v[80:83], a[236:239], a[156:159], v[80:83]\n"
    "v_mfma_f32_16x16x32_bf16 v[84:87], a[240:243], a[144:147], v[36:39]\n"
    "v_mfma_f32_16x16x32_bf16 v[84:87], a[244:247], a[148:151], v[84:87]\n"
    "v_mfma_f32_16x16x32_bf16 v[84:87], a[248:251], a[152:155], v[84:87]\n"
    "v_mfma_f32_16x16x32_bf16 v[84:87], a[252:255], a[156:159], v[84:87]\n"
    "s_nop 3\n"
    "v_max_f32 v9, v80, v80\n"
    "v_max3_f32 v9, v9, v80, v81\n"
    "v_max3_f32 v9, v9, v82, v83\n"
    "s_nop 0\n"
    "v_max3_f32 v9, v9, v84, v85\n"
    "v_max3_f32 v9, v9, v86, v87\n"
    "s_add_u32 s57, s56, 0x2000\n"
    "s_add_u32 s58, s56, 0x3000\n"
    "buffer_load_dwordx4 a[224:227], v76, s[40:43], s57 offen offset:0 sc1\n"
    "buffer_load_dwordx4 a[228:231], v76, s[40:43], s57 offen offset:1024 sc1\n"
    "buffer_load_dwordx4 a[232:235], v76, s[40:43], s57 offen offset:2048 sc1\n"
    "buffer_load_dwordx4 a[236:239], v76, s[40:43], s57 offen offset:3072 sc1\n"
    "buffer_load_dwordx4 a[240:243], v76, s[40:43], s58 offen offset:0 sc1\n"
    "buffer_load_dwordx4 a[244:247], v76, s[40:43], s58 offen offset:1024 sc1\n"
    "buffer_load_dwordx4 a[248:251], v76, s[40:43], s58 offen offset:2048 sc1\n"
    "buffer_load_dwordx4 a[252:255], v76, s[40:43], s58 offen offset:3072 sc1\n"
    "s_waitcnt vmcnt(0)\n"
    "v_mfma_f32_16x16x32_bf16 v[80:83], a[224:227], a[144:147], v[36:39]\n"
    "v_mfma_f32_16x16x32_bf16 v[80:83], a[228:231], a[148:151], v[80:83]\n"
    "v_mfma_f32_16x16x32_bf16 v[80:83], a[232:235], a[152:155], v[80:83]\n"
    "v_mfma_f32_16x16x32_bf16 v[80:83], a[236:239], a[156:159], v[80:83]\n"
    "v_mfma_f32_16x16x32_bf16 v[84:87], a[240:243], a[144:147], v[36:39]\n"
    "v_mfma_f32_16x16x32_bf16 v[84:87], a[244:247], a[148:151], v[84:87]\n"
    "v_mfma_f32_16x16x32_bf16 v[84:87], a[248:251], a[152:155], v[84:87]\n"
    "v_mfma_f32_16x16x32_bf16 v[84:87], a[252:255], a[156:159], v[84:87]\n"
    "s_nop 3\n"
    "v_max3_f32 v9, v9, v80, v81\n"
    "v_max3_f32 v9, v9, v82, v83\n"
    "s_nop 1\n"
    "v_max3_f32 v9, v9, v84, v85\n"
    "v_max3_f32 v9, v9, v86, v87\n"
    "s_add_u32 s57, s56, 0x4000\n"
    "s_add_u32 s58, s56, 0x5000\n"
    "buffer_load_dwordx4 a[224:227], v76, s[40:43], s57 offen offset:0 sc1\n"
    "buffer_load_dwordx4 a[228:231], v76, s[40:43], s57 offen offset:1024 sc1\n"
    "buffer_load_dwordx4 a[232:235], v76, s[40:43], s57 offen offset:2048 sc1\n"
    "buffer_load_dwordx4 a[236:239], v76, s[40:43], s57 offen offset:3072 sc1\n"
    "buffer_load_dwordx4 a[240:243], v76, s[40:43], s58 offen offset:0 sc1\n"
    "buffer_load_dwordx4 a[244:247], v76, s[40:43], s58 offen offset:1024 sc1\n"
    "buffer_load_dwordx4 a[248:251], v76, s[40:43], s58 offen offset:2048 sc1\n"
    "buffer_load_dwordx4 a[252:255], v76, s[40:43], s58 offen offset:3072 sc1\n"
    "s_waitcnt vmcnt(0)\n"
    "v_mfma_f32_16x16x32_bf16 v[80:83], a[224:227], a[144:147], v[36:39]\n"
    "v_mfma_f32_16x16x32_bf16 v[80:83], a[228:231], a[148:151], v[80:83]\n"
    "v_mfma_f32_16x16x32_bf16 v[80:83], a[232:235], a[152:155], v[80:83]\n"
    "v_mfma_f32_16x16x32_bf16 v[80:83], a[236:239], a[156:159], v[80:83]\n"
    "v_mfma_f32_16x16x32_bf16 v[84:87], a[240:243], a[144:147], v[36:39]\n"
    "v_mfma_f32_16x16x32_bf16 v[84:87], a[244:247], a[148:151], v[84:87]\n"
    "v_mfma_f32_16x16x32_bf16 v[84:87], a[248:251], a[152:155], v[84:87]\n"
    "v_mfma_f32_16x16x32_bf16 v[84:87], a[252:255], a[156:159], v[84:87]\n"
    "s_nop 3\n"
    "v_max3_f32 v9, v9, v80, v81\n"
    "v_max3_f32 v9, v9, v82, v83\n"
    "s_nop 1\n"
    "v_max3_f32 v9, v9, v84, v85\n"
    "v_max3_f32 v9, v9, v86, v87\n"
    "s_add_u32 s57, s56, 0x6000\n"
    "s_add_u32 s58, s56, 0x7000\n"
    "buffer_load_dwordx4 a[224:227], v76, s[40:43], s57 offen offset:0 sc1\n"
    "buffer_load_dwordx4 a[228:231], v76, s[40:43], s57 offen offset:1024 sc1\n"
    "buffer_load_dwordx4 a[232:235], v76, s[40:43], s57 offen offset:2048 sc1\n"
    "buffer_load_dwordx4 a[236:239], v76, s[40:43], s57 offen offset:3072 sc1\n"
    "buffer_load_dwordx4 a[240:243], v76, s[40:43], s58 offen offset:0 sc1\n"
    "buffer_load_dwordx4 a[244:247], v76, s[40:43], s58 offen offset:1024 sc1\n"
    "buffer_load_dwordx4 a[248:251], v76, s[40:43], s58 offen offset:2048 sc1\n"
    "buffer_load_dwordx4 a[252:255], v76, s[40:43], s58 offen offset:3072 sc1\n"
    "s_waitcnt vmcnt(0)\n"
    "v_mfma_f32_16x16x32_bf16 v[80:83], a[224:227], a[144:147], v[36:39]\n"
    "v_mfma_f32_16x16x32_bf16 v[80:83], a[228:231], a[148:151], v[80:83]\n"
    "v_mfma_f32_16x16x32_bf16 v[80:83], a[232:235], a[152:155], v[80:83]\n"
    "v_mfma_f32_16x16x32_bf16 v[80:83], a[236:239], a[156:159], v[80:83]\n"
    "v_mfma_f32_16x16x32_bf16 v[84:87], a[240:243], a[144:147], v[36:39]\n"
    "v_mfma_f32_16x16x32_bf16 v[84:87], a[244:247], a[148:151], v[84:87]\n"
    "v_mfma_f32_16x16x32_bf16 v[84:87], a[248:251], a[152:155], v[84:87]\n"
    "v_mfma_f32_16x16x32_bf16 v[84:87], a[252:255], a[156:159], v[84:87]\n"
    "s_nop 3\n"
    "v_max3_f32 v9, v9, v80, v81\n"
    "v_max3_f32 v9, v9, v82, v83\n"
    "s_nop 1\n"
    "v_max3_f32 v9, v9, v84, v85\n"
    "v_max3_f32 v9, v9, v86, v87\n"
    "v_mov_b32 v10, v9\n"
    "s_nop 1\n"
    "v_permlane16_swap_b32 v10, v9\n"
    "v_max_f32 v9, v10, v9\n"
    "v_mov_b32 v10, v9\n"
    "s_nop 1\n"
    "v_permlane32_swap_b32 v10, v9\n"
    "v_max_f32 v9, v10, v9\n"
    "v_max_f32 v9, 0, v9\n"
    "v_sub_f32 v36, v36, v9\n"
    "v_sub_f32 v37, v37, v9\n"
    "v_sub_f32 v38, v38, v9\n"
    "v_sub_f32 v39, v39, v9\n"
    "s_add_u32 s57, s56, 0x0\n"
    "s_add_u32 s58, s56, 0x1000\n"
    "buffer_load_dwordx4 a[224:227], v76, s[40:43], s57 offen offset:0 sc1\n"
    "buffer_load_dwordx4 a[228:231], v76, s[40:43], s57 offen offset:1024 sc1\n"
    "buffer_load_dwordx4 a[232:235], v76, s[40:43], s57 offen offset:2048 sc1\n"
    "buffer_load_dwordx4 a[236:239], v76, s[40:43], s57 offen offset:3072 sc1\n"
    "buffer_load_dwordx4 a[240:243], v76, s[40:43], s58 offen offset:0 sc1\n"
    "buffer_load_dwordx4 a[244:247], v76, s[40:43], s58 offen offset:1024 sc1\n"
    "buffer_load_dwordx4 a[248:251], v76, s[40:43], s58 offen offset:2048 sc1\n"
    "buffer_load_dwordx4 a[252:255], v76, s[40:43], s58 offen offset:3072 sc1\n"
    "s_waitcnt vmcnt(0)\n"
    "v_mfma_f32_16x16x32_bf16 v[80:83], a[224:227], a[144:147], v[36:39]\n"
    "v_mfma_f32_16x16x32_bf16 v[80:83], a[228:231], a[148:151], v[80:83]\n"
    "v_mfma_f32_16x16x32_bf16 v[80:83], a[232:235], a[152:155], v[80:83]\n"
    "v_mfma_f32_16x16x32_bf16 v[80:83], a[236:239], a[156:159], v[80:83]\n"
    "v_mfma_f32_16x16x32_bf16 v[84:87], a[240:243], a[144:147], v[36:39]\n"
    "v_mfma_f32_16x16x32_bf16 v[84:87], a[244:247], a[148:151], v[84:87]\n"
    "v_mfma_f32_16x16x32_bf16 v[84:87], a[248:251], a[152:155], v[84:87]\n"
    "v_mfma_f32_16x16x32_bf16 v[84:87], a[252:255], a[156:159], v[84:87]\n"
    "s_nop 3\n"
    "v_exp_f32 v80, v80\n"
    "v_exp_f32 v81, v81\n"
    "v_exp_f32 v82, v82\n"
    "v_exp_f32 v83, v83\n"
    "v_exp_f32 v84, v84\n"
    "v_exp_f32 v85, v85\n"
    "v_exp_f32 v86, v86\n"
    "v_exp_f32 v87, v87\n"
    "v_cvt_pk_bf16_f32 v144, v80, v81\n"
    "v_cvt_pk_bf16_f32 v145, v82, v83\n"
    "v_cvt_pk_bf16_f32 v146, v84, v85\n"
    "v_cvt_pk_bf16_f32 v147, v86, v87\n"
    "s_add_u32 s57, s56, 0x2000\n"
    "s_add_u32 s58, s56, 0x3000\n"
    "buffer_load_dwordx4 a[224:227], v76, s[40:43], s57 offen offset:0 sc1\n"
    "buffer_load_dwordx4 a[228:231], v76, s[40:43], s57 offen offset:1024 sc1\n"
    "buffer_load_dwordx4 a[232:235], v76, s[40:43], s57 offen offset:2048 sc1\n"
    "buffer_load_dwordx4 a[236:239], v76, s[40:43], s57 offen offset:3072 sc1\n"
    "buffer_load_dwordx4 a[240:243], v76, s[40:43], s58 offen offset:0 sc1\n"
    "buffer_load_dwordx4 a[244:247], v76, s[40:43], s58 offen offset:1024 sc1\n"
    "buffer_load_dwordx4 a[248:251], v76, s[40:43], s58 offen offset:2048 sc1\n"
    "buffer_load_dwordx4 a[252:255], v76, s[40:43], s58 offen offset:3072 sc1\n"
    "s_waitcnt vmcnt(0)\n"
    "v_mfma_f32_16x16x32_bf16 v[80:83], a[224:227], a[144:147], v[36:39]\n"
    "v_mfma_f32_16x16x32_bf16 v[80:83], a[228:231], a[148:151], v[80:83]\n"
    "v_mfma_f32_16x16x32_bf16 v[80:83], a[232:235], a[152:155], v[80:83]\n"
    "v_mfma_f32_16x16x32_bf16 v[80:83], a[236:239], a[156:159], v[80:83]\n"
    "v_mfma_f32_16x16x32_bf16 v[84:87], a[240:243], a[144:147], v[36:39]\n"
    "v_mfma_f32_16x16x32_bf16 v[84:87], a[244:247], a[148:151], v[84:87]\n"
    "v_mfma_f32_16x16x32_bf16 v[84:87], a[248:251], a[152:155], v[84:87]\n"
    "v_mfma_f32_16x16x32_bf16 v[84:87], a[252:255], a[156:159], v[84:87]\n"
    "s_nop 3\n"
    "v_exp_f32 v80, v80\n"
    "v_exp_f32 v81, v81\n"
    "v_exp_f32 v82, v82\n"
    "v_exp_f32 v83, v83\n"
    "v_exp_f32 v84, v84\n"
    "v_exp_f32 v85, v85\n"
    "v_exp_f32 v86, v86\n"
    "v_exp_f32 v87, v87\n"
    "v_cvt_pk_bf16_f32 v148, v80, v81\n"
    "v_cvt_pk_bf16_f32 v149, v82, v83\n"
    "v_cvt_pk_bf16_f32 v150, v84, v85\n"
    "v_cvt_pk_bf16_f32 v151, v86, v87\n"
    "s_add_u32 s57, s56, 0x4000\n"
    "s_add_u32 s58, s56, 0x5000\n"
    "buffer_load_dwordx4 a[224:227], v76, s[40:43], s57 offen offset:0 sc1\n"
    "buffer_load_dwordx4 a[228:231], v76, s[40:43], s57 offen offset:1024 sc1\n"
    "buffer_load_dwordx4 a[232:235], v76, s[40:43], s57 offen offset:2048 sc1\n"
    "buffer_load_dwordx4 a[236:239], v76, s[40:43], s57 offen offset:3072 sc1\n"
    "buffer_load_dwordx4 a[240:243], v76, s[40:43], s58 offen offset:0 sc1\n"
    "buffer_load_dwordx4 a[244:247], v76, s[40:43], s58 offen offset:1024 sc1\n"
    "buffer_load_dwordx4 a[248:251], v76, s[40:43], s58 offen offset:2048 sc1\n"
    "buffer_load_dwordx4 a[252:255], v76, s[40:43], s58 offen offset:3072 sc1\n"
    "s_waitcnt vmcnt(0)\n"
    "v_mfma_f32_16x16x32_bf16 v[80:83], a[224:227], a[144:147], v[36:39]\n"
    "v_mfma_f32_16x16x32_bf16 v[80:83], a[228:231], a[148:151], v[80:83]\n"
    "v_mfma_f32_16x16x32_bf16 v[80:83], a[232:235], a[152:155], v[80:83]\n"
    "v_mfma_f32_16x16x32_bf16 v[80:83], a[236:239], a[156:159], v[80:83]\n"
    "v_mfma_f32_16x16x32_bf16 v[84:87], a[240:243], a[144:147], v[36:39]\n"
    "v_mfma_f32_16x16x32_bf16 v[84:87], a[244:247], a[148:151], v[84:87]\n"
    "v_mfma_f32_16x16x32_bf16 v[84:87], a[248:251], a[152:155], v[84:87]\n"
    "v_mfma_f32_16x16x32_bf16 v[84:87], a[252:255], a[156:159], v[84:87]\n"
    "s_nop 3\n"
    "v_exp_f32 v80, v80\n"
    "v_exp_f32 v81, v81\n"
    "v_exp_f32 v82, v82\n"
    "v_exp_f32 v83, v83\n"
    "v_exp_f32 v84, v84\n"
    "v_exp_f32 v85, v85\n"
    "v_exp_f32 v86, v86\n"
    "v_exp_f32 v87, v87\n"
    "v_cvt_pk_bf16_f32 v152, v80, v81\n"
    "v_cvt_pk_bf16_f32 v153, v82, v83\n"
    "v_cvt_pk_bf16_f32 v154, v84, v85\n"
    "v_cvt_pk_bf16_f32 v155, v86, v87\n"
    "s_add_u32 s57, s56, 0x6000\n"
    "s_add_u32 s58, s56, 0x7000\n"
    "buffer_load_dwordx4 a[224:227], v76, s[40:43], s57 offen offset:0 sc1\n"
    "buffer_load_dwordx4 a[228:231], v76, s[40:43], s57 offen offset:1024 sc1\n"
    "buffer_load_dwordx4 a[232:235], v76, s[40:43], s57 offen offset:2048 sc1\n"
    "buffer_load_dwordx4 a[236:239], v76, s[40:43], s57 offen offset:3072 sc1\n"
    "buffer_load_dwordx4 a[240:243], v76, s[40:43], s58 offen offset:0 sc1\n"
    "buffer_load_dwordx4 a[244:247], v76, s[40:43], s58 offen offset:1024 sc1\n"
    "buffer_load_dwordx4 a[248:251], v76, s[40:43], s58 offen offset:2048 sc1\n"
    "buffer_load_dwordx4 a[252:255], v76, s[40:43], s58 offen offset:3072 sc1\n"
    "s_waitcnt vmcnt(0)\n"
    "v_mfma_f32_16x16x32_bf16 v[80:83], a[224:227], a[144:147], v[36:39]\n"
    "v_mfma_f32_16x16x32_bf16 v[80:83], a[228:231], a[148:151], v[80:83]\n"
    "v_mfma_f32_16x16x32_bf16 v[80:83], a[232:235], a[152:155], v[80:83]\n"
    "v_mfma_f32_16x16x32_bf16 v[80:83], a[236:239], a[156:159], v[80:83]\n"
    "v_mfma_f32_16x16x32_bf16 v[84:87], a[240:243], a[144:147], v[36:39]\n"
    "v_mfma_f32_16x16x32_bf16 v[84:87], a[244:247], a[148:151], v[84:87]\n"
    "v_mfma_f32_16x16x32_bf16 v[84:87], a[248:251], a[152:155], v[84:87]\n"
    "v_mfma_f32_16x16x32_bf16 v[84:87], a[252:255], a[156:159], v[84:87]\n"
    "s_nop 3\n"
    "v_exp_f32 v80, v80\n"
    "v_exp_f32 v81, v81\n"
    "v_exp_f32 v82, v82\n"
    "v_exp_f32 v83, v83\n"
    "v_exp_f32 v84, v84\n"
    "v_exp_f32 v85, v85\n"
    "v_exp_f32 v86, v86\n"
    "v_exp_f32 v87, v87\n"
    "v_cvt_pk_bf16_f32 v156, v80, v81\n"
    "v_cvt_pk_bf16_f32 v157, v82, v83\n"
    "v_cvt_pk_bf16_f32 v158, v84, v85\n"
    "v_cvt_pk_bf16_f32 v159, v86, v87\n"
    "v_mfma_f32_16x16x32_bf16 v[52:55], v[64:67], v[144:147], 0\n"
    "v_mfma_f32_16x16x32_bf16 v[52:55], v[64:67], v[148:151], v[52:55]\n"
    "v_mfma_f32_16x16x32_bf16 v[52:55], v[64:67], v[152:155], v[52:55]\n"
    "v_mfma_f32_16x16x32_bf16 v[52:55], v[64:67], v[156:159], v[52:55]\n"
    "v_sub_f32 v20, v20, v9\n"
    "v_sub_f32 v21, v21, v9\n"
    "v_sub_f32 v22, v22, v9\n"
    "v_sub_f32 v23, v23, v9\n"
    "v_sub_f32 v11, 0, v9\n"
    "v_exp_f32 v11, v11\n"
    "s_nop 0\n"
    "v_mul_f32 v69, v11, v69\n"
    "ds_bpermute_b32 v12, v99, v11 offset:0\n"
    "ds_bpermute_b32 v13, v99, v11 offset:4\n"
    "ds_bpermute_b32 v14, v99, v11 offset:8\n"
    "ds_bpermute_b32 v15, v99, v11 offset:12\n"
    "s_waitcnt lgkmcnt(0)\n"
    "v_accvgpr_read_b32 v80, a4\n"
    "v_accvgpr_read_b32 v81, a5\n"
    "v_accvgpr_read_b32 v82, a6\n"
    "v_accvgpr_read_b32 v83, a7\n"
    "v_accvgpr_read_b32 v84, a20\n"
    "v_accvgpr_read_b32 v85, a21\n"
    "v_accvgpr_read_b32 v86, a22\n"
    "v_accvgpr_read_b32 v87, a23\n"
    "v_mul_f32 v80, v12, v80\n"
    "v_mul_f32 v81, v13, v81\n"
    "v_mul_f32 v82, v14, v82\n"
    "v_mul_f32 v83, v15, v83\n"
    "v_mul_f32 v84, v12, v84\n"
    "v_mul_f32 v85, v13, v85\n"
    "v_mul_f32 v86, v14, v86\n"
    "v_mul_f32 v87, v15, v87\n"
    "v_accvgpr_write_b32 a4, v80\n"
    "v_accvgpr_write_b32 a5, v81\n"
    "v_accvgpr_write_b32 a6, v82\n"
    "v_accvgpr_write_b32 a7, v83\n"
    "v_accvgpr_write_b32 a20, v84\n"
    "v_accvgpr_write_b32 a21, v85\n"
    "v_accvgpr_write_b32 a22, v86\n"
    "v_accvgpr_write_b32 a23, v87\n"
    "v_accvgpr_read_b32 v80, a36\n"
    "v_accvgpr_read_b32 v81, a37\n"
    "v_accvgpr_read_b32 v82, a38\n"
    "v_accvgpr_read_b32 v83, a39\n"
    "v_accvgpr_read_b32 v84, a52\n"
    "v_accvgpr_read_b32 v85, a53\n"
    "v_accvgpr_read_b32 v86, a54\n"
    "v_accvgpr_read_b32 v87, a55\n"
    "v_mul_f32 v80, v12, v80\n"
    "v_mul_f32 v81, v13, v81\n"
    "v_mul_f32 v82, v14, v82\n"
    "v_mul_f32 v83, v15, v83\n"
    "v_mul_f32 v84, v12, v84\n"
    "v_mul_f32 v85, v13, v85\n"
    "v_mul_f32 v86, v14, v86\n"
    "v_mul_f32 v87, v15, v87\n"
    "v_accvgpr_write_b32 a36, v80\n"
    "v_accvgpr_write_b32 a37, v81\n"
    "v_accvgpr_write_b32 a38, v82\n"
    "v_accvgpr_write_b32 a39, v83\n"
    "v_accvgpr_write_b32 a52, v84\n"
    "v_accvgpr_write_b32 a53, v85\n"
    "v_accvgpr_write_b32 a54, v86\n"
    "v_accvgpr_write_b32 a55, v87\n"
    "v_accvgpr_read_b32 v80, a68\n"
    "v_accvgpr_read_b32 v81, a69\n"
    "v_accvgpr_read_b32 v82, a70\n"
    "v_accvgpr_read_b32 v83, a71\n"
    "v_accvgpr_read_b32 v84, a84\n"
    "v_accvgpr_read_b32 v85, a85\n"
    "v_accvgpr_read_b32 v86, a86\n"
    "v_accvgpr_read_b32 v87, a87\n"
    "v_mul_f32 v80, v12, v80\n"
    "v_mul_f32 v81, v13, v81\n"
    "v_mul_f32 v82, v14, v82\n"
    "v_mul_f32 v83, v15, v83\n"
    "v_mul_f32 v84, v12, v84\n"
    "v_mul_f32 v85, v13, v85\n"
    "v_mul_f32 v86, v14, v86\n"
    "v_mul_f32 v87, v15, v87\n"
    "v_accvgpr_write_b32 a68, v80\n"
    "v_accvgpr_write_b32 a69, v81\n"
    "v_accvgpr_write_b32 a70, v82\n"
    "v_accvgpr_write_b32 a71, v83\n"
    "v_accvgpr_write_b32 a84, v84\n"
    "v_accvgpr_write_b32 a85, v85\n"
    "v_accvgpr_write_b32 a86, v86\n"
    "v_accvgpr_write_b32 a87, v87\n"
    "v_accvgpr_read_b32 v80, a100\n"
    "v_accvgpr_read_b32 v81, a101\n"
    "v_accvgpr_read_b32 v82, a102\n"
    "v_accvgpr_read_b32 v83, a103\n"
    "v_accvgpr_read_b32 v84, a116\n"
    "v_accvgpr_read_b32 v85, a117\n"
    "v_accvgpr_read_b32 v86, a118\n"
    "v_accvgpr_read_b32 v87, a119\n"
    "v_mul_f32 v80, v12, v80\n"
    "v_mul_f32 v81, v13, v81\n"
    "v_mul_f32 v82, v14, v82\n"
    "v_mul_f32 v83, v15, v83\n"
    "v_mul_f32 v84, v12, v84\n"
    "v_mul_f32 v85, v13, v85\n"
    "v_mul_f32 v86, v14, v86\n"
    "v_mul_f32 v87, v15, v87\n"
    "v_accvgpr_write_b32 a100, v80\n"
    "v_accvgpr_write_b32 a101, v81\n"
    "v_accvgpr_write_b32 a102, v82\n"
    "v_accvgpr_write_b32 a103, v83\n"
    "v_accvgpr_write_b32 a116, v84\n"
    "v_accvgpr_write_b32 a117, v85\n"
    "v_accvgpr_write_b32 a118, v86\n"
    "v_accvgpr_write_b32 a119, v87\n"
    "s_nop 7\n"
    "s_nop 7\n"
    "s_branch L_ret_C1\n"
    "L_rare_C2:\n"
    "s_nop 7\n"
    "s_nop 7\n"
    "s_sub_u32 s56, s48, 0x20000\n"
    "s_add_u32 s57, s56, 0x0\n"
    "s_add_u32 s58, s56, 0x1000\n"
    "buffer_load_dwordx4 a[224:227], v76, s[40:43], s57 offen offset:0 sc1\n"
    "buffer_load_dwordx4 a[228:231], v76, s[40:43], s57 offen offset:1024 sc1\n"
    "buffer_load_dwordx4 a[232:235], v76, s[40:43], s57 offen offset:2048 sc1\n"
    "buffer_load_dwordx4 a[236:239], v76, s[40:43], s57 offen offset:3072 sc1\n"
    "buffer_load_dwordx4 a[240:243], v76, s[40:43], s58 offen offset:0 sc1\n"
    "buffer_load_dwordx4 a[244:247], v76, s[40:43], s58 offen offset:1024 sc1\n"
    "buffer_load_dwordx4 a[248:251], v76, s[40:43], s58 offen offset:2048 sc1\n"
    "buffer_load_dwordx4 a[252:255], v76, s[40:43], s58 offen offset:3072 sc1\n"
    "s_waitcnt vmcnt(0)\n"
    "v_mfma_f32_16x16x32_bf16 v[80:83], a[224:227], a[160:163], v[40:43]\n"
    "v_mfma_f32_16x16x32_bf16 v[80:83], a[228:231], a[164:167], v[80:83]\n"
    "v_mfma_f32_16x16x32_bf16 v[80:83], a[232:235], a[168:171], v[80:83]\n"
    "v_mfma_f32_16x16x32_bf16 v[80:83], a[236:239], a[172:175], v[80:83]\n"
    "v_mfma_f32_16x16x32_bf16 v[84:87], a[240:243], a[160:163], v[40:43]\n"
    "v_mfma_f32_16x16x32_bf16 v[84:87], a[244:247], a[164:167], v[84:87]\n"
    "v_mfma_f32_16x16x32_bf16 v[84:87], a[248:251], a[168:171], v[84:87]\n"
    "v_mfma_f32_16x16x32_bf16 v[84:87], a[252:255], a[172:175], v[84:87]\n"
    "s_nop 3\n"
    "v_max_f32 v9, v80, v80\n"
    "v_max3_f32 v9, v9, v80, v81\n"
    "v_max3_f32 v9, v9, v82, v83\n"
    "s_nop 0\n"
    "v_max3_f32 v9, v9, v84, v85\n"
    "v_max3_f32 v9, v9, v86, v87\n"
    "s_add_u32 s57, s56, 0x2000\n"
    "s_add_u32 s58, s56, 0x3000\n"
    "buffer_load_dwordx4 a[224:227], v76, s[40:43], s57 offen offset:0 sc1\n"
    "buffer_load_dwordx4 a[228:231], v76, s[40:43], s57 offen offset:1024 sc1\n"
    "buffer_load_dwordx4 a[232:235], v76, s[40:43], s57 offen offset:2048 sc1\n"
    "buffer_load_dwordx4 a[236:239], v76, s[40:43], s57 offen offset:3072 sc1\n"
    "buffer_load_dwordx4 a[240:243], v76, s[40:43], s58 offen offset:0 sc1\n"
    "buffer_load_dwordx4 a[244:247], v76, s[40:43], s58 offen offset:1024 sc1\n"
    "buffer_load_dwordx4 a[248:251], v76, s[40:43], s58 offen offset:2048 sc1\n"
    "buffer_load_dwordx4 a[252:255], v76, s[40:43], s58 offen offset:3072 sc1\n"
    "s_waitcnt vmcnt(0)\n"
    "v_mfma_f32_16x16x32_bf16 v[80:83], a[224:227], a[160:163], v[40:43]\n"
    "v_mfma_f32_16x16x32_bf16 v[80:83], a[228:231], a[164:167], v[80:83]\n"
    "v_mfma_f32_16x16x32_bf16 v[80:83], a[232:235], a[168:171], v[80:83]\n"
    "v_mfma_f32_16x16x32_bf16 v[80:83], a[236:239], a[172:175], v[80:83]\n"
    "v_mfma_f32_16x16x32_bf16 v[84:87], a[240:243], a[160:163], v[40:43]\n"
    "v_mfma_f32_16x16x32_bf16 v[84:87], a[244:247], a[164:167], v[84:87]\n"
    "v_mfma_f32_16x16x32_bf16 v[84:87], a[248:251], a[168:171], v[84:87]\n"
    "v_mfma_f32_16x16x32_bf16 v[84:87], a[252:255], a[172:175], v[84:87]\n"
    "s_nop 3\n"
    "v_max3_f32 v9, v9, v80, v81\n"
    "v_max3_f32 v9, v9, v82, v83\n"
    "s_nop 1\n"
    "v_max3_f32 v9, v9, v84, v85\n"
    "v_max3_f32 v9, v9, v86, v87\n"
    "s_add_u32 s57, s56, 0x4000\n"
    "s_add_u32 s58, s56, 0x5000\n"
    "buffer_load_dwordx4 a[224:227], v76, s[40:43], s57 offen offset:0 sc1\n"
    "buffer_load_dwordx4 a[228:231], v76, s[40:43], s57 offen offset:1024 sc1\n"
    "buffer_load_dwordx4 a[232:235], v76, s[40:43], s57 offen offset:2048 sc1\n"
    "buffer_load_dwordx4 a[236:239], v76, s[40:43], s57 offen offset:3072 sc1\n"
    "buffer_load_dwordx4 a[240:243], v76, s[40:43], s58 offen offset:0 sc1\n"
    "buffer_load_dwordx4 a[244:247], v76, s[40:43], s58 offen offset:1024 sc1\n"
    "buffer_load_dwordx4 a[248:251], v76, s[40:43], s58 offen offset:2048 sc1\n"
    "buffer_load_dwordx4 a[252:255], v76, s[40:43], s58 offen offset:3072 sc1\n"
    "s_waitcnt vmcnt(0)\n"
    "v_mfma_f32_16x16x32_bf16 v[80:83], a[224:227], a[160:163], v[40:43]\n"
    "v_mfma_f32_16x16x32_bf16 v[80:83], a[228:231], a[164:167], v[80:83]\n"
    "v_mfma_f32_16x16x32_bf16 v[80:83], a[232:235], a[168:171], v[80:83]\n"
    "v_mfma_f32_16x16x32_bf16 v[80:83], a[236:239], a[172:175], v[80:83]\n"
    "v_mfma_f32_16x16x32_bf16 v[84:87], a[240:243], a[160:163], v[40:43]\n"
    "v_mfma_f32_16x16x32_bf16 v[84:87], a[244:247], a[164:167], v[84:87]\n"
    "v_mfma_f32_16x16x32_bf16 v[84:87], a[248:251], a[168:171], v[84:87]\n"
    "v_mfma_f32_16x16x32_bf16 v[84:87], a[252:255], a[172:175], v[84:87]\n"
    "s_nop 3\n"
    "v_max3_f32 v9, v9, v80, v81\n"
    "v_max3_f32 v9, v9, v82, v83\n"
    "s_nop 1\n"
    "v_max3_f32 v9, v9, v84, v85\n"
    "v_max3_f32 v9, v9, v86, v87\n"
    "s_add_u32 s57, s56, 0x6000\n"
    "s_add_u32 s58, s56, 0x7000\n"
    "buffer_load_dwordx4 a[224:227], v76, s[40:43], s57 offen offset:0 sc1\n"
    "buffer_load_dwordx4 a[228:231], v76, s[40:43], s57 offen offset:1024 sc1\n"
    "buffer_load_dwordx4 a[232:235], v76, s[40:43], s57 offen offset:2048 sc1\n"
    "buffer_load_dwordx4 a[236:239], v76, s[40:43], s57 offen offset:3072 sc1\n"
    "buffer_load_dwordx4 a[240:243], v76, s[40:43], s58 offen offset:0 sc1\n"
    "buffer_load_dwordx4 a[244:247], v76, s[40:43], s58 offen offset:1024 sc1\n"
    "buffer_load_dwordx4 a[248:251], v76, s[40:43], s58 offen offset:2048 sc1\n"
    "buffer_load_dwordx4 a[252:255], v76, s[40:43], s58 offen offset:3072 sc1\n"
    "s_waitcnt vmcnt(0)\n"
    "v_mfma_f32_16x16x32_bf16 v[80:83], a[224:227], a[160:163], v[40:43]\n"
    "v_mfma_f32_16x16x32_bf16 v[80:83], a[228:231], a[164:167], v[80:83]\n"
    "v_mfma_f32_16x16x32_bf16 v[80:83], a[232:235], a[168:171], v[80:83]\n"
    "v_mfma_f32_16x16x32_bf16 v[80:83], a[236:239], a[172:175], v[80:83]\n"
    "v_mfma_f32_16x16x32_bf16 v[84:87], a[240:243], a[160:163], v[40:43]\n"
    "v_mfma_f32_16x16x32_bf16 v[84:87], a[244:247], a[164:167], v[84:87]\n"
    "v_mfma_f32_16x16x32_bf16 v[84:87], a[248:251], a[168:171], v[84:87]\n"
    "v_mfma_f32_16x16x32_bf16 v[84:87], a[252:255], a[172:175], v[84:87]\n"
    "s_nop 3\n"
    "v_max3_f32 v9, v9, v80, v81\n"
    "v_max3_f32 v9, v9, v82, v83\n"
    "s_nop 1\n"
    "v_max3_f32 v9, v9, v84, v85\n"
    "v_max3_f32 v9, v9, v86, v87\n"
    "v_mov_b32 v10, v9\n"
    "s_nop 1\n"
    "v_permlane16_swap_b32 v10, v9\n"
    "v_max_f32 v9, v10, v9\n"
    "v_mov_b32 v10, v9\n"
    "s_nop 1\n"
    "v_permlane32_swap_b32 v10, v9\n"
    "v_max_f32 v9, v10, v9\n"
    "v_max_f32 v9, 0, v9\n"
    "v_sub_f32 v40, v40, v9\n"
    "v_sub_f32 v41, v41, v9\n"
    "v_sub_f32 v42, v42, v9\n"
    "v_sub_f32 v43, v43, v9\n"
    "s_add_u32 s57, s56, 0x0\n"
    "s_add_u32 s58, s56, 0x1000\n"
    "buffer_load_dwordx4 a[224:227], v76, s[40:43], s57 offen offset:0 sc1\n"
    "buffer_load_dwordx4 a[228:231], v76, s[40:43], s57 offen offset:1024 sc1\n"
    "buffer_load_dwordx4 a[232:235], v76, s[40:43], s57 offen offset:2048 sc1\n"
    "buffer_load_dwordx4 a[236:239], v76, s[40:43], s57 offen offset:3072 sc1\n"
    "buffer_load_dwordx4 a[240:243], v76, s[40:43], s58 offen offset:0 sc1\n"
    "buffer_load_dwordx4 a[244:247], v76, s[40:43], s58 offen offset:1024 sc1\n"
    "buffer_load_dwordx4 a[248:251], v76, s[40:43], s58 offen offset:2048 sc1\n"
    "buffer_load_dwordx4 a[252:255], v76, s[40:43], s58 offen offset:3072 sc1\n"
    "s_waitcnt vmcnt(0)\n"
    "v_mfma_f32_16x16x32_bf16 v[80:83], a[224:227], a[160:163], v[40:43]\n"
    "v_mfma_f32_16x16x32_bf16 v[80:83], a[228:231], a[164:167], v[80:83]\n"
    "v_mfma_f32_16x16x32_bf16 v[80:83], a[232:235], a[168:171], v[80:83]\n"
    "v_mfma_f32_16x16x32_bf16 v[80:83], a[236:239], a[172:175], v[80:83]\n"
    "v_mfma_f32_16x16x32_bf16 v[84:87], a[240:243], a[160:163], v[40:43]\n"
    "v_mfma_f32_16x16x32_bf16 v[84:87], a[244:247], a[164:167], v[84:87]\n"
    "v_mfma_f32_16x16x32_bf16 v[84:87], a[248:251], a[168:171], v[84:87]\n"
    "v_mfma_f32_16x16x32_bf16 v[84:87], a[252:255], a[172:175], v[84:87]\n"
    "s_nop 3\n"
    "v_exp_f32 v80, v80\n"
    "v_exp_f32 v81, v81\n"
    "v_exp_f32 v82, v82\n"
    "v_exp_f32 v83, v83\n"
    "v_exp_f32 v84, v84\n"
    "v_exp_f32 v85, v85\n"
    "v_exp_f32 v86, v86\n"
    "v_exp_f32 v87, v87\n"
    "v_cvt_pk_bf16_f32 v160, v80, v81\n"
    "v_cvt_pk_bf16_f32 v161, v82, v83\n"
    "v_cvt_pk_bf16_f32 v162, v84, v85\n"
    "v_cvt_pk_bf16_f32 v163, v86, v87\n"
    "s_add_u32 s57, s56, 0x2000\n"
    "s_add_u32 s58, s56, 0x3000\n"
    "buffer_load_dwordx4 a[224:227], v76, s[40:43], s57 offen offset:0 sc1\n"
    "buffer_load_dwordx4 a[228:231], v76, s[40:43], s57 offen offset:1024 sc1\n"
    "buffer_load_dwordx4 a[232:235], v76, s[40:43], s57 offen offset:2048 sc1\n"
    "buffer_load_dwordx4 a[236:239], v76, s[40:43], s57 offen offset:3072 sc1\n"
    "buffer_load_dwordx4 a[240:243], v76, s[40:43], s58 offen offset:0 sc1\n"
    "buffer_load_dwordx4 a[244:247], v76, s[40:43], s58 offen offset:1024 sc1\n"
    "buffer_load_dwordx4 a[248:251], v76, s[40:43], s58 offen offset:2048 sc1\n"
    "buffer_load_dwordx4 a[252:255], v76, s[40:43], s58 offen offset:3072 sc1\n"
    "s_waitcnt vmcnt(0)\n"
    "v_mfma_f32_16x16x32_bf16 v[80:83], a[224:227], a[160:163], v[40:43]\n"
    "v_mfma_f32_16x16x32_bf16 v[80:83], a[228:231], a[164:167], v[80:83]\n"
    "v_mfma_f32_16x16x32_bf16 v[80:83], a[232:235], a[168:171], v[80:83]\n"
    "v_mfma_f32_16x16x32_bf16 v[80:83], a[236:239], a[172:175], v[80:83]\n"
    "v_mfma_f32_16x16x32_bf16 v[84:87], a[240:243], a[160:163], v[40:43]\n"
    "v_mfma_f32_16x16x32_bf16 v[84:87], a[244:247], a[164:167], v[84:87]\n"
    "v_mfma_f32_16x16x32_bf16 v[84:87], a[248:251], a[168:171], v[84:87]\n"
    "v_mfma_f32_16x16x32_bf16 v[84:87], a[252:255], a[172:175], v[84:87]\n"
    "s_nop 3\n"
    "v_exp_f32 v80, v80\n"
    "v_exp_f32 v81, v81\n"
    "v_exp_f32 v82, v82\n"
    "v_exp_f32 v83, v83\n"
    "v_exp_f32 v84, v84\n"
    "v_exp_f32 v85, v85\n"
    "v_exp_f32 v86, v86\n"
    "v_exp_f32 v87, v87\n"
    "v_cvt_pk_bf16_f32 v164, v80, v81\n"
    "v_cvt_pk_bf16_f32 v165, v82, v83\n"
    "v_cvt_pk_bf16_f32 v166, v84, v85\n"
    "v_cvt_pk_bf16_f32 v167, v86, v87\n"
    "s_add_u32 s57, s56, 0x4000\n"
    "s_add_u32 s58, s56, 0x5000\n"
    "buffer_load_dwordx4 a[224:227], v76, s[40:43], s57 offen offset:0 sc1\n"
    "buffer_load_dwordx4 a[228:231], v76, s[40:43], s57 offen offset:1024 sc1\n"
    "buffer_load_dwordx4 a[232:235], v76, s[40:43], s57 offen offset:2048 sc1\n"
    "buffer_load_dwordx4 a[236:239], v76, s[40:43], s57 offen offset:3072 sc1\n"
    "buffer_load_dwordx4 a[240:243], v76, s[40:43], s58 offen offset:0 sc1\n"
    "buffer_load_dwordx4 a[244:247], v76, s[40:43], s58 offen offset:1024 sc1\n"
    "buffer_load_dwordx4 a[248:251], v76, s[40:43], s58 offen offset:2048 sc1\n"
    "buffer_load_dwordx4 a[252:255], v76, s[40:43], s58 offen offset:3072 sc1\n"
    "s_waitcnt vmcnt(0)\n"
    "v_mfma_f32_16x16x32_bf16 v[80:83], a[224:227], a[160:163], v[40:43]\n"
    "v_mfma_f32_16x16x32_bf16 v[80:83], a[228:231], a[164:167], v[80:83]\n"
    "v_mfma_f32_16x16x32_bf16 v[80:83], a[232:235], a[168:171], v[80:83]\n"
    "v_mfma_f32_16x16x32_bf16 v[80:83], a[236:239], a[172:175], v[80:83]\n"
    "v_mfma_f32_16x16x32_bf16 v[84:87], a[240:243], a[160:163], v[40:43]\n"
    "v_mfma_f32_16x16x32_bf16 v[84:87], a[244:247], a[164:167], v[84:87]\n"
    "v_mfma_f32_16x16x32_bf16 v[84:87], a[248:251], a[168:171], v[84:87]\n"
    "v_mfma_f32_16x16x32_bf16 v[84:87], a[252:255], a[172:175], v[84:87]\n"
    "s_nop 3\n"
    "v_exp_f32 v80, v80\n"
    "v_exp_f32 v81, v81\n"
    "v_exp_f32 v82, v82\n"
    "v_exp_f32 v83, v83\n"
    "v_exp_f32 v84, v84\n"
    "v_exp_f32 v85, v85\n"
    "v_exp_f32 v86, v86\n"
    "v_exp_f32 v87, v87\n"
    "v_cvt_pk_bf16_f32 v168, v80, v81\n"
    "v_cvt_pk_bf16_f32 v169, v82, v83\n"
    "v_cvt_pk_bf16_f32 v170, v84, v85\n"
    "v_cvt_pk_bf16_f32 v171, v86, v87\n"
    "s_add_u32 s57, s56, 0x6000\n"
    "s_add_u32 s58, s56, 0x7000\n"
    "buffer_load_dwordx4 a[224:227], v76, s[40:43], s57 offen offset:0 sc1\n"
    "buffer_load_dwordx4 a[228:231], v76, s[40:43], s57 offen offset:1024 sc1\n"
    "buffer_load_dwordx4 a[232:235], v76, s[40:43], s57 offen offset:2048 sc1\n"
    "buffer_load_dwordx4 a[236:239], v76, s[40:43], s57 offen offset:3072 sc1\n"
    "buffer_load_dwordx4 a[240:243], v76, s[40:43], s58 offen offset:0 sc1\n"
    "buffer_load_dwordx4 a[244:247], v76, s[40:43], s58 offen offset:1024 sc1\n"
    "buffer_load_dwordx4 a[248:251], v76, s[40:43], s58 offen offset:2048 sc1\n"
    "buffer_load_dwordx4 a[252:255], v76, s[40:43], s58 offen offset:3072 sc1\n"
    "s_waitcnt vmcnt(0)\n"
    "v_mfma_f32_16x16x32_bf16 v[80:83], a[224:227], a[160:163], v[40:43]\n"
    "v_mfma_f32_16x16x32_bf16 v[80:83], a[228:231], a[164:167], v[80:83]\n"
    "v_mfma_f32_16x16x32_bf16 v[80:83], a[232:235], a[168:171], v[80:83]\n"
    "v_mfma_f32_16x16x32_bf16 v[80:83], a[236:239], a[172:175], v[80:83]\n"
    "v_mfma_f32_16x16x32_bf16 v[84:87], a[240:243], a[160:163], v[40:43]\n"
    "v_mfma_f32_16x16x32_bf16 v[84:87], a[244:247], a[164:167], v[84:87]\n"
    "v_mfma_f32_16x16x32_bf16 v[84:87], a[248:251], a[168:171], v[84:87]\n"
    "v_mfma_f32_16x16x32_bf16 v[84:87], a[252:255], a[172:175], v[84:87]\n"
    "s_nop 3\n"
    "v_exp_f32 v80, v80\n"
    "v_exp_f32 v81, v81\n"
    "v_exp_f32 v82, v82\n"
    "v_exp_f32 v83, v83\n"
    "v_exp_f32 v84, v84\n"
    "v_exp_f32 v85, v85\n"
    "v_exp_f32 v86, v86\n"
    "v_exp_f32 v87, v87\n"
    "v_cvt_pk_bf16_f32 v172, v80, v81\n"
    "v_cvt_pk_bf16_f32 v173, v82, v83\n"
    "v_cvt_pk_bf16_f32 v174, v84, v85\n"
    "v_cvt_pk_bf16_f32 v175, v86, v87\n"
    "v_mfma_f32_16x16x32_bf16 v[56:59], v[64:67], v[160:163], 0\n"
    "v_mfma_f32_16x16x32_bf16 v[56:59], v[64:67], v[164:167], v[56:59]\n"
    "v_mfma_f32_16x16x32_bf16 v[56:59], v[64:67], v[168:171], v[56:59]\n"
    "v_mfma_f32_16x16x32_bf16 v[56:59], v[64:67], v[172:175], v[56:59]\n"
    "v_sub_f32 v24, v24, v9\n"
    "v_sub_f32 v25, v25, v9\n"
    "v_sub_f32 v26, v26, v9\n"
    "v_sub_f32 v27, v27, v9\n"
    "v_sub_f32 v11, 0, v9\n"
    "v_exp_f32 v11, v11\n"
    "s_nop 0\n"
    "v_mul_f32 v70, v11, v70\n"
    "ds_bpermute_b32 v12, v99, v11 offset:0\n"
    "ds_bpermute_b32 v13, v99, v11 offset:4\n"
    "ds_bpermute_b32 v14, v99, v11 offset:8\n"
    "ds_bpermute_b32 v15, v99, v11 offset:12\n"
    "s_waitcnt lgkmcnt(0)\n"
    "v_accvgpr_read_b32 v80, a8\n"
    "v_accvgpr_read_b32 v81, a9\n"
    "v_accvgpr_read_b32 v82, a10\n"
    "v_accvgpr_read_b32 v83, a11\n"
    "v_accvgpr_read_b32 v84, a24\n"
    "v_accvgpr_read_b32 v85, a25\n"
    "v_accvgpr_read_b32 v86, a26\n"
    "v_accvgpr_read_b32 v87, a27\n"
    "v_mul_f32 v80, v12, v80\n"
    "v_mul_f32 v81, v13, v81\n"
    "v_mul_f32 v82, v14, v82\n"
    "v_mul_f32 v83, v15, v83\n"
    "v_mul_f32 v84, v12, v84\n"
    "v_mul_f32 v85, v13, v85\n"
    "v_mul_f32 v86, v14, v86\n"
    "v_mul_f32 v87, v15, v87\n"
    "v_accvgpr_write_b32 a8, v80\n"
    "v_accvgpr_write_b32 a9, v81\n"
    "v_accvgpr_write_b32 a10, v82\n"
    "v_accvgpr_write_b32 a11, v83\n"
    "v_accvgpr_write_b32 a24, v84\n"
    "v_accvgpr_write_b32 a25, v85\n"
    "v_accvgpr_write_b32 a26, v86\n"
    "v_accvgpr_write_b32 a27, v87\n"
    "v_accvgpr_read_b32 v80, a40\n"
    "v_accvgpr_read_b32 v81, a41\n"
    "v_accvgpr_read_b32 v82, a42\n"
    "v_accvgpr_read_b32 v83, a43\n"
    "v_accvgpr_read_b32 v84, a56\n"
    "v_accvgpr_read_b32 v85, a57\n"
    "v_accvgpr_read_b32 v86, a58\n"
    "v_accvgpr_read_b32 v87, a59\n"
    "v_mul_f32 v80, v12, v80\n"
    "v_mul_f32 v81, v13, v81\n"
    "v_mul_f32 v82, v14, v82\n"
    "v_mul_f32 v83, v15, v83\n"
    "v_mul_f32 v84, v12, v84\n"
    "v_mul_f32 v85, v13, v85\n"
    "v_mul_f32 v86, v14, v86\n"
    "v_mul_f32 v87, v15, v87\n"
    "v_accvgpr_write_b32 a40, v80\n"
    "v_accvgpr_write_b32 a41, v81\n"
    "v_accvgpr_write_b32 a42, v82\n"
    "v_accvgpr_write_b32 a43, v83\n"
    "v_accvgpr_write_b32 a56, v84\n"
    "v_accvgpr_write_b32 a57, v85\n"
    "v_accvgpr_write_b32 a58, v86\n"
    "v_accvgpr_write_b32 a59, v87\n"
    "v_accvgpr_read_b32 v80, a72\n"
    "v_accvgpr_read_b32 v81, a73\n"
    "v_accvgpr_read_b32 v82, a74\n"
    "v_accvgpr_read_b32 v83, a75\n"
    "v_accvgpr_read_b32 v84, a88\n"
    "v_accvgpr_read_b32 v85, a89\n"
    "v_accvgpr_read_b32 v86, a90\n"
    "v_accvgpr_read_b32 v87, a91\n"
    "v_mul_f32 v80, v12, v80\n"
    "v_mul_f32 v81, v13, v81\n"
    "v_mul_f32 v82, v14, v82\n"
    "v_mul_f32 v83, v15, v83\n"
    "v_mul_f32 v84, v12, v84\n"
    "v_mul_f32 v85, v13, v85\n"
    "v_mul_f32 v86, v14, v86\n"
    "v_mul_f32 v87, v15, v87\n"
    "v_accvgpr_write_b32 a72, v80\n"
    "v_accvgpr_write_b32 a73, v81\n"
    "v_accvgpr_write_b32 a74, v82\n"
    "v_accvgpr_write_b32 a75, v83\n"
    "v_accvgpr_write_b32 a88, v84\n"
    "v_accvgpr_write_b32 a89, v85\n"
    "v_accvgpr_write_b32 a90, v86\n"
    "v_accvgpr_write_b32 a91, v87\n"
    "v_accvgpr_read_b32 v80, a104\n"
    "v_accvgpr_read_b32 v81, a105\n"
    "v_accvgpr_read_b32 v82, a106\n"
    "v_accvgpr_read_b32 v83, a107\n"
    "v_accvgpr_read_b32 v84, a120\n"
    "v_accvgpr_read_b32 v85, a121\n"
    "v_accvgpr_read_b32 v86, a122\n"
    "v_accvgpr_read_b32 v87, a123\n"
    "v_mul_f32 v80, v12, v80\n"
    "v_mul_f32 v81, v13, v81\n"
    "v_mul_f32 v82, v14, v82\n"
    "v_mul_f32 v83, v15, v83\n"
    "v_mul_f32 v84, v12, v84\n"
    "v_mul_f32 v85, v13, v85\n"
    "v_mul_f32 v86, v14, v86\n"
    "v_mul_f32 v87, v15, v87\n"
    "v_accvgpr_write_b32 a104, v80\n"
    "v_accvgpr_write_b32 a105, v81\n"
    "v_accvgpr_write_b32 a106, v82\n"
    "v_accvgpr_write_b32 a107, v83\n"
    "v_accvgpr_write_b32 a120, v84\n"
    "v_accvgpr_write_b32 a121, v85\n"
    "v_accvgpr_write_b32 a122, v86\n"
    "v_accvgpr_write_b32 a123, v87\n"
    "s_nop 7\n"
    "s_nop 7\n"
    "s_branch L_ret_C2\n"
    "L_rare_C3:\n"
    "s_nop 7\n"
    "s_nop 7\n"
    "s_sub_u32 s56, s48, 0x20000\n"
    "s_add_u32 s57, s56, 0x0\n"
    "s_add_u32 s58, s56, 0x1000\n"
    "buffer_load_dwordx4 a[224:227], v76, s[40:43], s57 offen offset:0 sc1\n"
    "buffer_load_dwordx4 a[228:231], v76, s[40:43], s57 offen offset:1024 sc1\n"
    "buffer_load_dwordx4 a[232:235], v76, s[40:43], s57 offen offset:2048 sc1\n"
    "buffer_load_dwordx4 a[236:239], v76, s[40:43], s57 offen offset:3072 sc1\n"
    "buffer_load_dwordx4 a[240:243], v76, s[40:43], s58 offen offset:0 sc1\n"
    "buffer_load_dwordx4 a[244:247], v76, s[40:43], s58 offen offset:1024 sc1\n"
    "buffer_load_dwordx4 a[248:251], v76, s[40:43], s58 offen offset:2048 sc1\n"
    "buffer_load_dwordx4 a[252:255], v76, s[40:43], s58 offen offset:3072 sc1\n"
    "s_waitcnt vmcnt(0)\n"
    "v_mfma_f32_16x16x32_bf16 v[80:83], a[224:227], a[176:179], v[44:47]\n"
    "v_mfma_f32_16x16x32_bf16 v[80:83], a[228:231], a[180:183], v[80:83]\n"
    "v_mfma_f32_16x16x32_bf16 v[80:83], a[232:235], a[184:187], v[80:83]\n"
    "v_mfma_f32_16x16x32_bf16 v[80:83], a[236:239], a[188:191], v[80:83]\n"
    "v_mfma_f32_16x16x32_bf16 v[84:87], a[240:243], a[176:179], v[44:47]\n"
    "v_mfma_f32_16x16x32_bf16 v[84:87], a[244:247], a[180:183], v[84:87]\n"
    "v_mfma_f32_16x16x32_bf16 v[84:87], a[248:251], a[184:187], v[84:87]\n"
    "v_mfma_f32_16x16x32_bf16 v[84:87], a[252:255], a[188:191], v[84:87]\n"
    "s_nop 3\n"
    "v_max_f32 v9, v80, v80\n"
    "v_max3_f32 v9, v9, v80, v81\n"
    "v_max3_f32 v9, v9, v82, v83\n"
    "s_nop 0\n"
    "v_max3_f32 v9, v9, v84, v85\n"
    "v_max3_f32 v9, v9, v86, v87\n"
    "s_add_u32 s57, s56, 0x2000\n"
    "s_add_u32 s58, s56, 0x3000\n"
    "buffer_load_dwordx4 a[224:227], v76, s[40:43], s57 offen offset:0 sc1\n"
    "buffer_load_dwordx4 a[228:231], v76, s[40:43], s57 offen offset:1024 sc1\n"
    "buffer_load_dwordx4 a[232:235], v76, s[40:43], s57 offen offset:2048 sc1\n"
    "buffer_load_dwordx4 a[236:239], v76, s[40:43], s57 offen offset:3072 sc1\n"
    "buffer_load_dwordx4 a[240:243], v76, s[40:43], s58 offen offset:0 sc1\n"
    "buffer_load_dwordx4 a[244:247], v76, s[40:43], s58 offen offset:1024 sc1\n"
    "buffer_load_dwordx4 a[248:251], v76, s[40:43], s58 offen offset:2048 sc1\n"
    "buffer_load_dwordx4 a[252:255], v76, s[40:43], s58 offen offset:3072 sc1\n"
    "s_waitcnt vmcnt(0)\n"
    "v_mfma_f32_16x16x32_bf16 v[80:83], a[224:227], a[176:179], v[44:47]\n"
    "v_mfma_f32_16x16x32_bf16 v[80:83], a[228:231], a[180:183], v[80:83]\n"
    "v_mfma_f32_16x16x32_bf16 v[80:83], a[232:235], a[184:187], v[80:83]\n"
    "v_mfma_f32_16x16x32_bf16 v[80:83], a[236:239], a[188:191], v[80:83]\n"
    "v_mfma_f32_16x16x32_bf16 v[84:87], a[240:243], a[176:179], v[44:47]\n"
    "v_mfma_f32_16x16x32_bf16 v[84:87], a[244:247], a[180:183], v[84:87]\n"
    "v_mfma_f32_16x16x32_bf16 v[84:87], a[248:251], a[184:187], v[84:87]\n"
    "v_mfma_f32_16x16x32_bf16 v[84:87], a[252:255], a[188:191], v[84:87]\n"
    "s_nop 3\n"
    "v_max3_f32 v9, v9, v80, v81\n"
    "v_max3_f32 v9, v9, v82, v83\n"
    "s_nop 1\n"
    "v_max3_f32 v9, v9, v84, v85\n"
    "v_max3_f32 v9, v9, v86, v87\n"
    "s_add_u32 s57, s56, 0x4000\n"
    "s_add_u32 s58, s56, 0x5000\n"
    "buffer_load_dwordx4 a[224:227], v76, s[40:43], s57 offen offset:0 sc1\n"
    "buffer_load_dwordx4 a[228:231], v76, s[40:43], s57 offen offset:1024 sc1\n"
    "buffer_load_dwordx4 a[232:235], v76, s[40:43], s57 offen offset:2048 sc1\n"
    "buffer_load_dwordx4 a[236:239], v76, s[40:43], s57 offen offset:3072 sc1\n"
    "buffer_load_dwordx4 a[240:243], v76, s[40:43], s58 offen offset:0 sc1\n"
    "buffer_load_dwordx4 a[244:247], v76, s[40:43], s58 offen offset:1024 sc1\n"
    "buffer_load_dwordx4 a[248:251], v76, s[40:43], s58 offen offset:2048 sc1\n"
    "buffer_load_dwordx4 a[252:255], v76, s[40:43], s58 offen offset:3072 sc1\n"
    "s_waitcnt vmcnt(0)\n"
    "v_mfma_f32_16x16x32_bf16 v[80:83], a[224:227], a[176:179], v[44:47]\n"
    "v_mfma_f32_16x16x32_bf16 v[80:83], a[228:231], a[180:183], v[80:83]\n"
    "v_mfma_f32_16x16x32_bf16 v[80:83], a[232:235], a[184:187], v[80:83]\n"
    "v_mfma_f32_16x16x32_bf16 v[80:83], a[236:239], a[188:191], v[80:83]\n"
    "v_mfma_f32_16x16x32_bf16 v[84:87], a[240:243], a[176:179], v[44:47]\n"
    "v_mfma_f32_16x16x32_bf16 v[84:87], a[244:247], a[180:183], v[84:87]\n"
    "v_mfma_f32_16x16x32_bf16 v[84:87], a[248:251], a[184:187], v[84:87]\n"
    "v_mfma_f32_16x16x32_bf16 v[84:87], a[252:255], a[188:191], v[84:87]\n"
    "s_nop 3\n"
    "v_max3_f32 v9, v9, v80, v81\n"
    "v_max3_f32 v9, v9, v82, v83\n"
    "s_nop 1\n"
    "v_max3_f32 v9, v9, v84, v85\n"
    "v_max3_f32 v9, v9, v86, v87\n"
    "s_add_u32 s57, s56, 0x6000\n"
    "s_add_u32 s58, s56, 0x7000\n"
    "buffer_load_dwordx4 a[224:227], v76, s[40:43], s57 offen offset:0 sc1\n"
    "buffer_load_dwordx4 a[228:231], v76, s[40:43], s57 offen offset:1024 sc1\n"
    "buffer_load_dwordx4 a[232:235], v76, s[40:43], s57 offen offset:2048 sc1\n"
    "buffer_load_dwordx4 a[236:239], v76, s[40:43], s57 offen offset:3072 sc1\n"
    "buffer_load_dwordx4 a[240:243], v76, s[40:43], s58 offen offset:0 sc1\n"
    "buffer_load_dwordx4 a[244:247], v76, s[40:43], s58 offen offset:1024 sc1\n"
    "buffer_load_dwordx4 a[248:251], v76, s[40:43], s58 offen offset:2048 sc1\n"
    "buffer_load_dwordx4 a[252:255], v76, s[40:43], s58 offen offset:3072 sc1\n"
    "s_waitcnt vmcnt(0)\n"
    "v_mfma_f32_16x16x32_bf16 v[80:83], a[224:227], a[176:179], v[44:47]\n"
    "v_mfma_f32_16x16x32_bf16 v[80:83], a[228:231], a[180:183], v[80:83]\n"
    "v_mfma_f32_16x16x32_bf16 v[80:83], a[232:235], a[184:187], v[80:83]\n"
    "v_mfma_f32_16x16x32_bf16 v[80:83], a[236:239], a[188:191], v[80:83]\n"
    "v_mfma_f32_16x16x32_bf16 v[84:87], a[240:243], a[176:179], v[44:47]\n"
    "v_mfma_f32_16x16x32_bf16 v[84:87], a[244:247], a[180:183], v[84:87]\n"
    "v_mfma_f32_16x16x32_bf16 v[84:87], a[248:251], a[184:187], v[84:87]\n"
    "v_mfma_f32_16x16x32_bf16 v[84:87], a[252:255], a[188:191], v[84:87]\n"
    "s_nop 3\n"
    "v_max3_f32 v9, v9, v80, v81\n"
    "v_max3_f32 v9, v9, v82, v83\n"
    "s_nop 1\n"
    "v_max3_f32 v9, v9, v84, v85\n"
    "v_max3_f32 v9, v9, v86, v87\n"
    "v_mov_b32 v10, v9\n"
    "s_nop 1\n"
    "v_permlane16_swap_b32 v10, v9\n"
    "v_max_f32 v9, v10, v9\n"
    "v_mov_b32 v10, v9\n"
    "s_nop 1\n"
    "v_permlane32_swap_b32 v10, v9\n"
    "v_max_f32 v9, v10, v9\n"
    "v_max_f32 v9, 0, v9\n"
    "v_sub_f32 v44, v44, v9\n"
    "v_sub_f32 v45, v45, v9\n"
    "v_sub_f32 v46, v46, v9\n"
    "v_sub_f32 v47, v47, v9\n"
    "s_add_u32 s57, s56, 0x0\n"
    "s_add_u32 s58, s56, 0x1000\n"
    "buffer_load_dwordx4 a[224:227], v76, s[40:43], s57 offen offset:0 sc1\n"
    "buffer_load_dwordx4 a[228:231], v76, s[40:43], s57 offen offset:1024 sc1\n"
    "buffer_load_dwordx4 a[232:235], v76, s[40:43], s57 offen offset:2048 sc1\n"
    "buffer_load_dwordx4 a[236:239], v76, s[40:43], s57 offen offset:3072 sc1\n"
    "buffer_load_dwordx4 a[240:243], v76, s[40:43], s58 offen offset:0 sc1\n"
    "buffer_load_dwordx4 a[244:247], v76, s[40:43], s58 offen offset:1024 sc1\n"
    "buffer_load_dwordx4 a[248:251], v76, s[40:43], s58 offen offset:2048 sc1\n"
    "buffer_load_dwordx4 a[252:255], v76, s[40:43], s58 offen offset:3072 sc1\n"
    "s_waitcnt vmcnt(0)\n"
    "v_mfma_f32_16x16x32_bf16 v[80:83], a[224:227], a[176:179], v[44:47]\n"
    "v_mfma_f32_16x16x32_bf16 v[80:83], a[228:231], a[180:183], v[80:83]\n"
    "v_mfma_f32_16x16x32_bf16 v[80:83], a[232:235], a[184:187], v[80:83]\n"
    "v_mfma_f32_16x16x32_bf16 v[80:83], a[236:239], a[188:191], v[80:83]\n"
    "v_mfma_f32_16x16x32_bf16 v[84:87], a[240:243], a[176:179], v[44:47]\n"
    "v_mfma_f32_16x16x32_bf16 v[84:87], a[244:247], a[180:183], v[84:87]\n"
    "v_mfma_f32_16x16x32_bf16 v[84:87], a[248:251], a[184:187], v[84:87]\n"
    "v_mfma_f32_16x16x32_bf16 v[84:87], a[252:255], a[188:191], v[84:87]\n"
    "s_nop 3\n"
    "v_exp_f32 v80, v80\n"
    "v_exp_f32 v81, v81\n"
    "v_exp_f32 v82, v82\n"
    "v_exp_f32 v83, v83\n"
    "v_exp_f32 v84, v84\n"
    "v_exp_f32 v85, v85\n"
    "v_exp_f32 v86, v86\n"
    "v_exp_f32 v87, v87\n"
    "v_cvt_pk_bf16_f32 v176, v80, v81\n"
    "v_cvt_pk_bf16_f32 v177, v82, v83\n"
    "v_cvt_pk_bf16_f32 v178, v84, v85\n"
    "v_cvt_pk_bf16_f32 v179, v86, v87\n"
    "s_add_u32 s57, s56, 0x2000\n"
    "s_add_u32 s58, s56, 0x3000\n"
    "buffer_load_dwordx4 a[224:227], v76, s[40:43], s57 offen offset:0 sc1\n"
    "buffer_load_dwordx4 a[228:231], v76, s[40:43], s57 offen offset:1024 sc1\n"
    "buffer_load_dwordx4 a[232:235], v76, s[40:43], s57 offen offset:2048 sc1\n"
    "buffer_load_dwordx4 a[236:239], v76, s[40:43], s57 offen offset:3072 sc1\n"
    "buffer_load_dwordx4 a[240:243], v76, s[40:43], s58 offen offset:0 sc1\n"
    "buffer_load_dwordx4 a[244:247], v76, s[40:43], s58 offen offset:1024 sc1\n"
    "buffer_load_dwordx4 a[248:251], v76, s[40:43], s58 offen offset:2048 sc1\n"
    "buffer_load_dwordx4 a[252:255], v76, s[40:43], s58 offen offset:3072 sc1\n"
    "s_waitcnt vmcnt(0)\n"
    "v_mfma_f32_16x16x32_bf16 v[80:83], a[224:227], a[176:179], v[44:47]\n"
    "v_mfma_f32_16x16x32_bf16 v[80:83], a[228:231], a[180:183], v[80:83]\n"
    "v_mfma_f32_16x16x32_bf16 v[80:83], a[232:235], a[184:187], v[80:83]\n"
    "v_mfma_f32_16x16x32_bf16 v[80:83], a[236:239], a[188:191], v[80:83]\n"
    "v_mfma_f32_16x16x32_bf16 v[84:87], a[240:243], a[176:179], v[44:47]\n"
    "v_mfma_f32_16x16x32_bf16 v[84:87], a[244:247], a[180:183], v[84:87]\n"
    "v_mfma_f32_16x16x32_bf16 v[84:87], a[248:251], a[184:187], v[84:87]\n"
    "v_mfma_f32_16x16x32_bf16 v[84:87], a[252:255], a[188:191], v[84:87]\n"
    "s_nop 3\n"
    "v_exp_f32 v80, v80\n"
    "v_exp_f32 v81, v81\n"
    "v_exp_f32 v82, v82\n"
    "v_exp_f32 v83, v83\n"
    "v_exp_f32 v84, v84\n"
    "v_exp_f32 v85, v85\n"
    "v_exp_f32 v86, v86\n"
    "v_exp_f32 v87, v87\n"
    "v_cvt_pk_bf16_f32 v180, v80, v81\n"
    "v_cvt_pk_bf16_f32 v181, v82, v83\n"
    "v_cvt_pk_bf16_f32 v182, v84, v85\n"
    "v_cvt_pk_bf16_f32 v183, v86, v87\n"
    "s_add_u32 s57, s56, 0x4000\n"
    "s_add_u32 s58, s56, 0x5000\n"
    "buffer_load_dwordx4 a[224:227], v76, s[40:43], s57 offen offset:0 sc1\n"
    "buffer_load_dwordx4 a[228:231], v76, s[40:43], s57 offen offset:1024 sc1\n"
    "buffer_load_dwordx4 a[232:235], v76, s[40:43], s57 offen offset:2048 sc1\n"
    "buffer_load_dwordx4 a[236:239], v76, s[40:43], s57 offen offset:3072 sc1\n"
    "buffer_load_dwordx4 a[240:243], v76, s[40:43], s58 offen offset:0 sc1\n"
    "buffer_load_dwordx4 a[244:247], v76, s[40:43], s58 offen offset:1024 sc1\n"
    "buffer_load_dwordx4 a[248:251], v76, s[40:43], s58 offen offset:2048 sc1\n"
    "buffer_load_dwordx4 a[252:255], v76, s[40:43], s58 offen offset:3072 sc1\n"
    "s_waitcnt vmcnt(0)\n"
    "v_mfma_f32_16x16x32_bf16 v[80:83], a[224:227], a[176:179], v[44:47]\n"
    "v_mfma_f32_16x16x32_bf16 v[80:83], a[228:231], a[180:183], v[80:83]\n"
    "v_mfma_f32_16x16x32_bf16 v[80:83], a[232:235], a[184:187], v[80:83]\n"
    "v_mfma_f32_16x16x32_bf16 v[80:83], a[236:239], a[188:191], v[80:83]\n"
    "v_mfma_f32_16x16x32_bf16 v[84:87], a[240:243], a[176:179], v[44:47]\n"
    "v_mfma_f32_16x16x32_bf16 v[84:87], a[244:247], a[180:183], v[84:87]\n"
    "v_mfma_f32_16x16x32_bf16 v[84:87], a[248:251], a[184:187], v[84:87]\n"
    "v_mfma_f32_16x16x32_bf16 v[84:87], a[252:255], a[188:191], v[84:87]\n"
    "s_nop 3\n"
    "v_exp_f32 v80, v80\n"
    "v_exp_f32 v81, v81\n"
    "v_exp_f32 v82, v82\n"
    "v_exp_f32 v83, v83\n"
    "v_exp_f32 v84, v84\n"
    "v_exp_f32 v85, v85\n"
    "v_exp_f32 v86, v86\n"
    "v_exp_f32 v87, v87\n"
    "v_cvt_pk_bf16_f32 v184, v80, v81\n"
    "v_cvt_pk_bf16_f32 v185, v82, v83\n"
    "v_cvt_pk_bf16_f32 v186, v84, v85\n"
    "v_cvt_pk_bf16_f32 v187, v86, v87\n"
    "s_add_u32 s57, s56, 0x6000\n"
    "s_add_u32 s58, s56, 0x7000\n"
    "buffer_load_dwordx4 a[224:227], v76, s[40:43], s57 offen offset:0 sc1\n"
    "buffer_load_dwordx4 a[228:231], v76, s[40:43], s57 offen offset:1024 sc1\n"
    "buffer_load_dwordx4 a[232:235], v76, s[40:43], s57 offen offset:2048 sc1\n"
    "buffer_load_dwordx4 a[236:239], v76, s[40:43], s57 offen offset:3072 sc1\n"
    "buffer_load_dwordx4 a[240:243], v76, s[40:43], s58 offen offset:0 sc1\n"
    "buffer_load_dwordx4 a[244:247], v76, s[40:43], s58 offen offset:1024 sc1\n"
    "buffer_load_dwordx4 a[248:251], v76, s[40:43], s58 offen offset:2048 sc1\n"
    "buffer_load_dwordx4 a[252:255], v76, s[40:43], s58 offen offset:3072 sc1\n"
    "s_waitcnt vmcnt(0)\n"
    "v_mfma_f32_16x16x32_bf16 v[80:83], a[224:227], a[176:179], v[44:47]\n"
    "v_mfma_f32_16x16x32_bf16 v[80:83], a[228:231], a[180:183], v[80:83]\n"
    "v_mfma_f32_16x16x32_bf16 v[80:83], a[232:235], a[184:187], v[80:83]\n"
    "v_mfma_f32_16x16x32_bf16 v[80:83], a[236:239], a[188:191], v[80:83]\n"
    "v_mfma_f32_16x16x32_bf16 v[84:87], a[240:243], a[176:179], v[44:47]\n"
    "v_mfma_f32_16x16x32_bf16 v[84:87], a[244:247], a[180:183], v[84:87]\n"
    "v_mfma_f32_16x16x32_bf16 v[84:87], a[248:251], a[184:187], v[84:87]\n"
    "v_mfma_f32_16x16x32_bf16 v[84:87], a[252:255], a[188:191], v[84:87]\n"
    "s_nop 3\n"
    "v_exp_f32 v80, v80\n"
    "v_exp_f32 v81, v81\n"
    "v_exp_f32 v82, v82\n"
    "v_exp_f32 v83, v83\n"
    "v_exp_f32 v84, v84\n"
    "v_exp_f32 v85, v85\n"
    "v_exp_f32 v86, v86\n"
    "v_exp_f32 v87, v87\n"
    "v_cvt_pk_bf16_f32 v188, v80, v81\n"
    "v_cvt_pk_bf16_f32 v189, v82, v83\n"
    "v_cvt_pk_bf16_f32 v190, v84, v85\n"
    "v_cvt_pk_bf16_f32 v191, v86, v87\n"
    "v_mfma_f32_16x16x32_bf16 v[60:63], v[64:67], v[176:179], 0\n"
    "v_mfma_f32_16x16x32_bf16 v[60:63], v[64:67], v[180:183], v[60:63]\n"
    "v_mfma_f32_16x16x32_bf16 v[60:63], v[64:67], v[184:187], v[60:63]\n"
    "v_mfma_f32_16x16x32_bf16 v[60:63], v[64:67], v[188:191], v[60:63]\n"
    "v_sub_f32 v28, v28, v9\n"
    "v_sub_f32 v29, v29, v9\n"
    "v_sub_f32 v30, v30, v9\n"
    "v_sub_f32 v31, v31, v9\n"
    "v_sub_f32 v11, 0, v9\n"
    "v_exp_f32 v11, v11\n"
    "s_nop 0\n"
    "v_mul_f32 v71, v11, v71\n"
    "ds_bpermute_b32 v12, v99, v11 offset:0\n"
    "ds_bpermute_b32 v13, v99, v11 offset:4\n"
    "ds_bpermute_b32 v14, v99, v11 offset:8\n"
    "ds_bpermute_b32 v15, v99, v11 offset:12\n"
    "s_waitcnt lgkmcnt(0)\n"
    "v_accvgpr_read_b32 v80, a12\n"
    "v_accvgpr_read_b32 v81, a13\n"
    "v_accvgpr_read_b32 v82, a14\n"
    "v_accvgpr_read_b32 v83, a15\n"
    "v_accvgpr_read_b32 v84, a28\n"
    "v_accvgpr_read_b32 v85, a29\n"
    "v_accvgpr_read_b32 v86, a30\n"
    "v_accvgpr_read_b32 v87, a31\n"
    "v_mul_f32 v80, v12, v80\n"
    "v_mul_f32 v81, v13, v81\n"
    "v_mul_f32 v82, v14, v82\n"
    "v_mul_f32 v83, v15, v83\n"
    "v_mul_f32 v84, v12, v84\n"
    "v_mul_f32 v85, v13, v85\n"
    "v_mul_f32 v86, v14, v86\n"
    "v_mul_f32 v87, v15, v87\n"
    "v_accvgpr_write_b32 a12, v80\n"
    "v_accvgpr_write_b32 a13, v81\n"
    "v_accvgpr_write_b32 a14, v82\n"
    "v_accvgpr_write_b32 a15, v83\n"
    "v_accvgpr_write_b32 a28, v84\n"
    "v_accvgpr_write_b32 a29, v85\n"
    "v_accvgpr_write_b32 a30, v86\n"
    "v_accvgpr_write_b32 a31, v87\n"
    "v_accvgpr_read_b32 v80, a44\n"
    "v_accvgpr_read_b32 v81, a45\n"
    "v_accvgpr_read_b32 v82, a46\n"
    "v_accvgpr_read_b32 v83, a47\n"
    "v_accvgpr_read_b32 v84, a60\n"
    "v_accvgpr_read_b32 v85, a61\n"
    "v_accvgpr_read_b32 v86, a62\n"
    "v_accvgpr_read_b32 v87, a63\n"
    "v_mul_f32 v80, v12, v80\n"
    "v_mul_f32 v81, v13, v81\n"
    "v_mul_f32 v82, v14, v82\n"
    "v_mul_f32 v83, v15, v83\n"
    "v_mul_f32 v84, v12, v84\n"
    "v_mul_f32 v85, v13, v85\n"
    "v_mul_f32 v86, v14, v86\n"
    "v_mul_f32 v87, v15, v87\n"
    "v_accvgpr_write_b32 a44, v80\n"
    "v_accvgpr_write_b32 a45, v81\n"
    "v_accvgpr_write_b32 a46, v82\n"
    "v_accvgpr_write_b32 a47, v83\n"
    "v_accvgpr_write_b32 a60, v84\n"
    "v_accvgpr_write_b32 a61, v85\n"
    "v_accvgpr_write_b32 a62, v86\n"
    "v_accvgpr_write_b32 a63, v87\n"
    "v_accvgpr_read_b32 v80, a76\n"
    "v_accvgpr_read_b32 v81, a77\n"
    "v_accvgpr_read_b32 v82, a78\n"
    "v_accvgpr_read_b32 v83, a79\n"
    "v_accvgpr_read_b32 v84, a92\n"
    "v_accvgpr_read_b32 v85, a93\n"
    "v_accvgpr_read_b32 v86, a94\n"
    "v_accvgpr_read_b32 v87, a95\n"
    "v_mul_f32 v80, v12, v80\n"
    "v_mul_f32 v81, v13, v81\n"
    "v_mul_f32 v82, v14, v82\n"
    "v_mul_f32 v83, v15, v83\n"
    "v_mul_f32 v84, v12, v84\n"
    "v_mul_f32 v85, v13, v85\n"
    "v_mul_f32 v86, v14, v86\n"
    "v_mul_f32 v87, v15, v87\n"
    "v_accvgpr_write_b32 a76, v80\n"
    "v_accvgpr_write_b32 a77, v81\n"
    "v_accvgpr_write_b32 a78, v82\n"
    "v_accvgpr_write_b32 a79, v83\n"
    "v_accvgpr_write_b32 a92, v84\n"
    "v_accvgpr_write_b32 a93, v85\n"
    "v_accvgpr_write_b32 a94, v86\n"
    "v_accvgpr_write_b32 a95, v87\n"
    "v_accvgpr_read_b32 v80, a108\n"
    "v_accvgpr_read_b32 v81, a109\n"
    "v_accvgpr_read_b32 v82, a110\n"
    "v_accvgpr_read_b32 v83, a111\n"
    "v_accvgpr_read_b32 v84, a124\n"
    "v_accvgpr_read_b32 v85, a125\n"
    "v_accvgpr_read_b32 v86, a126\n"
    "v_accvgpr_read_b32 v87, a127\n"
    "v_mul_f32 v80, v12, v80\n"
    "v_mul_f32 v81, v13, v81\n"
    "v_mul_f32 v82, v14, v82\n"
    "v_mul_f32 v83, v15, v83\n"
    "v_mul_f32 v84, v12, v84\n"
    "v_mul_f32 v85, v13, v85\n"
    "v_mul_f32 v86, v14, v86\n"
    "v_mul_f32 v87, v15, v87\n"
    "v_accvgpr_write_b32 a108, v80\n"
    "v_accvgpr_write_b32 a109, v81\n"
    "v_accvgpr_write_b32 a110, v82\n"
    "v_accvgpr_write_b32 a111, v83\n"
    "v_accvgpr_write_b32 a124, v84\n"
    "v_accvgpr_write_b32 a125, v85\n"
    "v_accvgpr_write_b32 a126, v86\n"
    "v_accvgpr_write_b32 a127, v87\n"
    "s_nop 7\n"
    "s_nop 7\n"
    "s_branch L_ret_C3\n"
    "L_rare_D0:\n"
    "s_nop 7\n"
    "s_nop 7\n"
    "s_sub_u32 s56, s48, 0x20000\n"
    "s_add_u32 s57, s56, 0x0\n"
    "s_add_u32 s58, s56, 0x1000\n"
    "buffer_load_dwordx4 a[224:227], v76, s[40:43], s57 offen offset:0 sc1\n"
    "buffer_load_dwordx4 a[228:231], v76, s[40:43], s57 offen offset:1024 sc1\n"
    "buffer_load_dwordx4 a[232:235], v76, s[40:43], s57 offen offset:2048 sc1\n"
    "buffer_load_dwordx4 a[236:239], v76, s[40:43], s57 offen offset:3072 sc1\n"
    "buffer_load_dwordx4 a[240:243], v76, s[40:43], s58 offen offset:0 sc1\n"
    "buffer_load_dwordx4 a[244:247], v76, s[40:43], s58 offen offset:1024 sc1\n"
    "buffer_load_dwordx4 a[248:251], v76, s[40:43], s58 offen offset:2048 sc1\n"
    "buffer_load_dwordx4 a[252:255], v76, s[40:43], s58 offen offset:3072 sc1\n"
    "s_waitcnt vmcnt(0)\n"
    "v_mfma_f32_16x16x32_bf16 v[80:83], a[224:227], a[128:131], v[32:35]\n"
    "v_mfma_f32_16x16x32_bf16 v[80:83], a[228:231], a[132:135], v[80:83]\n"
    "v_mfma_f32_16x16x32_bf16 v[80:83], a[232:235], a[136:139], v[80:83]\n"
    "v_mfma_f32_16x16x32_bf16 v[80:83], a[236:239], a[140:143], v[80:83]\n"
    "v_mfma_f32_16x16x32_bf16 v[84:87], a[240:243], a[128:131], v[32:35]\n"
    "v_mfma_f32_16x16x32_bf16 v[84:87], a[244:247], a[132:135], v[84:87]\n"
    "v_mfma_f32_16x16x32_bf16 v[84:87], a[248:251], a[136:139], v[84:87]\n"
    "v_mfma_f32_16x16x32_bf16 v[84:87], a[252:255], a[140:143], v[84:87]\n"
    "s_nop 3\n"
    "v_max_f32 v9, v80, v80\n"
    "v_max3_f32 v9, v9, v80, v81\n"
    "v_max3_f32 v9, v9, v82, v83\n"
    "s_nop 0\n"
    "v_max3_f32 v9, v9, v84, v85\n"
    "v_max3_f32 v9, v9, v86, v87\n"
    "s_add_u32 s57, s56, 0x2000\n"
    "s_add_u32 s58, s56, 0x3000\n"
    "buffer_load_dwordx4 a[224:227], v76, s[40:43], s57 offen offset:0 sc1\n"
    "buffer_load_dwordx4 a[228:231], v76, s[40:43], s57 offen offset:1024 sc1\n"
    "buffer_load_dwordx4 a[232:235], v76, s[40:43], s57 offen offset:2048 sc1\n"
    "buffer_load_dwordx4 a[236:239], v76, s[40:43], s57 offen offset:3072 sc1\n"
    "buffer_load_dwordx4 a[240:243], v76, s[40:43], s58 offen offset:0 sc1\n"
    "buffer_load_dwordx4 a[244:247], v76, s[40:43], s58 offen offset:1024 sc1\n"
    "buffer_load_dwordx4 a[248:251], v76, s[40:43], s58 offen offset:2048 sc1\n"
    "buffer_load_dwordx4 a[252:255], v76, s[40:43], s58 offen offset:3072 sc1\n"
    "s_waitcnt vmcnt(0)\n"
    "v_mfma_f32_16x16x32_bf16 v[80:83], a[224:227], a[128:131], v[32:35]\n"
    "v_mfma_f32_16x16x32_bf16 v[80:83], a[228:231], a[132:135], v[80:83]\n"
    "v_mfma_f32_16x16x32_bf16 v[80:83], a[232:235], a[136:139], v[80:83]\n"
    "v_mfma_f32_16x16x32_bf16 v[80:83], a[236:239], a[140:143], v[80:83]\n"
    "v_mfma_f32_16x16x32_bf16 v[84:87], a[240:243], a[128:131], v[32:35]\n"
    "v_mfma_f32_16x16x32_bf16 v[84:87], a[244:247], a[132:135], v[84:87]\n"
    "v_mfma_f32_16x16x32_bf16 v[84:87], a[248:251], a[136:139], v[84:87]\n"
    "v_mfma_f32_16x16x32_bf16 v[84:87], a[252:255], a[140:143], v[84:87]\n"
    "s_nop 3\n"
    "v_max3_f32 v9, v9, v80, v81\n"
    "v_max3_f32 v9, v9, v82, v83\n"
    "s_nop 1\n"
    "v_max3_f32 v9, v9, v84, v85\n"
    "v_max3_f32 v9, v9, v86, v87\n"
    "s_add_u32 s57, s56, 0x4000\n"
    "s_add_u32 s58, s56, 0x5000\n"
    "buffer_load_dwordx4 a[224:227], v76, s[40:43], s57 offen offset:0 sc1\n"
    "buffer_load_dwordx4 a[228:231], v76, s[40:43], s57 offen offset:1024 sc1\n"
    "buffer_load_dwordx4 a[232:235], v76, s[40:43], s57 offen offset:2048 sc1\n"
    "buffer_load_dwordx4 a[236:239], v76, s[40:43], s57 offen offset:3072 sc1\n"
    "buffer_load_dwordx4 a[240:243], v76, s[40:43], s58 offen offset:0 sc1\n"
    "buffer_load_dwordx4 a[244:247], v76, s[40:43], s58 offen offset:1024 sc1\n"
    "buffer_load_dwordx4 a[248:251], v76, s[40:43], s58 offen offset:2048 sc1\n"
    "buffer_load_dwordx4 a[252:255], v76, s[40:43], s58 offen offset:3072 sc1\n"
    "s_waitcnt vmcnt(0)\n"
    "v_mfma_f32_16x16x32_bf16 v[80:83], a[224:227], a[128:131], v[32:35]\n"
    "v_mfma_f32_16x16x32_bf16 v[80:83], a[228:231], a[132:135], v[80:83]\n"
    "v_mfma_f32_16x16x32_bf16 v[80:83], a[232:235], a[136:139], v[80:83]\n"
    "v_mfma_f32_16x16x32_bf16 v[80:83], a[236:239], a[140:143], v[80:83]\n"
    "v_mfma_f32_16x16x32_bf16 v[84:87], a[240:243], a[128:131], v[32:35]\n"
    "v_mfma_f32_16x16x32_bf16 v[84:87], a[244:247], a[132:135], v[84:87]\n"
    "v_mfma_f32_16x16x32_bf16 v[84:87], a[248:251], a[136:139], v[84:87]\n"
    "v_mfma_f32_16x16x32_bf16 v[84:87], a[252:255], a[140:143], v[84:87]\n"
    "s_nop 3\n"
    "v_max3_f32 v9, v9, v80, v81\n"
    "v_max3_f32 v9, v9, v82, v83\n"
    "s_nop 1\n"
    "v_max3_f32 v9, v9, v84, v85\n"
    "v_max3_f32 v9, v9, v86, v87\n"
    "s_add_u32 s57, s56, 0x6000\n"
    "s_add_u32 s58, s56, 0x7000\n"
    "buffer_load_dwordx4 a[224:227], v76, s[40:43], s57 offen offset:0 sc1\n"
    "buffer_load_dwordx4 a[228:231], v76, s[40:43], s57 offen offset:1024 sc1\n"
    "buffer_load_dwordx4 a[232:235], v76, s[40:43], s57 offen offset:2048 sc1\n"
    "buffer_load_dwordx4 a[236:239], v76, s[40:43], s57 offen offset:3072 sc1\n"
    "buffer_load_dwordx4 a[240:243], v76, s[40:43], s58 offen offset:0 sc1\n"
    "buffer_load_dwordx4 a[244:247], v76, s[40:43], s58 offen offset:1024 sc1\n"
    "buffer_load_dwordx4 a[248:251], v76, s[40:43], s58 offen offset:2048 sc1\n"
    "buffer_load_dwordx4 a[252:255], v76, s[40:43], s58 offen offset:3072 sc1\n"
    "s_waitcnt vmcnt(0)\n"
    "v_mfma_f32_16x16x32_bf16 v[80:83], a[224:227], a[128:131], v[32:35]\n"
    "v_mfma_f32_16x16x32_bf16 v[80:83], a[228:231], a[132:135], v[80:83]\n"
    "v_mfma_f32_16x16x32_bf16 v[80:83], a[232:235], a[136:139], v[80:83]\n"
    "v_mfma_f32_16x16x32_bf16 v[80:83], a[236:239], a[140:143], v[80:83]\n"
    "v_mfma_f32_16x16x32_bf16 v[84:87], a[240:243], a[128:131], v[32:35]\n"
    "v_mfma_f32_16x16x32_bf16 v[84:87], a[244:247], a[132:135], v[84:87]\n"
    "v_mfma_f32_16x16x32_bf16 v[84:87], a[248:251], a[136:139], v[84:87]\n"
    "v_mfma_f32_16x16x32_bf16 v[84:87], a[252:255], a[140:143], v[84:87]\n"
    "s_nop 3\n"
    "v_max3_f32 v9, v9, v80, v81\n"
    "v_max3_f32 v9, v9, v82, v83\n"
    "s_nop 1\n"
    "v_max3_f32 v9, v9, v84, v85\n"
    "v_max3_f32 v9, v9, v86, v87\n"
    "v_mov_b32 v10, v9\n"
    "s_nop 1\n"
    "v_permlane16_swap_b32 v10, v9\n"
    "v_max_f32 v9, v10, v9\n"
    "v_mov_b32 v10, v9\n"
    "s_nop 1\n"
    "v_permlane32_swap_b32 v10, v9\n"
    "v_max_f32 v9, v10, v9\n"
    "v_max_f32 v9, 0, v9\n"
    "v_sub_f32 v32, v32, v9\n"
    "v_sub_f32 v33, v33, v9\n"
    "v_sub_f32 v34, v34, v9\n"
    "v_sub_f32 v35, v35, v9\n"
    "s_add_u32 s57, s56, 0x0\n"
    "s_add_u32 s58, s56, 0x1000\n"
    "buffer_load_dwordx4 a[224:227], v76, s[40:43], s57 offen offset:0 sc1\n"
    "buffer_load_dwordx4 a[228:231], v76, s[40:43], s57 offen offset:1024 sc1\n"
    "buffer_load_dwordx4 a[232:235], v76, s[40:43], s57 offen offset:2048 sc1\n"
    "buffer_load_dwordx4 a[236:239], v76, s[40:43], s57 offen offset:3072 sc1\n"
    "buffer_load_dwordx4 a[240:243], v76, s[40:43], s58 offen offset:0 sc1\n"
    "buffer_load_dwordx4 a[244:247], v76, s[40:43], s58 offen offset:1024 sc1\n"
    "buffer_load_dwordx4 a[248:251], v76, s[40:43], s58 offen offset:2048 sc1\n"
    "buffer_load_dwordx4 a[252:255], v76, s[40:43], s58 offen offset:3072 sc1\n"
    "s_waitcnt vmcnt(0)\n"
    "v_mfma_f32_16x16x32_bf16 v[80:83], a[224:227], a[128:131], v[32:35]\n"
    "v_mfma_f32_16x16x32_bf16 v[80:83], a[228:231], a[132:135], v[80:83]\n"
    "v_mfma_f32_16x16x32_bf16 v[80:83], a[232:235], a[136:139], v[80:83]\n"
    "v_mfma_f32_16x16x32_bf16 v[80:83], a[236:239], a[140:143], v[80:83]\n"
    "v_mfma_f32_16x16x32_bf16 v[84:87], a[240:243], a[128:131], v[32:35]\n"
    "v_mfma_f32_16x16x32_bf16 v[84:87], a[244:247], a[132:135], v[84:87]\n"
    "v_mfma_f32_16x16x32_bf16 v[84:87], a[248:251], a[136:139], v[84:87]\n"
    "v_mfma_f32_16x16x32_bf16 v[84:87], a[252:255], a[140:143], v[84:87]\n"
    "s_nop 3\n"
    "v_exp_f32 v80, v80\n"
    "v_exp_f32 v81, v81\n"
    "v_exp_f32 v82, v82\n"
    "v_exp_f32 v83, v83\n"
    "v_exp_f32 v84, v84\n"
    "v_exp_f32 v85, v85\n"
    "v_exp_f32 v86, v86\n"
    "v_exp_f32 v87, v87\n"
    "v_cvt_pk_bf16_f32 v192, v80, v81\n"
    "v_cvt_pk_bf16_f32 v193, v82, v83\n"
    "v_cvt_pk_bf16_f32 v194, v84, v85\n"
    "v_cvt_pk_bf16_f32 v195, v86, v87\n"
    "s_add_u32 s57, s56, 0x2000\n"
    "s_add_u32 s58, s56, 0x3000\n"
    "buffer_load_dwordx4 a[224:227], v76, s[40:43], s57 offen offset:0 sc1\n"
    "buffer_load_dwordx4 a[228:231], v76, s[40:43], s57 offen offset:1024 sc1\n"
    "buffer_load_dwordx4 a[232:235], v76, s[40:43], s57 offen offset:2048 sc1\n"
    "buffer_load_dwordx4 a[236:239], v76, s[40:43], s57 offen offset:3072 sc1\n"
    "buffer_load_dwordx4 a[240:243], v76, s[40:43], s58 offen offset:0 sc1\n"
    "buffer_load_dwordx4 a[244:247], v76, s[40:43], s58 offen offset:1024 sc1\n"
    "buffer_load_dwordx4 a[248:251], v76, s[40:43], s58 offen offset:2048 sc1\n"
    "buffer_load_dwordx4 a[252:255], v76, s[40:43], s58 offen offset:3072 sc1\n"
    "s_waitcnt vmcnt(0)\n"
    "v_mfma_f32_16x16x32_bf16 v[80:83], a[224:227], a[128:131], v[32:35]\n"
    "v_mfma_f32_16x16x32_bf16 v[80:83], a[228:231], a[132:135], v[80:83]\n"
    "v_mfma_f32_16x16x32_bf16 v[80:83], a[232:235], a[136:139], v[80:83]\n"
    "v_mfma_f32_16x16x32_bf16 v[80:83], a[236:239], a[140:143], v[80:83]\n"
    "v_mfma_f32_16x16x32_bf16 v[84:87], a[240:243], a[128:131], v[32:35]\n"
    "v_mfma_f32_16x16x32_bf16 v[84:87], a[244:247], a[132:135], v[84:87]\n"
    "v_mfma_f32_16x16x32_bf16 v[84:87], a[248:251], a[136:139], v[84:87]\n"
    "v_mfma_f32_16x16x32_bf16 v[84:87], a[252:255], a[140:143], v[84:87]\n"
    "s_nop 3\n"
    "v_exp_f32 v80, v80\n"
    "v_exp_f32 v81, v81\n"
    "v_exp_f32 v82, v82\n"
    "v_exp_f32 v83, v83\n"
    "v_exp_f32 v84, v84\n"
    "v_exp_f32 v85, v85\n"
    "v_exp_f32 v86, v86\n"
    "v_exp_f32 v87, v87\n"
    "v_cvt_pk_bf16_f32 v196, v80, v81\n"
    "v_cvt_pk_bf16_f32 v197, v82, v83\n"
    "v_cvt_pk_bf16_f32 v198, v84, v85\n"
    "v_cvt_pk_bf16_f32 v199, v86, v87\n"
    "s_add_u32 s57, s56, 0x4000\n"
    "s_add_u32 s58, s56, 0x5000\n"
    "buffer_load_dwordx4 a[224:227], v76, s[40:43], s57 offen offset:0 sc1\n"
    "buffer_load_dwordx4 a[228:231], v76, s[40:43], s57 offen offset:1024 sc1\n"
    "buffer_load_dwordx4 a[232:235], v76, s[40:43], s57 offen offset:2048 sc1\n"
    "buffer_load_dwordx4 a[236:239], v76, s[40:43], s57 offen offset:3072 sc1\n"
    "buffer_load_dwordx4 a[240:243], v76, s[40:43], s58 offen offset:0 sc1\n"
    "buffer_load_dwordx4 a[244:247], v76, s[40:43], s58 offen offset:1024 sc1\n"
    "buffer_load_dwordx4 a[248:251], v76, s[40:43], s58 offen offset:2048 sc1\n"
    "buffer_load_dwordx4 a[252:255], v76, s[40:43], s58 offen offset:3072 sc1\n"
    "s_waitcnt vmcnt(0)\n"
    "v_mfma_f32_16x16x32_bf16 v[80:83], a[224:227], a[128:131], v[32:35]\n"
    "v_mfma_f32_16x16x32_bf16 v[80:83], a[228:231], a[132:135], v[80:83]\n"
    "v_mfma_f32_16x16x32_bf16 v[80:83], a[232:235], a[136:139], v[80:83]\n"
    "v_mfma_f32_16x16x32_bf16 v[80:83], a[236:239], a[140:143], v[80:83]\n"
    "v_mfma_f32_16x16x32_bf16 v[84:87], a[240:243], a[128:131], v[32:35]\n"
    "v_mfma_f32_16x16x32_bf16 v[84:87], a[244:247], a[132:135], v[84:87]\n"
    "v_mfma_f32_16x16x32_bf16 v[84:87], a[248:251], a[136:139], v[84:87]\n"
    "v_mfma_f32_16x16x32_bf16 v[84:87], a[252:255], a[140:143], v[84:87]\n"
    "s_nop 3\n"
    "v_exp_f32 v80, v80\n"
    "v_exp_f32 v81, v81\n"
    "v_exp_f32 v82, v82\n"
    "v_exp_f32 v83, v83\n"
    "v_exp_f32 v84, v84\n"
    "v_exp_f32 v85, v85\n"
    "v_exp_f32 v86, v86\n"
    "v_exp_f32 v87, v87\n"
    "v_cvt_pk_bf16_f32 v200, v80, v81\n"
    "v_cvt_pk_bf16_f32 v201, v82, v83\n"
    "v_cvt_pk_bf16_f32 v202, v84, v85\n"
    "v_cvt_pk_bf16_f32 v203, v86, v87\n"
    "s_add_u32 s57, s56, 0x6000\n"
    "s_add_u32 s58, s56, 0x7000\n"
    "buffer_load_dwordx4 a[224:227], v76, s[40:43], s57 offen offset:0 sc1\n"
    "buffer_load_dwordx4 a[228:231], v76, s[40:43], s57 offen offset:1024 sc1\n"
    "buffer_load_dwordx4 a[232:235], v76, s[40:43], s57 offen offset:2048 sc1\n"
    "buffer_load_dwordx4 a[236:239], v76, s[40:43], s57 offen offset:3072 sc1\n"
    "buffer_load_dwordx4 a[240:243], v76, s[40:43], s58 offen offset:0 sc1\n"
    "buffer_load_dwordx4 a[244:247], v76, s[40:43], s58 offen offset:1024 sc1\n"
    "buffer_load_dwordx4 a[248:251], v76, s[40:43], s58 offen offset:2048 sc1\n"
    "buffer_load_dwordx4 a[252:255], v76, s[40:43], s58 offen offset:3072 sc1\n"
    "s_waitcnt vmcnt(0)\n"
    "v_mfma_f32_16x16x32_bf16 v[80:83], a[224:227], a[128:131], v[32:35]\n"
    "v_mfma_f32_16x16x32_bf16 v[80:83], a[228:231], a[132:135], v[80:83]\n"
    "v_mfma_f32_16x16x32_bf16 v[80:83], a[232:235], a[136:139], v[80:83]\n"
    "v_mfma_f32_16x16x32_bf16 v[80:83], a[236:239], a[140:143], v[80:83]\n"
    "v_mfma_f32_16x16x32_bf16 v[84:87], a[240:243], a[128:131], v[32:35]\n"
    "v_mfma_f32_16x16x32_bf16 v[84:87], a[244:247], a[132:135], v[84:87]\n"
    "v_mfma_f32_16x16x32_bf16 v[84:87], a[248:251], a[136:139], v[84:87]\n"
    "v_mfma_f32_16x16x32_bf16 v[84:87], a[252:255], a[140:143], v[84:87]\n"
    "s_nop 3\n"
    "v_exp_f32 v80, v80\n"
    "v_exp_f32 v81, v81\n"
    "v_exp_f32 v82, v82\n"
    "v_exp_f32 v83, v83\n"
    "v_exp_f32 v84, v84\n"
    "v_exp_f32 v85, v85\n"
    "v_exp_f32 v86, v86\n"
    "v_exp_f32 v87, v87\n"
    "v_cvt_pk_bf16_f32 v204, v80, v81\n"
    "v_cvt_pk_bf16_f32 v205, v82, v83\n"
    "v_cvt_pk_bf16_f32 v206, v84, v85\n"
    "v_cvt_pk_bf16_f32 v207, v86, v87\n"
    "v_mfma_f32_16x16x32_bf16 v[48:51], v[64:67], v[192:195], 0\n"
    "v_mfma_f32_16x16x32_bf16 v[48:51], v[64:67], v[196:199], v[48:51]\n"
    "v_mfma_f32_16x16x32_bf16 v[48:51], v[64:67], v[200:203], v[48:51]\n"
    "v_mfma_f32_16x16x32_bf16 v[48:51], v[64:67], v[204:207], v[48:51]\n"
    "v_sub_f32 v16, v16, v9\n"
    "v_sub_f32 v17, v17, v9\n"
    "v_sub_f32 v18, v18, v9\n"
    "v_sub_f32 v19, v19, v9\n"
    "v_sub_f32 v11, 0, v9\n"
    "v_exp_f32 v11, v11\n"
    "s_nop 0\n"
    "v_mul_f32 v68, v11, v68\n"
    "ds_bpermute_b32 v12, v99, v11 offset:0\n"
    "ds_bpermute_b32 v13, v99, v11 offset:4\n"
    "ds_bpermute_b32 v14, v99, v11 offset:8\n"
    "ds_bpermute_b32 v15, v99, v11 offset:12\n"
    "s_waitcnt lgkmcnt(0)\n"
    "v_accvgpr_read_b32 v80, a0\n"
    "v_accvgpr_read_b32 v81, a1\n"
    "v_accvgpr_read_b32 v82, a2\n"
    "v_accvgpr_read_b32 v83, a3\n"
    "v_accvgpr_read_b32 v84, a16\n"
    "v_accvgpr_read_b32 v85, a17\n"
    "v_accvgpr_read_b32 v86, a18\n"
    "v_accvgpr_read_b32 v87, a19\n"
    "v_mul_f32 v80, v12, v80\n"
    "v_mul_f32 v81, v13, v81\n"
    "v_mul_f32 v82, v14, v82\n"
    "v_mul_f32 v83, v15, v83\n"
    "v_mul_f32 v84, v12, v84\n"
    "v_mul_f32 v85, v13, v85\n"
    "v_mul_f32 v86, v14, v86\n"
    "v_mul_f32 v87, v15, v87\n"
    "v_accvgpr_write_b32 a0, v80\n"
    "v_accvgpr_write_b32 a1, v81\n"
    "v_accvgpr_write_b32 a2, v82\n"
    "v_accvgpr_write_b32 a3, v83\n"
    "v_accvgpr_write_b32 a16, v84\n"
    "v_accvgpr_write_b32 a17, v85\n"
    "v_accvgpr_write_b32 a18, v86\n"
    "v_accvgpr_write_b32 a19, v87\n"
    "v_accvgpr_read_b32 v80, a32\n"
    "v_accvgpr_read_b32 v81, a33\n"
    "v_accvgpr_read_b32 v82, a34\n"
    "v_accvgpr_read_b32 v83, a35\n"
    "v_accvgpr_read_b32 v84, a48\n"
    "v_accvgpr_read_b32 v85, a49\n"
    "v_accvgpr_read_b32 v86, a50\n"
    "v_accvgpr_read_b32 v87, a51\n"
    "v_mul_f32 v80, v12, v80\n"
    "v_mul_f32 v81, v13, v81\n"
    "v_mul_f32 v82, v14, v82\n"
    "v_mul_f32 v83, v15, v83\n"
    "v_mul_f32 v84, v12, v84\n"
    "v_mul_f32 v85, v13, v85\n"
    "v_mul_f32 v86, v14, v86\n"
    "v_mul_f32 v87, v15, v87\n"
    "v_accvgpr_write_b32 a32, v80\n"
    "v_accvgpr_write_b32 a33, v81\n"
    "v_accvgpr_write_b32 a34, v82\n"
    "v_accvgpr_write_b32 a35, v83\n"
    "v_accvgpr_write_b32 a48, v84\n"
    "v_accvgpr_write_b32 a49, v85\n"
    "v_accvgpr_write_b32 a50, v86\n"
    "v_accvgpr_write_b32 a51, v87\n"
    "v_accvgpr_read_b32 v80, a64\n"
    "v_accvgpr_read_b32 v81, a65\n"
    "v_accvgpr_read_b32 v82, a66\n"
    "v_accvgpr_read_b32 v83, a67\n"
    "v_accvgpr_read_b32 v84, a80\n"
    "v_accvgpr_read_b32 v85, a81\n"
    "v_accvgpr_read_b32 v86, a82\n"
    "v_accvgpr_read_b32 v87, a83\n"
    "v_mul_f32 v80, v12, v80\n"
    "v_mul_f32 v81, v13, v81\n"
    "v_mul_f32 v82, v14, v82\n"
    "v_mul_f32 v83, v15, v83\n"
    "v_mul_f32 v84, v12, v84\n"
    "v_mul_f32 v85, v13, v85\n"
    "v_mul_f32 v86, v14, v86\n"
    "v_mul_f32 v87, v15, v87\n"
    "v_accvgpr_write_b32 a64, v80\n"
    "v_accvgpr_write_b32 a65, v81\n"
    "v_accvgpr_write_b32 a66, v82\n"
    "v_accvgpr_write_b32 a67, v83\n"
    "v_accvgpr_write_b32 a80, v84\n"
    "v_accvgpr_write_b32 a81, v85\n"
    "v_accvgpr_write_b32 a82, v86\n"
    "v_accvgpr_write_b32 a83, v87\n"
    "v_accvgpr_read_b32 v80, a96\n"
    "v_accvgpr_read_b32 v81, a97\n"
    "v_accvgpr_read_b32 v82, a98\n"
    "v_accvgpr_read_b32 v83, a99\n"
    "v_accvgpr_read_b32 v84, a112\n"
    "v_accvgpr_read_b32 v85, a113\n"
    "v_accvgpr_read_b32 v86, a114\n"
    "v_accvgpr_read_b32 v87, a115\n"
    "v_mul_f32 v80, v12, v80\n"
    "v_mul_f32 v81, v13, v81\n"
    "v_mul_f32 v82, v14, v82\n"
    "v_mul_f32 v83, v15, v83\n"
    "v_mul_f32 v84, v12, v84\n"
    "v_mul_f32 v85, v13, v85\n"
    "v_mul_f32 v86, v14, v86\n"
    "v_mul_f32 v87, v15, v87\n"
    "v_accvgpr_write_b32 a96, v80\n"
    "v_accvgpr_write_b32 a97, v81\n"
    "v_accvgpr_write_b32 a98, v82\n"
    "v_accvgpr_write_b32 a99, v83\n"
    "v_accvgpr_write_b32 a112, v84\n"
    "v_accvgpr_write_b32 a113, v85\n"
    "v_accvgpr_write_b32 a114, v86\n"
    "v_accvgpr_write_b32 a115, v87\n"
    "s_nop 7\n"
    "s_nop 7\n"
    "s_branch L_ret_D0\n"
    "L_rare_D1:\n"
    "s_nop 7\n"
    "s_nop 7\n"
    "s_sub_u32 s56, s48, 0x20000\n"
    "s_add_u32 s57, s56, 0x0\n"
    "s_add_u32 s58, s56, 0x1000\n"
    "buffer_load_dwordx4 a[224:227], v76, s[40:43], s57 offen offset:0 sc1\n"
    "buffer_load_dwordx4 a[228:231], v76, s[40:43], s57 offen offset:1024 sc1\n"
    "buffer_load_dwordx4 a[232:235], v76, s[40:43], s57 offen offset:2048 sc1\n"
    "buffer_load_dwordx4 a[236:239], v76, s[40:43], s57 offen offset:3072 sc1\n"
    "buffer_load_dwordx4 a[240:243], v76, s[40:43], s58 offen offset:0 sc1\n"
    "buffer_load_dwordx4 a[244:247], v76, s[40:43], s58 offen offset:1024 sc1\n"
    "buffer_load_dwordx4 a[248:251], v76, s[40:43], s58 offen offset:2048 sc1\n"
    "buffer_load_dwordx4 a[252:255], v76, s[40:43], s58 offen offset:3072 sc1\n"
    "s_waitcnt vmcnt(0)\n"
    "v_mfma_f32_16x16x32_bf16 v[80:83], a[224:227], a[144:147], v[36:39]\n"
    "v_mfma_f32_16x16x32_bf16 v[80:83], a[228:231], a[148:151], v[80:83]\n"
    "v_mfma_f32_16x16x32_bf16 v[80:83], a[232:235], a[152:155], v[80:83]\n"
    "v_mfma_f32_16x16x32_bf16 v[80:83], a[236:239], a[156:159], v[80:83]\n"
    "v_mfma_f32_16x16x32_bf16 v[84:87], a[240:243], a[144:147], v[36:39]\n"
    "v_mfma_f32_16x16x32_bf16 v[84:87], a[244:247], a[148:151], v[84:87]\n"
    "v_mfma_f32_16x16x32_bf16 v[84:87], a[248:251], a[152:155], v[84:87]\n"
    "v_mfma_f32_16x16x32_bf16 v[84:87], a[252:255], a[156:159], v[84:87]\n"
    "s_nop 3\n"
    "v_max_f32 v9, v80, v80\n"
    "v_max3_f32 v9, v9, v80, v81\n"
    "v_max3_f32 v9, v9, v82, v83\n"
    "s_nop 0\n"
    "v_max3_f32 v9, v9, v84, v85\n"
    "v_max3_f32 v9, v9, v86, v87\n"
    "s_add_u32 s57, s56, 0x2000\n"
    "s_add_u32 s58, s56, 0x3000\n"
    "buffer_load_dwordx4 a[224:227], v76, s[40:43], s57 offen offset:0 sc1\n"
    "buffer_load_dwordx4 a[228:231], v76, s[40:43], s57 offen offset:1024 sc1\n"
    "buffer_load_dwordx4 a[232:235], v76, s[40:43], s57 offen offset:2048 sc1\n"
    "buffer_load_dwordx4 a[236:239], v76, s[40:43], s57 offen offset:3072 sc1\n"
    "buffer_load_dwordx4 a[240:243], v76, s[40:43], s58 offen offset:0 sc1\n"
    "buffer_load_dwordx4 a[244:247], v76, s[40:43], s58 offen offset:1024 sc1\n"
    "buffer_load_dwordx4 a[248:251], v76, s[40:43], s58 offen offset:2048 sc1\n"
    "buffer_load_dwordx4 a[252:255], v76, s[40:43], s58 offen offset:3072 sc1\n"
    "s_waitcnt vmcnt(0)\n"
    "v_mfma_f32_16x16x32_bf16 v[80:83], a[224:227], a[144:147], v[36:39]\n"
    "v_mfma_f32_16x16x32_bf16 v[80:83], a[228:231], a[148:151], v[80:83]\n"
    "v_mfma_f32_16x16x32_bf16 v[80:83], a[232:235], a[152:155], v[80:83]\n"
    "v_mfma_f32_16x16x32_bf16 v[80:83], a[236:239], a[156:159], v[80:83]\n"
    "v_mfma_f32_16x16x32_bf16 v[84:87], a[240:243], a[144:147], v[36:39]\n"
    "v_mfma_f32_16x16x32_bf16 v[84:87], a[244:247], a[148:151], v[84:87]\n"
    "v_mfma_f32_16x16x32_bf16 v[84:87], a[248:251], a[152:155], v[84:87]\n"
    "v_mfma_f32_16x16x32_bf16 v[84:87], a[252:255], a[156:159], v[84:87]\n"
    "s_nop 3\n"
    "v_max3_f32 v9, v9, v80, v81\n"
    "v_max3_f32 v9, v9, v82, v83\n"
    "s_nop 1\n"
    "v_max3_f32 v9, v9, v84, v85\n"
    "v_max3_f32 v9, v9, v86, v87\n"
    "s_add_u32 s57, s56, 0x4000\n"
    "s_add_u32 s58, s56, 0x5000\n"
    "buffer_load_dwordx4 a[224:227], v76, s[40:43], s57 offen offset:0 sc1\n"
    "buffer_load_dwordx4 a[228:231], v76, s[40:43], s57 offen offset:1024 sc1\n"
    "buffer_load_dwordx4 a[232:235], v76, s[40:43], s57 offen offset:2048 sc1\n"
    "buffer_load_dwordx4 a[236:239], v76, s[40:43], s57 offen offset:3072 sc1\n"
    "buffer_load_dwordx4 a[240:243], v76, s[40:43], s58 offen offset:0 sc1\n"
    "buffer_load_dwordx4 a[244:247], v76, s[40:43], s58 offen offset:1024 sc1\n"
    "buffer_load_dwordx4 a[248:251], v76, s[40:43], s58 offen offset:2048 sc1\n"
    "buffer_load_dwordx4 a[252:255], v76, s[40:43], s58 offen offset:3072 sc1\n"
    "s_waitcnt vmcnt(0)\n"
    "v_mfma_f32_16x16x32_bf16 v[80:83], a[224:227], a[144:147], v[36:39]\n"
    "v_mfma_f32_16x16x32_bf16 v[80:83], a[228:231], a[148:151], v[80:83]\n"
    "v_mfma_f32_16x16x32_bf16 v[80:83], a[232:235], a[152:155], v[80:83]\n"
    "v_mfma_f32_16x16x32_bf16 v[80:83], a[236:239], a[156:159], v[80:83]\n"
    "v_mfma_f32_16x16x32_bf16 v[84:87], a[240:243], a[144:147], v[36:39]\n"
    "v_mfma_f32_16x16x32_bf16 v[84:87], a[244:247], a[148:151], v[84:87]\n"
    "v_mfma_f32_16x16x32_bf16 v[84:87], a[248:251], a[152:155], v[84:87]\n"
    "v_mfma_f32_16x16x32_bf16 v[84:87], a[252:255], a[156:159], v[84:87]\n"
    "s_nop 3\n"
    "v_max3_f32 v9, v9, v80, v81\n"
    "v_max3_f32 v9, v9, v82, v83\n"
    "s_nop 1\n"
    "v_max3_f32 v9, v9, v84, v85\n"
    "v_max3_f32 v9, v9, v86, v87\n"
    "s_add_u32 s57, s56, 0x6000\n"
    "s_add_u32 s58, s56, 0x7000\n"
    "buffer_load_dwordx4 a[224:227], v76, s[40:43], s57 offen offset:0 sc1\n"
    "buffer_load_dwordx4 a[228:231], v76, s[40:43], s57 offen offset:1024 sc1\n"
    "buffer_load_dwordx4 a[232:235], v76, s[40:43], s57 offen offset:2048 sc1\n"
    "buffer_load_dwordx4 a[236:239], v76, s[40:43], s57 offen offset:3072 sc1\n"
    "buffer_load_dwordx4 a[240:243], v76, s[40:43], s58 offen offset:0 sc1\n"
    "buffer_load_dwordx4 a[244:247], v76, s[40:43], s58 offen offset:1024 sc1\n"
    "buffer_load_dwordx4 a[248:251], v76, s[40:43], s58 offen offset:2048 sc1\n"
    "buffer_load_dwordx4 a[252:255], v76, s[40:43], s58 offen offset:3072 sc1\n"
    "s_waitcnt vmcnt(0)\n"
    "v_mfma_f32_16x16x32_bf16 v[80:83], a[224:227], a[144:147], v[36:39]\n"
    "v_mfma_f32_16x16x32_bf16 v[80:83], a[228:231], a[148:151], v[80:83]\n"
    "v_mfma_f32_16x16x32_bf16 v[80:83], a[232:235], a[152:155], v[80:83]\n"
    "v_mfma_f32_16x16x32_bf16 v[80:83], a[236:239], a[156:159], v[80:83]\n"
    "v_mfma_f32_16x16x32_bf16 v[84:87], a[240:243], a[144:147], v[36:39]\n"
    "v_mfma_f32_16x16x32_bf16 v[84:87], a[244:247], a[148:151], v[84:87]\n"
    "v_mfma_f32_16x16x32_bf16 v[84:87], a[248:251], a[152:155], v[84:87]\n"
    "v_mfma_f32_16x16x32_bf16 v[84:87], a[252:255], a[156:159], v[84:87]\n"
    "s_nop 3\n"
    "v_max3_f32 v9, v9, v80, v81\n"
    "v_max3_f32 v9, v9, v82, v83\n"
    "s_nop 1\n"
    "v_max3_f32 v9, v9, v84, v85\n"
    "v_max3_f32 v9, v9, v86, v87\n"
    "v_mov_b32 v10, v9\n"
    "s_nop 1\n"
    "v_permlane16_swap_b32 v10, v9\n"
    "v_max_f32 v9, v10, v9\n"
    "v_mov_b32 v10, v9\n"
    "s_nop 1\n"
    "v_permlane32_swap_b32 v10, v9\n"
    "v_max_f32 v9, v10, v9\n"
    "v_max_f32 v9, 0, v9\n"
    "v_sub_f32 v36, v36, v9\n"
    "v_sub_f32 v37, v37, v9\n"
    "v_sub_f32 v38, v38, v9\n"
    "v_sub_f32 v39, v39, v9\n"
    "s_add_u32 s57, s56, 0x0\n"
    "s_add_u32 s58, s56, 0x1000\n"
    "buffer_load_dwordx4 a[224:227], v76, s[40:43], s57 offen offset:0 sc1\n"
    "buffer_load_dwordx4 a[228:231], v76, s[40:43], s57 offen offset:1024 sc1\n"
    "buffer_load_dwordx4 a[232:235], v76, s[40:43], s57 offen offset:2048 sc1\n"
    "buffer_load_dwordx4 a[236:239], v76, s[40:43], s57 offen offset:3072 sc1\n"
    "buffer_load_dwordx4 a[240:243], v76, s[40:43], s58 offen offset:0 sc1\n"
    "buffer_load_dwordx4 a[244:247], v76, s[40:43], s58 offen offset:1024 sc1\n"
    "buffer_load_dwordx4 a[248:251], v76, s[40:43], s58 offen offset:2048 sc1\n"
    "buffer_load_dwordx4 a[252:255], v76, s[40:43], s58 offen offset:3072 sc1\n"
    "s_waitcnt vmcnt(0)\n"
    "v_mfma_f32_16x16x32_bf16 v[80:83], a[224:227], a[144:147], v[36:39]\n"
    "v_mfma_f32_16x16x32_bf16 v[80:83], a[228:231], a[148:151], v[80:83]\n"
    "v_mfma_f32_16x16x32_bf16 v[80:83], a[232:235], a[152:155], v[80:83]\n"
    "v_mfma_f32_16x16x32_bf16 v[80:83], a[236:239], a[156:159], v[80:83]\n"
    "v_mfma_f32_16x16x32_bf16 v[84:87], a[240:243], a[144:147], v[36:39]\n"
    "v_mfma_f32_16x16x32_bf16 v[84:87], a[244:247], a[148:151], v[84:87]\n"
    "v_mfma_f32_16x16x32_bf16 v[84:87], a[248:251], a[152:155], v[84:87]\n"
    "v_mfma_f32_16x16x32_bf16 v[84:87], a[252:255], a[156:159], v[84:87]\n"
    "s_nop 3\n"
    "v_exp_f32 v80, v80\n"
    "v_exp_f32 v81, v81\n"
    "v_exp_f32 v82, v82\n"
    "v_exp_f32 v83, v83\n"
    "v_exp_f32 v84, v84\n"
    "v_exp_f32 v85, v85\n"
    "v_exp_f32 v86, v86\n"
    "v_exp_f32 v87, v87\n"
    "v_cvt_pk_bf16_f32 v208, v80, v81\n"
    "v_cvt_pk_bf16_f32 v209, v82, v83\n"
    "v_cvt_pk_bf16_f32 v210, v84, v85\n"
    "v_cvt_pk_bf16_f32 v211, v86, v87\n"
    "s_add_u32 s57, s56, 0x2000\n"
    "s_add_u32 s58, s56, 0x3000\n"
    "buffer_load_dwordx4 a[224:227], v76, s[40:43], s57 offen offset:0 sc1\n"
    "buffer_load_dwordx4 a[228:231], v76, s[40:43], s57 offen offset:1024 sc1\n"
    "buffer_load_dwordx4 a[232:235], v76, s[40:43], s57 offen offset:2048 sc1\n"
    "buffer_load_dwordx4 a[236:239], v76, s[40:43], s57 offen offset:3072 sc1\n"
    "buffer_load_dwordx4 a[240:243], v76, s[40:43], s58 offen offset:0 sc1\n"
    "buffer_load_dwordx4 a[244:247], v76, s[40:43], s58 offen offset:1024 sc1\n"
    "buffer_load_dwordx4 a[248:251], v76, s[40:43], s58 offen offset:2048 sc1\n"
    "buffer_load_dwordx4 a[252:255], v76, s[40:43], s58 offen offset:3072 sc1\n"
    "s_waitcnt vmcnt(0)\n"
    "v_mfma_f32_16x16x32_bf16 v[80:83], a[224:227], a[144:147], v[36:39]\n"
    "v_mfma_f32_16x16x32_bf16 v[80:83], a[228:231], a[148:151], v[80:83]\n"
    "v_mfma_f32_16x16x32_bf16 v[80:83], a[232:235], a[152:155], v[80:83]\n"
    "v_mfma_f32_16x16x32_bf16 v[80:83], a[236:239], a[156:159], v[80:83]\n"
    "v_mfma_f32_16x16x32_bf16 v[84:87], a[240:243], a[144:147], v[36:39]\n"
    "v_mfma_f32_16x16x32_bf16 v[84:87], a[244:247], a[148:151], v[84:87]\n"
    "v_mfma_f32_16x16x32_bf16 v[84:87], a[248:251], a[152:155], v[84:87]\n"
    "v_mfma_f32_16x16x32_bf16 v[84:87], a[252:255], a[156:159], v[84:87]\n"
    "s_nop 3\n"
    "v_exp_f32 v80, v80\n"
    "v_exp_f32 v81, v81\n"
    "v_exp_f32 v82, v82\n"
    "v_exp_f32 v83, v83\n"
    "v_exp_f32 v84, v84\n"
    "v_exp_f32 v85, v85\n"
    "v_exp_f32 v86, v86\n"
    "v_exp_f32 v87, v87\n"
    "v_cvt_pk_bf16_f32 v212, v80, v81\n"
    "v_cvt_pk_bf16_f32 v213, v82, v83\n"
    "v_cvt_pk_bf16_f32 v214, v84, v85\n"
    "v_cvt_pk_bf16_f32 v215, v86, v87\n"
    "s_add_u32 s57, s56, 0x4000\n"
    "s_add_u32 s58, s56, 0x5000\n"
    "buffer_load_dwordx4 a[224:227], v76, s[40:43], s57 offen offset:0 sc1\n"
    "buffer_load_dwordx4 a[228:231], v76, s[40:43], s57 offen offset:1024 sc1\n"
    "buffer_load_dwordx4 a[232:235], v76, s[40:43], s57 offen offset:2048 sc1\n"
    "buffer_load_dwordx4 a[236:239], v76, s[40:43], s57 offen offset:3072 sc1\n"
    "buffer_load_dwordx4 a[240:243], v76, s[40:43], s58 offen offset:0 sc1\n"
    "buffer_load_dwordx4 a[244:247], v76, s[40:43], s58 offen offset:1024 sc1\n"
    "buffer_load_dwordx4 a[248:251], v76, s[40:43], s58 offen offset:2048 sc1\n"
    "buffer_load_dwordx4 a[252:255], v76, s[40:43], s58 offen offset:3072 sc1\n"
    "s_waitcnt vmcnt(0)\n"
    "v_mfma_f32_16x16x32_bf16 v[80:83], a[224:227], a[144:147], v[36:39]\n"
    "v_mfma_f32_16x16x32_bf16 v[80:83], a[228:231], a[148:151], v[80:83]\n"
    "v_mfma_f32_16x16x32_bf16 v[80:83], a[232:235], a[152:155], v[80:83]\n"
    "v_mfma_f32_16x16x32_bf16 v[80:83], a[236:239], a[156:159], v[80:83]\n"
    "v_mfma_f32_16x16x32_bf16 v[84:87], a[240:243], a[144:147], v[36:39]\n"
    "v_mfma_f32_16x16x32_bf16 v[84:87], a[244:247], a[148:151], v[84:87]\n"
    "v_mfma_f32_16x16x32_bf16 v[84:87], a[248:251], a[152:155], v[84:87]\n"
    "v_mfma_f32_16x16x32_bf16 v[84:87], a[252:255], a[156:159], v[84:87]\n"
    "s_nop 3\n"
    "v_exp_f32 v80, v80\n"
    "v_exp_f32 v81, v81\n"
    "v_exp_f32 v82, v82\n"
    "v_exp_f32 v83, v83\n"
    "v_exp_f32 v84, v84\n"
    "v_exp_f32 v85, v85\n"
    "v_exp_f32 v86, v86\n"
    "v_exp_f32 v87, v87\n"
    "v_cvt_pk_bf16_f32 v216, v80, v81\n"
    "v_cvt_pk_bf16_f32 v217, v82, v83\n"
    "v_cvt_pk_bf16_f32 v218, v84, v85\n"
    "v_cvt_pk_bf16_f32 v219, v86, v87\n"
    "s_add_u32 s57, s56, 0x6000\n"
    "s_add_u32 s58, s56, 0x7000\n"
    "buffer_load_dwordx4 a[224:227], v76, s[40:43], s57 offen offset:0 sc1\n"
    "buffer_load_dwordx4 a[228:231], v76, s[40:43], s57 offen offset:1024 sc1\n"
    "buffer_load_dwordx4 a[232:235], v76, s[40:43], s57 offen offset:2048 sc1\n"
    "buffer_load_dwordx4 a[236:239], v76, s[40:43], s57 offen offset:3072 sc1\n"
    "buffer_load_dwordx4 a[240:243], v76, s[40:43], s58 offen offset:0 sc1\n"
    "buffer_load_dwordx4 a[244:247], v76, s[40:43], s58 offen offset:1024 sc1\n"
    "buffer_load_dwordx4 a[248:251], v76, s[40:43], s58 offen offset:2048 sc1\n"
    "buffer_load_dwordx4 a[252:255], v76, s[40:43], s58 offen offset:3072 sc1\n"
    "s_waitcnt vmcnt(0)\n"
    "v_mfma_f32_16x16x32_bf16 v[80:83], a[224:227], a[144:147], v[36:39]\n"
    "v_mfma_f32_16x16x32_bf16 v[80:83], a[228:231], a[148:151], v[80:83]\n"
    "v_mfma_f32_16x16x32_bf16 v[80:83], a[232:235], a[152:155], v[80:83]\n"
    "v_mfma_f32_16x16x32_bf16 v[80:83], a[236:239], a[156:159], v[80:83]\n"
    "v_mfma_f32_16x16x32_bf16 v[84:87], a[240:243], a[144:147], v[36:39]\n"
    "v_mfma_f32_16x16x32_bf16 v[84:87], a[244:247], a[148:151], v[84:87]\n"
    "v_mfma_f32_16x16x32_bf16 v[84:87], a[248:251], a[152:155], v[84:87]\n"
    "v_mfma_f32_16x16x32_bf16 v[84:87], a[252:255], a[156:159], v[84:87]\n"
    "s_nop 3\n"
    "v_exp_f32 v80, v80\n"
    "v_exp_f32 v81, v81\n"
    "v_exp_f32 v82, v82\n"
    "v_exp_f32 v83, v83\n"
    "v_exp_f32 v84, v84\n"
    "v_exp_f32 v85, v85\n"
    "v_exp_f32 v86, v86\n"
    "v_exp_f32 v87, v87\n"
    "v_cvt_pk_bf16_f32 v220, v80, v81\n"
    "v_cvt_pk_bf16_f32 v221, v82, v83\n"
    "v_cvt_pk_bf16_f32 v222, v84, v85\n"
    "v_cvt_pk_bf16_f32 v223, v86, v87\n"
    "v_mfma_f32_16x16x32_bf16 v[52:55], v[64:67], v[208:211], 0\n"
    "v_mfma_f32_16x16x32_bf16 v[52:55], v[64:67], v[212:215], v[52:55]\n"
    "v_mfma_f32_16x16x32_bf16 v[52:55], v[64:67], v[216:219], v[52:55]\n"
    "v_mfma_f32_16x16x32_bf16 v[52:55], v[64:67], v[220:223], v[52:55]\n"
    "v_sub_f32 v20, v20, v9\n"
    "v_sub_f32 v21, v21, v9\n"
    "v_sub_f32 v22, v22, v9\n"
    "v_sub_f32 v23, v23, v9\n"
    "v_sub_f32 v11, 0, v9\n"
    "v_exp_f32 v11, v11\n"
    "s_nop 0\n"
    "v_mul_f32 v69, v11, v69\n"
    "ds_bpermute_b32 v12, v99, v11 offset:0\n"
    "ds_bpermute_b32 v13, v99, v11 offset:4\n"
    "ds_bpermute_b32 v14, v99, v11 offset:8\n"
    "ds_bpermute_b32 v15, v99, v11 offset:12\n"
    "s_waitcnt lgkmcnt(0)\n"
    "v_accvgpr_read_b32 v80, a4\n"
    "v_accvgpr_read_b32 v81, a5\n"
    "v_accvgpr_read_b32 v82, a6\n"
    "v_accvgpr_read_b32 v83, a7\n"
    "v_accvgpr_read_b32 v84, a20\n"
    "v_accvgpr_read_b32 v85, a21\n"
    "v_accvgpr_read_b32 v86, a22\n"
    "v_accvgpr_read_b32 v87, a23\n"
    "v_mul_f32 v80, v12, v80\n"
    "v_mul_f32 v81, v13, v81\n"
    "v_mul_f32 v82, v14, v82\n"
    "v_mul_f32 v83, v15, v83\n"
    "v_mul_f32 v84, v12, v84\n"
    "v_mul_f32 v85, v13, v85\n"
    "v_mul_f32 v86, v14, v86\n"
    "v_mul_f32 v87, v15, v87\n"
    "v_accvgpr_write_b32 a4, v80\n"
    "v_accvgpr_write_b32 a5, v81\n"
    "v_accvgpr_write_b32 a6, v82\n"
    "v_accvgpr_write_b32 a7, v83\n"
    "v_accvgpr_write_b32 a20, v84\n"
    "v_accvgpr_write_b32 a21, v85\n"
    "v_accvgpr_write_b32 a22, v86\n"
    "v_accvgpr_write_b32 a23, v87\n"
    "v_accvgpr_read_b32 v80, a36\n"
    "v_accvgpr_read_b32 v81, a37\n"
    "v_accvgpr_read_b32 v82, a38\n"
    "v_accvgpr_read_b32 v83, a39\n"
    "v_accvgpr_read_b32 v84, a52\n"
    "v_accvgpr_read_b32 v85, a53\n"
    "v_accvgpr_read_b32 v86, a54\n"
    "v_accvgpr_read_b32 v87, a55\n"
    "v_mul_f32 v80, v12, v80\n"
    "v_mul_f32 v81, v13, v81\n"
    "v_mul_f32 v82, v14, v82\n"
    "v_mul_f32 v83, v15, v83\n"
    "v_mul_f32 v84, v12, v84\n"
    "v_mul_f32 v85, v13, v85\n"
    "v_mul_f32 v86, v14, v86\n"
    "v_mul_f32 v87, v15, v87\n"
    "v_accvgpr_write_b32 a36, v80\n"
    "v_accvgpr_write_b32 a37, v81\n"
    "v_accvgpr_write_b32 a38, v82\n"
    "v_accvgpr_write_b32 a39, v83\n"
    "v_accvgpr_write_b32 a52, v84\n"
    "v_accvgpr_write_b32 a53, v85\n"
    "v_accvgpr_write_b32 a54, v86\n"
    "v_accvgpr_write_b32 a55, v87\n"
    "v_accvgpr_read_b32 v80, a68\n"
    "v_accvgpr_read_b32 v81, a69\n"
    "v_accvgpr_read_b32 v82, a70\n"
    "v_accvgpr_read_b32 v83, a71\n"
    "v_accvgpr_read_b32 v84, a84\n"
    "v_accvgpr_read_b32 v85, a85\n"
    "v_accvgpr_read_b32 v86, a86\n"
    "v_accvgpr_read_b32 v87, a87\n"
    "v_mul_f32 v80, v12, v80\n"
    "v_mul_f32 v81, v13, v81\n"
    "v_mul_f32 v82, v14, v82\n"
    "v_mul_f32 v83, v15, v83\n"
    "v_mul_f32 v84, v12, v84\n"
    "v_mul_f32 v85, v13, v85\n"
    "v_mul_f32 v86, v14, v86\n"
    "v_mul_f32 v87, v15, v87\n"
    "v_accvgpr_write_b32 a68, v80\n"
    "v_accvgpr_write_b32 a69, v81\n"
    "v_accvgpr_write_b32 a70, v82\n"
    "v_accvgpr_write_b32 a71, v83\n"
    "v_accvgpr_write_b32 a84, v84\n"
    "v_accvgpr_write_b32 a85, v85\n"
    "v_accvgpr_write_b32 a86, v86\n"
    "v_accvgpr_write_b32 a87, v87\n"
    "v_accvgpr_read_b32 v80, a100\n"
    "v_accvgpr_read_b32 v81, a101\n"
    "v_accvgpr_read_b32 v82, a102\n"
    "v_accvgpr_read_b32 v83, a103\n"
    "v_accvgpr_read_b32 v84, a116\n"
    "v_accvgpr_read_b32 v85, a117\n"
    "v_accvgpr_read_b32 v86, a118\n"
    "v_accvgpr_read_b32 v87, a119\n"
    "v_mul_f32 v80, v12, v80\n"
    "v_mul_f32 v81, v13, v81\n"
    "v_mul_f32 v82, v14, v82\n"
    "v_mul_f32 v83, v15, v83\n"
    "v_mul_f32 v84, v12, v84\n"
    "v_mul_f32 v85, v13, v85\n"
    "v_mul_f32 v86, v14, v86\n"
    "v_mul_f32 v87, v15, v87\n"
    "v_accvgpr_write_b32 a100, v80\n"
    "v_accvgpr_write_b32 a101, v81\n"
    "v_accvgpr_write_b32 a102, v82\n"
    "v_accvgpr_write_b32 a103, v83\n"
    "v_accvgpr_write_b32 a116, v84\n"
    "v_accvgpr_write_b32 a117, v85\n"
    "v_accvgpr_write_b32 a118, v86\n"
    "v_accvgpr_write_b32 a119, v87\n"
    "s_nop 7\n"
    "s_nop 7\n"
    "s_branch L_ret_D1\n"
    "L_rare_D2:\n"
    "s_nop 7\n"
    "s_nop 7\n"
    "s_sub_u32 s56, s48, 0x20000\n"
    "s_add_u32 s57, s56, 0x0\n"
    "s_add_u32 s58, s56, 0x1000\n"
    "buffer_load_dwordx4 a[224:227], v76, s[40:43], s57 offen offset:0 sc1\n"
    "buffer_load_dwordx4 a[228:231], v76, s[40:43], s57 offen offset:1024 sc1\n"
    "buffer_load_dwordx4 a[232:235], v76, s[40:43], s57 offen offset:2048 sc1\n"
    "buffer_load_dwordx4 a[236:239], v76, s[40:43], s57 offen offset:3072 sc1\n"
    "buffer_load_dwordx4 a[240:243], v76, s[40:43], s58 offen offset:0 sc1\n"
    "buffer_load_dwordx4 a[244:247], v76, s[40:43], s58 offen offset:1024 sc1\n"
    "buffer_load_dwordx4 a[248:251], v76, s[40:43], s58 offen offset:2048 sc1\n"
    "buffer_load_dwordx4 a[252:255], v76, s[40:43], s58 offen offset:3072 sc1\n"
    "s_waitcnt vmcnt(0)\n"
    "v_mfma_f32_16x16x32_bf16 v[80:83], a[224:227], a[160:163], v[40:43]\n"
    "v_mfma_f32_16x16x32_bf16 v[80:83], a[228:231], a[164:167], v[80:83]\n"
    "v_mfma_f32_16x16x32_bf16 v[80:83], a[232:235], a[168:171], v[80:83]\n"
    "v_mfma_f32_16x16x32_bf16 v[80:83], a[236:239], a[172:175], v[80:83]\n"
    "v_mfma_f32_16x16x32_bf16 v[84:87], a[240:243], a[160:163], v[40:43]\n"
    "v_mfma_f32_16x16x32_bf16 v[84:87], a[244:247], a[164:167], v[84:87]\n"
    "v_mfma_f32_16x16x32_bf16 v[84:87], a[248:251], a[168:171], v[84:87]\n"
    "v_mfma_f32_16x16x32_bf16 v[84:87], a[252:255], a[172:175], v[84:87]\n"
    "s_nop 3\n"
    "v_max_f32 v9, v80, v80\n"
    "v_max3_f32 v9, v9, v80, v81\n"
    "v_max3_f32 v9, v9, v82, v83\n"
    "s_nop 0\n"
    "v_max3_f32 v9, v9, v84, v85\n"
    "v_max3_f32 v9, v9, v86, v87\n"
    "s_add_u32 s57, s56, 0x2000\n"
    "s_add_u32 s58, s56, 0x3000\n"
    "buffer_load_dwordx4 a[224:227], v76, s[40:43], s57 offen offset:0 sc1\n"
    "buffer_load_dwordx4 a[228:231], v76, s[40:43], s57 offen offset:1024 sc1\n"
    "buffer_load_dwordx4 a[232:235], v76, s[40:43], s57 offen offset:2048 sc1\n"
    "buffer_load_dwordx4 a[236:239], v76, s[40:43], s57 offen offset:3072 sc1\n"
    "buffer_load_dwordx4 a[240:243], v76, s[40:43], s58 offen offset:0 sc1\n"
    "buffer_load_dwordx4 a[244:247], v76, s[40:43], s58 offen offset:1024 sc1\n"
    "buffer_load_dwordx4 a[248:251], v76, s[40:43], s58 offen offset:2048 sc1\n"
    "buffer_load_dwordx4 a[252:255], v76, s[40:43], s58 offen offset:3072 sc1\n"
    "s_waitcnt vmcnt(0)\n"
    "v_mfma_f32_16x16x32_bf16 v[80:83], a[224:227], a[160:163], v[40:43]\n"
    "v_mfma_f32_16x16x32_bf16 v[80:83], a[228:231], a[164:167], v[80:83]\n"
    "v_mfma_f32_16x16x32_bf16 v[80:83], a[232:235], a[168:171], v[80:83]\n"
    "v_mfma_f32_16x16x32_bf16 v[80:83], a[236:239], a[172:175], v[80:83]\n"
    "v_mfma_f32_16x16x32_bf16 v[84:87], a[240:243], a[160:163], v[40:43]\n"
    "v_mfma_f32_16x16x32_bf16 v[84:87], a[244:247], a[164:167], v[84:87]\n"
    "v_mfma_f32_16x16x32_bf16 v[84:87], a[248:251], a[168:171], v[84:87]\n"
    "v_mfma_f32_16x16x32_bf16 v[84:87], a[252:255], a[172:175], v[84:87]\n"
    "s_nop 3\n"
    "v_max3_f32 v9, v9, v80, v81\n"
    "v_max3_f32 v9, v9, v82, v83\n"
    "s_nop 1\n"
    "v_max3_f32 v9, v9, v84, v85\n"
    "v_max3_f32 v9, v9, v86, v87\n"
    "s_add_u32 s57, s56, 0x4000\n"
    "s_add_u32 s58, s56, 0x5000\n"
    "buffer_load_dwordx4 a[224:227], v76, s[40:43], s57 offen offset:0 sc1\n"
    "buffer_load_dwordx4 a[228:231], v76, s[40:43], s57 offen offset:1024 sc1\n"
    "buffer_load_dwordx4 a[232:235], v76, s[40:43], s57 offen offset:2048 sc1\n"
    "buffer_load_dwordx4 a[236:239], v76, s[40:43], s57 offen offset:3072 sc1\n"
    "buffer_load_dwordx4 a[240:243], v76, s[40:43], s58 offen offset:0 sc1\n"
    "buffer_load_dwordx4 a[244:247], v76, s[40:43], s58 offen offset:1024 sc1\n"
    "buffer_load_dwordx4 a[248:251], v76, s[40:43], s58 offen offset:2048 sc1\n"
    "buffer_load_dwordx4 a[252:255], v76, s[40:43], s58 offen offset:3072 sc1\n"
    "s_waitcnt vmcnt(0)\n"
    "v_mfma_f32_16x16x32_bf16 v[80:83], a[224:227], a[160:163], v[40:43]\n"
    "v_mfma_f32_16x16x32_bf16 v[80:83], a[228:231], a[164:167], v[80:83]\n"
    "v_mfma_f32_16x16x32_bf16 v[80:83], a[232:235], a[168:171], v[80:83]\n"
    "v_mfma_f32_16x16x32_bf16 v[80:83], a[236:239], a[172:175], v[80:83]\n"
    "v_mfma_f32_16x16x32_bf16 v[84:87], a[240:243], a[160:163], v[40:43]\n"
    "v_mfma_f32_16x16x32_bf16 v[84:87], a[244:247], a[164:167], v[84:87]\n"
    "v_mfma_f32_16x16x32_bf16 v[84:87], a[248:251], a[168:171], v[84:87]\n"
    "v_mfma_f32_16x16x32_bf16 v[84:87], a[252:255], a[172:175], v[84:87]\n"
    "s_nop 3\n"
    "v_max3_f32 v9, v9, v80, v81\n"
    "v_max3_f32 v9, v9, v82, v83\n"
    "s_nop 1\n"
    "v_max3_f32 v9, v9, v84, v85\n"
    "v_max3_f32 v9, v9, v86, v87\n"
    "s_add_u32 s57, s56, 0x6000\n"
    "s_add_u32 s58, s56, 0x7000\n"
    "buffer_load_dwordx4 a[224:227], v76, s[40:43], s57 offen offset:0 sc1\n"
    "buffer_load_dwordx4 a[228:231], v76, s[40:43], s57 offen offset:1024 sc1\n"
    "buffer_load_dwordx4 a[232:235], v76, s[40:43], s57 offen offset:2048 sc1\n"
    "buffer_load_dwordx4 a[236:239], v76, s[40:43], s57 offen offset:3072 sc1\n"
    "buffer_load_dwordx4 a[240:243], v76, s[40:43], s58 offen offset:0 sc1\n"
    "buffer_load_dwordx4 a[244:247], v76, s[40:43], s58 offen offset:1024 sc1\n"
    "buffer_load_dwordx4 a[248:251], v76, s[40:43], s58 offen offset:2048 sc1\n"
    "buffer_load_dwordx4 a[252:255], v76, s[40:43], s58 offen offset:3072 sc1\n"
    "s_waitcnt vmcnt(0)\n"
    "v_mfma_f32_16x16x32_bf16 v[80:83], a[224:227], a[160:163], v[40:43]\n"
    "v_mfma_f32_16x16x32_bf16 v[80:83], a[228:231], a[164:167], v[80:83]\n"
    "v_mfma_f32_16x16x32_bf16 v[80:83], a[232:235], a[168:171], v[80:83]\n"
    "v_mfma_f32_16x16x32_bf16 v[80:83], a[236:239], a[172:175], v[80:83]\n"
    "v_mfma_f32_16x16x32_bf16 v[84:87], a[240:243], a[160:163], v[40:43]\n"
    "v_mfma_f32_16x16x32_bf16 v[84:87], a[244:247], a[164:167], v[84:87]\n"
    "v_mfma_f32_16x16x32_bf16 v[84:87], a[248:251], a[168:171], v[84:87]\n"
    "v_mfma_f32_16x16x32_bf16 v[84:87], a[252:255], a[172:175], v[84:87]\n"
    "s_nop 3\n"
    "v_max3_f32 v9, v9, v80, v81\n"
    "v_max3_f32 v9, v9, v82, v83\n"
    "s_nop 1\n"
    "v_max3_f32 v9, v9, v84, v85\n"
    "v_max3_f32 v9, v9, v86, v87\n"
    "v_mov_b32 v10, v9\n"
    "s_nop 1\n"
    "v_permlane16_swap_b32 v10, v9\n"
    "v_max_f32 v9, v10, v9\n"
    "v_mov_b32 v10, v9\n"
    "s_nop 1\n"
    "v_permlane32_swap_b32 v10, v9\n"
    "v_max_f32 v9, v10, v9\n"
    "v_max_f32 v9, 0, v9\n"
    "v_sub_f32 v40, v40, v9\n"
    "v_sub_f32 v41, v41, v9\n"
    "v_sub_f32 v42, v42, v9\n"
    "v_sub_f32 v43, v43, v9\n"
    "s_add_u32 s57, s56, 0x0\n"
    "s_add_u32 s58, s56, 0x1000\n"
    "buffer_load_dwordx4 a[224:227], v76, s[40:43], s57 offen offset:0 sc1\n"
    "buffer_load_dwordx4 a[228:231], v76, s[40:43], s57 offen offset:1024 sc1\n"
    "buffer_load_dwordx4 a[232:235], v76, s[40:43], s57 offen offset:2048 sc1\n"
    "buffer_load_dwordx4 a[236:239], v76, s[40:43], s57 offen offset:3072 sc1\n"
    "buffer_load_dwordx4 a[240:243], v76, s[40:43], s58 offen offset:0 sc1\n"
    "buffer_load_dwordx4 a[244:247], v76, s[40:43], s58 offen offset:1024 sc1\n"
    "buffer_load_dwordx4 a[248:251], v76, s[40:43], s58 offen offset:2048 sc1\n"
    "buffer_load_dwordx4 a[252:255], v76, s[40:43], s58 offen offset:3072 sc1\n"
    "s_waitcnt vmcnt(0)\n"
    "v_mfma_f32_16x16x32_bf16 v[80:83], a[224:227], a[160:163], v[40:43]\n"
    "v_mfma_f32_16x16x32_bf16 v[80:83], a[228:231], a[164:167], v[80:83]\n"
    "v_mfma_f32_16x16x32_bf16 v[80:83], a[232:235], a[168:171], v[80:83]\n"
    "v_mfma_f32_16x16x32_bf16 v[80:83], a[236:239], a[172:175], v[80:83]\n"
    "v_mfma_f32_16x16x32_bf16 v[84:87], a[240:243], a[160:163], v[40:43]\n"
    "v_mfma_f32_16x16x32_bf16 v[84:87], a[244:247], a[164:167], v[84:87]\n"
    "v_mfma_f32_16x16x32_bf16 v[84:87], a[248:251], a[168:171], v[84:87]\n"
    "v_mfma_f32_16x16x32_bf16 v[84:87], a[252:255], a[172:175], v[84:87]\n"
    "s_nop 3\n"
    "v_exp_f32 v80, v80\n"
    "v_exp_f32 v81, v81\n"
    "v_exp_f32 v82, v82\n"
    "v_exp_f32 v83, v83\n"
    "v_exp_f32 v84, v84\n"
    "v_exp_f32 v85, v85\n"
    "v_exp_f32 v86, v86\n"
    "v_exp_f32 v87, v87\n"
    "v_cvt_pk_bf16_f32 v224, v80, v81\n"
    "v_cvt_pk_bf16_f32 v225, v82, v83\n"
    "v_cvt_pk_bf16_f32 v226, v84, v85\n"
    "v_cvt_pk_bf16_f32 v227, v86, v87\n"
    "s_add_u32 s57, s56, 0x2000\n"
    "s_add_u32 s58, s56, 0x3000\n"
    "buffer_load_dwordx4 a[224:227], v76, s[40:43], s57 offen offset:0 sc1\n"
    "buffer_load_dwordx4 a[228:231], v76, s[40:43], s57 offen offset:1024 sc1\n"
    "buffer_load_dwordx4 a[232:235], v76, s[40:43], s57 offen offset:2048 sc1\n"
    "buffer_load_dwordx4 a[236:239], v76, s[40:43], s57 offen offset:3072 sc1\n"
    "buffer_load_dwordx4 a[240:243], v76, s[40:43], s58 offen offset:0 sc1\n"
    "buffer_load_dwordx4 a[244:247], v76, s[40:43], s58 offen offset:1024 sc1\n"
    "buffer_load_dwordx4 a[248:251], v76, s[40:43], s58 offen offset:2048 sc1\n"
    "buffer_load_dwordx4 a[252:255], v76, s[40:43], s58 offen offset:3072 sc1\n"
    "s_waitcnt vmcnt(0)\n"
    "v_mfma_f32_16x16x32_bf16 v[80:83], a[224:227], a[160:163], v[40:43]\n"
    "v_mfma_f32_16x16x32_bf16 v[80:83], a[228:231], a[164:167], v[80:83]\n"
    "v_mfma_f32_16x16x32_bf16 v[80:83], a[232:235], a[168:171], v[80:83]\n"
    "v_mfma_f32_16x16x32_bf16 v[80:83], a[236:239], a[172:175], v[80:83]\n"
    "v_mfma_f32_16x16x32_bf16 v[84:87], a[240:243], a[160:163], v[40:43]\n"
    "v_mfma_f32_16x16x32_bf16 v[84:87], a[244:247], a[164:167], v[84:87]\n"
    "v_mfma_f32_16x16x32_bf16 v[84:87], a[248:251], a[168:171], v[84:87]\n"
    "v_mfma_f32_16x16x32_bf16 v[84:87], a[252:255], a[172:175], v[84:87]\n"
    "s_nop 3\n"
    "v_exp_f32 v80, v80\n"
    "v_exp_f32 v81, v81\n"
    "v_exp_f32 v82, v82\n"
    "v_exp_f32 v83, v83\n"
    "v_exp_f32 v84, v84\n"
    "v_exp_f32 v85, v85\n"
    "v_exp_f32 v86, v86\n"
    "v_exp_f32 v87, v87\n"
    "v_cvt_pk_bf16_f32 v228, v80, v81\n"
    "v_cvt_pk_bf16_f32 v229, v82, v83\n"
    "v_cvt_pk_bf16_f32 v230, v84, v85\n"
    "v_cvt_pk_bf16_f32 v231, v86, v87\n"
    "s_add_u32 s57, s56, 0x4000\n"
    "s_add_u32 s58, s56, 0x5000\n"
    "buffer_load_dwordx4 a[224:227], v76, s[40:43], s57 offen offset:0 sc1\n"
    "buffer_load_dwordx4 a[228:231], v76, s[40:43], s57 offen offset:1024 sc1\n"
    "buffer_load_dwordx4 a[232:235], v76, s[40:43], s57 offen offset:2048 sc1\n"
    "buffer_load_dwordx4 a[236:239], v76, s[40:43], s57 offen offset:3072 sc1\n"
    "buffer_load_dwordx4 a[240:243], v76, s[40:43], s58 offen offset:0 sc1\n"
    "buffer_load_dwordx4 a[244:247], v76, s[40:43], s58 offen offset:1024 sc1\n"
    "buffer_load_dwordx4 a[248:251], v76, s[40:43], s58 offen offset:2048 sc1\n"
    "buffer_load_dwordx4 a[252:255], v76, s[40:43], s58 offen offset:3072 sc1\n"
    "s_waitcnt vmcnt(0)\n"
    "v_mfma_f32_16x16x32_bf16 v[80:83], a[224:227], a[160:163], v[40:43]\n"
    "v_mfma_f32_16x16x32_bf16 v[80:83], a[228:231], a[164:167], v[80:83]\n"
    "v_mfma_f32_16x16x32_bf16 v[80:83], a[232:235], a[168:171], v[80:83]\n"
    "v_mfma_f32_16x16x32_bf16 v[80:83], a[236:239], a[172:175], v[80:83]\n"
    "v_mfma_f32_16x16x32_bf16 v[84:87], a[240:243], a[160:163], v[40:43]\n"
    "v_mfma_f32_16x16x32_bf16 v[84:87], a[244:247], a[164:167], v[84:87]\n"
    "v_mfma_f32_16x16x32_bf16 v[84:87], a[248:251], a[168:171], v[84:87]\n"
    "v_mfma_f32_16x16x32_bf16 v[84:87], a[252:255], a[172:175], v[84:87]\n"
    "s_nop 3\n"
    "v_exp_f32 v80, v80\n"
    "v_exp_f32 v81, v81\n"
    "v_exp_f32 v82, v82\n"
    "v_exp_f32 v83, v83\n"
    "v_exp_f32 v84, v84\n"
    "v_exp_f32 v85, v85\n"
    "v_exp_f32 v86, v86\n"
    "v_exp_f32 v87, v87\n"
    "v_cvt_pk_bf16_f32 v232, v80, v81\n"
    "v_cvt_pk_bf16_f32 v233, v82, v83\n"
    "v_cvt_pk_bf16_f32 v234, v84, v85\n"
    "v_cvt_pk_bf16_f32 v235, v86, v87\n"
    "s_add_u32 s57, s56, 0x6000\n"
    "s_add_u32 s58, s56, 0x7000\n"
    "buffer_load_dwordx4 a[224:227], v76, s[40:43], s57 offen offset:0 sc1\n"
    "buffer_load_dwordx4 a[228:231], v76, s[40:43], s57 offen offset:1024 sc1\n"
    "buffer_load_dwordx4 a[232:235], v76, s[40:43], s57 offen offset:2048 sc1\n"
    "buffer_load_dwordx4 a[236:239], v76, s[40:43], s57 offen offset:3072 sc1\n"
    "buffer_load_dwordx4 a[240:243], v76, s[40:43], s58 offen offset:0 sc1\n"
    "buffer_load_dwordx4 a[244:247], v76, s[40:43], s58 offen offset:1024 sc1\n"
    "buffer_load_dwordx4 a[248:251], v76, s[40:43], s58 offen offset:2048 sc1\n"
    "buffer_load_dwordx4 a[252:255], v76, s[40:43], s58 offen offset:3072 sc1\n"
    "s_waitcnt vmcnt(0)\n"
    "v_mfma_f32_16x16x32_bf16 v[80:83], a[224:227], a[160:163], v[40:43]\n"
    "v_mfma_f32_16x16x32_bf16 v[80:83], a[228:231], a[164:167], v[80:83]\n"
    "v_mfma_f32_16x16x32_bf16 v[80:83], a[232:235], a[168:171], v[80:83]\n"
    "v_mfma_f32_16x16x32_bf16 v[80:83], a[236:239], a[172:175], v[80:83]\n"
    "v_mfma_f32_16x16x32_bf16 v[84:87], a[240:243], a[160:163], v[40:43]\n"
    "v_mfma_f32_16x16x32_bf16 v[84:87], a[244:247], a[164:167], v[84:87]\n"
    "v_mfma_f32_16x16x32_bf16 v[84:87], a[248:251], a[168:171], v[84:87]\n"
    "v_mfma_f32_16x16x32_bf16 v[84:87], a[252:255], a[172:175], v[84:87]\n"
    "s_nop 3\n"
    "v_exp_f32 v80, v80\n"
    "v_exp_f32 v81, v81\n"
    "v_exp_f32 v82, v82\n"
    "v_exp_f32 v83, v83\n"
    "v_exp_f32 v84, v84\n"
    "v_exp_f32 v85, v85\n"
    "v_exp_f32 v86, v86\n"
    "v_exp_f32 v87, v87\n"
    "v_cvt_pk_bf16_f32 v236, v80, v81\n"
    "v_cvt_pk_bf16_f32 v237, v82, v83\n"
    "v_cvt_pk_bf16_f32 v238, v84, v85\n"
    "v_cvt_pk_bf16_f32 v239, v86, v87\n"
    "v_mfma_f32_16x16x32_bf16 v[56:59], v[64:67], v[224:227], 0\n"
    "v_mfma_f32_16x16x32_bf16 v[56:59], v[64:67], v[228:231], v[56:59]\n"
    "v_mfma_f32_16x16x32_bf16 v[56:59], v[64:67], v[232:235], v[56:59]\n"
    "v_mfma_f32_16x16x32_bf16 v[56:59], v[64:67], v[236:239], v[56:59]\n"
    "v_sub_f32 v24, v24, v9\n"
    "v_sub_f32 v25, v25, v9\n"
    "v_sub_f32 v26, v26, v9\n"
    "v_sub_f32 v27, v27, v9\n"
    "v_sub_f32 v11, 0, v9\n"
    "v_exp_f32 v11, v11\n"
    "s_nop 0\n"
    "v_mul_f32 v70, v11, v70\n"
    "ds_bpermute_b32 v12, v99, v11 offset:0\n"
    "ds_bpermute_b32 v13, v99, v11 offset:4\n"
    "ds_bpermute_b32 v14, v99, v11 offset:8\n"
    "ds_bpermute_b32 v15, v99, v11 offset:12\n"
    "s_waitcnt lgkmcnt(0)\n"
    "v_accvgpr_read_b32 v80, a8\n"
    "v_accvgpr_read_b32 v81, a9\n"
    "v_accvgpr_read_b32 v82, a10\n"
    "v_accvgpr_read_b32 v83, a11\n"
    "v_accvgpr_read_b32 v84, a24\n"
    "v_accvgpr_read_b32 v85, a25\n"
    "v_accvgpr_read_b32 v86, a26\n"
    "v_accvgpr_read_b32 v87, a27\n"
    "v_mul_f32 v80, v12, v80\n"
    "v_mul_f32 v81, v13, v81\n"
    "v_mul_f32 v82, v14, v82\n"
    "v_mul_f32 v83, v15, v83\n"
    "v_mul_f32 v84, v12, v84\n"
    "v_mul_f32 v85, v13, v85\n"
    "v_mul_f32 v86, v14, v86\n"
    "v_mul_f32 v87, v15, v87\n"
    "v_accvgpr_write_b32 a8, v80\n"
    "v_accvgpr_write_b32 a9, v81\n"
    "v_accvgpr_write_b32 a10, v82\n"
    "v_accvgpr_write_b32 a11, v83\n"
    "v_accvgpr_write_b32 a24, v84\n"
    "v_accvgpr_write_b32 a25, v85\n"
    "v_accvgpr_write_b32 a26, v86\n"
    "v_accvgpr_write_b32 a27, v87\n"
    "v_accvgpr_read_b32 v80, a40\n"
    "v_accvgpr_read_b32 v81, a41\n"
    "v_accvgpr_read_b32 v82, a42\n"
    "v_accvgpr_read_b32 v83, a43\n"
    "v_accvgpr_read_b32 v84, a56\n"
    "v_accvgpr_read_b32 v85, a57\n"
    "v_accvgpr_read_b32 v86, a58\n"
    "v_accvgpr_read_b32 v87, a59\n"
    "v_mul_f32 v80, v12, v80\n"
    "v_mul_f32 v81, v13, v81\n"
    "v_mul_f32 v82, v14, v82\n"
    "v_mul_f32 v83, v15, v83\n"
    "v_mul_f32 v84, v12, v84\n"
    "v_mul_f32 v85, v13, v85\n"
    "v_mul_f32 v86, v14, v86\n"
    "v_mul_f32 v87, v15, v87\n"
    "v_accvgpr_write_b32 a40, v80\n"
    "v_accvgpr_write_b32 a41, v81\n"
    "v_accvgpr_write_b32 a42, v82\n"
    "v_accvgpr_write_b32 a43, v83\n"
    "v_accvgpr_write_b32 a56, v84\n"
    "v_accvgpr_write_b32 a57, v85\n"
    "v_accvgpr_write_b32 a58, v86\n"
    "v_accvgpr_write_b32 a59, v87\n"
    "v_accvgpr_read_b32 v80, a72\n"
    "v_accvgpr_read_b32 v81, a73\n"
    "v_accvgpr_read_b32 v82, a74\n"
    "v_accvgpr_read_b32 v83, a75\n"
    "v_accvgpr_read_b32 v84, a88\n"
    "v_accvgpr_read_b32 v85, a89\n"
    "v_accvgpr_read_b32 v86, a90\n"
    "v_accvgpr_read_b32 v87, a91\n"
    "v_mul_f32 v80, v12, v80\n"
    "v_mul_f32 v81, v13, v81\n"
    "v_mul_f32 v82, v14, v82\n"
    "v_mul_f32 v83, v15, v83\n"
    "v_mul_f32 v84, v12, v84\n"
    "v_mul_f32 v85, v13, v85\n"
    "v_mul_f32 v86, v14, v86\n"
    "v_mul_f32 v87, v15, v87\n"
    "v_accvgpr_write_b32 a72, v80\n"
    "v_accvgpr_write_b32 a73, v81\n"
    "v_accvgpr_write_b32 a74, v82\n"
    "v_accvgpr_write_b32 a75, v83\n"
    "v_accvgpr_write_b32 a88, v84\n"
    "v_accvgpr_write_b32 a89, v85\n"
    "v_accvgpr_write_b32 a90, v86\n"
    "v_accvgpr_write_b32 a91, v87\n"
    "v_accvgpr_read_b32 v80, a104\n"
    "v_accvgpr_read_b32 v81, a105\n"
    "v_accvgpr_read_b32 v82, a106\n"
    "v_accvgpr_read_b32 v83, a107\n"
    "v_accvgpr_read_b32 v84, a120\n"
    "v_accvgpr_read_b32 v85, a121\n"
    "v_accvgpr_read_b32 v86, a122\n"
    "v_accvgpr_read_b32 v87, a123\n"
    "v_mul_f32 v80, v12, v80\n"
    "v_mul_f32 v81, v13, v81\n"
    "v_mul_f32 v82, v14, v82\n"
    "v_mul_f32 v83, v15, v83\n"
    "v_mul_f32 v84, v12, v84\n"
    "v_mul_f32 v85, v13, v85\n"
    "v_mul_f32 v86, v14, v86\n"
    "v_mul_f32 v87, v15, v87\n"
    "v_accvgpr_write_b32 a104, v80\n"
    "v_accvgpr_write_b32 a105, v81\n"
    "v_accvgpr_write_b32 a106, v82\n"
    "v_accvgpr_write_b32 a107, v83\n"
    "v_accvgpr_write_b32 a120, v84\n"
    "v_accvgpr_write_b32 a121, v85\n"
    "v_accvgpr_write_b32 a122, v86\n"
    "v_accvgpr_write_b32 a123, v87\n"
    "s_nop 7\n"
    "s_nop 7\n"
    "s_branch L_ret_D2\n"
    "L_rare_D3:\n"
    "s_nop 7\n"
    "s_nop 7\n"
    "s_sub_u32 s56, s48, 0x20000\n"
    "s_add_u32 s57, s56, 0x0\n"
    "s_add_u32 s58, s56, 0x1000\n"
    "buffer_load_dwordx4 a[224:227], v76, s[40:43], s57 offen offset:0 sc1\n"
    "buffer_load_dwordx4 a[228:231], v76, s[40:43], s57 offen offset:1024 sc1\n"
    "buffer_load_dwordx4 a[232:235], v76, s[40:43], s57 offen offset:2048 sc1\n"
    "buffer_load_dwordx4 a[236:239], v76, s[40:43], s57 offen offset:3072 sc1\n"
    "buffer_load_dwordx4 a[240:243], v76, s[40:43], s58 offen offset:0 sc1\n"
    "buffer_load_dwordx4 a[244:247], v76, s[40:43], s58 offen offset:1024 sc1\n"
    "buffer_load_dwordx4 a[248:251], v76, s[40:43], s58 offen offset:2048 sc1\n"
    "buffer_load_dwordx4 a[252:255], v76, s[40:43], s58 offen offset:3072 sc1\n"
    "s_waitcnt vmcnt(0)\n"
    "v_mfma_f32_16x16x32_bf16 v[80:83], a[224:227], a[176:179], v[44:47]\n"
    "v_mfma_f32_16x16x32_bf16 v[80:83], a[228:231], a[180:183], v[80:83]\n"
    "v_mfma_f32_16x16x32_bf16 v[80:83], a[232:235], a[184:187], v[80:83]\n"
    "v_mfma_f32_16x16x32_bf16 v[80:83], a[236:239], a[188:191], v[80:83]\n"
    "v_mfma_f32_16x16x32_bf16 v[84:87], a[240:243], a[176:179], v[44:47]\n"
    "v_mfma_f32_16x16x32_bf16 v[84:87], a[244:247], a[180:183], v[84:87]\n"
    "v_mfma_f32_16x16x32_bf16 v[84:87], a[248:251], a[184:187], v[84:87]\n"
    "v_mfma_f32_16x16x32_bf16 v[84:87], a[252:255], a[188:191], v[84:87]\n"
    "s_nop 3\n"
    "v_max_f32 v9, v80, v80\n"
    "v_max3_f32 v9, v9, v80, v81\n"
    "v_max3_f32 v9, v9, v82, v83\n"
    "s_nop 0\n"
    "v_max3_f32 v9, v9, v84, v85\n"
    "v_max3_f32 v9, v9, v86, v87\n"
    "s_add_u32 s57, s56, 0x2000\n"
    "s_add_u32 s58, s56, 0x3000\n"
    "buffer_load_dwordx4 a[224:227], v76, s[40:43], s57 offen offset:0 sc1\n"
    "buffer_load_dwordx4 a[228:231], v76, s[40:43], s57 offen offset:1024 sc1\n"
    "buffer_load_dwordx4 a[232:235], v76, s[40:43], s57 offen offset:2048 sc1\n"
    "buffer_load_dwordx4 a[236:239], v76, s[40:43], s57 offen offset:3072 sc1\n"
    "buffer_load_dwordx4 a[240:243], v76, s[40:43], s58 offen offset:0 sc1\n"
    "buffer_load_dwordx4 a[244:247], v76, s[40:43], s58 offen offset:1024 sc1\n"
    "buffer_load_dwordx4 a[248:251], v76, s[40:43], s58 offen offset:2048 sc1\n"
    "buffer_load_dwordx4 a[252:255], v76, s[40:43], s58 offen offset:3072 sc1\n"
    "s_waitcnt vmcnt(0)\n"
    "v_mfma_f32_16x16x32_bf16 v[80:83], a[224:227], a[176:179], v[44:47]\n"
    "v_mfma_f32_16x16x32_bf16 v[80:83], a[228:231], a[180:183], v[80:83]\n"
    "v_mfma_f32_16x16x32_bf16 v[80:83], a[232:235], a[184:187], v[80:83]\n"
    "v_mfma_f32_16x16x32_bf16 v[80:83], a[236:239], a[188:191], v[80:83]\n"
    "v_mfma_f32_16x16x32_bf16 v[84:87], a[240:243], a[176:179], v[44:47]\n"
    "v_mfma_f32_16x16x32_bf16 v[84:87], a[244:247], a[180:183], v[84:87]\n"
    "v_mfma_f32_16x16x32_bf16 v[84:87], a[248:251], a[184:187], v[84:87]\n"
    "v_mfma_f32_16x16x32_bf16 v[84:87], a[252:255], a[188:191], v[84:87]\n"
    "s_nop 3\n"
    "v_max3_f32 v9, v9, v80, v81\n"
    "v_max3_f32 v9, v9, v82, v83\n"
    "s_nop 1\n"
    "v_max3_f32 v9, v9, v84, v85\n"
    "v_max3_f32 v9, v9, v86, v87\n"
    "s_add_u32 s57, s56, 0x4000\n"
    "s_add_u32 s58, s56, 0x5000\n"
    "buffer_load_dwordx4 a[224:227], v76, s[40:43], s57 offen offset:0 sc1\n"
    "buffer_load_dwordx4 a[228:231], v76, s[40:43], s57 offen offset:1024 sc1\n"
    "buffer_load_dwordx4 a[232:235], v76, s[40:43], s57 offen offset:2048 sc1\n"
    "buffer_load_dwordx4 a[236:239], v76, s[40:43], s57 offen offset:3072 sc1\n"
    "buffer_load_dwordx4 a[240:243], v76, s[40:43], s58 offen offset:0 sc1\n"
    "buffer_load_dwordx4 a[244:247], v76, s[40:43], s58 offen offset:1024 sc1\n"
    "buffer_load_dwordx4 a[248:251], v76, s[40:43], s58 offen offset:2048 sc1\n"
    "buffer_load_dwordx4 a[252:255], v76, s[40:43], s58 offen offset:3072 sc1\n"
    "s_waitcnt vmcnt(0)\n"
    "v_mfma_f32_16x16x32_bf16 v[80:83], a[224:227], a[176:179], v[44:47]\n"
    "v_mfma_f32_16x16x32_bf16 v[80:83], a[228:231], a[180:183], v[80:83]\n"
    "v_mfma_f32_16x16x32_bf16 v[80:83], a[232:235], a[184:187], v[80:83]\n"
    "v_mfma_f32_16x16x32_bf16 v[80:83], a[236:239], a[188:191], v[80:83]\n"
    "v_mfma_f32_16x16x32_bf16 v[84:87], a[240:243], a[176:179], v[44:47]\n"
    "v_mfma_f32_16x16x32_bf16 v[84:87], a[244:247], a[180:183], v[84:87]\n"
    "v_mfma_f32_16x16x32_bf16 v[84:87], a[248:251], a[184:187], v[84:87]\n"
    "v_mfma_f32_16x16x32_bf16 v[84:87], a[252:255], a[188:191], v[84:87]\n"
    "s_nop 3\n"
    "v_max3_f32 v9, v9, v80, v81\n"
    "v_max3_f32 v9, v9, v82, v83\n"
    "s_nop 1\n"
    "v_max3_f32 v9, v9, v84, v85\n"
    "v_max3_f32 v9, v9, v86, v87\n"
    "s_add_u32 s57, s56, 0x6000\n"
    "s_add_u32 s58, s56, 0x7000\n"
    "buffer_load_dwordx4 a[224:227], v76, s[40:43], s57 offen offset:0 sc1\n"
    "buffer_load_dwordx4 a[228:231], v76, s[40:43], s57 offen offset:1024 sc1\n"
    "buffer_load_dwordx4 a[232:235], v76, s[40:43], s57 offen offset:2048 sc1\n"
    "buffer_load_dwordx4 a[236:239], v76, s[40:43], s57 offen offset:3072 sc1\n"
    "buffer_load_dwordx4 a[240:243], v76, s[40:43], s58 offen offset:0 sc1\n"
    "buffer_load_dwordx4 a[244:247], v76, s[40:43], s58 offen offset:1024 sc1\n"
    "buffer_load_dwordx4 a[248:251], v76, s[40:43], s58 offen offset:2048 sc1\n"
    "buffer_load_dwordx4 a[252:255], v76, s[40:43], s58 offen offset:3072 sc1\n"
    "s_waitcnt vmcnt(0)\n"
    "v_mfma_f32_16x16x32_bf16 v[80:83], a[224:227], a[176:179], v[44:47]\n"
    "v_mfma_f32_16x16x32_bf16 v[80:83], a[228:231], a[180:183], v[80:83]\n"
    "v_mfma_f32_16x16x32_bf16 v[80:83], a[232:235], a[184:187], v[80:83]\n"
    "v_mfma_f32_16x16x32_bf16 v[80:83], a[236:239], a[188:191], v[80:83]\n"
    "v_mfma_f32_16x16x32_bf16 v[84:87], a[240:243], a[176:179], v[44:47]\n"
    "v_mfma_f32_16x16x32_bf16 v[84:87], a[244:247], a[180:183], v[84:87]\n"
    "v_mfma_f32_16x16x32_bf16 v[84:87], a[248:251], a[184:187], v[84:87]\n"
    "v_mfma_f32_16x16x32_bf16 v[84:87], a[252:255], a[188:191], v[84:87]\n"
    "s_nop 3\n"
    "v_max3_f32 v9, v9, v80, v81\n"
    "v_max3_f32 v9, v9, v82, v83\n"
    "s_nop 1\n"
    "v_max3_f32 v9, v9, v84, v85\n"
    "v_max3_f32 v9, v9, v86, v87\n"
    "v_mov_b32 v10, v9\n"
    "s_nop 1\n"
    "v_permlane16_swap_b32 v10, v9\n"
    "v_max_f32 v9, v10, v9\n"
    "v_mov_b32 v10, v9\n"
    "s_nop 1\n"
    "v_permlane32_swap_b32 v10, v9\n"
    "v_max_f32 v9, v10, v9\n"
    "v_max_f32 v9, 0, v9\n"
    "v_sub_f32 v44, v44, v9\n"
    "v_sub_f32 v45, v45, v9\n"
    "v_sub_f32 v46, v46, v9\n"
    "v_sub_f32 v47, v47, v9\n"
    "s_add_u32 s57, s56, 0x0\n"
    "s_add_u32 s58, s56, 0x1000\n"
    "buffer_load_dwordx4 a[224:227], v76, s[40:43], s57 offen offset:0 sc1\n"
    "buffer_load_dwordx4 a[228:231], v76, s[40:43], s57 offen offset:1024 sc1\n"
    "buffer_load_dwordx4 a[232:235], v76, s[40:43], s57 offen offset:2048 sc1\n"
    "buffer_load_dwordx4 a[236:239], v76, s[40:43], s57 offen offset:3072 sc1\n"
    "buffer_load_dwordx4 a[240:243], v76, s[40:43], s58 offen offset:0 sc1\n"
    "buffer_load_dwordx4 a[244:247], v76, s[40:43], s58 offen offset:1024 sc1\n"
    "buffer_load_dwordx4 a[248:251], v76, s[40:43], s58 offen offset:2048 sc1\n"
    "buffer_load_dwordx4 a[252:255], v76, s[40:43], s58 offen offset:3072 sc1\n"
    "s_waitcnt vmcnt(0)\n"
    "v_mfma_f32_16x16x32_bf16 v[80:83], a[224:227], a[176:179], v[44:47]\n"
    "v_mfma_f32_16x16x32_bf16 v[80:83], a[228:231], a[180:183], v[80:83]\n"
    "v_mfma_f32_16x16x32_bf16 v[80:83], a[232:235], a[184:187], v[80:83]\n"
    "v_mfma_f32_16x16x32_bf16 v[80:83], a[236:239], a[188:191], v[80:83]\n"
    "v_mfma_f32_16x16x32_bf16 v[84:87], a[240:243], a[176:179], v[44:47]\n"
    "v_mfma_f32_16x16x32_bf16 v[84:87], a[244:247], a[180:183], v[84:87]\n"
    "v_mfma_f32_16x16x32_bf16 v[84:87], a[248:251], a[184:187], v[84:87]\n"
    "v_mfma_f32_16x16x32_bf16 v[84:87], a[252:255], a[188:191], v[84:87]\n"
    "s_nop 3\n"
    "v_exp_f32 v80, v80\n"
    "v_exp_f32 v81, v81\n"
    "v_exp_f32 v82, v82\n"
    "v_exp_f32 v83, v83\n"
    "v_exp_f32 v84, v84\n"
    "v_exp_f32 v85, v85\n"
    "v_exp_f32 v86, v86\n"
    "v_exp_f32 v87, v87\n"
    "v_cvt_pk_bf16_f32 v240, v80, v81\n"
    "v_cvt_pk_bf16_f32 v241, v82, v83\n"
    "v_cvt_pk_bf16_f32 v242, v84, v85\n"
    "v_cvt_pk_bf16_f32 v243, v86, v87\n"
    "s_add_u32 s57, s56, 0x2000\n"
    "s_add_u32 s58, s56, 0x3000\n"
    "buffer_load_dwordx4 a[224:227], v76, s[40:43], s57 offen offset:0 sc1\n"
    "buffer_load_dwordx4 a[228:231], v76, s[40:43], s57 offen offset:1024 sc1\n"
    "buffer_load_dwordx4 a[232:235], v76, s[40:43], s57 offen offset:2048 sc1\n"
    "buffer_load_dwordx4 a[236:239], v76, s[40:43], s57 offen offset:3072 sc1\n"
    "buffer_load_dwordx4 a[240:243], v76, s[40:43], s58 offen offset:0 sc1\n"
    "buffer_load_dwordx4 a[244:247], v76, s[40:43], s58 offen offset:1024 sc1\n"
    "buffer_load_dwordx4 a[248:251], v76, s[40:43], s58 offen offset:2048 sc1\n"
    "buffer_load_dwordx4 a[252:255], v76, s[40:43], s58 offen offset:3072 sc1\n"
    "s_waitcnt vmcnt(0)\n"
    "v_mfma_f32_16x16x32_bf16 v[80:83], a[224:227], a[176:179], v[44:47]\n"
    "v_mfma_f32_16x16x32_bf16 v[80:83], a[228:231], a[180:183], v[80:83]\n"
    "v_mfma_f32_16x16x32_bf16 v[80:83], a[232:235], a[184:187], v[80:83]\n"
    "v_mfma_f32_16x16x32_bf16 v[80:83], a[236:239], a[188:191], v[80:83]\n"
    "v_mfma_f32_16x16x32_bf16 v[84:87], a[240:243], a[176:179], v[44:47]\n"
    "v_mfma_f32_16x16x32_bf16 v[84:87], a[244:247], a[180:183], v[84:87]\n"
    "v_mfma_f32_16x16x32_bf16 v[84:87], a[248:251], a[184:187], v[84:87]\n"
    "v_mfma_f32_16x16x32_bf16 v[84:87], a[252:255], a[188:191], v[84:87]\n"
    "s_nop 3\n"
    "v_exp_f32 v80, v80\n"
    "v_exp_f32 v81, v81\n"
    "v_exp_f32 v82, v82\n"
    "v_exp_f32 v83, v83\n"
    "v_exp_f32 v84, v84\n"
    "v_exp_f32 v85, v85\n"
    "v_exp_f32 v86, v86\n"
    "v_exp_f32 v87, v87\n"
    "v_cvt_pk_bf16_f32 v244, v80, v81\n"
    "v_cvt_pk_bf16_f32 v245, v82, v83\n"
    "v_cvt_pk_bf16_f32 v246, v84, v85\n"
    "v_cvt_pk_bf16_f32 v247, v86, v87\n"
    "s_add_u32 s57, s56, 0x4000\n"
    "s_add_u32 s58, s56, 0x5000\n"
    "buffer_load_dwordx4 a[224:227], v76, s[40:43], s57 offen offset:0 sc1\n"
    "buffer_load_dwordx4 a[228:231], v76, s[40:43], s57 offen offset:1024 sc1\n"
    "buffer_load_dwordx4 a[232:235], v76, s[40:43], s57 offen offset:2048 sc1\n"
    "buffer_load_dwordx4 a[236:239], v76, s[40:43], s57 offen offset:3072 sc1\n"
    "buffer_load_dwordx4 a[240:243], v76, s[40:43], s58 offen offset:0 sc1\n"
    "buffer_load_dwordx4 a[244:247], v76, s[40:43], s58 offen offset:1024 sc1\n"
    "buffer_load_dwordx4 a[248:251], v76, s[40:43], s58 offen offset:2048 sc1\n"
    "buffer_load_dwordx4 a[252:255], v76, s[40:43], s58 offen offset:3072 sc1\n"
    "s_waitcnt vmcnt(0)\n"
    "v_mfma_f32_16x16x32_bf16 v[80:83], a[224:227], a[176:179], v[44:47]\n"
    "v_mfma_f32_16x16x32_bf16 v[80:83], a[228:231], a[180:183], v[80:83]\n"
    "v_mfma_f32_16x16x32_bf16 v[80:83], a[232:235], a[184:187], v[80:83]\n"
    "v_mfma_f32_16x16x32_bf16 v[80:83], a[236:239], a[188:191], v[80:83]\n"
    "v_mfma_f32_16x16x32_bf16 v[84:87], a[240:243], a[176:179], v[44:47]\n"
    "v_mfma_f32_16x16x32_bf16 v[84:87], a[244:247], a[180:183], v[84:87]\n"
    "v_mfma_f32_16x16x32_bf16 v[84:87], a[248:251], a[184:187], v[84:87]\n"
    "v_mfma_f32_16x16x32_bf16 v[84:87], a[252:255], a[188:191], v[84:87]\n"
    "s_nop 3\n"
    "v_exp_f32 v80, v80\n"
    "v_exp_f32 v81, v81\n"
    "v_exp_f32 v82, v82\n"
    "v_exp_f32 v83, v83\n"
    "v_exp_f32 v84, v84\n"
    "v_exp_f32 v85, v85\n"
    "v_exp_f32 v86, v86\n"
    "v_exp_f32 v87, v87\n"
    "v_cvt_pk_bf16_f32 v248, v80, v81\n"
    "v_cvt_pk_bf16_f32 v249, v82, v83\n"
    "v_cvt_pk_bf16_f32 v250, v84, v85\n"
    "v_cvt_pk_bf16_f32 v251, v86, v87\n"
    "s_add_u32 s57, s56, 0x6000\n"
    "s_add_u32 s58, s56, 0x7000\n"
    "buffer_load_dwordx4 a[224:227], v76, s[40:43], s57 offen offset:0 sc1\n"
    "buffer_load_dwordx4 a[228:231], v76, s[40:43], s57 offen offset:1024 sc1\n"
    "buffer_load_dwordx4 a[232:235], v76, s[40:43], s57 offen offset:2048 sc1\n"
    "buffer_load_dwordx4 a[236:239], v76, s[40:43], s57 offen offset:3072 sc1\n"
    "buffer_load_dwordx4 a[240:243], v76, s[40:43], s58 offen offset:0 sc1\n"
    "buffer_load_dwordx4 a[244:247], v76, s[40:43], s58 offen offset:1024 sc1\n"
    "buffer_load_dwordx4 a[248:251], v76, s[40:43], s58 offen offset:2048 sc1\n"
    "buffer_load_dwordx4 a[252:255], v76, s[40:43], s58 offen offset:3072 sc1\n"
    "s_waitcnt vmcnt(0)\n"
    "v_mfma_f32_16x16x32_bf16 v[80:83], a[224:227], a[176:179], v[44:47]\n"
    "v_mfma_f32_16x16x32_bf16 v[80:83], a[228:231], a[180:183], v[80:83]\n"
    "v_mfma_f32_16x16x32_bf16 v[80:83], a[232:235], a[184:187], v[80:83]\n"
    "v_mfma_f32_16x16x32_bf16 v[80:83], a[236:239], a[188:191], v[80:83]\n"
    "v_mfma_f32_16x16x32_bf16 v[84:87], a[240:243], a[176:179], v[44:47]\n"
    "v_mfma_f32_16x16x32_bf16 v[84:87], a[244:247], a[180:183], v[84:87]\n"
    "v_mfma_f32_16x16x32_bf16 v[84:87], a[248:251], a[184:187], v[84:87]\n"
    "v_mfma_f32_16x16x32_bf16 v[84:87], a[252:255], a[188:191], v[84:87]\n"
    "s_nop 3\n"
    "v_exp_f32 v80, v80\n"
    "v_exp_f32 v81, v81\n"
    "v_exp_f32 v82, v82\n"
    "v_exp_f32 v83, v83\n"
    "v_exp_f32 v84, v84\n"
    "v_exp_f32 v85, v85\n"
    "v_exp_f32 v86, v86\n"
    "v_exp_f32 v87, v87\n"
    "v_cvt_pk_bf16_f32 v252, v80, v81\n"
    "v_cvt_pk_bf16_f32 v253, v82, v83\n"
    "v_cvt_pk_bf16_f32 v254, v84, v85\n"
    "v_cvt_pk_bf16_f32 v255, v86, v87\n"
    "v_mfma_f32_16x16x32_bf16 v[60:63], v[64:67], v[240:243], 0\n"
    "v_mfma_f32_16x16x32_bf16 v[60:63], v[64:67], v[244:247], v[60:63]\n"
    "v_mfma_f32_16x16x32_bf16 v[60:63], v[64:67], v[248:251], v[60:63]\n"
    "v_mfma_f32_16x16x32_bf16 v[60:63], v[64:67], v[252:255], v[60:63]\n"
    "v_sub_f32 v28, v28, v9\n"
    "v_sub_f32 v29, v29, v9\n"
    "v_sub_f32 v30, v30, v9\n"
    "v_sub_f32 v31, v31, v9\n"
    "v_sub_f32 v11, 0, v9\n"
    "v_exp_f32 v11, v11\n"
    "s_nop 0\n"
    "v_mul_f32 v71, v11, v71\n"
    "ds_bpermute_b32 v12, v99, v11 offset:0\n"
    "ds_bpermute_b32 v13, v99, v11 offset:4\n"
    "ds_bpermute_b32 v14, v99, v11 offset:8\n"
    "ds_bpermute_b32 v15, v99, v11 offset:12\n"
    "s_waitcnt lgkmcnt(0)\n"
    "v_accvgpr_read_b32 v80, a12\n"
    "v_accvgpr_read_b32 v81, a13\n"
    "v_accvgpr_read_b32 v82, a14\n"
    "v_accvgpr_read_b32 v83, a15\n"
    "v_accvgpr_read_b32 v84, a28\n"
    "v_accvgpr_read_b32 v85, a29\n"
    "v_accvgpr_read_b32 v86, a30\n"
    "v_accvgpr_read_b32 v87, a31\n"
    "v_mul_f32 v80, v12, v80\n"
    "v_mul_f32 v81, v13, v81\n"
    "v_mul_f32 v82, v14, v82\n"
    "v_mul_f32 v83, v15, v83\n"
    "v_mul_f32 v84, v12, v84\n"
    "v_mul_f32 v85, v13, v85\n"
    "v_mul_f32 v86, v14, v86\n"
    "v_mul_f32 v87, v15, v87\n"
    "v_accvgpr_write_b32 a12, v80\n"
    "v_accvgpr_write_b32 a13, v81\n"
    "v_accvgpr_write_b32 a14, v82\n"
    "v_accvgpr_write_b32 a15, v83\n"
    "v_accvgpr_write_b32 a28, v84\n"
    "v_accvgpr_write_b32 a29, v85\n"
    "v_accvgpr_write_b32 a30, v86\n"
    "v_accvgpr_write_b32 a31, v87\n"
    "v_accvgpr_read_b32 v80, a44\n"
    "v_accvgpr_read_b32 v81, a45\n"
    "v_accvgpr_read_b32 v82, a46\n"
    "v_accvgpr_read_b32 v83, a47\n"
    "v_accvgpr_read_b32 v84, a60\n"
    "v_accvgpr_read_b32 v85, a61\n"
    "v_accvgpr_read_b32 v86, a62\n"
    "v_accvgpr_read_b32 v87, a63\n"
    "v_mul_f32 v80, v12, v80\n"
    "v_mul_f32 v81, v13, v81\n"
    "v_mul_f32 v82, v14, v82\n"
    "v_mul_f32 v83, v15, v83\n"
    "v_mul_f32 v84, v12, v84\n"
    "v_mul_f32 v85, v13, v85\n"
    "v_mul_f32 v86, v14, v86\n"
    "v_mul_f32 v87, v15, v87\n"
    "v_accvgpr_write_b32 a44, v80\n"
    "v_accvgpr_write_b32 a45, v81\n"
    "v_accvgpr_write_b32 a46, v82\n"
    "v_accvgpr_write_b32 a47, v83\n"
    "v_accvgpr_write_b32 a60, v84\n"
    "v_accvgpr_write_b32 a61, v85\n"
    "v_accvgpr_write_b32 a62, v86\n"
    "v_accvgpr_write_b32 a63, v87\n"
    "v_accvgpr_read_b32 v80, a76\n"
    "v_accvgpr_read_b32 v81, a77\n"
    "v_accvgpr_read_b32 v82, a78\n"
    "v_accvgpr_read_b32 v83, a79\n"
    "v_accvgpr_read_b32 v84, a92\n"
    "v_accvgpr_read_b32 v85, a93\n"
    "v_accvgpr_read_b32 v86, a94\n"
    "v_accvgpr_read_b32 v87, a95\n"
    "v_mul_f32 v80, v12, v80\n"
    "v_mul_f32 v81, v13, v81\n"
    "v_mul_f32 v82, v14, v82\n"
    "v_mul_f32 v83, v15, v83\n"
    "v_mul_f32 v84, v12, v84\n"
    "v_mul_f32 v85, v13, v85\n"
    "v_mul_f32 v86, v14, v86\n"
    "v_mul_f32 v87, v15, v87\n"
    "v_accvgpr_write_b32 a76, v80\n"
    "v_accvgpr_write_b32 a77, v81\n"
    "v_accvgpr_write_b32 a78, v82\n"
    "v_accvgpr_write_b32 a79, v83\n"
    "v_accvgpr_write_b32 a92, v84\n"
    "v_accvgpr_write_b32 a93, v85\n"
    "v_accvgpr_write_b32 a94, v86\n"
    "v_accvgpr_write_b32 a95, v87\n"
    "v_accvgpr_read_b32 v80, a108\n"
    "v_accvgpr_read_b32 v81, a109\n"
    "v_accvgpr_read_b32 v82, a110\n"
    "v_accvgpr_read_b32 v83, a111\n"
    "v_accvgpr_read_b32 v84, a124\n"
    "v_accvgpr_read_b32 v85, a125\n"
    "v_accvgpr_read_b32 v86, a126\n"
    "v_accvgpr_read_b32 v87, a127\n"
    "v_mul_f32 v80, v12, v80\n"
    "v_mul_f32 v81, v13, v81\n"
    "v_mul_f32 v82, v14, v82\n"
    "v_mul_f32 v83, v15, v83\n"
    "v_mul_f32 v84, v12, v84\n"
    "v_mul_f32 v85, v13, v85\n"
    "v_mul_f32 v86, v14, v86\n"
    "v_mul_f32 v87, v15, v87\n"
    "v_accvgpr_write_b32 a108, v80\n"
    "v_accvgpr_write_b32 a109, v81\n"
    "v_accvgpr_write_b32 a110, v82\n"
    "v_accvgpr_write_b32 a111, v83\n"
    "v_accvgpr_write_b32 a124, v84\n"
    "v_accvgpr_write_b32 a125, v85\n"
    "v_accvgpr_write_b32 a126, v86\n"
    "v_accvgpr_write_b32 a127, v87\n"
    "s_nop 7\n"
    "s_nop 7\n"
    "s_branch L_ret_D3\n"
    "L_rare_E0:\n"
    "s_nop 7\n"
    "s_nop 7\n"
    "s_sub_u32 s56, s48, 0x20000\n"
    "s_add_u32 s57, s56, 0x0\n"
    "s_add_u32 s58, s56, 0x1000\n"
    "buffer_load_dwordx4 a[224:227], v76, s[40:43], s57 offen offset:0 sc1\n"
    "buffer_load_dwordx4 a[228:231], v76, s[40:43], s57 offen offset:1024 sc1\n"
    "buffer_load_dwordx4 a[232:235], v76, s[40:43], s57 offen offset:2048 sc1\n"
    "buffer_load_dwordx4 a[236:239], v76, s[40:43], s57 offen offset:3072 sc1\n"
    "buffer_load_dwordx4 a[240:243], v76, s[40:43], s58 offen offset:0 sc1\n"
    "buffer_load_dwordx4 a[244:247], v76, s[40:43], s58 offen offset:1024 sc1\n"
    "buffer_load_dwordx4 a[248:251], v76, s[40:43], s58 offen offset:2048 sc1\n"
    "buffer_load_dwordx4 a[252:255], v76, s[40:43], s58 offen offset:3072 sc1\n"
    "s_waitcnt vmcnt(0)\n"
    "v_mfma_f32_16x16x32_bf16 v[80:83], a[224:227], a[128:131], v[32:35]\n"
    "v_mfma_f32_16x16x32_bf16 v[80:83], a[228:231], a[132:135], v[80:83]\n"
    "v_mfma_f32_16x16x32_bf16 v[80:83], a[232:235], a[136:139], v[80:83]\n"
    "v_mfma_f32_16x16x32_bf16 v[80:83], a[236:239], a[140:143], v[80:83]\n"
    "v_mfma_f32_16x16x32_bf16 v[84:87], a[240:243], a[128:131], v[32:35]\n"
    "v_mfma_f32_16x16x32_bf16 v[84:87], a[244:247], a[132:135], v[84:87]\n"
    "v_mfma_f32_16x16x32_bf16 v[84:87], a[248:251], a[136:139], v[84:87]\n"
    "v_mfma_f32_16x16x32_bf16 v[84:87], a[252:255], a[140:143], v[84:87]\n"
    "s_nop 3\n"
    "v_max_f32 v9, v80, v80\n"
    "v_max3_f32 v9, v9, v80, v81\n"
    "v_max3_f32 v9, v9, v82, v83\n"
    "s_nop 0\n"
    "v_max3_f32 v9, v9, v84, v85\n"
    "v_max3_f32 v9, v9, v86, v87\n"
    "s_add_u32 s57, s56, 0x2000\n"
    "s_add_u32 s58, s56, 0x3000\n"
    "buffer_load_dwordx4 a[224:227], v76, s[40:43], s57 offen offset:0 sc1\n"
    "buffer_load_dwordx4 a[228:231], v76, s[40:43], s57 offen offset:1024 sc1\n"
    "buffer_load_dwordx4 a[232:235], v76, s[40:43], s57 offen offset:2048 sc1\n"
    "buffer_load_dwordx4 a[236:239], v76, s[40:43], s57 offen offset:3072 sc1\n"
    "buffer_load_dwordx4 a[240:243], v76, s[40:43], s58 offen offset:0 sc1\n"
    "buffer_load_dwordx4 a[244:247], v76, s[40:43], s58 offen offset:1024 sc1\n"
    "buffer_load_dwordx4 a[248:251], v76, s[40:43], s58 offen offset:2048 sc1\n"
    "buffer_load_dwordx4 a[252:255], v76, s[40:43], s58 offen offset:3072 sc1\n"
    "s_waitcnt vmcnt(0)\n"
    "v_mfma_f32_16x16x32_bf16 v[80:83], a[224:227], a[128:131], v[32:35]\n"
    "v_mfma_f32_16x16x32_bf16 v[80:83], a[228:231], a[132:135], v[80:83]\n"
    "v_mfma_f32_16x16x32_bf16 v[80:83], a[232:235], a[136:139], v[80:83]\n"
    "v_mfma_f32_16x16x32_bf16 v[80:83], a[236:239], a[140:143], v[80:83]\n"
    "v_mfma_f32_16x16x32_bf16 v[84:87], a[240:243], a[128:131], v[32:35]\n"
    "v_mfma_f32_16x16x32_bf16 v[84:87], a[244:247], a[132:135], v[84:87]\n"
    "v_mfma_f32_16x16x32_bf16 v[84:87], a[248:251], a[136:139], v[84:87]\n"
    "v_mfma_f32_16x16x32_bf16 v[84:87], a[252:255], a[140:143], v[84:87]\n"
    "s_nop 3\n"
    "v_max3_f32 v9, v9, v80, v81\n"
    "v_max3_f32 v9, v9, v82, v83\n"
    "s_nop 1\n"
    "v_max3_f32 v9, v9, v84, v85\n"
    "v_max3_f32 v9, v9, v86, v87\n"
    "s_add_u32 s57, s56, 0x4000\n"
    "s_add_u32 s58, s56, 0x5000\n"
    "buffer_load_dwordx4 a[224:227], v76, s[40:43], s57 offen offset:0 sc1\n"
    "buffer_load_dwordx4 a[228:231], v76, s[40:43], s57 offen offset:1024 sc1\n"
    "buffer_load_dwordx4 a[232:235], v76, s[40:43], s57 offen offset:2048 sc1\n"
    "buffer_load_dwordx4 a[236:239], v76, s[40:43], s57 offen offset:3072 sc1\n"
    "buffer_load_dwordx4 a[240:243], v76, s[40:43], s58 offen offset:0 sc1\n"
    "buffer_load_dwordx4 a[244:247], v76, s[40:43], s58 offen offset:1024 sc1\n"
    "buffer_load_dwordx4 a[248:251], v76, s[40:43], s58 offen offset:2048 sc1\n"
    "buffer_load_dwordx4 a[252:255], v76, s[40:43], s58 offen offset:3072 sc1\n"
    "s_waitcnt vmcnt(0)\n"
    "v_mfma_f32_16x16x32_bf16 v[80:83], a[224:227], a[128:131], v[32:35]\n"
    "v_mfma_f32_16x16x32_bf16 v[80:83], a[228:231], a[132:135], v[80:83]\n"
    "v_mfma_f32_16x16x32_bf16 v[80:83], a[232:235], a[136:139], v[80:83]\n"
    "v_mfma_f32_16x16x32_bf16 v[80:83], a[236:239], a[140:143], v[80:83]\n"
    "v_mfma_f32_16x16x32_bf16 v[84:87], a[240:243], a[128:131], v[32:35]\n"
    "v_mfma_f32_16x16x32_bf16 v[84:87], a[244:247], a[132:135], v[84:87]\n"
    "v_mfma_f32_16x16x32_bf16 v[84:87], a[248:251], a[136:139], v[84:87]\n"
    "v_mfma_f32_16x16x32_bf16 v[84:87], a[252:255], a[140:143], v[84:87]\n"
    "s_nop 3\n"
    "v_max3_f32 v9, v9, v80, v81\n"
    "v_max3_f32 v9, v9, v82, v83\n"
    "s_nop 1\n"
    "v_max3_f32 v9, v9, v84, v85\n"
    "v_max3_f32 v9, v9, v86, v87\n"
    "s_add_u32 s57, s56, 0x6000\n"
    "s_add_u32 s58, s56, 0x7000\n"
    "buffer_load_dwordx4 a[224:227], v76, s[40:43], s57 offen offset:0 sc1\n"
    "buffer_load_dwordx4 a[228:231], v76, s[40:43], s57 offen offset:1024 sc1\n"
    "buffer_load_dwordx4 a[232:235], v76, s[40:43], s57 offen offset:2048 sc1\n"
    "buffer_load_dwordx4 a[236:239], v76, s[40:43], s57 offen offset:3072 sc1\n"
    "buffer_load_dwordx4 a[240:243], v76, s[40:43], s58 offen offset:0 sc1\n"
    "buffer_load_dwordx4 a[244:247], v76, s[40:43], s58 offen offset:1024 sc1\n"
    "buffer_load_dwordx4 a[248:251], v76, s[40:43], s58 offen offset:2048 sc1\n"
    "buffer_load_dwordx4 a[252:255], v76, s[40:43], s58 offen offset:3072 sc1\n"
    "s_waitcnt vmcnt(0)\n"
    "v_mfma_f32_16x16x32_bf16 v[80:83], a[224:227], a[128:131], v[32:35]\n"
    "v_mfma_f32_16x16x32_bf16 v[80:83], a[228:231], a[132:135], v[80:83]\n"
    "v_mfma_f32_16x16x32_bf16 v[80:83], a[232:235], a[136:139], v[80:83]\n"
    "v_mfma_f32_16x16x32_bf16 v[80:83], a[236:239], a[140:143], v[80:83]\n"
    "v_mfma_f32_16x16x32_bf16 v[84:87], a[240:243], a[128:131], v[32:35]\n"
    "v_mfma_f32_16x16x32_bf16 v[84:87], a[244:247], a[132:135], v[84:87]\n"
    "v_mfma_f32_16x16x32_bf16 v[84:87], a[248:251], a[136:139], v[84:87]\n"
    "v_mfma_f32_16x16x32_bf16 v[84:87], a[252:255], a[140:143], v[84:87]\n"
    "s_nop 3\n"
    "v_max3_f32 v9, v9, v80, v81\n"
    "v_max3_f32 v9, v9, v82, v83\n"
    "s_nop 1\n"
    "v_max3_f32 v9, v9, v84, v85\n"
    "v_max3_f32 v9, v9, v86, v87\n"
    "v_mov_b32 v10, v9\n"
    "s_nop 1\n"
    "v_permlane16_swap_b32 v10, v9\n"
    "v_max_f32 v9, v10, v9\n"
    "v_mov_b32 v10, v9\n"
    "s_nop 1\n"
    "v_permlane32_swap_b32 v10, v9\n"
    "v_max_f32 v9, v10, v9\n"
    "v_max_f32 v9, 0, v9\n"
    "v_sub_f32 v32, v32, v9\n"
    "v_sub_f32 v33, v33, v9\n"
    "v_sub_f32 v34, v34, v9\n"
    "v_sub_f32 v35, v35, v9\n"
    "s_add_u32 s57, s56, 0x0\n"
    "s_add_u32 s58, s56, 0x1000\n"
    "buffer_load_dwordx4 a[224:227], v76, s[40:43], s57 offen offset:0 sc1\n"
    "buffer_load_dwordx4 a[228:231], v76, s[40:43], s57 offen offset:1024 sc1\n"
    "buffer_load_dwordx4 a[232:235], v76, s[40:43], s57 offen offset:2048 sc1\n"
    "buffer_load_dwordx4 a[236:239], v76, s[40:43], s57 offen offset:3072 sc1\n"
    "buffer_load_dwordx4 a[240:243], v76, s[40:43], s58 offen offset:0 sc1\n"
    "buffer_load_dwordx4 a[244:247], v76, s[40:43], s58 offen offset:1024 sc1\n"
    "buffer_load_dwordx4 a[248:251], v76, s[40:43], s58 offen offset:2048 sc1\n"
    "buffer_load_dwordx4 a[252:255], v76, s[40:43], s58 offen offset:3072 sc1\n"
    "s_waitcnt vmcnt(0)\n"
    "v_mfma_f32_16x16x32_bf16 v[80:83], a[224:227], a[128:131], v[32:35]\n"
    "v_mfma_f32_16x16x32_bf16 v[80:83], a[228:231], a[132:135], v[80:83]\n"
    "v_mfma_f32_16x16x32_bf16 v[80:83], a[232:235], a[136:139], v[80:83]\n"
    "v_mfma_f32_16x16x32_bf16 v[80:83], a[236:239], a[140:143], v[80:83]\n"
    "v_mfma_f32_16x16x32_bf16 v[84:87], a[240:243], a[128:131], v[32:35]\n"
    "v_mfma_f32_16x16x32_bf16 v[84:87], a[244:247], a[132:135], v[84:87]\n"
    "v_mfma_f32_16x16x32_bf16 v[84:87], a[248:251], a[136:139], v[84:87]\n"
    "v_mfma_f32_16x16x32_bf16 v[84:87], a[252:255], a[140:143], v[84:87]\n"
    "s_nop 3\n"
    "v_exp_f32 v80, v80\n"
    "v_exp_f32 v81, v81\n"
    "v_exp_f32 v82, v82\n"
    "v_exp_f32 v83, v83\n"
    "v_exp_f32 v84, v84\n"
    "v_exp_f32 v85, v85\n"
    "v_exp_f32 v86, v86\n"
    "v_exp_f32 v87, v87\n"
    "v_cvt_pk_bf16_f32 v128, v80, v81\n"
    "v_cvt_pk_bf16_f32 v129, v82, v83\n"
    "v_cvt_pk_bf16_f32 v130, v84, v85\n"
    "v_cvt_pk_bf16_f32 v131, v86, v87\n"
    "s_add_u32 s57, s56, 0x2000\n"
    "s_add_u32 s58, s56, 0x3000\n"
    "buffer_load_dwordx4 a[224:227], v76, s[40:43], s57 offen offset:0 sc1\n"
    "buffer_load_dwordx4 a[228:231], v76, s[40:43], s57 offen offset:1024 sc1\n"
    "buffer_load_dwordx4 a[232:235], v76, s[40:43], s57 offen offset:2048 sc1\n"
    "buffer_load_dwordx4 a[236:239], v76, s[40:43], s57 offen offset:3072 sc1\n"
    "buffer_load_dwordx4 a[240:243], v76, s[40:43], s58 offen offset:0 sc1\n"
    "buffer_load_dwordx4 a[244:247], v76, s[40:43], s58 offen offset:1024 sc1\n"
    "buffer_load_dwordx4 a[248:251], v76, s[40:43], s58 offen offset:2048 sc1\n"
    "buffer_load_dwordx4 a[252:255], v76, s[40:43], s58 offen offset:3072 sc1\n"
    "s_waitcnt vmcnt(0)\n"
    "v_mfma_f32_16x16x32_bf16 v[80:83], a[224:227], a[128:131], v[32:35]\n"
    "v_mfma_f32_16x16x32_bf16 v[80:83], a[228:231], a[132:135], v[80:83]\n"
    "v_mfma_f32_16x16x32_bf16 v[80:83], a[232:235], a[136:139], v[80:83]\n"
    "v_mfma_f32_16x16x32_bf16 v[80:83], a[236:239], a[140:143], v[80:83]\n"
    "v_mfma_f32_16x16x32_bf16 v[84:87], a[240:243], a[128:131], v[32:35]\n"
    "v_mfma_f32_16x16x32_bf16 v[84:87], a[244:247], a[132:135], v[84:87]\n"
    "v_mfma_f32_16x16x32_bf16 v[84:87], a[248:251], a[136:139], v[84:87]\n"
    "v_mfma_f32_16x16x32_bf16 v[84:87], a[252:255], a[140:143], v[84:87]\n"
    "s_nop 3\n"
    "v_exp_f32 v80, v80\n"
    "v_exp_f32 v81, v81\n"
    "v_exp_f32 v82, v82\n"
    "v_exp_f32 v83, v83\n"
    "v_exp_f32 v84, v84\n"
    "v_exp_f32 v85, v85\n"
    "v_exp_f32 v86, v86\n"
    "v_exp_f32 v87, v87\n"
    "v_cvt_pk_bf16_f32 v132, v80, v81\n"
    "v_cvt_pk_bf16_f32 v133, v82, v83\n"
    "v_cvt_pk_bf16_f32 v134, v84, v85\n"
    "v_cvt_pk_bf16_f32 v135, v86, v87\n"
    "s_add_u32 s57, s56, 0x4000\n"
    "s_add_u32 s58, s56, 0x5000\n"
    "buffer_load_dwordx4 a[224:227], v76, s[40:43], s57 offen offset:0 sc1\n"
    "buffer_load_dwordx4 a[228:231], v76, s[40:43], s57 offen offset:1024 sc1\n"
    "buffer_load_dwordx4 a[232:235], v76, s[40:43], s57 offen offset:2048 sc1\n"
    "buffer_load_dwordx4 a[236:239], v76, s[40:43], s57 offen offset:3072 sc1\n"
    "buffer_load_dwordx4 a[240:243], v76, s[40:43], s58 offen offset:0 sc1\n"
    "buffer_load_dwordx4 a[244:247], v76, s[40:43], s58 offen offset:1024 sc1\n"
    "buffer_load_dwordx4 a[248:251], v76, s[40:43], s58 offen offset:2048 sc1\n"
    "buffer_load_dwordx4 a[252:255], v76, s[40:43], s58 offen offset:3072 sc1\n"
    "s_waitcnt vmcnt(0)\n"
    "v_mfma_f32_16x16x32_bf16 v[80:83], a[224:227], a[128:131], v[32:35]\n"
    "v_mfma_f32_16x16x32_bf16 v[80:83], a[228:231], a[132:135], v[80:83]\n"
    "v_mfma_f32_16x16x32_bf16 v[80:83], a[232:235], a[136:139], v[80:83]\n"
    "v_mfma_f32_16x16x32_bf16 v[80:83], a[236:239], a[140:143], v[80:83]\n"
    "v_mfma_f32_16x16x32_bf16 v[84:87], a[240:243], a[128:131], v[32:35]\n"
    "v_mfma_f32_16x16x32_bf16 v[84:87], a[244:247], a[132:135], v[84:87]\n"
    "v_mfma_f32_16x16x32_bf16 v[84:87], a[248:251], a[136:139], v[84:87]\n"
    "v_mfma_f32_16x16x32_bf16 v[84:87], a[252:255], a[140:143], v[84:87]\n"
    "s_nop 3\n"
    "v_exp_f32 v80, v80\n"
    "v_exp_f32 v81, v81\n"
    "v_exp_f32 v82, v82\n"
    "v_exp_f32 v83, v83\n"
    "v_exp_f32 v84, v84\n"
    "v_exp_f32 v85, v85\n"
    "v_exp_f32 v86, v86\n"
    "v_exp_f32 v87, v87\n"
    "v_cvt_pk_bf16_f32 v136, v80, v81\n"
    "v_cvt_pk_bf16_f32 v137, v82, v83\n"
    "v_cvt_pk_bf16_f32 v138, v84, v85\n"
    "v_cvt_pk_bf16_f32 v139, v86, v87\n"
    "s_add_u32 s57, s56, 0x6000\n"
    "s_add_u32 s58, s56, 0x7000\n"
    "buffer_load_dwordx4 a[224:227], v76, s[40:43], s57 offen offset:0 sc1\n"
    "buffer_load_dwordx4 a[228:231], v76, s[40:43], s57 offen offset:1024 sc1\n"
    "buffer_load_dwordx4 a[232:235], v76, s[40:43], s57 offen offset:2048 sc1\n"
    "buffer_load_dwordx4 a[236:239], v76, s[40:43], s57 offen offset:3072 sc1\n"
    "buffer_load_dwordx4 a[240:243], v76, s[40:43], s58 offen offset:0 sc1\n"
    "buffer_load_dwordx4 a[244:247], v76, s[40:43], s58 offen offset:1024 sc1\n"
    "buffer_load_dwordx4 a[248:251], v76, s[40:43], s58 offen offset:2048 sc1\n"
    "buffer_load_dwordx4 a[252:255], v76, s[40:43], s58 offen offset:3072 sc1\n"
    "s_waitcnt vmcnt(0)\n"
    "v_mfma_f32_16x16x32_bf16 v[80:83], a[224:227], a[128:131], v[32:35]\n"
    "v_mfma_f32_16x16x32_bf16 v[80:83], a[228:231], a[132:135], v[80:83]\n"
    "v_mfma_f32_16x16x32_bf16 v[80:83], a[232:235], a[136:139], v[80:83]\n"
    "v_mfma_f32_16x16x32_bf16 v[80:83], a[236:239], a[140:143], v[80:83]\n"
    "v_mfma_f32_16x16x32_bf16 v[84:87], a[240:243], a[128:131], v[32:35]\n"
    "v_mfma_f32_16x16x32_bf16 v[84:87], a[244:247], a[132:135], v[84:87]\n"
    "v_mfma_f32_16x16x32_bf16 v[84:87], a[248:251], a[136:139], v[84:87]\n"
    "v_mfma_f32_16x16x32_bf16 v[84:87], a[252:255], a[140:143], v[84:87]\n"
    "s_nop 3\n"
    "v_exp_f32 v80, v80\n"
    "v_exp_f32 v81, v81\n"
    "v_exp_f32 v82, v82\n"
    "v_exp_f32 v83, v83\n"
    "v_exp_f32 v84, v84\n"
    "v_exp_f32 v85, v85\n"
    "v_exp_f32 v86, v86\n"
    "v_exp_f32 v87, v87\n"
    "v_cvt_pk_bf16_f32 v140, v80, v81\n"
    "v_cvt_pk_bf16_f32 v141, v82, v83\n"
    "v_cvt_pk_bf16_f32 v142, v84, v85\n"
    "v_cvt_pk_bf16_f32 v143, v86, v87\n"
    "v_mfma_f32_16x16x32_bf16 v[48:51], v[64:67], v[128:131], 0\n"
    "v_mfma_f32_16x16x32_bf16 v[48:51], v[64:67], v[132:135], v[48:51]\n"
    "v_mfma_f32_16x16x32_bf16 v[48:51], v[64:67], v[136:139], v[48:51]\n"
    "v_mfma_f32_16x16x32_bf16 v[48:51], v[64:67], v[140:143], v[48:51]\n"
    "v_sub_f32 v100, v100, v9\n"
    "v_sub_f32 v101, v101, v9\n"
    "v_sub_f32 v102, v102, v9\n"
    "v_sub_f32 v103, v103, v9\n"
    "v_sub_f32 v11, 0, v9\n"
    "v_exp_f32 v11, v11\n"
    "s_nop 0\n"
    "v_mul_f32 v68, v11, v68\n"
    "ds_bpermute_b32 v12, v99, v11 offset:0\n"
    "ds_bpermute_b32 v13, v99, v11 offset:4\n"
    "ds_bpermute_b32 v14, v99, v11 offset:8\n"
    "ds_bpermute_b32 v15, v99, v11 offset:12\n"
    "s_waitcnt lgkmcnt(0)\n"
    "v_accvgpr_read_b32 v80, a0\n"
    "v_accvgpr_read_b32 v81, a1\n"
    "v_accvgpr_read_b32 v82, a2\n"
    "v_accvgpr_read_b32 v83, a3\n"
    "v_accvgpr_read_b32 v84, a16\n"
    "v_accvgpr_read_b32 v85, a17\n"
    "v_accvgpr_read_b32 v86, a18\n"
    "v_accvgpr_read_b32 v87, a19\n"
    "v_mul_f32 v80, v12, v80\n"
    "v_mul_f32 v81, v13, v81\n"
    "v_mul_f32 v82, v14, v82\n"
    "v_mul_f32 v83, v15, v83\n"
    "v_mul_f32 v84, v12, v84\n"
    "v_mul_f32 v85, v13, v85\n"
    "v_mul_f32 v86, v14, v86\n"
    "v_mul_f32 v87, v15, v87\n"
    "v_accvgpr_write_b32 a0, v80\n"
    "v_accvgpr_write_b32 a1, v81\n"
    "v_accvgpr_write_b32 a2, v82\n"
    "v_accvgpr_write_b32 a3, v83\n"
    "v_accvgpr_write_b32 a16, v84\n"
    "v_accvgpr_write_b32 a17, v85\n"
    "v_accvgpr_write_b32 a18, v86\n"
    "v_accvgpr_write_b32 a19, v87\n"
    "v_accvgpr_read_b32 v80, a32\n"
    "v_accvgpr_read_b32 v81, a33\n"
    "v_accvgpr_read_b32 v82, a34\n"
    "v_accvgpr_read_b32 v83, a35\n"
    "v_accvgpr_read_b32 v84, a48\n"
    "v_accvgpr_read_b32 v85, a49\n"
    "v_accvgpr_read_b32 v86, a50\n"
    "v_accvgpr_read_b32 v87, a51\n"
    "v_mul_f32 v80, v12, v80\n"
    "v_mul_f32 v81, v13, v81\n"
    "v_mul_f32 v82, v14, v82\n"
    "v_mul_f32 v83, v15, v83\n"
    "v_mul_f32 v84, v12, v84\n"
    "v_mul_f32 v85, v13, v85\n"
    "v_mul_f32 v86, v14, v86\n"
    "v_mul_f32 v87, v15, v87\n"
    "v_accvgpr_write_b32 a32, v80\n"
    "v_accvgpr_write_b32 a33, v81\n"
    "v_accvgpr_write_b32 a34, v82\n"
    "v_accvgpr_write_b32 a35, v83\n"
    "v_accvgpr_write_b32 a48, v84\n"
    "v_accvgpr_write_b32 a49, v85\n"
    "v_accvgpr_write_b32 a50, v86\n"
    "v_accvgpr_write_b32 a51, v87\n"
    "v_accvgpr_read_b32 v80, a64\n"
    "v_accvgpr_read_b32 v81, a65\n"
    "v_accvgpr_read_b32 v82, a66\n"
    "v_accvgpr_read_b32 v83, a67\n"
    "v_accvgpr_read_b32 v84, a80\n"
    "v_accvgpr_read_b32 v85, a81\n"
    "v_accvgpr_read_b32 v86, a82\n"
    "v_accvgpr_read_b32 v87, a83\n"
    "v_mul_f32 v80, v12, v80\n"
    "v_mul_f32 v81, v13, v81\n"
    "v_mul_f32 v82, v14, v82\n"
    "v_mul_f32 v83, v15, v83\n"
    "v_mul_f32 v84, v12, v84\n"
    "v_mul_f32 v85, v13, v85\n"
    "v_mul_f32 v86, v14, v86\n"
    "v_mul_f32 v87, v15, v87\n"
    "v_accvgpr_write_b32 a64, v80\n"
    "v_accvgpr_write_b32 a65, v81\n"
    "v_accvgpr_write_b32 a66, v82\n"
    "v_accvgpr_write_b32 a67, v83\n"
    "v_accvgpr_write_b32 a80, v84\n"
    "v_accvgpr_write_b32 a81, v85\n"
    "v_accvgpr_write_b32 a82, v86\n"
    "v_accvgpr_write_b32 a83, v87\n"
    "v_accvgpr_read_b32 v80, a96\n"
    "v_accvgpr_read_b32 v81, a97\n"
    "v_accvgpr_read_b32 v82, a98\n"
    "v_accvgpr_read_b32 v83, a99\n"
    "v_accvgpr_read_b32 v84, a112\n"
    "v_accvgpr_read_b32 v85, a113\n"
    "v_accvgpr_read_b32 v86, a114\n"
    "v_accvgpr_read_b32 v87, a115\n"
    "v_mul_f32 v80, v12, v80\n"
    "v_mul_f32 v81, v13, v81\n"
    "v_mul_f32 v82, v14, v82\n"
    "v_mul_f32 v83, v15, v83\n"
    "v_mul_f32 v84, v12, v84\n"
    "v_mul_f32 v85, v13, v85\n"
    "v_mul_f32 v86, v14, v86\n"
    "v_mul_f32 v87, v15, v87\n"
    "v_accvgpr_write_b32 a96, v80\n"
    "v_accvgpr_write_b32 a97, v81\n"
    "v_accvgpr_write_b32 a98, v82\n"
    "v_accvgpr_write_b32 a99, v83\n"
    "v_accvgpr_write_b32 a112, v84\n"
    "v_accvgpr_write_b32 a113, v85\n"
    "v_accvgpr_write_b32 a114, v86\n"
    "v_accvgpr_write_b32 a115, v87\n"
    "s_nop 7\n"
    "s_nop 7\n"
    "s_branch L_ret_E0\n"
    "L_rare_E1:\n"
    "s_nop 7\n"
    "s_nop 7\n"
    "s_sub_u32 s56, s48, 0x20000\n"
    "s_add_u32 s57, s56, 0x0\n"
    "s_add_u32 s58, s56, 0x1000\n"
    "buffer_load_dwordx4 a[224:227], v76, s[40:43], s57 offen offset:0 sc1\n"
    "buffer_load_dwordx4 a[228:231], v76, s[40:43], s57 offen offset:1024 sc1\n"
    "buffer_load_dwordx4 a[232:235], v76, s[40:43], s57 offen offset:2048 sc1\n"
    "buffer_load_dwordx4 a[236:239], v76, s[40:43], s57 offen offset:3072 sc1\n"
    "buffer_load_dwordx4 a[240:243], v76, s[40:43], s58 offen offset:0 sc1\n"
    "buffer_load_dwordx4 a[244:247], v76, s[40:43], s58 offen offset:1024 sc1\n"
    "buffer_load_dwordx4 a[248:251], v76, s[40:43], s58 offen offset:2048 sc1\n"
    "buffer_load_dwordx4 a[252:255], v76, s[40:43], s58 offen offset:3072 sc1\n"
    "s_waitcnt vmcnt(0)\n"
    "v_mfma_f32_16x16x32_bf16 v[80:83], a[224:227], a[144:147], v[36:39]\n"
    "v_mfma_f32_16x16x32_bf16 v[80:83], a[228:231], a[148:151], v[80:83]\n"
    "v_mfma_f32_16x16x32_bf16 v[80:83], a[232:235], a[152:155], v[80:83]\n"
    "v_mfma_f32_16x16x32_bf16 v[80:83], a[236:239], a[156:159], v[80:83]\n"
    "v_mfma_f32_16x16x32_bf16 v[84:87], a[240:243], a[144:147], v[36:39]\n"
    "v_mfma_f32_16x16x32_bf16 v[84:87], a[244:247], a[148:151], v[84:87]\n"
    "v_mfma_f32_16x16x32_bf16 v[84:87], a[248:251], a[152:155], v[84:87]\n"
    "v_mfma_f32_16x16x32_bf16 v[84:87], a[252:255], a[156:159], v[84:87]\n"
    "s_nop 3\n"
    "v_max_f32 v9, v80, v80\n"
    "v_max3_f32 v9, v9, v80, v81\n"
    "v_max3_f32 v9, v9, v82, v83\n"
    "s_nop 0\n"
    "v_max3_f32 v9, v9, v84, v85\n"
    "v_max3_f32 v9, v9, v86, v87\n"
    "s_add_u32 s57, s56, 0x2000\n"
    "s_add_u32 s58, s56, 0x3000\n"
    "buffer_load_dwordx4 a[224:227], v76, s[40:43], s57 offen offset:0 sc1\n"
    "buffer_load_dwordx4 a[228:231], v76, s[40:43], s57 offen offset:1024 sc1\n"
    "buffer_load_dwordx4 a[232:235], v76, s[40:43], s57 offen offset:2048 sc1\n"
    "buffer_load_dwordx4 a[236:239], v76, s[40:43], s57 offen offset:3072 sc1\n"
    "buffer_load_dwordx4 a[240:243], v76, s[40:43], s58 offen offset:0 sc1\n"
    "buffer_load_dwordx4 a[244:247], v76, s[40:43], s58 offen offset:1024 sc1\n"
    "buffer_load_dwordx4 a[248:251], v76, s[40:43], s58 offen offset:2048 sc1\n"
    "buffer_load_dwordx4 a[252:255], v76, s[40:43], s58 offen offset:3072 sc1\n"
    "s_waitcnt vmcnt(0)\n"
    "v_mfma_f32_16x16x32_bf16 v[80:83], a[224:227], a[144:147], v[36:39]\n"
    "v_mfma_f32_16x16x32_bf16 v[80:83], a[228:231], a[148:151], v[80:83]\n"
    "v_mfma_f32_16x16x32_bf16 v[80:83], a[232:235], a[152:155], v[80:83]\n"
    "v_mfma_f32_16x16x32_bf16 v[80:83], a[236:239], a[156:159], v[80:83]\n"
    "v_mfma_f32_16x16x32_bf16 v[84:87], a[240:243], a[144:147], v[36:39]\n"
    "v_mfma_f32_16x16x32_bf16 v[84:87], a[244:247], a[148:151], v[84:87]\n"
    "v_mfma_f32_16x16x32_bf16 v[84:87], a[248:251], a[152:155], v[84:87]\n"
    "v_mfma_f32_16x16x32_bf16 v[84:87], a[252:255], a[156:159], v[84:87]\n"
    "s_nop 3\n"
    "v_max3_f32 v9, v9, v80, v81\n"
    "v_max3_f32 v9, v9, v82, v83\n"
    "s_nop 1\n"
    "v_max3_f32 v9, v9, v84, v85\n"
    "v_max3_f32 v9, v9, v86, v87\n"
    "s_add_u32 s57, s56, 0x4000\n"
    "s_add_u32 s58, s56, 0x5000\n"
    "buffer_load_dwordx4 a[224:227], v76, s[40:43], s57 offen offset:0 sc1\n"
    "buffer_load_dwordx4 a[228:231], v76, s[40:43], s57 offen offset:1024 sc1\n"
    "buffer_load_dwordx4 a[232:235], v76, s[40:43], s57 offen offset:2048 sc1\n"
    "buffer_load_dwordx4 a[236:239], v76, s[40:43], s57 offen offset:3072 sc1\n"
    "buffer_load_dwordx4 a[240:243], v76, s[40:43], s58 offen offset:0 sc1\n"
    "buffer_load_dwordx4 a[244:247], v76, s[40:43], s58 offen offset:1024 sc1\n"
    "buffer_load_dwordx4 a[248:251], v76, s[40:43], s58 offen offset:2048 sc1\n"
    "buffer_load_dwordx4 a[252:255], v76, s[40:43], s58 offen offset:3072 sc1\n"
    "s_waitcnt vmcnt(0)\n"
    "v_mfma_f32_16x16x32_bf16 v[80:83], a[224:227], a[144:147], v[36:39]\n"
    "v_mfma_f32_16x16x32_bf16 v[80:83], a[228:231], a[148:151], v[80:83]\n"
    "v_mfma_f32_16x16x32_bf16 v[80:83], a[232:235], a[152:155], v[80:83]\n"
    "v_mfma_f32_16x16x32_bf16 v[80:83], a[236:239], a[156:159], v[80:83]\n"
    "v_mfma_f32_16x16x32_bf16 v[84:87], a[240:243], a[144:147], v[36:39]\n"
    "v_mfma_f32_16x16x32_bf16 v[84:87], a[244:247], a[148:151], v[84:87]\n"
    "v_mfma_f32_16x16x32_bf16 v[84:87], a[248:251], a[152:155], v[84:87]\n"
    "v_mfma_f32_16x16x32_bf16 v[84:87], a[252:255], a[156:159], v[84:87]\n"
    "s_nop 3\n"
    "v_max3_f32 v9, v9, v80, v81\n"
    "v_max3_f32 v9, v9, v82, v83\n"
    "s_nop 1\n"
    "v_max3_f32 v9, v9, v84, v85\n"
    "v_max3_f32 v9, v9, v86, v87\n"
    "s_add_u32 s57, s56, 0x6000\n"
    "s_add_u32 s58, s56, 0x7000\n"
    "buffer_load_dwordx4 a[224:227], v76, s[40:43], s57 offen offset:0 sc1\n"
    "buffer_load_dwordx4 a[228:231], v76, s[40:43], s57 offen offset:1024 sc1\n"
    "buffer_load_dwordx4 a[232:235], v76, s[40:43], s57 offen offset:2048 sc1\n"
    "buffer_load_dwordx4 a[236:239], v76, s[40:43], s57 offen offset:3072 sc1\n"
    "buffer_load_dwordx4 a[240:243], v76, s[40:43], s58 offen offset:0 sc1\n"
    "buffer_load_dwordx4 a[244:247], v76, s[40:43], s58 offen offset:1024 sc1\n"
    "buffer_load_dwordx4 a[248:251], v76, s[40:43], s58 offen offset:2048 sc1\n"
    "buffer_load_dwordx4 a[252:255], v76, s[40:43], s58 offen offset:3072 sc1\n"
    "s_waitcnt vmcnt(0)\n"
    "v_mfma_f32_16x16x32_bf16 v[80:83], a[224:227], a[144:147], v[36:39]\n"
    "v_mfma_f32_16x16x32_bf16 v[80:83], a[228:231], a[148:151], v[80:83]\n"
    "v_mfma_f32_16x16x32_bf16 v[80:83], a[232:235], a[152:155], v[80:83]\n"
    "v_mfma_f32_16x16x32_bf16 v[80:83], a[236:239], a[156:159], v[80:83]\n"
    "v_mfma_f32_16x16x32_bf16 v[84:87], a[240:243], a[144:147], v[36:39]\n"
    "v_mfma_f32_16x16x32_bf16 v[84:87], a[244:247], a[148:151], v[84:87]\n"
    "v_mfma_f32_16x16x32_bf16 v[84:87], a[248:251], a[152:155], v[84:87]\n"
    "v_mfma_f32_16x16x32_bf16 v[84:87], a[252:255], a[156:159], v[84:87]\n"
    "s_nop 3\n"
    "v_max3_f32 v9, v9, v80, v81\n"
    "v_max3_f32 v9, v9, v82, v83\n"
    "s_nop 1\n"
    "v_max3_f32 v9, v9, v84, v85\n"
    "v_max3_f32 v9, v9, v86, v87\n"
    "v_mov_b32 v10, v9\n"
    "s_nop 1\n"
    "v_permlane16_swap_b32 v10, v9\n"
    "v_max_f32 v9, v10, v9\n"
    "v_mov_b32 v10, v9\n"
    "s_nop 1\n"
    "v_permlane32_swap_b32 v10, v9\n"
    "v_max_f32 v9, v10, v9\n"
    "v_max_f32 v9, 0, v9\n"
    "v_sub_f32 v36, v36, v9\n"
    "v_sub_f32 v37, v37, v9\n"
    "v_sub_f32 v38, v38, v9\n"
    "v_sub_f32 v39, v39, v9\n"
    "s_add_u32 s57, s56, 0x0\n"
    "s_add_u32 s58, s56, 0x1000\n"
    "buffer_load_dwordx4 a[224:227], v76, s[40:43], s57 offen offset:0 sc1\n"
    "buffer_load_dwordx4 a[228:231], v76, s[40:43], s57 offen offset:1024 sc1\n"
    "buffer_load_dwordx4 a[232:235], v76, s[40:43], s57 offen offset:2048 sc1\n"
    "buffer_load_dwordx4 a[236:239], v76, s[40:43], s57 offen offset:3072 sc1\n"
    "buffer_load_dwordx4 a[240:243], v76, s[40:43], s58 offen offset:0 sc1\n"
    "buffer_load_dwordx4 a[244:247], v76, s[40:43], s58 offen offset:1024 sc1\n"
    "buffer_load_dwordx4 a[248:251], v76, s[40:43], s58 offen offset:2048 sc1\n"
    "buffer_load_dwordx4 a[252:255], v76, s[40:43], s58 offen offset:3072 sc1\n"
    "s_waitcnt vmcnt(0)\n"
    "v_mfma_f32_16x16x32_bf16 v[80:83], a[224:227], a[144:147], v[36:39]\n"
    "v_mfma_f32_16x16x32_bf16 v[80:83], a[228:231], a[148:151], v[80:83]\n"
    "v_mfma_f32_16x16x32_bf16 v[80:83], a[232:235], a[152:155], v[80:83]\n"
    "v_mfma_f32_16x16x32_bf16 v[80:83], a[236:239], a[156:159], v[80:83]\n"
    "v_mfma_f32_16x16x32_bf16 v[84:87], a[240:243], a[144:147], v[36:39]\n"
    "v_mfma_f32_16x16x32_bf16 v[84:87], a[244:247], a[148:151], v[84:87]\n"
    "v_mfma_f32_16x16x32_bf16 v[84:87], a[248:251], a[152:155], v[84:87]\n"
    "v_mfma_f32_16x16x32_bf16 v[84:87], a[252:255], a[156:159], v[84:87]\n"
    "s_nop 3\n"
    "v_exp_f32 v80, v80\n"
    "v_exp_f32 v81, v81\n"
    "v_exp_f32 v82, v82\n"
    "v_exp_f32 v83, v83\n"
    "v_exp_f32 v84, v84\n"
    "v_exp_f32 v85, v85\n"
    "v_exp_f32 v86, v86\n"
    "v_exp_f32 v87, v87\n"
    "v_cvt_pk_bf16_f32 v144, v80, v81\n"
    "v_cvt_pk_bf16_f32 v145, v82, v83\n"
    "v_cvt_pk_bf16_f32 v146, v84, v85\n"
    "v_cvt_pk_bf16_f32 v147, v86, v87\n"
    "s_add_u32 s57, s56, 0x2000\n"
    "s_add_u32 s58, s56, 0x3000\n"
    "buffer_load_dwordx4 a[224:227], v76, s[40:43], s57 offen offset:0 sc1\n"
    "buffer_load_dwordx4 a[228:231], v76, s[40:43], s57 offen offset:1024 sc1\n"
    "buffer_load_dwordx4 a[232:235], v76, s[40:43], s57 offen offset:2048 sc1\n"
    "buffer_load_dwordx4 a[236:239], v76, s[40:43], s57 offen offset:3072 sc1\n"
    "buffer_load_dwordx4 a[240:243], v76, s[40:43], s58 offen offset:0 sc1\n"
    "buffer_load_dwordx4 a[244:247], v76, s[40:43], s58 offen offset:1024 sc1\n"
    "buffer_load_dwordx4 a[248:251], v76, s[40:43], s58 offen offset:2048 sc1\n"
    "buffer_load_dwordx4 a[252:255], v76, s[40:43], s58 offen offset:3072 sc1\n"
    "s_waitcnt vmcnt(0)\n"
    "v_mfma_f32_16x16x32_bf16 v[80:83], a[224:227], a[144:147], v[36:39]\n"
    "v_mfma_f32_16x16x32_bf16 v[80:83], a[228:231], a[148:151], v[80:83]\n"
    "v_mfma_f32_16x16x32_bf16 v[80:83], a[232:235], a[152:155], v[80:83]\n"
    "v_mfma_f32_16x16x32_bf16 v[80:83], a[236:239], a[156:159], v[80:83]\n"
    "v_mfma_f32_16x16x32_bf16 v[84:87], a[240:243], a[144:147], v[36:39]\n"
    "v_mfma_f32_16x16x32_bf16 v[84:87], a[244:247], a[148:151], v[84:87]\n"
    "v_mfma_f32_16x16x32_bf16 v[84:87], a[248:251], a[152:155], v[84:87]\n"
    "v_mfma_f32_16x16x32_bf16 v[84:87], a[252:255], a[156:159], v[84:87]\n"
    "s_nop 3\n"
    "v_exp_f32 v80, v80\n"
    "v_exp_f32 v81, v81\n"
    "v_exp_f32 v82, v82\n"
    "v_exp_f32 v83, v83\n"
    "v_exp_f32 v84, v84\n"
    "v_exp_f32 v85, v85\n"
    "v_exp_f32 v86, v86\n"
    "v_exp_f32 v87, v87\n"
    "v_cvt_pk_bf16_f32 v148, v80, v81\n"
    "v_cvt_pk_bf16_f32 v149, v82, v83\n"
    "v_cvt_pk_bf16_f32 v150, v84, v85\n"
    "v_cvt_pk_bf16_f32 v151, v86, v87\n"
    "s_add_u32 s57, s56, 0x4000\n"
    "s_add_u32 s58, s56, 0x5000\n"
    "buffer_load_dwordx4 a[224:227], v76, s[40:43], s57 offen offset:0 sc1\n"
    "buffer_load_dwordx4 a[228:231], v76, s[40:43], s57 offen offset:1024 sc1\n"
    "buffer_load_dwordx4 a[232:235], v76, s[40:43], s57 offen offset:2048 sc1\n"
    "buffer_load_dwordx4 a[236:239], v76, s[40:43], s57 offen offset:3072 sc1\n"
    "buffer_load_dwordx4 a[240:243], v76, s[40:43], s58 offen offset:0 sc1\n"
    "buffer_load_dwordx4 a[244:247], v76, s[40:43], s58 offen offset:1024 sc1\n"
    "buffer_load_dwordx4 a[248:251], v76, s[40:43], s58 offen offset:2048 sc1\n"
    "buffer_load_dwordx4 a[252:255], v76, s[40:43], s58 offen offset:3072 sc1\n"
    "s_waitcnt vmcnt(0)\n"
    "v_mfma_f32_16x16x32_bf16 v[80:83], a[224:227], a[144:147], v[36:39]\n"
    "v_mfma_f32_16x16x32_bf16 v[80:83], a[228:231], a[148:151], v[80:83]\n"
    "v_mfma_f32_16x16x32_bf16 v[80:83], a[232:235], a[152:155], v[80:83]\n"
    "v_mfma_f32_16x16x32_bf16 v[80:83], a[236:239], a[156:159], v[80:83]\n"
    "v_mfma_f32_16x16x32_bf16 v[84:87], a[240:243], a[144:147], v[36:39]\n"
    "v_mfma_f32_16x16x32_bf16 v[84:87], a[244:247], a[148:151], v[84:87]\n"
    "v_mfma_f32_16x16x32_bf16 v[84:87], a[248:251], a[152:155], v[84:87]\n"
    "v_mfma_f32_16x16x32_bf16 v[84:87], a[252:255], a[156:159], v[84:87]\n"
    "s_nop 3\n"
    "v_exp_f32 v80, v80\n"
    "v_exp_f32 v81, v81\n"
    "v_exp_f32 v82, v82\n"
    "v_exp_f32 v83, v83\n"
    "v_exp_f32 v84, v84\n"
    "v_exp_f32 v85, v85\n"
    "v_exp_f32 v86, v86\n"
    "v_exp_f32 v87, v87\n"
    "v_cvt_pk_bf16_f32 v152, v80, v81\n"
    "v_cvt_pk_bf16_f32 v153, v82, v83\n"
    "v_cvt_pk_bf16_f32 v154, v84, v85\n"
    "v_cvt_pk_bf16_f32 v155, v86, v87\n"
    "s_add_u32 s57, s56, 0x6000\n"
    "s_add_u32 s58, s56, 0x7000\n"
    "buffer_load_dwordx4 a[224:227], v76, s[40:43], s57 offen offset:0 sc1\n"
    "buffer_load_dwordx4 a[228:231], v76, s[40:43], s57 offen offset:1024 sc1\n"
    "buffer_load_dwordx4 a[232:235], v76, s[40:43], s57 offen offset:2048 sc1\n"
    "buffer_load_dwordx4 a[236:239], v76, s[40:43], s57 offen offset:3072 sc1\n"
    "buffer_load_dwordx4 a[240:243], v76, s[40:43], s58 offen offset:0 sc1\n"
    "buffer_load_dwordx4 a[244:247], v76, s[40:43], s58 offen offset:1024 sc1\n"
    "buffer_load_dwordx4 a[248:251], v76, s[40:43], s58 offen offset:2048 sc1\n"
    "buffer_load_dwordx4 a[252:255], v76, s[40:43], s58 offen offset:3072 sc1\n"
    "s_waitcnt vmcnt(0)\n"
    "v_mfma_f32_16x16x32_bf16 v[80:83], a[224:227], a[144:147], v[36:39]\n"
    "v_mfma_f32_16x16x32_bf16 v[80:83], a[228:231], a[148:151], v[80:83]\n"
    "v_mfma_f32_16x16x32_bf16 v[80:83], a[232:235], a[152:155], v[80:83]\n"
    "v_mfma_f32_16x16x32_bf16 v[80:83], a[236:239], a[156:159], v[80:83]\n"
    "v_mfma_f32_16x16x32_bf16 v[84:87], a[240:243], a[144:147], v[36:39]\n"
    "v_mfma_f32_16x16x32_bf16 v[84:87], a[244:247], a[148:151], v[84:87]\n"
    "v_mfma_f32_16x16x32_bf16 v[84:87], a[248:251], a[152:155], v[84:87]\n"
    "v_mfma_f32_16x16x32_bf16 v[84:87], a[252:255], a[156:159], v[84:87]\n"
    "s_nop 3\n"
    "v_exp_f32 v80, v80\n"
    "v_exp_f32 v81, v81\n"
    "v_exp_f32 v82, v82\n"
    "v_exp_f32 v83, v83\n"
    "v_exp_f32 v84, v84\n"
    "v_exp_f32 v85, v85\n"
    "v_exp_f32 v86, v86\n"
    "v_exp_f32 v87, v87\n"
    "v_cvt_pk_bf16_f32 v156, v80, v81\n"
    "v_cvt_pk_bf16_f32 v157, v82, v83\n"
    "v_cvt_pk_bf16_f32 v158, v84, v85\n"
    "v_cvt_pk_bf16_f32 v159, v86, v87\n"
    "v_mfma_f32_16x16x32_bf16 v[52:55], v[64:67], v[144:147], 0\n"
    "v_mfma_f32_16x16x32_bf16 v[52:55], v[64:67], v[148:151], v[52:55]\n"
    "v_mfma_f32_16x16x32_bf16 v[52:55], v[64:67], v[152:155], v[52:55]\n"
    "v_mfma_f32_16x16x32_bf16 v[52:55], v[64:67], v[156:159], v[52:55]\n"
    "v_sub_f32 v104, v104, v9\n"
    "v_sub_f32 v105, v105, v9\n"
    "v_sub_f32 v106, v106, v9\n"
    "v_sub_f32 v107, v107, v9\n"
    "v_sub_f32 v11, 0, v9\n"
    "v_exp_f32 v11, v11\n"
    "s_nop 0\n"
    "v_mul_f32 v69, v11, v69\n"
    "ds_bpermute_b32 v12, v99, v11 offset:0\n"
    "ds_bpermute_b32 v13, v99, v11 offset:4\n"
    "ds_bpermute_b32 v14, v99, v11 offset:8\n"
    "ds_bpermute_b32 v15, v99, v11 offset:12\n"
    "s_waitcnt lgkmcnt(0)\n"
    "v_accvgpr_read_b32 v80, a4\n"
    "v_accvgpr_read_b32 v81, a5\n"
    "v_accvgpr_read_b32 v82, a6\n"
    "v_accvgpr_read_b32 v83, a7\n"
    "v_accvgpr_read_b32 v84, a20\n"
    "v_accvgpr_read_b32 v85, a21\n"
    "v_accvgpr_read_b32 v86, a22\n"
    "v_accvgpr_read_b32 v87, a23\n"
    "v_mul_f32 v80, v12, v80\n"
    "v_mul_f32 v81, v13, v81\n"
    "v_mul_f32 v82, v14, v82\n"
    "v_mul_f32 v83, v15, v83\n"
    "v_mul_f32 v84, v12, v84\n"
    "v_mul_f32 v85, v13, v85\n"
    "v_mul_f32 v86, v14, v86\n"
    "v_mul_f32 v87, v15, v87\n"
    "v_accvgpr_write_b32 a4, v80\n"
    "v_accvgpr_write_b32 a5, v81\n"
    "v_accvgpr_write_b32 a6, v82\n"
    "v_accvgpr_write_b32 a7, v83\n"
    "v_accvgpr_write_b32 a20, v84\n"
    "v_accvgpr_write_b32 a21, v85\n"
    "v_accvgpr_write_b32 a22, v86\n"
    "v_accvgpr_write_b32 a23, v87\n"
    "v_accvgpr_read_b32 v80, a36\n"
    "v_accvgpr_read_b32 v81, a37\n"
    "v_accvgpr_read_b32 v82, a38\n"
    "v_accvgpr_read_b32 v83, a39\n"
    "v_accvgpr_read_b32 v84, a52\n"
    "v_accvgpr_read_b32 v85, a53\n"
    "v_accvgpr_read_b32 v86, a54\n"
    "v_accvgpr_read_b32 v87, a55\n"
    "v_mul_f32 v80, v12, v80\n"
    "v_mul_f32 v81, v13, v81\n"
    "v_mul_f32 v82, v14, v82\n"
    "v_mul_f32 v83, v15, v83\n"
    "v_mul_f32 v84, v12, v84\n"
    "v_mul_f32 v85, v13, v85\n"
    "v_mul_f32 v86, v14, v86\n"
    "v_mul_f32 v87, v15, v87\n"
    "v_accvgpr_write_b32 a36, v80\n"
    "v_accvgpr_write_b32 a37, v81\n"
    "v_accvgpr_write_b32 a38, v82\n"
    "v_accvgpr_write_b32 a39, v83\n"
    "v_accvgpr_write_b32 a52, v84\n"
    "v_accvgpr_write_b32 a53, v85\n"
    "v_accvgpr_write_b32 a54, v86\n"
    "v_accvgpr_write_b32 a55, v87\n"
    "v_accvgpr_read_b32 v80, a68\n"
    "v_accvgpr_read_b32 v81, a69\n"
    "v_accvgpr_read_b32 v82, a70\n"
    "v_accvgpr_read_b32 v83, a71\n"
    "v_accvgpr_read_b32 v84, a84\n"
    "v_accvgpr_read_b32 v85, a85\n"
    "v_accvgpr_read_b32 v86, a86\n"
    "v_accvgpr_read_b32 v87, a87\n"
    "v_mul_f32 v80, v12, v80\n"
    "v_mul_f32 v81, v13, v81\n"
    "v_mul_f32 v82, v14, v82\n"
    "v_mul_f32 v83, v15, v83\n"
    "v_mul_f32 v84, v12, v84\n"
    "v_mul_f32 v85, v13, v85\n"
    "v_mul_f32 v86, v14, v86\n"
    "v_mul_f32 v87, v15, v87\n"
    "v_accvgpr_write_b32 a68, v80\n"
    "v_accvgpr_write_b32 a69, v81\n"
    "v_accvgpr_write_b32 a70, v82\n"
    "v_accvgpr_write_b32 a71, v83\n"
    "v_accvgpr_write_b32 a84, v84\n"
    "v_accvgpr_write_b32 a85, v85\n"
    "v_accvgpr_write_b32 a86, v86\n"
    "v_accvgpr_write_b32 a87, v87\n"
    "v_accvgpr_read_b32 v80, a100\n"
    "v_accvgpr_read_b32 v81, a101\n"
    "v_accvgpr_read_b32 v82, a102\n"
    "v_accvgpr_read_b32 v83, a103\n"
    "v_accvgpr_read_b32 v84, a116\n"
    "v_accvgpr_read_b32 v85, a117\n"
    "v_accvgpr_read_b32 v86, a118\n"
    "v_accvgpr_read_b32 v87, a119\n"
    "v_mul_f32 v80, v12, v80\n"
    "v_mul_f32 v81, v13, v81\n"
    "v_mul_f32 v82, v14, v82\n"
    "v_mul_f32 v83, v15, v83\n"
    "v_mul_f32 v84, v12, v84\n"
    "v_mul_f32 v85, v13, v85\n"
    "v_mul_f32 v86, v14, v86\n"
    "v_mul_f32 v87, v15, v87\n"
    "v_accvgpr_write_b32 a100, v80\n"
    "v_accvgpr_write_b32 a101, v81\n"
    "v_accvgpr_write_b32 a102, v82\n"
    "v_accvgpr_write_b32 a103, v83\n"
    "v_accvgpr_write_b32 a116, v84\n"
    "v_accvgpr_write_b32 a117, v85\n"
    "v_accvgpr_write_b32 a118, v86\n"
    "v_accvgpr_write_b32 a119, v87\n"
    "s_nop 7\n"
    "s_nop 7\n"
    "s_branch L_ret_E1\n"
    "L_rare_E2:\n"
    "s_nop 7\n"
    "s_nop 7\n"
    "s_sub_u32 s56, s48, 0x20000\n"
    "s_add_u32 s57, s56, 0x0\n"
    "s_add_u32 s58, s56, 0x1000\n"
    "buffer_load_dwordx4 a[224:227], v76, s[40:43], s57 offen offset:0 sc1\n"
    "buffer_load_dwordx4 a[228:231], v76, s[40:43], s57 offen offset:1024 sc1\n"
    "buffer_load_dwordx4 a[232:235], v76, s[40:43], s57 offen offset:2048 sc1\n"
    "buffer_load_dwordx4 a[236:239], v76, s[40:43], s57 offen offset:3072 sc1\n"
    "buffer_load_dwordx4 a[240:243], v76, s[40:43], s58 offen offset:0 sc1\n"
    "buffer_load_dwordx4 a[244:247], v76, s[40:43], s58 offen offset:1024 sc1\n"
    "buffer_load_dwordx4 a[248:251], v76, s[40:43], s58 offen offset:2048 sc1\n"
    "buffer_load_dwordx4 a[252:255], v76, s[40:43], s58 offen offset:3072 sc1\n"
    "s_waitcnt vmcnt(0)\n"
    "v_mfma_f32_16x16x32_bf16 v[80:83], a[224:227], a[160:163], v[40:43]\n"
    "v_mfma_f32_16x16x32_bf16 v[80:83], a[228:231], a[164:167], v[80:83]\n"
    "v_mfma_f32_16x16x32_bf16 v[80:83], a[232:235], a[168:171], v[80:83]\n"
    "v_mfma_f32_16x16x32_bf16 v[80:83], a[236:239], a[172:175], v[80:83]\n"
    "v_mfma_f32_16x16x32_bf16 v[84:87], a[240:243], a[160:163], v[40:43]\n"
    "v_mfma_f32_16x16x32_bf16 v[84:87], a[244:247], a[164:167], v[84:87]\n"
    "v_mfma_f32_16x16x32_bf16 v[84:87], a[248:251], a[168:171], v[84:87]\n"
    "v_mfma_f32_16x16x32_bf16 v[84:87], a[252:255], a[172:175], v[84:87]\n"
    "s_nop 3\n"
    "v_max_f32 v9, v80, v80\n"
    "v_max3_f32 v9, v9, v80, v81\n"
    "v_max3_f32 v9, v9, v82, v83\n"
    "s_nop 0\n"
    "v_max3_f32 v9, v9, v84, v85\n"
    "v_max3_f32 v9, v9, v86, v87\n"
    "s_add_u32 s57, s56, 0x2000\n"
    "s_add_u32 s58, s56, 0x3000\n"
    "buffer_load_dwordx4 a[224:227], v76, s[40:43], s57 offen offset:0 sc1\n"
    "buffer_load_dwordx4 a[228:231], v76, s[40:43], s57 offen offset:1024 sc1\n"
    "buffer_load_dwordx4 a[232:235], v76, s[40:43], s57 offen offset:2048 sc1\n"
    "buffer_load_dwordx4 a[236:239], v76, s[40:43], s57 offen offset:3072 sc1\n"
    "buffer_load_dwordx4 a[240:243], v76, s[40:43], s58 offen offset:0 sc1\n"
    "buffer_load_dwordx4 a[244:247], v76, s[40:43], s58 offen offset:1024 sc1\n"
    "buffer_load_dwordx4 a[248:251], v76, s[40:43], s58 offen offset:2048 sc1\n"
    "buffer_load_dwordx4 a[252:255], v76, s[40:43], s58 offen offset:3072 sc1\n"
    "s_waitcnt vmcnt(0)\n"
    "v_mfma_f32_16x16x32_bf16 v[80:83], a[224:227], a[160:163], v[40:43]\n"
    "v_mfma_f32_16x16x32_bf16 v[80:83], a[228:231], a[164:167], v[80:83]\n"
    "v_mfma_f32_16x16x32_bf16 v[80:83], a[232:235], a[168:171], v[80:83]\n"
    "v_mfma_f32_16x16x32_bf16 v[80:83], a[236:239], a[172:175], v[80:83]\n"
    "v_mfma_f32_16x16x32_bf16 v[84:87], a[240:243], a[160:163], v[40:43]\n"
    "v_mfma_f32_16x16x32_bf16 v[84:87], a[244:247], a[164:167], v[84:87]\n"
    "v_mfma_f32_16x16x32_bf16 v[84:87], a[248:251], a[168:171], v[84:87]\n"
    "v_mfma_f32_16x16x32_bf16 v[84:87], a[252:255], a[172:175], v[84:87]\n"
    "s_nop 3\n"
    "v_max3_f32 v9, v9, v80, v81\n"
    "v_max3_f32 v9, v9, v82, v83\n"
    "s_nop 1\n"
    "v_max3_f32 v9, v9, v84, v85\n"
    "v_max3_f32 v9, v9, v86, v87\n"
    "s_add_u32 s57, s56, 0x4000\n"
    "s_add_u32 s58, s56, 0x5000\n"
    "buffer_load_dwordx4 a[224:227], v76, s[40:43], s57 offen offset:0 sc1\n"
    "buffer_load_dwordx4 a[228:231], v76, s[40:43], s57 offen offset:1024 sc1\n"
    "buffer_load_dwordx4 a[232:235], v76, s[40:43], s57 offen offset:2048 sc1\n"
    "buffer_load_dwordx4 a[236:239], v76, s[40:43], s57 offen offset:3072 sc1\n"
    "buffer_load_dwordx4 a[240:243], v76, s[40:43], s58 offen offset:0 sc1\n"
    "buffer_load_dwordx4 a[244:247], v76, s[40:43], s58 offen offset:1024 sc1\n"
    "buffer_load_dwordx4 a[248:251], v76, s[40:43], s58 offen offset:2048 sc1\n"
    "buffer_load_dwordx4 a[252:255], v76, s[40:43], s58 offen offset:3072 sc1\n"
    "s_waitcnt vmcnt(0)\n"
    "v_mfma_f32_16x16x32_bf16 v[80:83], a[224:227], a[160:163], v[40:43]\n"
    "v_mfma_f32_16x16x32_bf16 v[80:83], a[228:231], a[164:167], v[80:83]\n"
    "v_mfma_f32_16x16x32_bf16 v[80:83], a[232:235], a[168:171], v[80:83]\n"
    "v_mfma_f32_16x16x32_bf16 v[80:83], a[236:239], a[172:175], v[80:83]\n"
    "v_mfma_f32_16x16x32_bf16 v[84:87], a[240:243], a[160:163], v[40:43]\n"
    "v_mfma_f32_16x16x32_bf16 v[84:87], a[244:247], a[164:167], v[84:87]\n"
    "v_mfma_f32_16x16x32_bf16 v[84:87], a[248:251], a[168:171], v[84:87]\n"
    "v_mfma_f32_16x16x32_bf16 v[84:87], a[252:255], a[172:175], v[84:87]\n"
    "s_nop 3\n"
    "v_max3_f32 v9, v9, v80, v81\n"
    "v_max3_f32 v9, v9, v82, v83\n"
    "s_nop 1\n"
    "v_max3_f32 v9, v9, v84, v85\n"
    "v_max3_f32 v9, v9, v86, v87\n"
    "s_add_u32 s57, s56, 0x6000\n"
    "s_add_u32 s58, s56, 0x7000\n"
    "buffer_load_dwordx4 a[224:227], v76, s[40:43], s57 offen offset:0 sc1\n"
    "buffer_load_dwordx4 a[228:231], v76, s[40:43], s57 offen offset:1024 sc1\n"
    "buffer_load_dwordx4 a[232:235], v76, s[40:43], s57 offen offset:2048 sc1\n"
    "buffer_load_dwordx4 a[236:239], v76, s[40:43], s57 offen offset:3072 sc1\n"
    "buffer_load_dwordx4 a[240:243], v76, s[40:43], s58 offen offset:0 sc1\n"
    "buffer_load_dwordx4 a[244:247], v76, s[40:43], s58 offen offset:1024 sc1\n"
    "buffer_load_dwordx4 a[248:251], v76, s[40:43], s58 offen offset:2048 sc1\n"
    "buffer_load_dwordx4 a[252:255], v76, s[40:43], s58 offen offset:3072 sc1\n"
    "s_waitcnt vmcnt(0)\n"
    "v_mfma_f32_16x16x32_bf16 v[80:83], a[224:227], a[160:163], v[40:43]\n"
    "v_mfma_f32_16x16x32_bf16 v[80:83], a[228:231], a[164:167], v[80:83]\n"
    "v_mfma_f32_16x16x32_bf16 v[80:83], a[232:235], a[168:171], v[80:83]\n"
    "v_mfma_f32_16x16x32_bf16 v[80:83], a[236:239], a[172:175], v[80:83]\n"
    "v_mfma_f32_16x16x32_bf16 v[84:87], a[240:243], a[160:163], v[40:43]\n"
    "v_mfma_f32_16x16x32_bf16 v[84:87], a[244:247], a[164:167], v[84:87]\n"
    "v_mfma_f32_16x16x32_bf16 v[84:87], a[248:251], a[168:171], v[84:87]\n"
    "v_mfma_f32_16x16x32_bf16 v[84:87], a[252:255], a[172:175], v[84:87]\n"
    "s_nop 3\n"
    "v_max3_f32 v9, v9, v80, v81\n"
    "v_max3_f32 v9, v9, v82, v83\n"
    "s_nop 1\n"
    "v_max3_f32 v9, v9, v84, v85\n"
    "v_max3_f32 v9, v9, v86, v87\n"
    "v_mov_b32 v10, v9\n"
    "s_nop 1\n"
    "v_permlane16_swap_b32 v10, v9\n"
    "v_max_f32 v9, v10, v9\n"
    "v_mov_b32 v10, v9\n"
    "s_nop 1\n"
    "v_permlane32_swap_b32 v10, v9\n"
    "v_max_f32 v9, v10, v9\n"
    "v_max_f32 v9, 0, v9\n"
    "v_sub_f32 v40, v40, v9\n"
    "v_sub_f32 v41, v41, v9\n"
    "v_sub_f32 v42, v42, v9\n"
    "v_sub_f32 v43, v43, v9\n"
    "s_add_u32 s57, s56, 0x0\n"
    "s_add_u32 s58, s56, 0x1000\n"
    "buffer_load_dwordx4 a[224:227], v76, s[40:43], s57 offen offset:0 sc1\n"
    "buffer_load_dwordx4 a[228:231], v76, s[40:43], s57 offen offset:1024 sc1\n"
    "buffer_load_dwordx4 a[232:235], v76, s[40:43], s57 offen offset:2048 sc1\n"
    "buffer_load_dwordx4 a[236:239], v76, s[40:43], s57 offen offset:3072 sc1\n"
    "buffer_load_dwordx4 a[240:243], v76, s[40:43], s58 offen offset:0 sc1\n"
    "buffer_load_dwordx4 a[244:247], v76, s[40:43], s58 offen offset:1024 sc1\n"
    "buffer_load_dwordx4 a[248:251], v76, s[40:43], s58 offen offset:2048 sc1\n"
    "buffer_load_dwordx4 a[252:255], v76, s[40:43], s58 offen offset:3072 sc1\n"
    "s_waitcnt vmcnt(0)\n"
    "v_mfma_f32_16x16x32_bf16 v[80:83], a[224:227], a[160:163], v[40:43]\n"
    "v_mfma_f32_16x16x32_bf16 v[80:83], a[228:231], a[164:167], v[80:83]\n"
    "v_mfma_f32_16x16x32_bf16 v[80:83], a[232:235], a[168:171], v[80:83]\n"
    "v_mfma_f32_16x16x32_bf16 v[80:83], a[236:239], a[172:175], v[80:83]\n"
    "v_mfma_f32_16x16x32_bf16 v[84:87], a[240:243], a[160:163], v[40:43]\n"
    "v_mfma_f32_16x16x32_bf16 v[84:87], a[244:247], a[164:167], v[84:87]\n"
    "v_mfma_f32_16x16x32_bf16 v[84:87], a[248:251], a[168:171], v[84:87]\n"
    "v_mfma_f32_16x16x32_bf16 v[84:87], a[252:255], a[172:175], v[84:87]\n"
    "s_nop 3\n"
    "v_exp_f32 v80, v80\n"
    "v_exp_f32 v81, v81\n"
    "v_exp_f32 v82, v82\n"
    "v_exp_f32 v83, v83\n"
    "v_exp_f32 v84, v84\n"
    "v_exp_f32 v85, v85\n"
    "v_exp_f32 v86, v86\n"
    "v_exp_f32 v87, v87\n"
    "v_cvt_pk_bf16_f32 v160, v80, v81\n"
    "v_cvt_pk_bf16_f32 v161, v82, v83\n"
    "v_cvt_pk_bf16_f32 v162, v84, v85\n"
    "v_cvt_pk_bf16_f32 v163, v86, v87\n"
    "s_add_u32 s57, s56, 0x2000\n"
    "s_add_u32 s58, s56, 0x3000\n"
    "buffer_load_dwordx4 a[224:227], v76, s[40:43], s57 offen offset:0 sc1\n"
    "buffer_load_dwordx4 a[228:231], v76, s[40:43], s57 offen offset:1024 sc1\n"
    "buffer_load_dwordx4 a[232:235], v76, s[40:43], s57 offen offset:2048 sc1\n"
    "buffer_load_dwordx4 a[236:239], v76, s[40:43], s57 offen offset:3072 sc1\n"
    "buffer_load_dwordx4 a[240:243], v76, s[40:43], s58 offen offset:0 sc1\n"
    "buffer_load_dwordx4 a[244:247], v76, s[40:43], s58 offen offset:1024 sc1\n"
    "buffer_load_dwordx4 a[248:251], v76, s[40:43], s58 offen offset:2048 sc1\n"
    "buffer_load_dwordx4 a[252:255], v76, s[40:43], s58 offen offset:3072 sc1\n"
    "s_waitcnt vmcnt(0)\n"
    "v_mfma_f32_16x16x32_bf16 v[80:83], a[224:227], a[160:163], v[40:43]\n"
    "v_mfma_f32_16x16x32_bf16 v[80:83], a[228:231], a[164:167], v[80:83]\n"
    "v_mfma_f32_16x16x32_bf16 v[80:83], a[232:235], a[168:171], v[80:83]\n"
    "v_mfma_f32_16x16x32_bf16 v[80:83], a[236:239], a[172:175], v[80:83]\n"
    "v_mfma_f32_16x16x32_bf16 v[84:87], a[240:243], a[160:163], v[40:43]\n"
    "v_mfma_f32_16x16x32_bf16 v[84:87], a[244:247], a[164:167], v[84:87]\n"
    "v_mfma_f32_16x16x32_bf16 v[84:87], a[248:251], a[168:171], v[84:87]\n"
    "v_mfma_f32_16x16x32_bf16 v[84:87], a[252:255], a[172:175], v[84:87]\n"
    "s_nop 3\n"
    "v_exp_f32 v80, v80\n"
    "v_exp_f32 v81, v81\n"
    "v_exp_f32 v82, v82\n"
    "v_exp_f32 v83, v83\n"
    "v_exp_f32 v84, v84\n"
    "v_exp_f32 v85, v85\n"
    "v_exp_f32 v86, v86\n"
    "v_exp_f32 v87, v87\n"
    "v_cvt_pk_bf16_f32 v164, v80, v81\n"
    "v_cvt_pk_bf16_f32 v165, v82, v83\n"
    "v_cvt_pk_bf16_f32 v166, v84, v85\n"
    "v_cvt_pk_bf16_f32 v167, v86, v87\n"
    "s_add_u32 s57, s56, 0x4000\n"
    "s_add_u32 s58, s56, 0x5000\n"
    "buffer_load_dwordx4 a[224:227], v76, s[40:43], s57 offen offset:0 sc1\n"
    "buffer_load_dwordx4 a[228:231], v76, s[40:43], s57 offen offset:1024 sc1\n"
    "buffer_load_dwordx4 a[232:235], v76, s[40:43], s57 offen offset:2048 sc1\n"
    "buffer_load_dwordx4 a[236:239], v76, s[40:43], s57 offen offset:3072 sc1\n"
    "buffer_load_dwordx4 a[240:243], v76, s[40:43], s58 offen offset:0 sc1\n"
    "buffer_load_dwordx4 a[244:247], v76, s[40:43], s58 offen offset:1024 sc1\n"
    "buffer_load_dwordx4 a[248:251], v76, s[40:43], s58 offen offset:2048 sc1\n"
    "buffer_load_dwordx4 a[252:255], v76, s[40:43], s58 offen offset:3072 sc1\n"
    "s_waitcnt vmcnt(0)\n"
    "v_mfma_f32_16x16x32_bf16 v[80:83], a[224:227], a[160:163], v[40:43]\n"
    "v_mfma_f32_16x16x32_bf16 v[80:83], a[228:231], a[164:167], v[80:83]\n"
    "v_mfma_f32_16x16x32_bf16 v[80:83], a[232:235], a[168:171], v[80:83]\n"
    "v_mfma_f32_16x16x32_bf16 v[80:83], a[236:239], a[172:175], v[80:83]\n"
    "v_mfma_f32_16x16x32_bf16 v[84:87], a[240:243], a[160:163], v[40:43]\n"
    "v_mfma_f32_16x16x32_bf16 v[84:87], a[244:247], a[164:167], v[84:87]\n"
    "v_mfma_f32_16x16x32_bf16 v[84:87], a[248:251], a[168:171], v[84:87]\n"
    "v_mfma_f32_16x16x32_bf16 v[84:87], a[252:255], a[172:175], v[84:87]\n"
    "s_nop 3\n"
    "v_exp_f32 v80, v80\n"
    "v_exp_f32 v81, v81\n"
    "v_exp_f32 v82, v82\n"
    "v_exp_f32 v83, v83\n"
    "v_exp_f32 v84, v84\n"
    "v_exp_f32 v85, v85\n"
    "v_exp_f32 v86, v86\n"
    "v_exp_f32 v87, v87\n"
    "v_cvt_pk_bf16_f32 v168, v80, v81\n"
    "v_cvt_pk_bf16_f32 v169, v82, v83\n"
    "v_cvt_pk_bf16_f32 v170, v84, v85\n"
    "v_cvt_pk_bf16_f32 v171, v86, v87\n"
    "s_add_u32 s57, s56, 0x6000\n"
    "s_add_u32 s58, s56, 0x7000\n"
    "buffer_load_dwordx4 a[224:227], v76, s[40:43], s57 offen offset:0 sc1\n"
    "buffer_load_dwordx4 a[228:231], v76, s[40:43], s57 offen offset:1024 sc1\n"
    "buffer_load_dwordx4 a[232:235], v76, s[40:43], s57 offen offset:2048 sc1\n"
    "buffer_load_dwordx4 a[236:239], v76, s[40:43], s57 offen offset:3072 sc1\n"
    "buffer_load_dwordx4 a[240:243], v76, s[40:43], s58 offen offset:0 sc1\n"
    "buffer_load_dwordx4 a[244:247], v76, s[40:43], s58 offen offset:1024 sc1\n"
    "buffer_load_dwordx4 a[248:251], v76, s[40:43], s58 offen offset:2048 sc1\n"
    "buffer_load_dwordx4 a[252:255], v76, s[40:43], s58 offen offset:3072 sc1\n"
    "s_waitcnt vmcnt(0)\n"
    "v_mfma_f32_16x16x32_bf16 v[80:83], a[224:227], a[160:163], v[40:43]\n"
    "v_mfma_f32_16x16x32_bf16 v[80:83], a[228:231], a[164:167], v[80:83]\n"
    "v_mfma_f32_16x16x32_bf16 v[80:83], a[232:235], a[168:171], v[80:83]\n"
    "v_mfma_f32_16x16x32_bf16 v[80:83], a[236:239], a[172:175], v[80:83]\n"
    "v_mfma_f32_16x16x32_bf16 v[84:87], a[240:243], a[160:163], v[40:43]\n"
    "v_mfma_f32_16x16x32_bf16 v[84:87], a[244:247], a[164:167], v[84:87]\n"
    "v_mfma_f32_16x16x32_bf16 v[84:87], a[248:251], a[168:171], v[84:87]\n"
    "v_mfma_f32_16x16x32_bf16 v[84:87], a[252:255], a[172:175], v[84:87]\n"
    "s_nop 3\n"
    "v_exp_f32 v80, v80\n"
    "v_exp_f32 v81, v81\n"
    "v_exp_f32 v82, v82\n"
    "v_exp_f32 v83, v83\n"
    "v_exp_f32 v84, v84\n"
    "v_exp_f32 v85, v85\n"
    "v_exp_f32 v86, v86\n"
    "v_exp_f32 v87, v87\n"
    "v_cvt_pk_bf16_f32 v172, v80, v81\n"
    "v_cvt_pk_bf16_f32 v173, v82, v83\n"
    "v_cvt_pk_bf16_f32 v174, v84, v85\n"
    "v_cvt_pk_bf16_f32 v175, v86, v87\n"
    "v_mfma_f32_16x16x32_bf16 v[56:59], v[64:67], v[160:163], 0\n"
    "v_mfma_f32_16x16x32_bf16 v[56:59], v[64:67], v[164:167], v[56:59]\n"
    "v_mfma_f32_16x16x32_bf16 v[56:59], v[64:67], v[168:171], v[56:59]\n"
    "v_mfma_f32_16x16x32_bf16 v[56:59], v[64:67], v[172:175], v[56:59]\n"
    "v_sub_f32 v108, v108, v9\n"
    "v_sub_f32 v109, v109, v9\n"
    "v_sub_f32 v110, v110, v9\n"
    "v_sub_f32 v111, v111, v9\n"
    "v_sub_f32 v11, 0, v9\n"
    "v_exp_f32 v11, v11\n"
    "s_nop 0\n"
    "v_mul_f32 v70, v11, v70\n"
    "ds_bpermute_b32 v12, v99, v11 offset:0\n"
    "ds_bpermute_b32 v13, v99, v11 offset:4\n"
    "ds_bpermute_b32 v14, v99, v11 offset:8\n"
    "ds_bpermute_b32 v15, v99, v11 offset:12\n"
    "s_waitcnt lgkmcnt(0)\n"
    "v_accvgpr_read_b32 v80, a8\n"
    "v_accvgpr_read_b32 v81, a9\n"
    "v_accvgpr_read_b32 v82, a10\n"
    "v_accvgpr_read_b32 v83, a11\n"
    "v_accvgpr_read_b32 v84, a24\n"
    "v_accvgpr_read_b32 v85, a25\n"
    "v_accvgpr_read_b32 v86, a26\n"
    "v_accvgpr_read_b32 v87, a27\n"
    "v_mul_f32 v80, v12, v80\n"
    "v_mul_f32 v81, v13, v81\n"
    "v_mul_f32 v82, v14, v82\n"
    "v_mul_f32 v83, v15, v83\n"
    "v_mul_f32 v84, v12, v84\n"
    "v_mul_f32 v85, v13, v85\n"
    "v_mul_f32 v86, v14, v86\n"
    "v_mul_f32 v87, v15, v87\n"
    "v_accvgpr_write_b32 a8, v80\n"
    "v_accvgpr_write_b32 a9, v81\n"
    "v_accvgpr_write_b32 a10, v82\n"
    "v_accvgpr_write_b32 a11, v83\n"
    "v_accvgpr_write_b32 a24, v84\n"
    "v_accvgpr_write_b32 a25, v85\n"
    "v_accvgpr_write_b32 a26, v86\n"
    "v_accvgpr_write_b32 a27, v87\n"
    "v_accvgpr_read_b32 v80, a40\n"
    "v_accvgpr_read_b32 v81, a41\n"
    "v_accvgpr_read_b32 v82, a42\n"
    "v_accvgpr_read_b32 v83, a43\n"
    "v_accvgpr_read_b32 v84, a56\n"
    "v_accvgpr_read_b32 v85, a57\n"
    "v_accvgpr_read_b32 v86, a58\n"
    "v_accvgpr_read_b32 v87, a59\n"
    "v_mul_f32 v80, v12, v80\n"
    "v_mul_f32 v81, v13, v81\n"
    "v_mul_f32 v82, v14, v82\n"
    "v_mul_f32 v83, v15, v83\n"
    "v_mul_f32 v84, v12, v84\n"
    "v_mul_f32 v85, v13, v85\n"
    "v_mul_f32 v86, v14, v86\n"
    "v_mul_f32 v87, v15, v87\n"
    "v_accvgpr_write_b32 a40, v80\n"
    "v_accvgpr_write_b32 a41, v81\n"
    "v_accvgpr_write_b32 a42, v82\n"
    "v_accvgpr_write_b32 a43, v83\n"
    "v_accvgpr_write_b32 a56, v84\n"
    "v_accvgpr_write_b32 a57, v85\n"
    "v_accvgpr_write_b32 a58, v86\n"
    "v_accvgpr_write_b32 a59, v87\n"
    "v_accvgpr_read_b32 v80, a72\n"
    "v_accvgpr_read_b32 v81, a73\n"
    "v_accvgpr_read_b32 v82, a74\n"
    "v_accvgpr_read_b32 v83, a75\n"
    "v_accvgpr_read_b32 v84, a88\n"
    "v_accvgpr_read_b32 v85, a89\n"
    "v_accvgpr_read_b32 v86, a90\n"
    "v_accvgpr_read_b32 v87, a91\n"
    "v_mul_f32 v80, v12, v80\n"
    "v_mul_f32 v81, v13, v81\n"
    "v_mul_f32 v82, v14, v82\n"
    "v_mul_f32 v83, v15, v83\n"
    "v_mul_f32 v84, v12, v84\n"
    "v_mul_f32 v85, v13, v85\n"
    "v_mul_f32 v86, v14, v86\n"
    "v_mul_f32 v87, v15, v87\n"
    "v_accvgpr_write_b32 a72, v80\n"
    "v_accvgpr_write_b32 a73, v81\n"
    "v_accvgpr_write_b32 a74, v82\n"
    "v_accvgpr_write_b32 a75, v83\n"
    "v_accvgpr_write_b32 a88, v84\n"
    "v_accvgpr_write_b32 a89, v85\n"
    "v_accvgpr_write_b32 a90, v86\n"
    "v_accvgpr_write_b32 a91, v87\n"
    "v_accvgpr_read_b32 v80, a104\n"
    "v_accvgpr_read_b32 v81, a105\n"
    "v_accvgpr_read_b32 v82, a106\n"
    "v_accvgpr_read_b32 v83, a107\n"
    "v_accvgpr_read_b32 v84, a120\n"
    "v_accvgpr_read_b32 v85, a121\n"
    "v_accvgpr_read_b32 v86, a122\n"
    "v_accvgpr_read_b32 v87, a123\n"
    "v_mul_f32 v80, v12, v80\n"
    "v_mul_f32 v81, v13, v81\n"
    "v_mul_f32 v82, v14, v82\n"
    "v_mul_f32 v83, v15, v83\n"
    "v_mul_f32 v84, v12, v84\n"
    "v_mul_f32 v85, v13, v85\n"
    "v_mul_f32 v86, v14, v86\n"
    "v_mul_f32 v87, v15, v87\n"
    "v_accvgpr_write_b32 a104, v80\n"
    "v_accvgpr_write_b32 a105, v81\n"
    "v_accvgpr_write_b32 a106, v82\n"
    "v_accvgpr_write_b32 a107, v83\n"
    "v_accvgpr_write_b32 a120, v84\n"
    "v_accvgpr_write_b32 a121, v85\n"
    "v_accvgpr_write_b32 a122, v86\n"
    "v_accvgpr_write_b32 a123, v87\n"
    "s_nop 7\n"
    "s_nop 7\n"
    "s_branch L_ret_E2\n"
    "L_rare_E3:\n"
    "s_nop 7\n"
    "s_nop 7\n"
    "s_sub_u32 s56, s48, 0x20000\n"
    "s_add_u32 s57, s56, 0x0\n"
    "s_add_u32 s58, s56, 0x1000\n"
    "buffer_load_dwordx4 a[224:227], v76, s[40:43], s57 offen offset:0 sc1\n"
    "buffer_load_dwordx4 a[228:231], v76, s[40:43], s57 offen offset:1024 sc1\n"
    "buffer_load_dwordx4 a[232:235], v76, s[40:43], s57 offen offset:2048 sc1\n"
    "buffer_load_dwordx4 a[236:239], v76, s[40:43], s57 offen offset:3072 sc1\n"
    "buffer_load_dwordx4 a[240:243], v76, s[40:43], s58 offen offset:0 sc1\n"
    "buffer_load_dwordx4 a[244:247], v76, s[40:43], s58 offen offset:1024 sc1\n"
    "buffer_load_dwordx4 a[248:251], v76, s[40:43], s58 offen offset:2048 sc1\n"
    "buffer_load_dwordx4 a[252:255], v76, s[40:43], s58 offen offset:3072 sc1\n"
    "s_waitcnt vmcnt(0)\n"
    "v_mfma_f32_16x16x32_bf16 v[80:83], a[224:227], a[176:179], v[44:47]\n"
    "v_mfma_f32_16x16x32_bf16 v[80:83], a[228:231], a[180:183], v[80:83]\n"
    "v_mfma_f32_16x16x32_bf16 v[80:83], a[232:235], a[184:187], v[80:83]\n"
    "v_mfma_f32_16x16x32_bf16 v[80:83], a[236:239], a[188:191], v[80:83]\n"
    "v_mfma_f32_16x16x32_bf16 v[84:87], a[240:243], a[176:179], v[44:47]\n"
    "v_mfma_f32_16x16x32_bf16 v[84:87], a[244:247], a[180:183], v[84:87]\n"
    "v_mfma_f32_16x16x32_bf16 v[84:87], a[248:251], a[184:187], v[84:87]\n"
    "v_mfma_f32_16x16x32_bf16 v[84:87], a[252:255], a[188:191], v[84:87]\n"
    "s_nop 3\n"
    "v_max_f32 v9, v80, v80\n"
    "v_max3_f32 v9, v9, v80, v81\n"
    "v_max3_f32 v9, v9, v82, v83\n"
    "s_nop 0\n"
    "v_max3_f32 v9, v9, v84, v85\n"
    "v_max3_f32 v9, v9, v86, v87\n"
    "s_add_u32 s57, s56, 0x2000\n"
    "s_add_u32 s58, s56, 0x3000\n"
    "buffer_load_dwordx4 a[224:227], v76, s[40:43], s57 offen offset:0 sc1\n"
    "buffer_load_dwordx4 a[228:231], v76, s[40:43], s57 offen offset:1024 sc1\n"
    "buffer_load_dwordx4 a[232:235], v76, s[40:43], s57 offen offset:2048 sc1\n"
    "buffer_load_dwordx4 a[236:239], v76, s[40:43], s57 offen offset:3072 sc1\n"
    "buffer_load_dwordx4 a[240:243], v76, s[40:43], s58 offen offset:0 sc1\n"
    "buffer_load_dwordx4 a[244:247], v76, s[40:43], s58 offen offset:1024 sc1\n"
    "buffer_load_dwordx4 a[248:251], v76, s[40:43], s58 offen offset:2048 sc1\n"
    "buffer_load_dwordx4 a[252:255], v76, s[40:43], s58 offen offset:3072 sc1\n"
    "s_waitcnt vmcnt(0)\n"
    "v_mfma_f32_16x16x32_bf16 v[80:83], a[224:227], a[176:179], v[44:47]\n"
    "v_mfma_f32_16x16x32_bf16 v[80:83], a[228:231], a[180:183], v[80:83]\n"
    "v_mfma_f32_16x16x32_bf16 v[80:83], a[232:235], a[184:187], v[80:83]\n"
    "v_mfma_f32_16x16x32_bf16 v[80:83], a[236:239], a[188:191], v[80:83]\n"
    "v_mfma_f32_16x16x32_bf16 v[84:87], a[240:243], a[176:179], v[44:47]\n"
    "v_mfma_f32_16x16x32_bf16 v[84:87], a[244:247], a[180:183], v[84:87]\n"
    "v_mfma_f32_16x16x32_bf16 v[84:87], a[248:251], a[184:187], v[84:87]\n"
    "v_mfma_f32_16x16x32_bf16 v[84:87], a[252:255], a[188:191], v[84:87]\n"
    "s_nop 3\n"
    "v_max3_f32 v9, v9, v80, v81\n"
    "v_max3_f32 v9, v9, v82, v83\n"
    "s_nop 1\n"
    "v_max3_f32 v9, v9, v84, v85\n"
    "v_max3_f32 v9, v9, v86, v87\n"
    "s_add_u32 s57, s56, 0x4000\n"
    "s_add_u32 s58, s56, 0x5000\n"
    "buffer_load_dwordx4 a[224:227], v76, s[40:43], s57 offen offset:0 sc1\n"
    "buffer_load_dwordx4 a[228:231], v76, s[40:43], s57 offen offset:1024 sc1\n"
    "buffer_load_dwordx4 a[232:235], v76, s[40:43], s57 offen offset:2048 sc1\n"
    "buffer_load_dwordx4 a[236:239], v76, s[40:43], s57 offen offset:3072 sc1\n"
    "buffer_load_dwordx4 a[240:243], v76, s[40:43], s58 offen offset:0 sc1\n"
    "buffer_load_dwordx4 a[244:247], v76, s[40:43], s58 offen offset:1024 sc1\n"
    "buffer_load_dwordx4 a[248:251], v76, s[40:43], s58 offen offset:2048 sc1\n"
    "buffer_load_dwordx4 a[252:255], v76, s[40:43], s58 offen offset:3072 sc1\n"
    "s_waitcnt vmcnt(0)\n"
    "v_mfma_f32_16x16x32_bf16 v[80:83], a[224:227], a[176:179], v[44:47]\n"
    "v_mfma_f32_16x16x32_bf16 v[80:83], a[228:231], a[180:183], v[80:83]\n"
    "v_mfma_f32_16x16x32_bf16 v[80:83], a[232:235], a[184:187], v[80:83]\n"
    "v_mfma_f32_16x16x32_bf16 v[80:83], a[236:239], a[188:191], v[80:83]\n"
    "v_mfma_f32_16x16x32_bf16 v[84:87], a[240:243], a[176:179], v[44:47]\n"
    "v_mfma_f32_16x16x32_bf16 v[84:87], a[244:247], a[180:183], v[84:87]\n"
    "v_mfma_f32_16x16x32_bf16 v[84:87], a[248:251], a[184:187], v[84:87]\n"
    "v_mfma_f32_16x16x32_bf16 v[84:87], a[252:255], a[188:191], v[84:87]\n"
    "s_nop 3\n"
    "v_max3_f32 v9, v9, v80, v81\n"
    "v_max3_f32 v9, v9, v82, v83\n"
    "s_nop 1\n"
    "v_max3_f32 v9, v9, v84, v85\n"
    "v_max3_f32 v9, v9, v86, v87\n"
    "s_add_u32 s57, s56, 0x6000\n"
    "s_add_u32 s58, s56, 0x7000\n"
    "buffer_load_dwordx4 a[224:227], v76, s[40:43], s57 offen offset:0 sc1\n"
    "buffer_load_dwordx4 a[228:231], v76, s[40:43], s57 offen offset:1024 sc1\n"
    "buffer_load_dwordx4 a[232:235], v76, s[40:43], s57 offen offset:2048 sc1\n"
    "buffer_load_dwordx4 a[236:239], v76, s[40:43], s57 offen offset:3072 sc1\n"
    "buffer_load_dwordx4 a[240:243], v76, s[40:43], s58 offen offset:0 sc1\n"
    "buffer_load_dwordx4 a[244:247], v76, s[40:43], s58 offen offset:1024 sc1\n"
    "buffer_load_dwordx4 a[248:251], v76, s[40:43], s58 offen offset:2048 sc1\n"
    "buffer_load_dwordx4 a[252:255], v76, s[40:43], s58 offen offset:3072 sc1\n"
    "s_waitcnt vmcnt(0)\n"
    "v_mfma_f32_16x16x32_bf16 v[80:83], a[224:227], a[176:179], v[44:47]\n"
    "v_mfma_f32_16x16x32_bf16 v[80:83], a[228:231], a[180:183], v[80:83]\n"
    "v_mfma_f32_16x16x32_bf16 v[80:83], a[232:235], a[184:187], v[80:83]\n"
    "v_mfma_f32_16x16x32_bf16 v[80:83], a[236:239], a[188:191], v[80:83]\n"
    "v_mfma_f32_16x16x32_bf16 v[84:87], a[240:243], a[176:179], v[44:47]\n"
    "v_mfma_f32_16x16x32_bf16 v[84:87], a[244:247], a[180:183], v[84:87]\n"
    "v_mfma_f32_16x16x32_bf16 v[84:87], a[248:251], a[184:187], v[84:87]\n"
    "v_mfma_f32_16x16x32_bf16 v[84:87], a[252:255], a[188:191], v[84:87]\n"
    "s_nop 3\n"
    "v_max3_f32 v9, v9, v80, v81\n"
    "v_max3_f32 v9, v9, v82, v83\n"
    "s_nop 1\n"
    "v_max3_f32 v9, v9, v84, v85\n"
    "v_max3_f32 v9, v9, v86, v87\n"
    "v_mov_b32 v10, v9\n"
    "s_nop 1\n"
    "v_permlane16_swap_b32 v10, v9\n"
    "v_max_f32 v9, v10, v9\n"
    "v_mov_b32 v10, v9\n"
    "s_nop 1\n"
    "v_permlane32_swap_b32 v10, v9\n"
    "v_max_f32 v9, v10, v9\n"
    "v_max_f32 v9, 0, v9\n"
    "v_sub_f32 v44, v44, v9\n"
    "v_sub_f32 v45, v45, v9\n"
    "v_sub_f32 v46, v46, v9\n"
    "v_sub_f32 v47, v47, v9\n"
    "s_add_u32 s57, s56, 0x0\n"
    "s_add_u32 s58, s56, 0x1000\n"
    "buffer_load_dwordx4 a[224:227], v76, s[40:43], s57 offen offset:0 sc1\n"
    "buffer_load_dwordx4 a[228:231], v76, s[40:43], s57 offen offset:1024 sc1\n"
    "buffer_load_dwordx4 a[232:235], v76, s[40:43], s57 offen offset:2048 sc1\n"
    "buffer_load_dwordx4 a[236:239], v76, s[40:43], s57 offen offset:3072 sc1\n"
    "buffer_load_dwordx4 a[240:243], v76, s[40:43], s58 offen offset:0 sc1\n"
    "buffer_load_dwordx4 a[244:247], v76, s[40:43], s58 offen offset:1024 sc1\n"
    "buffer_load_dwordx4 a[248:251], v76, s[40:43], s58 offen offset:2048 sc1\n"
    "buffer_load_dwordx4 a[252:255], v76, s[40:43], s58 offen offset:3072 sc1\n"
    "s_waitcnt vmcnt(0)\n"
    "v_mfma_f32_16x16x32_bf16 v[80:83], a[224:227], a[176:179], v[44:47]\n"
    "v_mfma_f32_16x16x32_bf16 v[80:83], a[228:231], a[180:183], v[80:83]\n"
    "v_mfma_f32_16x16x32_bf16 v[80:83], a[232:235], a[184:187], v[80:83]\n"
    "v_mfma_f32_16x16x32_bf16 v[80:83], a[236:239], a[188:191], v[80:83]\n"
    "v_mfma_f32_16x16x32_bf16 v[84:87], a[240:243], a[176:179], v[44:47]\n"
    "v_mfma_f32_16x16x32_bf16 v[84:87], a[244:247], a[180:183], v[84:87]\n"
    "v_mfma_f32_16x16x32_bf16 v[84:87], a[248:251], a[184:187], v[84:87]\n"
    "v_mfma_f32_16x16x32_bf16 v[84:87], a[252:255], a[188:191], v[84:87]\n"
    "s_nop 3\n"
    "v_exp_f32 v80, v80\n"
    "v_exp_f32 v81, v81\n"
    "v_exp_f32 v82, v82\n"
    "v_exp_f32 v83, v83\n"
    "v_exp_f32 v84, v84\n"
    "v_exp_f32 v85, v85\n"
    "v_exp_f32 v86, v86\n"
    "v_exp_f32 v87, v87\n"
    "v_cvt_pk_bf16_f32 v176, v80, v81\n"
    "v_cvt_pk_bf16_f32 v177, v82, v83\n"
    "v_cvt_pk_bf16_f32 v178, v84, v85\n"
    "v_cvt_pk_bf16_f32 v179, v86, v87\n"
    "s_add_u32 s57, s56, 0x2000\n"
    "s_add_u32 s58, s56, 0x3000\n"
    "buffer_load_dwordx4 a[224:227], v76, s[40:43], s57 offen offset:0 sc1\n"
    "buffer_load_dwordx4 a[228:231], v76, s[40:43], s57 offen offset:1024 sc1\n"
    "buffer_load_dwordx4 a[232:235], v76, s[40:43], s57 offen offset:2048 sc1\n"
    "buffer_load_dwordx4 a[236:239], v76, s[40:43], s57 offen offset:3072 sc1\n"
    "buffer_load_dwordx4 a[240:243], v76, s[40:43], s58 offen offset:0 sc1\n"
    "buffer_load_dwordx4 a[244:247], v76, s[40:43], s58 offen offset:1024 sc1\n"
    "buffer_load_dwordx4 a[248:251], v76, s[40:43], s58 offen offset:2048 sc1\n"
    "buffer_load_dwordx4 a[252:255], v76, s[40:43], s58 offen offset:3072 sc1\n"
    "s_waitcnt vmcnt(0)\n"
    "v_mfma_f32_16x16x32_bf16 v[80:83], a[224:227], a[176:179], v[44:47]\n"
    "v_mfma_f32_16x16x32_bf16 v[80:83], a[228:231], a[180:183], v[80:83]\n"
    "v_mfma_f32_16x16x32_bf16 v[80:83], a[232:235], a[184:187], v[80:83]\n"
    "v_mfma_f32_16x16x32_bf16 v[80:83], a[236:239], a[188:191], v[80:83]\n"
    "v_mfma_f32_16x16x32_bf16 v[84:87], a[240:243], a[176:179], v[44:47]\n"
    "v_mfma_f32_16x16x32_bf16 v[84:87], a[244:247], a[180:183], v[84:87]\n"
    "v_mfma_f32_16x16x32_bf16 v[84:87], a[248:251], a[184:187], v[84:87]\n"
    "v_mfma_f32_16x16x32_bf16 v[84:87], a[252:255], a[188:191], v[84:87]\n"
    "s_nop 3\n"
    "v_exp_f32 v80, v80\n"
    "v_exp_f32 v81, v81\n"
    "v_exp_f32 v82, v82\n"
    "v_exp_f32 v83, v83\n"
    "v_exp_f32 v84, v84\n"
    "v_exp_f32 v85, v85\n"
    "v_exp_f32 v86, v86\n"
    "v_exp_f32 v87, v87\n"
    "v_cvt_pk_bf16_f32 v180, v80, v81\n"
    "v_cvt_pk_bf16_f32 v181, v82, v83\n"
    "v_cvt_pk_bf16_f32 v182, v84, v85\n"
    "v_cvt_pk_bf16_f32 v183, v86, v87\n"
    "s_add_u32 s57, s56, 0x4000\n"
    "s_add_u32 s58, s56, 0x5000\n"
    "buffer_load_dwordx4 a[224:227], v76, s[40:43], s57 offen offset:0 sc1\n"
    "buffer_load_dwordx4 a[228:231], v76, s[40:43], s57 offen offset:1024 sc1\n"
    "buffer_load_dwordx4 a[232:235], v76, s[40:43], s57 offen offset:2048 sc1\n"
    "buffer_load_dwordx4 a[236:239], v76, s[40:43], s57 offen offset:3072 sc1\n"
    "buffer_load_dwordx4 a[240:243], v76, s[40:43], s58 offen offset:0 sc1\n"
    "buffer_load_dwordx4 a[244:247], v76, s[40:43], s58 offen offset:1024 sc1\n"
    "buffer_load_dwordx4 a[248:251], v76, s[40:43], s58 offen offset:2048 sc1\n"
    "buffer_load_dwordx4 a[252:255], v76, s[40:43], s58 offen offset:3072 sc1\n"
    "s_waitcnt vmcnt(0)\n"
    "v_mfma_f32_16x16x32_bf16 v[80:83], a[224:227], a[176:179], v[44:47]\n"
    "v_mfma_f32_16x16x32_bf16 v[80:83], a[228:231], a[180:183], v[80:83]\n"
    "v_mfma_f32_16x16x32_bf16 v[80:83], a[232:235], a[184:187], v[80:83]\n"
    "v_mfma_f32_16x16x32_bf16 v[80:83], a[236:239], a[188:191], v[80:83]\n"
    "v_mfma_f32_16x16x32_bf16 v[84:87], a[240:243], a[176:179], v[44:47]\n"
    "v_mfma_f32_16x16x32_bf16 v[84:87], a[244:247], a[180:183], v[84:87]\n"
    "v_mfma_f32_16x16x32_bf16 v[84:87], a[248:251], a[184:187], v[84:87]\n"
    "v_mfma_f32_16x16x32_bf16 v[84:87], a[252:255], a[188:191], v[84:87]\n"
    "s_nop 3\n"
    "v_exp_f32 v80, v80\n"
    "v_exp_f32 v81, v81\n"
    "v_exp_f32 v82, v82\n"
    "v_exp_f32 v83, v83\n"
    "v_exp_f32 v84, v84\n"
    "v_exp_f32 v85, v85\n"
    "v_exp_f32 v86, v86\n"
    "v_exp_f32 v87, v87\n"
    "v_cvt_pk_bf16_f32 v184, v80, v81\n"
    "v_cvt_pk_bf16_f32 v185, v82, v83\n"
    "v_cvt_pk_bf16_f32 v186, v84, v85\n"
    "v_cvt_pk_bf16_f32 v187, v86, v87\n"
    "s_add_u32 s57, s56, 0x6000\n"
    "s_add_u32 s58, s56, 0x7000\n"
    "buffer_load_dwordx4 a[224:227], v76, s[40:43], s57 offen offset:0 sc1\n"
    "buffer_load_dwordx4 a[228:231], v76, s[40:43], s57 offen offset:1024 sc1\n"
    "buffer_load_dwordx4 a[232:235], v76, s[40:43], s57 offen offset:2048 sc1\n"
    "buffer_load_dwordx4 a[236:239], v76, s[40:43], s57 offen offset:3072 sc1\n"
    "buffer_load_dwordx4 a[240:243], v76, s[40:43], s58 offen offset:0 sc1\n"
    "buffer_load_dwordx4 a[244:247], v76, s[40:43], s58 offen offset:1024 sc1\n"
    "buffer_load_dwordx4 a[248:251], v76, s[40:43], s58 offen offset:2048 sc1\n"
    "buffer_load_dwordx4 a[252:255], v76, s[40:43], s58 offen offset:3072 sc1\n"
    "s_waitcnt vmcnt(0)\n"
    "v_mfma_f32_16x16x32_bf16 v[80:83], a[224:227], a[176:179], v[44:47]\n"
    "v_mfma_f32_16x16x32_bf16 v[80:83], a[228:231], a[180:183], v[80:83]\n"
    "v_mfma_f32_16x16x32_bf16 v[80:83], a[232:235], a[184:187], v[80:83]\n"
    "v_mfma_f32_16x16x32_bf16 v[80:83], a[236:239], a[188:191], v[80:83]\n"
    "v_mfma_f32_16x16x32_bf16 v[84:87], a[240:243], a[176:179], v[44:47]\n"
    "v_mfma_f32_16x16x32_bf16 v[84:87], a[244:247], a[180:183], v[84:87]\n"
    "v_mfma_f32_16x16x32_bf16 v[84:87], a[248:251], a[184:187], v[84:87]\n"
    "v_mfma_f32_16x16x32_bf16 v[84:87], a[252:255], a[188:191], v[84:87]\n"
    "s_nop 3\n"
    "v_exp_f32 v80, v80\n"
    "v_exp_f32 v81, v81\n"
    "v_exp_f32 v82, v82\n"
    "v_exp_f32 v83, v83\n"
    "v_exp_f32 v84, v84\n"
    "v_exp_f32 v85, v85\n"
    "v_exp_f32 v86, v86\n"
    "v_exp_f32 v87, v87\n"
    "v_cvt_pk_bf16_f32 v188, v80, v81\n"
    "v_cvt_pk_bf16_f32 v189, v82, v83\n"
    "v_cvt_pk_bf16_f32 v190, v84, v85\n"
    "v_cvt_pk_bf16_f32 v191, v86, v87\n"
    "v_mfma_f32_16x16x32_bf16 v[60:63], v[64:67], v[176:179], 0\n"
    "v_mfma_f32_16x16x32_bf16 v[60:63], v[64:67], v[180:183], v[60:63]\n"
    "v_mfma_f32_16x16x32_bf16 v[60:63], v[64:67], v[184:187], v[60:63]\n"
    "v_mfma_f32_16x16x32_bf16 v[60:63], v[64:67], v[188:191], v[60:63]\n"
    "v_sub_f32 v112, v112, v9\n"
    "v_sub_f32 v113, v113, v9\n"
    "v_sub_f32 v114, v114, v9\n"
    "v_sub_f32 v115, v115, v9\n"
    "v_sub_f32 v11, 0, v9\n"
    "v_exp_f32 v11, v11\n"
    "s_nop 0\n"
    "v_mul_f32 v71, v11, v71\n"
    "ds_bpermute_b32 v12, v99, v11 offset:0\n"
    "ds_bpermute_b32 v13, v99, v11 offset:4\n"
    "ds_bpermute_b32 v14, v99, v11 offset:8\n"
    "ds_bpermute_b32 v15, v99, v11 offset:12\n"
    "s_waitcnt lgkmcnt(0)\n"
    "v_accvgpr_read_b32 v80, a12\n"
    "v_accvgpr_read_b32 v81, a13\n"
    "v_accvgpr_read_b32 v82, a14\n"
    "v_accvgpr_read_b32 v83, a15\n"
    "v_accvgpr_read_b32 v84, a28\n"
    "v_accvgpr_read_b32 v85, a29\n"
    "v_accvgpr_read_b32 v86, a30\n"
    "v_accvgpr_read_b32 v87, a31\n"
    "v_mul_f32 v80, v12, v80\n"
    "v_mul_f32 v81, v13, v81\n"
    "v_mul_f32 v82, v14, v82\n"
    "v_mul_f32 v83, v15, v83\n"
    "v_mul_f32 v84, v12, v84\n"
    "v_mul_f32 v85, v13, v85\n"
    "v_mul_f32 v86, v14, v86\n"
    "v_mul_f32 v87, v15, v87\n"
    "v_accvgpr_write_b32 a12, v80\n"
    "v_accvgpr_write_b32 a13, v81\n"
    "v_accvgpr_write_b32 a14, v82\n"
    "v_accvgpr_write_b32 a15, v83\n"
    "v_accvgpr_write_b32 a28, v84\n"
    "v_accvgpr_write_b32 a29, v85\n"
    "v_accvgpr_write_b32 a30, v86\n"
    "v_accvgpr_write_b32 a31, v87\n"
    "v_accvgpr_read_b32 v80, a44\n"
    "v_accvgpr_read_b32 v81, a45\n"
    "v_accvgpr_read_b32 v82, a46\n"
    "v_accvgpr_read_b32 v83, a47\n"
    "v_accvgpr_read_b32 v84, a60\n"
    "v_accvgpr_read_b32 v85, a61\n"
    "v_accvgpr_read_b32 v86, a62\n"
    "v_accvgpr_read_b32 v87, a63\n"
    "v_mul_f32 v80, v12, v80\n"
    "v_mul_f32 v81, v13, v81\n"
    "v_mul_f32 v82, v14, v82\n"
    "v_mul_f32 v83, v15, v83\n"
    "v_mul_f32 v84, v12, v84\n"
    "v_mul_f32 v85, v13, v85\n"
    "v_mul_f32 v86, v14, v86\n"
    "v_mul_f32 v87, v15, v87\n"
    "v_accvgpr_write_b32 a44, v80\n"
    "v_accvgpr_write_b32 a45, v81\n"
    "v_accvgpr_write_b32 a46, v82\n"
    "v_accvgpr_write_b32 a47, v83\n"
    "v_accvgpr_write_b32 a60, v84\n"
    "v_accvgpr_write_b32 a61, v85\n"
    "v_accvgpr_write_b32 a62, v86\n"
    "v_accvgpr_write_b32 a63, v87\n"
    "v_accvgpr_read_b32 v80, a76\n"
    "v_accvgpr_read_b32 v81, a77\n"
    "v_accvgpr_read_b32 v82, a78\n"
    "v_accvgpr_read_b32 v83, a79\n"
    "v_accvgpr_read_b32 v84, a92\n"
    "v_accvgpr_read_b32 v85, a93\n"
    "v_accvgpr_read_b32 v86, a94\n"
    "v_accvgpr_read_b32 v87, a95\n"
    "v_mul_f32 v80, v12, v80\n"
    "v_mul_f32 v81, v13, v81\n"
    "v_mul_f32 v82, v14, v82\n"
    "v_mul_f32 v83, v15, v83\n"
    "v_mul_f32 v84, v12, v84\n"
    "v_mul_f32 v85, v13, v85\n"
    "v_mul_f32 v86, v14, v86\n"
    "v_mul_f32 v87, v15, v87\n"
    "v_accvgpr_write_b32 a76, v80\n"
    "v_accvgpr_write_b32 a77, v81\n"
    "v_accvgpr_write_b32 a78, v82\n"
    "v_accvgpr_write_b32 a79, v83\n"
    "v_accvgpr_write_b32 a92, v84\n"
    "v_accvgpr_write_b32 a93, v85\n"
    "v_accvgpr_write_b32 a94, v86\n"
    "v_accvgpr_write_b32 a95, v87\n"
    "v_accvgpr_read_b32 v80, a108\n"
    "v_accvgpr_read_b32 v81, a109\n"
    "v_accvgpr_read_b32 v82, a110\n"
    "v_accvgpr_read_b32 v83, a111\n"
    "v_accvgpr_read_b32 v84, a124\n"
    "v_accvgpr_read_b32 v85, a125\n"
    "v_accvgpr_read_b32 v86, a126\n"
    "v_accvgpr_read_b32 v87, a127\n"
    "v_mul_f32 v80, v12, v80\n"
    "v_mul_f32 v81, v13, v81\n"
    "v_mul_f32 v82, v14, v82\n"
    "v_mul_f32 v83, v15, v83\n"
    "v_mul_f32 v84, v12, v84\n"
    "v_mul_f32 v85, v13, v85\n"
    "v_mul_f32 v86, v14, v86\n"
    "v_mul_f32 v87, v15, v87\n"
    "v_accvgpr_write_b32 a108, v80\n"
    "v_accvgpr_write_b32 a109, v81\n"
    "v_accvgpr_write_b32 a110, v82\n"
    "v_accvgpr_write_b32 a111, v83\n"
    "v_accvgpr_write_b32 a124, v84\n"
    "v_accvgpr_write_b32 a125, v85\n"
    "v_accvgpr_write_b32 a126, v86\n"
    "v_accvgpr_write_b32 a127, v87\n"
    "s_nop 7\n"
    "s_nop 7\n"
    "s_branch L_ret_E3\n"
    "L_rare_F0:\n"
    "s_nop 7\n"
    "s_nop 7\n"
    "s_sub_u32 s56, s48, 0x10000\n"
    "s_add_u32 s57, s56, 0x0\n"
    "s_add_u32 s58, s56, 0x1000\n"
    "buffer_load_dwordx4 a[224:227], v76, s[40:43], s57 offen offset:0 sc1\n"
    "buffer_load_dwordx4 a[228:231], v76, s[40:43], s57 offen offset:1024 sc1\n"
    "buffer_load_dwordx4 a[232:235], v76, s[40:43], s57 offen offset:2048 sc1\n"
    "buffer_load_dwordx4 a[236:239], v76, s[40:43], s57 offen offset:3072 sc1\n"
    "buffer_load_dwordx4 a[240:243], v76, s[40:43], s58 offen offset:0 sc1\n"
    "buffer_load_dwordx4 a[244:247], v76, s[40:43], s58 offen offset:1024 sc1\n"
    "buffer_load_dwordx4 a[248:251], v76, s[40:43], s58 offen offset:2048 sc1\n"
    "buffer_load_dwordx4 a[252:255], v76, s[40:43], s58 offen offset:3072 sc1\n"
    "s_waitcnt vmcnt(0)\n"
    "v_mfma_f32_16x16x32_bf16 v[80:83], a[224:227], a[128:131], v[32:35]\n"
    "v_mfma_f32_16x16x32_bf16 v[80:83], a[228:231], a[132:135], v[80:83]\n"
    "v_mfma_f32_16x16x32_bf16 v[80:83], a[232:235], a[136:139], v[80:83]\n"
    "v_mfma_f32_16x16x32_bf16 v[80:83], a[236:239], a[140:143], v[80:83]\n"
    "v_mfma_f32_16x16x32_bf16 v[84:87], a[240:243], a[128:131], v[32:35]\n"
    "v_mfma_f32_16x16x32_bf16 v[84:87], a[244:247], a[132:135], v[84:87]\n"
    "v_mfma_f32_16x16x32_bf16 v[84:87], a[248:251], a[136:139], v[84:87]\n"
    "v_mfma_f32_16x16x32_bf16 v[84:87], a[252:255], a[140:143], v[84:87]\n"
    "s_nop 3\n"
    "v_max_f32 v9, v80, v80\n"
    "v_max3_f32 v9, v9, v80, v81\n"
    "v_max3_f32 v9, v9, v82, v83\n"
    "s_nop 0\n"
    "v_max3_f32 v9, v9, v84, v85\n"
    "v_max3_f32 v9, v9, v86, v87\n"
    "s_add_u32 s57, s56, 0x2000\n"
    "s_add_u32 s58, s56, 0x3000\n"
    "buffer_load_dwordx4 a[224:227], v76, s[40:43], s57 offen offset:0 sc1\n"
    "buffer_load_dwordx4 a[228:231], v76, s[40:43], s57 offen offset:1024 sc1\n"
    "buffer_load_dwordx4 a[232:235], v76, s[40:43], s57 offen offset:2048 sc1\n"
    "buffer_load_dwordx4 a[236:239], v76, s[40:43], s57 offen offset:3072 sc1\n"
    "buffer_load_dwordx4 a[240:243], v76, s[40:43], s58 offen offset:0 sc1\n"
    "buffer_load_dwordx4 a[244:247], v76, s[40:43], s58 offen offset:1024 sc1\n"
    "buffer_load_dwordx4 a[248:251], v76, s[40:43], s58 offen offset:2048 sc1\n"
    "buffer_load_dwordx4 a[252:255], v76, s[40:43], s58 offen offset:3072 sc1\n"
    "s_waitcnt vmcnt(0)\n"
    "v_mfma_f32_16x16x32_bf16 v[80:83], a[224:227], a[128:131], v[32:35]\n"
    "v_mfma_f32_16x16x32_bf16 v[80:83], a[228:231], a[132:135], v[80:83]\n"
    "v_mfma_f32_16x16x32_bf16 v[80:83], a[232:235], a[136:139], v[80:83]\n"
    "v_mfma_f32_16x16x32_bf16 v[80:83], a[236:239], a[140:143], v[80:83]\n"
    "v_mfma_f32_16x16x32_bf16 v[84:87], a[240:243], a[128:131], v[32:35]\n"
    "v_mfma_f32_16x16x32_bf16 v[84:87], a[244:247], a[132:135], v[84:87]\n"
    "v_mfma_f32_16x16x32_bf16 v[84:87], a[248:251], a[136:139], v[84:87]\n"
    "v_mfma_f32_16x16x32_bf16 v[84:87], a[252:255], a[140:143], v[84:87]\n"
    "s_nop 3\n"
    "v_max3_f32 v9, v9, v80, v81\n"
    "v_max3_f32 v9, v9, v82, v83\n"
    "s_nop 1\n"
    "v_max3_f32 v9, v9, v84, v85\n"
    "v_max3_f32 v9, v9, v86, v87\n"
    "s_add_u32 s57, s56, 0x4000\n"
    "s_add_u32 s58, s56, 0x5000\n"
    "buffer_load_dwordx4 a[224:227], v76, s[40:43], s57 offen offset:0 sc1\n"
    "buffer_load_dwordx4 a[228:231], v76, s[40:43], s57 offen offset:1024 sc1\n"
    "buffer_load_dwordx4 a[232:235], v76, s[40:43], s57 offen offset:2048 sc1\n"
    "buffer_load_dwordx4 a[236:239], v76, s[40:43], s57 offen offset:3072 sc1\n"
    "buffer_load_dwordx4 a[240:243], v76, s[40:43], s58 offen offset:0 sc1\n"
    "buffer_load_dwordx4 a[244:247], v76, s[40:43], s58 offen offset:1024 sc1\n"
    "buffer_load_dwordx4 a[248:251], v76, s[40:43], s58 offen offset:2048 sc1\n"
    "buffer_load_dwordx4 a[252:255], v76, s[40:43], s58 offen offset:3072 sc1\n"
    "s_waitcnt vmcnt(0)\n"
    "v_mfma_f32_16x16x32_bf16 v[80:83], a[224:227], a[128:131], v[32:35]\n"
    "v_mfma_f32_16x16x32_bf16 v[80:83], a[228:231], a[132:135], v[80:83]\n"
    "v_mfma_f32_16x16x32_bf16 v[80:83], a[232:235], a[136:139], v[80:83]\n"
    "v_mfma_f32_16x16x32_bf16 v[80:83], a[236:239], a[140:143], v[80:83]\n"
    "v_mfma_f32_16x16x32_bf16 v[84:87], a[240:243], a[128:131], v[32:35]\n"
    "v_mfma_f32_16x16x32_bf16 v[84:87], a[244:247], a[132:135], v[84:87]\n"
    "v_mfma_f32_16x16x32_bf16 v[84:87], a[248:251], a[136:139], v[84:87]\n"
    "v_mfma_f32_16x16x32_bf16 v[84:87], a[252:255], a[140:143], v[84:87]\n"
    "s_nop 3\n"
    "v_max3_f32 v9, v9, v80, v81\n"
    "v_max3_f32 v9, v9, v82, v83\n"
    "s_nop 1\n"
    "v_max3_f32 v9, v9, v84, v85\n"
    "v_max3_f32 v9, v9, v86, v87\n"
    "s_add_u32 s57, s56, 0x6000\n"
    "s_add_u32 s58, s56, 0x7000\n"
    "buffer_load_dwordx4 a[224:227], v76, s[40:43], s57 offen offset:0 sc1\n"
    "buffer_load_dwordx4 a[228:231], v76, s[40:43], s57 offen offset:1024 sc1\n"
    "buffer_load_dwordx4 a[232:235], v76, s[40:43], s57 offen offset:2048 sc1\n"
    "buffer_load_dwordx4 a[236:239], v76, s[40:43], s57 offen offset:3072 sc1\n"
    "buffer_load_dwordx4 a[240:243], v76, s[40:43], s58 offen offset:0 sc1\n"
    "buffer_load_dwordx4 a[244:247], v76, s[40:43], s58 offen offset:1024 sc1\n"
    "buffer_load_dwordx4 a[248:251], v76, s[40:43], s58 offen offset:2048 sc1\n"
    "buffer_load_dwordx4 a[252:255], v76, s[40:43], s58 offen offset:3072 sc1\n"
    "s_waitcnt vmcnt(0)\n"
    "v_mfma_f32_16x16x32_bf16 v[80:83], a[224:227], a[128:131], v[32:35]\n"
    "v_mfma_f32_16x16x32_bf16 v[80:83], a[228:231], a[132:135], v[80:83]\n"
    "v_mfma_f32_16x16x32_bf16 v[80:83], a[232:235], a[136:139], v[80:83]\n"
    "v_mfma_f32_16x16x32_bf16 v[80:83], a[236:239], a[140:143], v[80:83]\n"
    "v_mfma_f32_16x16x32_bf16 v[84:87], a[240:243], a[128:131], v[32:35]\n"
    "v_mfma_f32_16x16x32_bf16 v[84:87], a[244:247], a[132:135], v[84:87]\n"
    "v_mfma_f32_16x16x32_bf16 v[84:87], a[248:251], a[136:139], v[84:87]\n"
    "v_mfma_f32_16x16x32_bf16 v[84:87], a[252:255], a[140:143], v[84:87]\n"
    "s_nop 3\n"
    "v_max3_f32 v9, v9, v80, v81\n"
    "v_max3_f32 v9, v9, v82, v83\n"
    "s_nop 1\n"
    "v_max3_f32 v9, v9, v84, v85\n"
    "v_max3_f32 v9, v9, v86, v87\n"
    "v_mov_b32 v10, v9\n"
    "s_nop 1\n"
    "v_permlane16_swap_b32 v10, v9\n"
    "v_max_f32 v9, v10, v9\n"
    "v_mov_b32 v10, v9\n"
    "s_nop 1\n"
    "v_permlane32_swap_b32 v10, v9\n"
    "v_max_f32 v9, v10, v9\n"
    "v_max_f32 v9, 0, v9\n"
    "v_sub_f32 v32, v32, v9\n"
    "v_sub_f32 v33, v33, v9\n"
    "v_sub_f32 v34, v34, v9\n"
    "v_sub_f32 v35, v35, v9\n"
    "s_add_u32 s57, s56, 0x0\n"
    "s_add_u32 s58, s56, 0x1000\n"
    "buffer_load_dwordx4 a[224:227], v76, s[40:43], s57 offen offset:0 sc1\n"
    "buffer_load_dwordx4 a[228:231], v76, s[40:43], s57 offen offset:1024 sc1\n"
    "buffer_load_dwordx4 a[232:235], v76, s[40:43], s57 offen offset:2048 sc1\n"
    "buffer_load_dwordx4 a[236:239], v76, s[40:43], s57 offen offset:3072 sc1\n"
    "buffer_load_dwordx4 a[240:243], v76, s[40:43], s58 offen offset:0 sc1\n"
    "buffer_load_dwordx4 a[244:247], v76, s[40:43], s58 offen offset:1024 sc1\n"
    "buffer_load_dwordx4 a[248:251], v76, s[40:43], s58 offen offset:2048 sc1\n"
    "buffer_load_dwordx4 a[252:255], v76, s[40:43], s58 offen offset:3072 sc1\n"
    "s_waitcnt vmcnt(0)\n"
    "v_mfma_f32_16x16x32_bf16 v[80:83], a[224:227], a[128:131], v[32:35]\n"
    "v_mfma_f32_16x16x32_bf16 v[80:83], a[228:231], a[132:135], v[80:83]\n"
    "v_mfma_f32_16x16x32_bf16 v[80:83], a[232:235], a[136:139], v[80:83]\n"
    "v_mfma_f32_16x16x32_bf16 v[80:83], a[236:239], a[140:143], v[80:83]\n"
    "v_mfma_f32_16x16x32_bf16 v[84:87], a[240:243], a[128:131], v[32:35]\n"
    "v_mfma_f32_16x16x32_bf16 v[84:87], a[244:247], a[132:135], v[84:87]\n"
    "v_mfma_f32_16x16x32_bf16 v[84:87], a[248:251], a[136:139], v[84:87]\n"
    "v_mfma_f32_16x16x32_bf16 v[84:87], a[252:255], a[140:143], v[84:87]\n"
    "s_nop 3\n"
    "v_exp_f32 v80, v80\n"
    "v_exp_f32 v81, v81\n"
    "v_exp_f32 v82, v82\n"
    "v_exp_f32 v83, v83\n"
    "v_exp_f32 v84, v84\n"
    "v_exp_f32 v85, v85\n"
    "v_exp_f32 v86, v86\n"
    "v_exp_f32 v87, v87\n"
    "v_cvt_pk_bf16_f32 v192, v80, v81\n"
    "v_cvt_pk_bf16_f32 v193, v82, v83\n"
    "v_cvt_pk_bf16_f32 v194, v84, v85\n"
    "v_cvt_pk_bf16_f32 v195, v86, v87\n"
    "s_add_u32 s57, s56, 0x2000\n"
    "s_add_u32 s58, s56, 0x3000\n"
    "buffer_load_dwordx4 a[224:227], v76, s[40:43], s57 offen offset:0 sc1\n"
    "buffer_load_dwordx4 a[228:231], v76, s[40:43], s57 offen offset:1024 sc1\n"
    "buffer_load_dwordx4 a[232:235], v76, s[40:43], s57 offen offset:2048 sc1\n"
    "buffer_load_dwordx4 a[236:239], v76, s[40:43], s57 offen offset:3072 sc1\n"
    "buffer_load_dwordx4 a[240:243], v76, s[40:43], s58 offen offset:0 sc1\n"
    "buffer_load_dwordx4 a[244:247], v76, s[40:43], s58 offen offset:1024 sc1\n"
    "buffer_load_dwordx4 a[248:251], v76, s[40:43], s58 offen offset:2048 sc1\n"
    "buffer_load_dwordx4 a[252:255], v76, s[40:43], s58 offen offset:3072 sc1\n"
    "s_waitcnt vmcnt(0)\n"
    "v_mfma_f32_16x16x32_bf16 v[80:83], a[224:227], a[128:131], v[32:35]\n"
    "v_mfma_f32_16x16x32_bf16 v[80:83], a[228:231], a[132:135], v[80:83]\n"
    "v_mfma_f32_16x16x32_bf16 v[80:83], a[232:235], a[136:139], v[80:83]\n"
    "v_mfma_f32_16x16x32_bf16 v[80:83], a[236:239], a[140:143], v[80:83]\n"
    "v_mfma_f32_16x16x32_bf16 v[84:87], a[240:243], a[128:131], v[32:35]\n"
    "v_mfma_f32_16x16x32_bf16 v[84:87], a[244:247], a[132:135], v[84:87]\n"
    "v_mfma_f32_16x16x32_bf16 v[84:87], a[248:251], a[136:139], v[84:87]\n"
    "v_mfma_f32_16x16x32_bf16 v[84:87], a[252:255], a[140:143], v[84:87]\n"
    "s_nop 3\n"
    "v_exp_f32 v80, v80\n"
    "v_exp_f32 v81, v81\n"
    "v_exp_f32 v82, v82\n"
    "v_exp_f32 v83, v83\n"
    "v_exp_f32 v84, v84\n"
    "v_exp_f32 v85, v85\n"
    "v_exp_f32 v86, v86\n"
    "v_exp_f32 v87, v87\n"
    "v_cvt_pk_bf16_f32 v196, v80, v81\n"
    "v_cvt_pk_bf16_f32 v197, v82, v83\n"
    "v_cvt_pk_bf16_f32 v198, v84, v85\n"
    "v_cvt_pk_bf16_f32 v199, v86, v87\n"
    "s_add_u32 s57, s56, 0x4000\n"
    "s_add_u32 s58, s56, 0x5000\n"
    "buffer_load_dwordx4 a[224:227], v76, s[40:43], s57 offen offset:0 sc1\n"
    "buffer_load_dwordx4 a[228:231], v76, s[40:43], s57 offen offset:1024 sc1\n"
    "buffer_load_dwordx4 a[232:235], v76, s[40:43], s57 offen offset:2048 sc1\n"
    "buffer_load_dwordx4 a[236:239], v76, s[40:43], s57 offen offset:3072 sc1\n"
    "buffer_load_dwordx4 a[240:243], v76, s[40:43], s58 offen offset:0 sc1\n"
    "buffer_load_dwordx4 a[244:247], v76, s[40:43], s58 offen offset:1024 sc1\n"
    "buffer_load_dwordx4 a[248:251], v76, s[40:43], s58 offen offset:2048 sc1\n"
    "buffer_load_dwordx4 a[252:255], v76, s[40:43], s58 offen offset:3072 sc1\n"
    "s_waitcnt vmcnt(0)\n"
    "v_mfma_f32_16x16x32_bf16 v[80:83], a[224:227], a[128:131], v[32:35]\n"
    "v_mfma_f32_16x16x32_bf16 v[80:83], a[228:231], a[132:135], v[80:83]\n"
    "v_mfma_f32_16x16x32_bf16 v[80:83], a[232:235], a[136:139], v[80:83]\n"
    "v_mfma_f32_16x16x32_bf16 v[80:83], a[236:239], a[140:143], v[80:83]\n"
    "v_mfma_f32_16x16x32_bf16 v[84:87], a[240:243], a[128:131], v[32:35]\n"
    "v_mfma_f32_16x16x32_bf16 v[84:87], a[244:247], a[132:135], v[84:87]\n"
    "v_mfma_f32_16x16x32_bf16 v[84:87], a[248:251], a[136:139], v[84:87]\n"
    "v_mfma_f32_16x16x32_bf16 v[84:87], a[252:255], a[140:143], v[84:87]\n"
    "s_nop 3\n"
    "v_exp_f32 v80, v80\n"
    "v_exp_f32 v81, v81\n"
    "v_exp_f32 v82, v82\n"
    "v_exp_f32 v83, v83\n"
    "v_exp_f32 v84, v84\n"
    "v_exp_f32 v85, v85\n"
    "v_exp_f32 v86, v86\n"
    "v_exp_f32 v87, v87\n"
    "v_cvt_pk_bf16_f32 v200, v80, v81\n"
    "v_cvt_pk_bf16_f32 v201, v82, v83\n"
    "v_cvt_pk_bf16_f32 v202, v84, v85\n"
    "v_cvt_pk_bf16_f32 v203, v86, v87\n"
    "s_add_u32 s57, s56, 0x6000\n"
    "s_add_u32 s58, s56, 0x7000\n"
    "buffer_load_dwordx4 a[224:227], v76, s[40:43], s57 offen offset:0 sc1\n"
    "buffer_load_dwordx4 a[228:231], v76, s[40:43], s57 offen offset:1024 sc1\n"
    "buffer_load_dwordx4 a[232:235], v76, s[40:43], s57 offen offset:2048 sc1\n"
    "buffer_load_dwordx4 a[236:239], v76, s[40:43], s57 offen offset:3072 sc1\n"
    "buffer_load_dwordx4 a[240:243], v76, s[40:43], s58 offen offset:0 sc1\n"
    "buffer_load_dwordx4 a[244:247], v76, s[40:43], s58 offen offset:1024 sc1\n"
    "buffer_load_dwordx4 a[248:251], v76, s[40:43], s58 offen offset:2048 sc1\n"
    "buffer_load_dwordx4 a[252:255], v76, s[40:43], s58 offen offset:3072 sc1\n"
    "s_waitcnt vmcnt(0)\n"
    "v_mfma_f32_16x16x32_bf16 v[80:83], a[224:227], a[128:131], v[32:35]\n"
    "v_mfma_f32_16x16x32_bf16 v[80:83], a[228:231], a[132:135], v[80:83]\n"
    "v_mfma_f32_16x16x32_bf16 v[80:83], a[232:235], a[136:139], v[80:83]\n"
    "v_mfma_f32_16x16x32_bf16 v[80:83], a[236:239], a[140:143], v[80:83]\n"
    "v_mfma_f32_16x16x32_bf16 v[84:87], a[240:243], a[128:131], v[32:35]\n"
    "v_mfma_f32_16x16x32_bf16 v[84:87], a[244:247], a[132:135], v[84:87]\n"
    "v_mfma_f32_16x16x32_bf16 v[84:87], a[248:251], a[136:139], v[84:87]\n"
    "v_mfma_f32_16x16x32_bf16 v[84:87], a[252:255], a[140:143], v[84:87]\n"
    "s_nop 3\n"
    "v_exp_f32 v80, v80\n"
    "v_exp_f32 v81, v81\n"
    "v_exp_f32 v82, v82\n"
    "v_exp_f32 v83, v83\n"
    "v_exp_f32 v84, v84\n"
    "v_exp_f32 v85, v85\n"
    "v_exp_f32 v86, v86\n"
    "v_exp_f32 v87, v87\n"
    "v_cvt_pk_bf16_f32 v204, v80, v81\n"
    "v_cvt_pk_bf16_f32 v205, v82, v83\n"
    "v_cvt_pk_bf16_f32 v206, v84, v85\n"
    "v_cvt_pk_bf16_f32 v207, v86, v87\n"
    "v_mfma_f32_16x16x32_bf16 v[48:51], v[64:67], v[192:195], 0\n"
    "v_mfma_f32_16x16x32_bf16 v[48:51], v[64:67], v[196:199], v[48:51]\n"
    "v_mfma_f32_16x16x32_bf16 v[48:51], v[64:67], v[200:203], v[48:51]\n"
    "v_mfma_f32_16x16x32_bf16 v[48:51], v[64:67], v[204:207], v[48:51]\n"
    "s_add_u32 s57, s56, 0x8000\n"
    "s_add_u32 s58, s56, 0x9000\n"
    "buffer_load_dwordx4 a[224:227], v76, s[40:43], s57 offen offset:0 sc1\n"
    "buffer_load_dwordx4 a[228:231], v76, s[40:43], s57 offen offset:1024 sc1\n"
    "buffer_load_dwordx4 a[232:235], v76, s[40:43], s57 offen offset:2048 sc1\n"
    "buffer_load_dwordx4 a[236:239], v76, s[40:43], s57 offen offset:3072 sc1\n"
    "buffer_load_dwordx4 a[240:243], v76, s[40:43], s58 offen offset:0 sc1\n"
    "buffer_load_dwordx4 a[244:247], v76, s[40:43], s58 offen offset:1024 sc1\n"
    "buffer_load_dwordx4 a[248:251], v76, s[40:43], s58 offen offset:2048 sc1\n"
    "buffer_load_dwordx4 a[252:255], v76, s[40:43], s58 offen offset:3072 sc1\n"
    "s_waitcnt vmcnt(0)\n"
    "v_mfma_f32_16x16x32_bf16 a[0:3], v[128:131], a[224:227], a[0:3]\n"
    "v_mfma_f32_16x16x32_bf16 a[0:3], v[132:135], a[228:231], a[0:3]\n"
    "v_mfma_f32_16x16x32_bf16 a[0:3], v[136:139], a[232:235], a[0:3]\n"
    "v_mfma_f32_16x16x32_bf16 a[0:3], v[140:143], a[236:239], a[0:3]\n"
    "v_mfma_f32_16x16x32_bf16 a[16:19], v[128:131], a[240:243], a[16:19]\n"
    "v_mfma_f32_16x16x32_bf16 a[16:19], v[132:135], a[244:247], a[16:19]\n"
    "v_mfma_f32_16x16x32_bf16 a[16:19], v[136:139], a[248:251], a[16:19]\n"
    "v_mfma_f32_16x16x32_bf16 a[16:19], v[140:143], a[252:255], a[16:19]\n"
    "s_nop 7\n"
    "s_add_u32 s57, s56, 0xa000\n"
    "s_add_u32 s58, s56, 0xb000\n"
    "buffer_load_dwordx4 a[224:227], v76, s[40:43], s57 offen offset:0 sc1\n"
    "buffer_load_dwordx4 a[228:231], v76, s[40:43], s57 offen offset:1024 sc1\n"
    "buffer_load_dwordx4 a[232:235], v76, s[40:43], s57 offen offset:2048 sc1\n"
    "buffer_load_dwordx4 a[236:239], v76, s[40:43], s57 offen offset:3072 sc1\n"
    "buffer_load_dwordx4 a[240:243], v76, s[40:43], s58 offen offset:0 sc1\n"
    "buffer_load_dwordx4 a[244:247], v76, s[40:43], s58 offen offset:1024 sc1\n"
    "buffer_load_dwordx4 a[248:251], v76, s[40:43], s58 offen offset:2048 sc1\n"
    "buffer_load_dwordx4 a[252:255], v76, s[40:43], s58 offen offset:3072 sc1\n"
    "s_waitcnt vmcnt(0)\n"
    "v_mfma_f32_16x16x32_bf16 a[32:35], v[128:131], a[224:227], a[32:35]\n"
    "v_mfma_f32_16x16x32_bf16 a[32:35], v[132:135], a[228:231], a[32:35]\n"
    "v_mfma_f32_16x16x32_bf16 a[32:35], v[136:139], a[232:235], a[32:35]\n"
    "v_mfma_f32_16x16x32_bf16 a[32:35], v[140:143], a[236:239], a[32:35]\n"
    "v_mfma_f32_16x16x32_bf16 a[48:51], v[128:131], a[240:243], a[48:51]\n"
    "v_mfma_f32_16x16x32_bf16 a[48:51], v[132:135], a[244:247], a[48:51]\n"
    "v_mfma_f32_16x16x32_bf16 a[48:51], v[136:139], a[248:251], a[48:51]\n"
    "v_mfma_f32_16x16x32_bf16 a[48:51], v[140:143], a[252:255], a[48:51]\n"
    "s_nop 7\n"
    "s_add_u32 s57, s56, 0xc000\n"
    "s_add_u32 s58, s56, 0xd000\n"
    "buffer_load_dwordx4 a[224:227], v76, s[40:43], s57 offen offset:0 sc1\n"
    "buffer_load_dwordx4 a[228:231], v76, s[40:43], s57 offen offset:1024 sc1\n"
    "buffer_load_dwordx4 a[232:235], v76, s[40:43], s57 offen offset:2048 sc1\n"
    "buffer_load_dwordx4 a[236:239], v76, s[40:43], s57 offen offset:3072 sc1\n"
    "buffer_load_dwordx4 a[240:243], v76, s[40:43], s58 offen offset:0 sc1\n"
    "buffer_load_dwordx4 a[244:247], v76, s[40:43], s58 offen offset:1024 sc1\n"
    "buffer_load_dwordx4 a[248:251], v76, s[40:43], s58 offen offset:2048 sc1\n"
    "buffer_load_dwordx4 a[252:255], v76, s[40:43], s58 offen offset:3072 sc1\n"
    "s_waitcnt vmcnt(0)\n"
    "v_mfma_f32_16x16x32_bf16 a[64:67], v[128:131], a[224:227], a[64:67]\n"
    "v_mfma_f32_16x16x32_bf16 a[64:67], v[132:135], a[228:231], a[64:67]\n"
    "v_mfma_f32_16x16x32_bf16 a[64:67], v[136:139], a[232:235], a[64:67]\n"
    "v_mfma_f32_16x16x32_bf16 a[64:67], v[140:143], a[236:239], a[64:67]\n"
    "v_mfma_f32_16x16x32_bf16 a[80:83], v[128:131], a[240:243], a[80:83]\n"
    "v_mfma_f32_16x16x32_bf16 a[80:83], v[132:135], a[244:247], a[80:83]\n"
    "v_mfma_f32_16x16x32_bf16 a[80:83], v[136:139], a[248:251], a[80:83]\n"
    "v_mfma_f32_16x16x32_bf16 a[80:83], v[140:143], a[252:255], a[80:83]\n"
    "s_nop 7\n"
    "s_add_u32 s57, s56, 0xe000\n"
    "s_add_u32 s58, s56, 0xf000\n"
    "buffer_load_dwordx4 a[224:227], v76, s[40:43], s57 offen offset:0 sc1\n"
    "buffer_load_dwordx4 a[228:231], v76, s[40:43], s57 offen offset:1024 sc1\n"
    "buffer_load_dwordx4 a[232:235], v76, s[40:43], s57 offen offset:2048 sc1\n"
    "buffer_load_dwordx4 a[236:239], v76, s[40:43], s57 offen offset:3072 sc1\n"
    "buffer_load_dwordx4 a[240:243], v76, s[40:43], s58 offen offset:0 sc1\n"
    "buffer_load_dwordx4 a[244:247], v76, s[40:43], s58 offen offset:1024 sc1\n"
    "buffer_load_dwordx4 a[248:251], v76, s[40:43], s58 offen offset:2048 sc1\n"
    "buffer_load_dwordx4 a[252:255], v76, s[40:43], s58 offen offset:3072 sc1\n"
    "s_waitcnt vmcnt(0)\n"
    "v_mfma_f32_16x16x32_bf16 a[96:99], v[128:131], a[224:227], a[96:99]\n"
    "v_mfma_f32_16x16x32_bf16 a[96:99], v[132:135], a[228:231], a[96:99]\n"
    "v_mfma_f32_16x16x32_bf16 a[96:99], v[136:139], a[232:235], a[96:99]\n"
    "v_mfma_f32_16x16x32_bf16 a[96:99], v[140:143], a[236:239], a[96:99]\n"
    "v_mfma_f32_16x16x32_bf16 a[112:115], v[128:131], a[240:243], a[112:115]\n"
    "v_mfma_f32_16x16x32_bf16 a[112:115], v[132:135], a[244:247], a[112:115]\n"
    "v_mfma_f32_16x16x32_bf16 a[112:115], v[136:139], a[248:251], a[112:115]\n"
    "v_mfma_f32_16x16x32_bf16 a[112:115], v[140:143], a[252:255], a[112:115]\n"
    "s_nop 7\n"
    "v_mov_b32 v128, 0\n"
    "v_mov_b32 v129, 0\n"
    "v_mov_b32 v130, 0\n"
    "v_mov_b32 v131, 0\n"
    "v_mov_b32 v132, 0\n"
    "v_mov_b32 v133, 0\n"
    "v_mov_b32 v134, 0\n"
    "v_mov_b32 v135, 0\n"
    "v_mov_b32 v136, 0\n"
    "v_mov_b32 v137, 0\n"
    "v_mov_b32 v138, 0\n"
    "v_mov_b32 v139, 0\n"
    "v_mov_b32 v140, 0\n"
    "v_mov_b32 v141, 0\n"
    "v_mov_b32 v142, 0\n"
    "v_mov_b32 v143, 0\n"
    "v_sub_f32 v11, 0, v9\n"
    "v_exp_f32 v11, v11\n"
    "s_nop 0\n"
    "v_mul_f32 v68, v11, v68\n"
    "ds_bpermute_b32 v12, v99, v11 offset:0\n"
    "ds_bpermute_b32 v13, v99, v11 offset:4\n"
    "ds_bpermute_b32 v14, v99, v11 offset:8\n"
    "ds_bpermute_b32 v15, v99, v11 offset:12\n"
    "s_waitcnt lgkmcnt(0)\n"
    "v_accvgpr_read_b32 v80, a0\n"
    "v_accvgpr_read_b32 v81, a1\n"
    "v_accvgpr_read_b32 v82, a2\n"
    "v_accvgpr_read_b32 v83, a3\n"
    "v_accvgpr_read_b32 v84, a16\n"
    "v_accvgpr_read_b32 v85, a17\n"
    "v_accvgpr_read_b32 v86, a18\n"
    "v_accvgpr_read_b32 v87, a19\n"
    "v_mul_f32 v80, v12, v80\n"
    "v_mul_f32 v81, v13, v81\n"
    "v_mul_f32 v82, v14, v82\n"
    "v_mul_f32 v83, v15, v83\n"
    "v_mul_f32 v84, v12, v84\n"
    "v_mul_f32 v85, v13, v85\n"
    "v_mul_f32 v86, v14, v86\n"
    "v_mul_f32 v87, v15, v87\n"
    "v_accvgpr_write_b32 a0, v80\n"
    "v_accvgpr_write_b32 a1, v81\n"
    "v_accvgpr_write_b32 a2, v82\n"
    "v_accvgpr_write_b32 a3, v83\n"
    "v_accvgpr_write_b32 a16, v84\n"
    "v_accvgpr_write_b32 a17, v85\n"
    "v_accvgpr_write_b32 a18, v86\n"
    "v_accvgpr_write_b32 a19, v87\n"
    "v_accvgpr_read_b32 v80, a32\n"
    "v_accvgpr_read_b32 v81, a33\n"
    "v_accvgpr_read_b32 v82, a34\n"
    "v_accvgpr_read_b32 v83, a35\n"
    "v_accvgpr_read_b32 v84, a48\n"
    "v_accvgpr_read_b32 v85, a49\n"
    "v_accvgpr_read_b32 v86, a50\n"
    "v_accvgpr_read_b32 v87, a51\n"
    "v_mul_f32 v80, v12, v80\n"
    "v_mul_f32 v81, v13, v81\n"
    "v_mul_f32 v82, v14, v82\n"
    "v_mul_f32 v83, v15, v83\n"
    "v_mul_f32 v84, v12, v84\n"
    "v_mul_f32 v85, v13, v85\n"
    "v_mul_f32 v86, v14, v86\n"
    "v_mul_f32 v87, v15, v87\n"
    "v_accvgpr_write_b32 a32, v80\n"
    "v_accvgpr_write_b32 a33, v81\n"
    "v_accvgpr_write_b32 a34, v82\n"
    "v_accvgpr_write_b32 a35, v83\n"
    "v_accvgpr_write_b32 a48, v84\n"
    "v_accvgpr_write_b32 a49, v85\n"
    "v_accvgpr_write_b32 a50, v86\n"
    "v_accvgpr_write_b32 a51, v87\n"
    "v_accvgpr_read_b32 v80, a64\n"
    "v_accvgpr_read_b32 v81, a65\n"
    "v_accvgpr_read_b32 v82, a66\n"
    "v_accvgpr_read_b32 v83, a67\n"
    "v_accvgpr_read_b32 v84, a80\n"
    "v_accvgpr_read_b32 v85, a81\n"
    "v_accvgpr_read_b32 v86, a82\n"
    "v_accvgpr_read_b32 v87, a83\n"
    "v_mul_f32 v80, v12, v80\n"
    "v_mul_f32 v81, v13, v81\n"
    "v_mul_f32 v82, v14, v82\n"
    "v_mul_f32 v83, v15, v83\n"
    "v_mul_f32 v84, v12, v84\n"
    "v_mul_f32 v85, v13, v85\n"
    "v_mul_f32 v86, v14, v86\n"
    "v_mul_f32 v87, v15, v87\n"
    "v_accvgpr_write_b32 a64, v80\n"
    "v_accvgpr_write_b32 a65, v81\n"
    "v_accvgpr_write_b32 a66, v82\n"
    "v_accvgpr_write_b32 a67, v83\n"
    "v_accvgpr_write_b32 a80, v84\n"
    "v_accvgpr_write_b32 a81, v85\n"
    "v_accvgpr_write_b32 a82, v86\n"
    "v_accvgpr_write_b32 a83, v87\n"
    "v_accvgpr_read_b32 v80, a96\n"
    "v_accvgpr_read_b32 v81, a97\n"
    "v_accvgpr_read_b32 v82, a98\n"
    "v_accvgpr_read_b32 v83, a99\n"
    "v_accvgpr_read_b32 v84, a112\n"
    "v_accvgpr_read_b32 v85, a113\n"
    "v_accvgpr_read_b32 v86, a114\n"
    "v_accvgpr_read_b32 v87, a115\n"
    "v_mul_f32 v80, v12, v80\n"
    "v_mul_f32 v81, v13, v81\n"
    "v_mul_f32 v82, v14, v82\n"
    "v_mul_f32 v83, v15, v83\n"
    "v_mul_f32 v84, v12, v84\n"
    "v_mul_f32 v85, v13, v85\n"
    "v_mul_f32 v86, v14, v86\n"
    "v_mul_f32 v87, v15, v87\n"
    "v_accvgpr_write_b32 a96, v80\n"
    "v_accvgpr_write_b32 a97, v81\n"
    "v_accvgpr_write_b32 a98, v82\n"
    "v_accvgpr_write_b32 a99, v83\n"
    "v_accvgpr_write_b32 a112, v84\n"
    "v_accvgpr_write_b32 a113, v85\n"
    "v_accvgpr_write_b32 a114, v86\n"
    "v_accvgpr_write_b32 a115, v87\n"
    "s_nop 7\n"
    "s_nop 7\n"
    "s_branch L_ret_F0\n"
    "L_rare_F1:\n"
    "s_nop 7\n"
    "s_nop 7\n"
    "s_sub_u32 s56, s48, 0x10000\n"
    "s_add_u32 s57, s56, 0x0\n"
    "s_add_u32 s58, s56, 0x1000\n"
    "buffer_load_dwordx4 a[224:227], v76, s[40:43], s57 offen offset:0 sc1\n"
    "buffer_load_dwordx4 a[228:231], v76, s[40:43], s57 offen offset:1024 sc1\n"
    "buffer_load_dwordx4 a[232:235], v76, s[40:43], s57 offen offset:2048 sc1\n"
    "buffer_load_dwordx4 a[236:239], v76, s[40:43], s57 offen offset:3072 sc1\n"
    "buffer_load_dwordx4 a[240:243], v76, s[40:43], s58 offen offset:0 sc1\n"
    "buffer_load_dwordx4 a[244:247], v76, s[40:43], s58 offen offset:1024 sc1\n"
    "buffer_load_dwordx4 a[248:251], v76, s[40:43], s58 offen offset:2048 sc1\n"
    "buffer_load_dwordx4 a[252:255], v76, s[40:43], s58 offen offset:3072 sc1\n"
    "s_waitcnt vmcnt(0)\n"
    "v_mfma_f32_16x16x32_bf16 v[80:83], a[224:227], a[144:147], v[36:39]\n"
    "v_mfma_f32_16x16x32_bf16 v[80:83], a[228:231], a[148:151], v[80:83]\n"
    "v_mfma_f32_16x16x32_bf16 v[80:83], a[232:235], a[152:155], v[80:83]\n"
    "v_mfma_f32_16x16x32_bf16 v[80:83], a[236:239], a[156:159], v[80:83]\n"
    "v_mfma_f32_16x16x32_bf16 v[84:87], a[240:243], a[144:147], v[36:39]\n"
    "v_mfma_f32_16x16x32_bf16 v[84:87], a[244:247], a[148:151], v[84:87]\n"
    "v_mfma_f32_16x16x32_bf16 v[84:87], a[248:251], a[152:155], v[84:87]\n"
    "v_mfma_f32_16x16x32_bf16 v[84:87], a[252:255], a[156:159], v[84:87]\n"
    "s_nop 3\n"
    "v_max_f32 v9, v80, v80\n"
    "v_max3_f32 v9, v9, v80, v81\n"
    "v_max3_f32 v9, v9, v82, v83\n"
    "s_nop 0\n"
    "v_max3_f32 v9, v9, v84, v85\n"
    "v_max3_f32 v9, v9, v86, v87\n"
    "s_add_u32 s57, s56, 0x2000\n"
    "s_add_u32 s58, s56, 0x3000\n"
    "buffer_load_dwordx4 a[224:227], v76, s[40:43], s57 offen offset:0 sc1\n"
    "buffer_load_dwordx4 a[228:231], v76, s[40:43], s57 offen offset:1024 sc1\n"
    "buffer_load_dwordx4 a[232:235], v76, s[40:43], s57 offen offset:2048 sc1\n"
    "buffer_load_dwordx4 a[236:239], v76, s[40:43], s57 offen offset:3072 sc1\n"
    "buffer_load_dwordx4 a[240:243], v76, s[40:43], s58 offen offset:0 sc1\n"
    "buffer_load_dwordx4 a[244:247], v76, s[40:43], s58 offen offset:1024 sc1\n"
    "buffer_load_dwordx4 a[248:251], v76, s[40:43], s58 offen offset:2048 sc1\n"
    "buffer_load_dwordx4 a[252:255], v76, s[40:43], s58 offen offset:3072 sc1\n"
    "s_waitcnt vmcnt(0)\n"
    "v_mfma_f32_16x16x32_bf16 v[80:83], a[224:227], a[144:147], v[36:39]\n"
    "v_mfma_f32_16x16x32_bf16 v[80:83], a[228:231], a[148:151], v[80:83]\n"
    "v_mfma_f32_16x16x32_bf16 v[80:83], a[232:235], a[152:155], v[80:83]\n"
    "v_mfma_f32_16x16x32_bf16 v[80:83], a[236:239], a[156:159], v[80:83]\n"
    "v_mfma_f32_16x16x32_bf16 v[84:87], a[240:243], a[144:147], v[36:39]\n"
    "v_mfma_f32_16x16x32_bf16 v[84:87], a[244:247], a[148:151], v[84:87]\n"
    "v_mfma_f32_16x16x32_bf16 v[84:87], a[248:251], a[152:155], v[84:87]\n"
    "v_mfma_f32_16x16x32_bf16 v[84:87], a[252:255], a[156:159], v[84:87]\n"
    "s_nop 3\n"
    "v_max3_f32 v9, v9, v80, v81\n"
    "v_max3_f32 v9, v9, v82, v83\n"
    "s_nop 1\n"
    "v_max3_f32 v9, v9, v84, v85\n"
    "v_max3_f32 v9, v9, v86, v87\n"
    "s_add_u32 s57, s56, 0x4000\n"
    "s_add_u32 s58, s56, 0x5000\n"
    "buffer_load_dwordx4 a[224:227], v76, s[40:43], s57 offen offset:0 sc1\n"
    "buffer_load_dwordx4 a[228:231], v76, s[40:43], s57 offen offset:1024 sc1\n"
    "buffer_load_dwordx4 a[232:235], v76, s[40:43], s57 offen offset:2048 sc1\n"
    "buffer_load_dwordx4 a[236:239], v76, s[40:43], s57 offen offset:3072 sc1\n"
    "buffer_load_dwordx4 a[240:243], v76, s[40:43], s58 offen offset:0 sc1\n"
    "buffer_load_dwordx4 a[244:247], v76, s[40:43], s58 offen offset:1024 sc1\n"
    "buffer_load_dwordx4 a[248:251], v76, s[40:43], s58 offen offset:2048 sc1\n"
    "buffer_load_dwordx4 a[252:255], v76, s[40:43], s58 offen offset:3072 sc1\n"
    "s_waitcnt vmcnt(0)\n"
    "v_mfma_f32_16x16x32_bf16 v[80:83], a[224:227], a[144:147], v[36:39]\n"
    "v_mfma_f32_16x16x32_bf16 v[80:83], a[228:231], a[148:151], v[80:83]\n"
    "v_mfma_f32_16x16x32_bf16 v[80:83], a[232:235], a[152:155], v[80:83]\n"
    "v_mfma_f32_16x16x32_bf16 v[80:83], a[236:239], a[156:159], v[80:83]\n"
    "v_mfma_f32_16x16x32_bf16 v[84:87], a[240:243], a[144:147], v[36:39]\n"
    "v_mfma_f32_16x16x32_bf16 v[84:87], a[244:247], a[148:151], v[84:87]\n"
    "v_mfma_f32_16x16x32_bf16 v[84:87], a[248:251], a[152:155], v[84:87]\n"
    "v_mfma_f32_16x16x32_bf16 v[84:87], a[252:255], a[156:159], v[84:87]\n"
    "s_nop 3\n"
    "v_max3_f32 v9, v9, v80, v81\n"
    "v_max3_f32 v9, v9, v82, v83\n"
    "s_nop 1\n"
    "v_max3_f32 v9, v9, v84, v85\n"
    "v_max3_f32 v9, v9, v86, v87\n"
    "s_add_u32 s57, s56, 0x6000\n"
    "s_add_u32 s58, s56, 0x7000\n"
    "buffer_load_dwordx4 a[224:227], v76, s[40:43], s57 offen offset:0 sc1\n"
    "buffer_load_dwordx4 a[228:231], v76, s[40:43], s57 offen offset:1024 sc1\n"
    "buffer_load_dwordx4 a[232:235], v76, s[40:43], s57 offen offset:2048 sc1\n"
    "buffer_load_dwordx4 a[236:239], v76, s[40:43], s57 offen offset:3072 sc1\n"
    "buffer_load_dwordx4 a[240:243], v76, s[40:43], s58 offen offset:0 sc1\n"
    "buffer_load_dwordx4 a[244:247], v76, s[40:43], s58 offen offset:1024 sc1\n"
    "buffer_load_dwordx4 a[248:251], v76, s[40:43], s58 offen offset:2048 sc1\n"
    "buffer_load_dwordx4 a[252:255], v76, s[40:43], s58 offen offset:3072 sc1\n"
    "s_waitcnt vmcnt(0)\n"
    "v_mfma_f32_16x16x32_bf16 v[80:83], a[224:227], a[144:147], v[36:39]\n"
    "v_mfma_f32_16x16x32_bf16 v[80:83], a[228:231], a[148:151], v[80:83]\n"
    "v_mfma_f32_16x16x32_bf16 v[80:83], a[232:235], a[152:155], v[80:83]\n"
    "v_mfma_f32_16x16x32_bf16 v[80:83], a[236:239], a[156:159], v[80:83]\n"
    "v_mfma_f32_16x16x32_bf16 v[84:87], a[240:243], a[144:147], v[36:39]\n"
    "v_mfma_f32_16x16x32_bf16 v[84:87], a[244:247], a[148:151], v[84:87]\n"
    "v_mfma_f32_16x16x32_bf16 v[84:87], a[248:251], a[152:155], v[84:87]\n"
    "v_mfma_f32_16x16x32_bf16 v[84:87], a[252:255], a[156:159], v[84:87]\n"
    "s_nop 3\n"
    "v_max3_f32 v9, v9, v80, v81\n"
    "v_max3_f32 v9, v9, v82, v83\n"
    "s_nop 1\n"
    "v_max3_f32 v9, v9, v84, v85\n"
    "v_max3_f32 v9, v9, v86, v87\n"
    "v_mov_b32 v10, v9\n"
    "s_nop 1\n"
    "v_permlane16_swap_b32 v10, v9\n"
    "v_max_f32 v9, v10, v9\n"
    "v_mov_b32 v10, v9\n"
    "s_nop 1\n"
    "v_permlane32_swap_b32 v10, v9\n"
    "v_max_f32 v9, v10, v9\n"
    "v_max_f32 v9, 0, v9\n"
    "v_sub_f32 v36, v36, v9\n"
    "v_sub_f32 v37, v37, v9\n"
    "v_sub_f32 v38, v38, v9\n"
    "v_sub_f32 v39, v39, v9\n"
    "s_add_u32 s57, s56, 0x0\n"
    "s_add_u32 s58, s56, 0x1000\n"
    "buffer_load_dwordx4 a[224:227], v76, s[40:43], s57 offen offset:0 sc1\n"
    "buffer_load_dwordx4 a[228:231], v76, s[40:43], s57 offen offset:1024 sc1\n"
    "buffer_load_dwordx4 a[232:235], v76, s[40:43], s57 offen offset:2048 sc1\n"
    "buffer_load_dwordx4 a[236:239], v76, s[40:43], s57 offen offset:3072 sc1\n"
    "buffer_load_dwordx4 a[240:243], v76, s[40:43], s58 offen offset:0 sc1\n"
    "buffer_load_dwordx4 a[244:247], v76, s[40:43], s58 offen offset:1024 sc1\n"
    "buffer_load_dwordx4 a[248:251], v76, s[40:43], s58 offen offset:2048 sc1\n"
    "buffer_load_dwordx4 a[252:255], v76, s[40:43], s58 offen offset:3072 sc1\n"
    "s_waitcnt vmcnt(0)\n"
    "v_mfma_f32_16x16x32_bf16 v[80:83], a[224:227], a[144:147], v[36:39]\n"
    "v_mfma_f32_16x16x32_bf16 v[80:83], a[228:231], a[148:151], v[80:83]\n"
    "v_mfma_f32_16x16x32_bf16 v[80:83], a[232:235], a[152:155], v[80:83]\n"
    "v_mfma_f32_16x16x32_bf16 v[80:83], a[236:239], a[156:159], v[80:83]\n"
    "v_mfma_f32_16x16x32_bf16 v[84:87], a[240:243], a[144:147], v[36:39]\n"
    "v_mfma_f32_16x16x32_bf16 v[84:87], a[244:247], a[148:151], v[84:87]\n"
    "v_mfma_f32_16x16x32_bf16 v[84:87], a[248:251], a[152:155], v[84:87]\n"
    "v_mfma_f32_16x16x32_bf16 v[84:87], a[252:255], a[156:159], v[84:87]\n"
    "s_nop 3\n"
    "v_exp_f32 v80, v80\n"
    "v_exp_f32 v81, v81\n"
    "v_exp_f32 v82, v82\n"
    "v_exp_f32 v83, v83\n"
    "v_exp_f32 v84, v84\n"
    "v_exp_f32 v85, v85\n"
    "v_exp_f32 v86, v86\n"
    "v_exp_f32 v87, v87\n"
    "v_cvt_pk_bf16_f32 v208, v80, v81\n"
    "v_cvt_pk_bf16_f32 v209, v82, v83\n"
    "v_cvt_pk_bf16_f32 v210, v84, v85\n"
    "v_cvt_pk_bf16_f32 v211, v86, v87\n"
    "s_add_u32 s57, s56, 0x2000\n"
    "s_add_u32 s58, s56, 0x3000\n"
    "buffer_load_dwordx4 a[224:227], v76, s[40:43], s57 offen offset:0 sc1\n"
    "buffer_load_dwordx4 a[228:231], v76, s[40:43], s57 offen offset:1024 sc1\n"
    "buffer_load_dwordx4 a[232:235], v76, s[40:43], s57 offen offset:2048 sc1\n"
    "buffer_load_dwordx4 a[236:239], v76, s[40:43], s57 offen offset:3072 sc1\n"
    "buffer_load_dwordx4 a[240:243], v76, s[40:43], s58 offen offset:0 sc1\n"
    "buffer_load_dwordx4 a[244:247], v76, s[40:43], s58 offen offset:1024 sc1\n"
    "buffer_load_dwordx4 a[248:251], v76, s[40:43], s58 offen offset:2048 sc1\n"
    "buffer_load_dwordx4 a[252:255], v76, s[40:43], s58 offen offset:3072 sc1\n"
    "s_waitcnt vmcnt(0)\n"
    "v_mfma_f32_16x16x32_bf16 v[80:83], a[224:227], a[144:147], v[36:39]\n"
    "v_mfma_f32_16x16x32_bf16 v[80:83], a[228:231], a[148:151], v[80:83]\n"
    "v_mfma_f32_16x16x32_bf16 v[80:83], a[232:235], a[152:155], v[80:83]\n"
    "v_mfma_f32_16x16x32_bf16 v[80:83], a[236:239], a[156:159], v[80:83]\n"
    "v_mfma_f32_16x16x32_bf16 v[84:87], a[240:243], a[144:147], v[36:39]\n"
    "v_mfma_f32_16x16x32_bf16 v[84:87], a[244:247], a[148:151], v[84:87]\n"
    "v_mfma_f32_16x16x32_bf16 v[84:87], a[248:251], a[152:155], v[84:87]\n"
    "v_mfma_f32_16x16x32_bf16 v[84:87], a[252:255], a[156:159], v[84:87]\n"
    "s_nop 3\n"
    "v_exp_f32 v80, v80\n"
    "v_exp_f32 v81, v81\n"
    "v_exp_f32 v82, v82\n"
    "v_exp_f32 v83, v83\n"
    "v_exp_f32 v84, v84\n"
    "v_exp_f32 v85, v85\n"
    "v_exp_f32 v86, v86\n"
    "v_exp_f32 v87, v87\n"
    "v_cvt_pk_bf16_f32 v212, v80, v81\n"
    "v_cvt_pk_bf16_f32 v213, v82, v83\n"
    "v_cvt_pk_bf16_f32 v214, v84, v85\n"
    "v_cvt_pk_bf16_f32 v215, v86, v87\n"
    "s_add_u32 s57, s56, 0x4000\n"
    "s_add_u32 s58, s56, 0x5000\n"
    "buffer_load_dwordx4 a[224:227], v76, s[40:43], s57 offen offset:0 sc1\n"
    "buffer_load_dwordx4 a[228:231], v76, s[40:43], s57 offen offset:1024 sc1\n"
    "buffer_load_dwordx4 a[232:235], v76, s[40:43], s57 offen offset:2048 sc1\n"
    "buffer_load_dwordx4 a[236:239], v76, s[40:43], s57 offen offset:3072 sc1\n"
    "buffer_load_dwordx4 a[240:243], v76, s[40:43], s58 offen offset:0 sc1\n"
    "buffer_load_dwordx4 a[244:247], v76, s[40:43], s58 offen offset:1024 sc1\n"
    "buffer_load_dwordx4 a[248:251], v76, s[40:43], s58 offen offset:2048 sc1\n"
    "buffer_load_dwordx4 a[252:255], v76, s[40:43], s58 offen offset:3072 sc1\n"
    "s_waitcnt vmcnt(0)\n"
    "v_mfma_f32_16x16x32_bf16 v[80:83], a[224:227], a[144:147], v[36:39]\n"
    "v_mfma_f32_16x16x32_bf16 v[80:83], a[228:231], a[148:151], v[80:83]\n"
    "v_mfma_f32_16x16x32_bf16 v[80:83], a[232:235], a[152:155], v[80:83]\n"
    "v_mfma_f32_16x16x32_bf16 v[80:83], a[236:239], a[156:159], v[80:83]\n"
    "v_mfma_f32_16x16x32_bf16 v[84:87], a[240:243], a[144:147], v[36:39]\n"
    "v_mfma_f32_16x16x32_bf16 v[84:87], a[244:247], a[148:151], v[84:87]\n"
    "v_mfma_f32_16x16x32_bf16 v[84:87], a[248:251], a[152:155], v[84:87]\n"
    "v_mfma_f32_16x16x32_bf16 v[84:87], a[252:255], a[156:159], v[84:87]\n"
    "s_nop 3\n"
    "v_exp_f32 v80, v80\n"
    "v_exp_f32 v81, v81\n"
    "v_exp_f32 v82, v82\n"
    "v_exp_f32 v83, v83\n"
    "v_exp_f32 v84, v84\n"
    "v_exp_f32 v85, v85\n"
    "v_exp_f32 v86, v86\n"
    "v_exp_f32 v87, v87\n"
    "v_cvt_pk_bf16_f32 v216, v80, v81\n"
    "v_cvt_pk_bf16_f32 v217, v82, v83\n"
    "v_cvt_pk_bf16_f32 v218, v84, v85\n"
    "v_cvt_pk_bf16_f32 v219, v86, v87\n"
    "s_add_u32 s57, s56, 0x6000\n"
    "s_add_u32 s58, s56, 0x7000\n"
    "buffer_load_dwordx4 a[224:227], v76, s[40:43], s57 offen offset:0 sc1\n"
    "buffer_load_dwordx4 a[228:231], v76, s[40:43], s57 offen offset:1024 sc1\n"
    "buffer_load_dwordx4 a[232:235], v76, s[40:43], s57 offen offset:2048 sc1\n"
    "buffer_load_dwordx4 a[236:239], v76, s[40:43], s57 offen offset:3072 sc1\n"
    "buffer_load_dwordx4 a[240:243], v76, s[40:43], s58 offen offset:0 sc1\n"
    "buffer_load_dwordx4 a[244:247], v76, s[40:43], s58 offen offset:1024 sc1\n"
    "buffer_load_dwordx4 a[248:251], v76, s[40:43], s58 offen offset:2048 sc1\n"
    "buffer_load_dwordx4 a[252:255], v76, s[40:43], s58 offen offset:3072 sc1\n"
    "s_waitcnt vmcnt(0)\n"
    "v_mfma_f32_16x16x32_bf16 v[80:83], a[224:227], a[144:147], v[36:39]\n"
    "v_mfma_f32_16x16x32_bf16 v[80:83], a[228:231], a[148:151], v[80:83]\n"
    "v_mfma_f32_16x16x32_bf16 v[80:83], a[232:235], a[152:155], v[80:83]\n"
    "v_mfma_f32_16x16x32_bf16 v[80:83], a[236:239], a[156:159], v[80:83]\n"
    "v_mfma_f32_16x16x32_bf16 v[84:87], a[240:243], a[144:147], v[36:39]\n"
    "v_mfma_f32_16x16x32_bf16 v[84:87], a[244:247], a[148:151], v[84:87]\n"
    "v_mfma_f32_16x16x32_bf16 v[84:87], a[248:251], a[152:155], v[84:87]\n"
    "v_mfma_f32_16x16x32_bf16 v[84:87], a[252:255], a[156:159], v[84:87]\n"
    "s_nop 3\n"
    "v_exp_f32 v80, v80\n"
    "v_exp_f32 v81, v81\n"
    "v_exp_f32 v82, v82\n"
    "v_exp_f32 v83, v83\n"
    "v_exp_f32 v84, v84\n"
    "v_exp_f32 v85, v85\n"
    "v_exp_f32 v86, v86\n"
    "v_exp_f32 v87, v87\n"
    "v_cvt_pk_bf16_f32 v220, v80, v81\n"
    "v_cvt_pk_bf16_f32 v221, v82, v83\n"
    "v_cvt_pk_bf16_f32 v222, v84, v85\n"
    "v_cvt_pk_bf16_f32 v223, v86, v87\n"
    "v_mfma_f32_16x16x32_bf16 v[52:55], v[64:67], v[208:211], 0\n"
    "v_mfma_f32_16x16x32_bf16 v[52:55], v[64:67], v[212:215], v[52:55]\n"
    "v_mfma_f32_16x16x32_bf16 v[52:55], v[64:67], v[216:219], v[52:55]\n"
    "v_mfma_f32_16x16x32_bf16 v[52:55], v[64:67], v[220:223], v[52:55]\n"
    "s_add_u32 s57, s56, 0x8000\n"
    "s_add_u32 s58, s56, 0x9000\n"
    "buffer_load_dwordx4 a[224:227], v76, s[40:43], s57 offen offset:0 sc1\n"
    "buffer_load_dwordx4 a[228:231], v76, s[40:43], s57 offen offset:1024 sc1\n"
    "buffer_load_dwordx4 a[232:235], v76, s[40:43], s57 offen offset:2048 sc1\n"
    "buffer_load_dwordx4 a[236:239], v76, s[40:43], s57 offen offset:3072 sc1\n"
    "buffer_load_dwordx4 a[240:243], v76, s[40:43], s58 offen offset:0 sc1\n"
    "buffer_load_dwordx4 a[244:247], v76, s[40:43], s58 offen offset:1024 sc1\n"
    "buffer_load_dwordx4 a[248:251], v76, s[40:43], s58 offen offset:2048 sc1\n"
    "buffer_load_dwordx4 a[252:255], v76, s[40:43], s58 offen offset:3072 sc1\n"
    "s_waitcnt vmcnt(0)\n"
    "v_mfma_f32_16x16x32_bf16 a[4:7], v[144:147], a[224:227], a[4:7]\n"
    "v_mfma_f32_16x16x32_bf16 a[4:7], v[148:151], a[228:231], a[4:7]\n"
    "v_mfma_f32_16x16x32_bf16 a[4:7], v[152:155], a[232:235], a[4:7]\n"
    "v_mfma_f32_16x16x32_bf16 a[4:7], v[156:159], a[236:239], a[4:7]\n"
    "v_mfma_f32_16x16x32_bf16 a[20:23], v[144:147], a[240:243], a[20:23]\n"
    "v_mfma_f32_16x16x32_bf16 a[20:23], v[148:151], a[244:247], a[20:23]\n"
    "v_mfma_f32_16x16x32_bf16 a[20:23], v[152:155], a[248:251], a[20:23]\n"
    "v_mfma_f32_16x16x32_bf16 a[20:23], v[156:159], a[252:255], a[20:23]\n"
    "s_nop 7\n"
    "s_add_u32 s57, s56, 0xa000\n"
    "s_add_u32 s58, s56, 0xb000\n"
    "buffer_load_dwordx4 a[224:227], v76, s[40:43], s57 offen offset:0 sc1\n"
    "buffer_load_dwordx4 a[228:231], v76, s[40:43], s57 offen offset:1024 sc1\n"
    "buffer_load_dwordx4 a[232:235], v76, s[40:43], s57 offen offset:2048 sc1\n"
    "buffer_load_dwordx4 a[236:239], v76, s[40:43], s57 offen offset:3072 sc1\n"
    "buffer_load_dwordx4 a[240:243], v76, s[40:43], s58 offen offset:0 sc1\n"
    "buffer_load_dwordx4 a[244:247], v76, s[40:43], s58 offen offset:1024 sc1\n"
    "buffer_load_dwordx4 a[248:251], v76, s[40:43], s58 offen offset:2048 sc1\n"
    "buffer_load_dwordx4 a[252:255], v76, s[40:43], s58 offen offset:3072 sc1\n"
    "s_waitcnt vmcnt(0)\n"
    "v_mfma_f32_16x16x32_bf16 a[36:39], v[144:147], a[224:227], a[36:39]\n"
    "v_mfma_f32_16x16x32_bf16 a[36:39], v[148:151], a[228:231], a[36:39]\n"
    "v_mfma_f32_16x16x32_bf16 a[36:39], v[152:155], a[232:235], a[36:39]\n"
    "v_mfma_f32_16x16x32_bf16 a[36:39], v[156:159], a[236:239], a[36:39]\n"
    "v_mfma_f32_16x16x32_bf16 a[52:55], v[144:147], a[240:243], a[52:55]\n"
    "v_mfma_f32_16x16x32_bf16 a[52:55], v[148:151], a[244:247], a[52:55]\n"
    "v_mfma_f32_16x16x32_bf16 a[52:55], v[152:155], a[248:251], a[52:55]\n"
    "v_mfma_f32_16x16x32_bf16 a[52:55], v[156:159], a[252:255], a[52:55]\n"
    "s_nop 7\n"
    "s_add_u32 s57, s56, 0xc000\n"
    "s_add_u32 s58, s56, 0xd000\n"
    "buffer_load_dwordx4 a[224:227], v76, s[40:43], s57 offen offset:0 sc1\n"
    "buffer_load_dwordx4 a[228:231], v76, s[40:43], s57 offen offset:1024 sc1\n"
    "buffer_load_dwordx4 a[232:235], v76, s[40:43], s57 offen offset:2048 sc1\n"
    "buffer_load_dwordx4 a[236:239], v76, s[40:43], s57 offen offset:3072 sc1\n"
    "buffer_load_dwordx4 a[240:243], v76, s[40:43], s58 offen offset:0 sc1\n"
    "buffer_load_dwordx4 a[244:247], v76, s[40:43], s58 offen offset:1024 sc1\n"
    "buffer_load_dwordx4 a[248:251], v76, s[40:43], s58 offen offset:2048 sc1\n"
    "buffer_load_dwordx4 a[252:255], v76, s[40:43], s58 offen offset:3072 sc1\n"
    "s_waitcnt vmcnt(0)\n"
    "v_mfma_f32_16x16x32_bf16 a[68:71], v[144:147], a[224:227], a[68:71]\n"
    "v_mfma_f32_16x16x32_bf16 a[68:71], v[148:151], a[228:231], a[68:71]\n"
    "v_mfma_f32_16x16x32_bf16 a[68:71], v[152:155], a[232:235], a[68:71]\n"
    "v_mfma_f32_16x16x32_bf16 a[68:71], v[156:159], a[236:239], a[68:71]\n"
    "v_mfma_f32_16x16x32_bf16 a[84:87], v[144:147], a[240:243], a[84:87]\n"
    "v_mfma_f32_16x16x32_bf16 a[84:87], v[148:151], a[244:247], a[84:87]\n"
    "v_mfma_f32_16x16x32_bf16 a[84:87], v[152:155], a[248:251], a[84:87]\n"
    "v_mfma_f32_16x16x32_bf16 a[84:87], v[156:159], a[252:255], a[84:87]\n"
    "s_nop 7\n"
    "s_add_u32 s57, s56, 0xe000\n"
    "s_add_u32 s58, s56, 0xf000\n"
    "buffer_load_dwordx4 a[224:227], v76, s[40:43], s57 offen offset:0 sc1\n"
    "buffer_load_dwordx4 a[228:231], v76, s[40:43], s57 offen offset:1024 sc1\n"
    "buffer_load_dwordx4 a[232:235], v76, s[40:43], s57 offen offset:2048 sc1\n"
    "buffer_load_dwordx4 a[236:239], v76, s[40:43], s57 offen offset:3072 sc1\n"
    "buffer_load_dwordx4 a[240:243], v76, s[40:43], s58 offen offset:0 sc1\n"
    "buffer_load_dwordx4 a[244:247], v76, s[40:43], s58 offen offset:1024 sc1\n"
    "buffer_load_dwordx4 a[248:251], v76, s[40:43], s58 offen offset:2048 sc1\n"
    "buffer_load_dwordx4 a[252:255], v76, s[40:43], s58 offen offset:3072 sc1\n"
    "s_waitcnt vmcnt(0)\n"
    "v_mfma_f32_16x16x32_bf16 a[100:103], v[144:147], a[224:227], a[100:103]\n"
    "v_mfma_f32_16x16x32_bf16 a[100:103], v[148:151], a[228:231], a[100:103]\n"
    "v_mfma_f32_16x16x32_bf16 a[100:103], v[152:155], a[232:235], a[100:103]\n"
    "v_mfma_f32_16x16x32_bf16 a[100:103], v[156:159], a[236:239], a[100:103]\n"
    "v_mfma_f32_16x16x32_bf16 a[116:119], v[144:147], a[240:243], a[116:119]\n"
    "v_mfma_f32_16x16x32_bf16 a[116:119], v[148:151], a[244:247], a[116:119]\n"
    "v_mfma_f32_16x16x32_bf16 a[116:119], v[152:155], a[248:251], a[116:119]\n"
    "v_mfma_f32_16x16x32_bf16 a[116:119], v[156:159], a[252:255], a[116:119]\n"
    "s_nop 7\n"
    "v_mov_b32 v144, 0\n"
    "v_mov_b32 v145, 0\n"
    "v_mov_b32 v146, 0\n"
    "v_mov_b32 v147, 0\n"
    "v_mov_b32 v148, 0\n"
    "v_mov_b32 v149, 0\n"
    "v_mov_b32 v150, 0\n"
    "v_mov_b32 v151, 0\n"
    "v_mov_b32 v152, 0\n"
    "v_mov_b32 v153, 0\n"
    "v_mov_b32 v154, 0\n"
    "v_mov_b32 v155, 0\n"
    "v_mov_b32 v156, 0\n"
    "v_mov_b32 v157, 0\n"
    "v_mov_b32 v158, 0\n"
    "v_mov_b32 v159, 0\n"
    "v_sub_f32 v11, 0, v9\n"
    "v_exp_f32 v11, v11\n"
    "s_nop 0\n"
    "v_mul_f32 v69, v11, v69\n"
    "ds_bpermute_b32 v12, v99, v11 offset:0\n"
    "ds_bpermute_b32 v13, v99, v11 offset:4\n"
    "ds_bpermute_b32 v14, v99, v11 offset:8\n"
    "ds_bpermute_b32 v15, v99, v11 offset:12\n"
    "s_waitcnt lgkmcnt(0)\n"
    "v_accvgpr_read_b32 v80, a4\n"
    "v_accvgpr_read_b32 v81, a5\n"
    "v_accvgpr_read_b32 v82, a6\n"
    "v_accvgpr_read_b32 v83, a7\n"
    "v_accvgpr_read_b32 v84, a20\n"
    "v_accvgpr_read_b32 v85, a21\n"
    "v_accvgpr_read_b32 v86, a22\n"
    "v_accvgpr_read_b32 v87, a23\n"
    "v_mul_f32 v80, v12, v80\n"
    "v_mul_f32 v81, v13, v81\n"
    "v_mul_f32 v82, v14, v82\n"
    "v_mul_f32 v83, v15, v83\n"
    "v_mul_f32 v84, v12, v84\n"
    "v_mul_f32 v85, v13, v85\n"
    "v_mul_f32 v86, v14, v86\n"
    "v_mul_f32 v87, v15, v87\n"
    "v_accvgpr_write_b32 a4, v80\n"
    "v_accvgpr_write_b32 a5, v81\n"
    "v_accvgpr_write_b32 a6, v82\n"
    "v_accvgpr_write_b32 a7, v83\n"
    "v_accvgpr_write_b32 a20, v84\n"
    "v_accvgpr_write_b32 a21, v85\n"
    "v_accvgpr_write_b32 a22, v86\n"
    "v_accvgpr_write_b32 a23, v87\n"
    "v_accvgpr_read_b32 v80, a36\n"
    "v_accvgpr_read_b32 v81, a37\n"
    "v_accvgpr_read_b32 v82, a38\n"
    "v_accvgpr_read_b32 v83, a39\n"
    "v_accvgpr_read_b32 v84, a52\n"
    "v_accvgpr_read_b32 v85, a53\n"
    "v_accvgpr_read_b32 v86, a54\n"
    "v_accvgpr_read_b32 v87, a55\n"
    "v_mul_f32 v80, v12, v80\n"
    "v_mul_f32 v81, v13, v81\n"
    "v_mul_f32 v82, v14, v82\n"
    "v_mul_f32 v83, v15, v83\n"
    "v_mul_f32 v84, v12, v84\n"
    "v_mul_f32 v85, v13, v85\n"
    "v_mul_f32 v86, v14, v86\n"
    "v_mul_f32 v87, v15, v87\n"
    "v_accvgpr_write_b32 a36, v80\n"
    "v_accvgpr_write_b32 a37, v81\n"
    "v_accvgpr_write_b32 a38, v82\n"
    "v_accvgpr_write_b32 a39, v83\n"
    "v_accvgpr_write_b32 a52, v84\n"
    "v_accvgpr_write_b32 a53, v85\n"
    "v_accvgpr_write_b32 a54, v86\n"
    "v_accvgpr_write_b32 a55, v87\n"
    "v_accvgpr_read_b32 v80, a68\n"
    "v_accvgpr_read_b32 v81, a69\n"
    "v_accvgpr_read_b32 v82, a70\n"
    "v_accvgpr_read_b32 v83, a71\n"
    "v_accvgpr_read_b32 v84, a84\n"
    "v_accvgpr_read_b32 v85, a85\n"
    "v_accvgpr_read_b32 v86, a86\n"
    "v_accvgpr_read_b32 v87, a87\n"
    "v_mul_f32 v80, v12, v80\n"
    "v_mul_f32 v81, v13, v81\n"
    "v_mul_f32 v82, v14, v82\n"
    "v_mul_f32 v83, v15, v83\n"
    "v_mul_f32 v84, v12, v84\n"
    "v_mul_f32 v85, v13, v85\n"
    "v_mul_f32 v86, v14, v86\n"
    "v_mul_f32 v87, v15, v87\n"
    "v_accvgpr_write_b32 a68, v80\n"
    "v_accvgpr_write_b32 a69, v81\n"
    "v_accvgpr_write_b32 a70, v82\n"
    "v_accvgpr_write_b32 a71, v83\n"
    "v_accvgpr_write_b32 a84, v84\n"
    "v_accvgpr_write_b32 a85, v85\n"
    "v_accvgpr_write_b32 a86, v86\n"
    "v_accvgpr_write_b32 a87, v87\n"
    "v_accvgpr_read_b32 v80, a100\n"
    "v_accvgpr_read_b32 v81, a101\n"
    "v_accvgpr_read_b32 v82, a102\n"
    "v_accvgpr_read_b32 v83, a103\n"
    "v_accvgpr_read_b32 v84, a116\n"
    "v_accvgpr_read_b32 v85, a117\n"
    "v_accvgpr_read_b32 v86, a118\n"
    "v_accvgpr_read_b32 v87, a119\n"
    "v_mul_f32 v80, v12, v80\n"
    "v_mul_f32 v81, v13, v81\n"
    "v_mul_f32 v82, v14, v82\n"
    "v_mul_f32 v83, v15, v83\n"
    "v_mul_f32 v84, v12, v84\n"
    "v_mul_f32 v85, v13, v85\n"
    "v_mul_f32 v86, v14, v86\n"
    "v_mul_f32 v87, v15, v87\n"
    "v_accvgpr_write_b32 a100, v80\n"
    "v_accvgpr_write_b32 a101, v81\n"
    "v_accvgpr_write_b32 a102, v82\n"
    "v_accvgpr_write_b32 a103, v83\n"
    "v_accvgpr_write_b32 a116, v84\n"
    "v_accvgpr_write_b32 a117, v85\n"
    "v_accvgpr_write_b32 a118, v86\n"
    "v_accvgpr_write_b32 a119, v87\n"
    "s_nop 7\n"
    "s_nop 7\n"
    "s_branch L_ret_F1\n"
    "L_rare_F2:\n"
    "s_nop 7\n"
    "s_nop 7\n"
    "s_sub_u32 s56, s48, 0x10000\n"
    "s_add_u32 s57, s56, 0x0\n"
    "s_add_u32 s58, s56, 0x1000\n"
    "buffer_load_dwordx4 a[224:227], v76, s[40:43], s57 offen offset:0 sc1\n"
    "buffer_load_dwordx4 a[228:231], v76, s[40:43], s57 offen offset:1024 sc1\n"
    "buffer_load_dwordx4 a[232:235], v76, s[40:43], s57 offen offset:2048 sc1\n"
    "buffer_load_dwordx4 a[236:239], v76, s[40:43], s57 offen offset:3072 sc1\n"
    "buffer_load_dwordx4 a[240:243], v76, s[40:43], s58 offen offset:0 sc1\n"
    "buffer_load_dwordx4 a[244:247], v76, s[40:43], s58 offen offset:1024 sc1\n"
    "buffer_load_dwordx4 a[248:251], v76, s[40:43], s58 offen offset:2048 sc1\n"
    "buffer_load_dwordx4 a[252:255], v76, s[40:43], s58 offen offset:3072 sc1\n"
    "s_waitcnt vmcnt(0)\n"
    "v_mfma_f32_16x16x32_bf16 v[80:83], a[224:227], a[160:163], v[40:43]\n"
    "v_mfma_f32_16x16x32_bf16 v[80:83], a[228:231], a[164:167], v[80:83]\n"
    "v_mfma_f32_16x16x32_bf16 v[80:83], a[232:235], a[168:171], v[80:83]\n"
    "v_mfma_f32_16x16x32_bf16 v[80:83], a[236:239], a[172:175], v[80:83]\n"
    "v_mfma_f32_16x16x32_bf16 v[84:87], a[240:243], a[160:163], v[40:43]\n"
    "v_mfma_f32_16x16x32_bf16 v[84:87], a[244:247], a[164:167], v[84:87]\n"
    "v_mfma_f32_16x16x32_bf16 v[84:87], a[248:251], a[168:171], v[84:87]\n"
    "v_mfma_f32_16x16x32_bf16 v[84:87], a[252:255], a[172:175], v[84:87]\n"
    "s_nop 3\n"
    "v_max_f32 v9, v80, v80\n"
    "v_max3_f32 v9, v9, v80, v81\n"
    "v_max3_f32 v9, v9, v82, v83\n"
    "s_nop 0\n"
    "v_max3_f32 v9, v9, v84, v85\n"
    "v_max3_f32 v9, v9, v86, v87\n"
    "s_add_u32 s57, s56, 0x2000\n"
    "s_add_u32 s58, s56, 0x3000\n"
    "buffer_load_dwordx4 a[224:227], v76, s[40:43], s57 offen offset:0 sc1\n"
    "buffer_load_dwordx4 a[228:231], v76, s[40:43], s57 offen offset:1024 sc1\n"
    "buffer_load_dwordx4 a[232:235], v76, s[40:43], s57 offen offset:2048 sc1\n"
    "buffer_load_dwordx4 a[236:239], v76, s[40:43], s57 offen offset:3072 sc1\n"
    "buffer_load_dwordx4 a[240:243], v76, s[40:43], s58 offen offset:0 sc1\n"
    "buffer_load_dwordx4 a[244:247], v76, s[40:43], s58 offen offset:1024 sc1\n"
    "buffer_load_dwordx4 a[248:251], v76, s[40:43], s58 offen offset:2048 sc1\n"
    "buffer_load_dwordx4 a[252:255], v76, s[40:43], s58 offen offset:3072 sc1\n"
    "s_waitcnt vmcnt(0)\n"
    "v_mfma_f32_16x16x32_bf16 v[80:83], a[224:227], a[160:163], v[40:43]\n"
    "v_mfma_f32_16x16x32_bf16 v[80:83], a[228:231], a[164:167], v[80:83]\n"
    "v_mfma_f32_16x16x32_bf16 v[80:83], a[232:235], a[168:171], v[80:83]\n"
    "v_mfma_f32_16x16x32_bf16 v[80:83], a[236:239], a[172:175], v[80:83]\n"
    "v_mfma_f32_16x16x32_bf16 v[84:87], a[240:243], a[160:163], v[40:43]\n"
    "v_mfma_f32_16x16x32_bf16 v[84:87], a[244:247], a[164:167], v[84:87]\n"
    "v_mfma_f32_16x16x32_bf16 v[84:87], a[248:251], a[168:171], v[84:87]\n"
    "v_mfma_f32_16x16x32_bf16 v[84:87], a[252:255], a[172:175], v[84:87]\n"
    "s_nop 3\n"
    "v_max3_f32 v9, v9, v80, v81\n"
    "v_max3_f32 v9, v9, v82, v83\n"
    "s_nop 1\n"
    "v_max3_f32 v9, v9, v84, v85\n"
    "v_max3_f32 v9, v9, v86, v87\n"
    "s_add_u32 s57, s56, 0x4000\n"
    "s_add_u32 s58, s56, 0x5000\n"
    "buffer_load_dwordx4 a[224:227], v76, s[40:43], s57 offen offset:0 sc1\n"
    "buffer_load_dwordx4 a[228:231], v76, s[40:43], s57 offen offset:1024 sc1\n"
    "buffer_load_dwordx4 a[232:235], v76, s[40:43], s57 offen offset:2048 sc1\n"
    "buffer_load_dwordx4 a[236:239], v76, s[40:43], s57 offen offset:3072 sc1\n"
    "buffer_load_dwordx4 a[240:243], v76, s[40:43], s58 offen offset:0 sc1\n"
    "buffer_load_dwordx4 a[244:247], v76, s[40:43], s58 offen offset:1024 sc1\n"
    "buffer_load_dwordx4 a[248:251], v76, s[40:43], s58 offen offset:2048 sc1\n"
    "buffer_load_dwordx4 a[252:255], v76, s[40:43], s58 offen offset:3072 sc1\n"
    "s_waitcnt vmcnt(0)\n"
    "v_mfma_f32_16x16x32_bf16 v[80:83], a[224:227], a[160:163], v[40:43]\n"
    "v_mfma_f32_16x16x32_bf16 v[80:83], a[228:231], a[164:167], v[80:83]\n"
    "v_mfma_f32_16x16x32_bf16 v[80:83], a[232:235], a[168:171], v[80:83]\n"
    "v_mfma_f32_16x16x32_bf16 v[80:83], a[236:239], a[172:175], v[80:83]\n"
    "v_mfma_f32_16x16x32_bf16 v[84:87], a[240:243], a[160:163], v[40:43]\n"
    "v_mfma_f32_16x16x32_bf16 v[84:87], a[244:247], a[164:167], v[84:87]\n"
    "v_mfma_f32_16x16x32_bf16 v[84:87], a[248:251], a[168:171], v[84:87]\n"
    "v_mfma_f32_16x16x32_bf16 v[84:87], a[252:255], a[172:175], v[84:87]\n"
    "s_nop 3\n"
    "v_max3_f32 v9, v9, v80, v81\n"
    "v_max3_f32 v9, v9, v82, v83\n"
    "s_nop 1\n"
    "v_max3_f32 v9, v9, v84, v85\n"
    "v_max3_f32 v9, v9, v86, v87\n"
    "s_add_u32 s57, s56, 0x6000\n"
    "s_add_u32 s58, s56, 0x7000\n"
    "buffer_load_dwordx4 a[224:227], v76, s[40:43], s57 offen offset:0 sc1\n"
    "buffer_load_dwordx4 a[228:231], v76, s[40:43], s57 offen offset:1024 sc1\n"
    "buffer_load_dwordx4 a[232:235], v76, s[40:43], s57 offen offset:2048 sc1\n"
    "buffer_load_dwordx4 a[236:239], v76, s[40:43], s57 offen offset:3072 sc1\n"
    "buffer_load_dwordx4 a[240:243], v76, s[40:43], s58 offen offset:0 sc1\n"
    "buffer_load_dwordx4 a[244:247], v76, s[40:43], s58 offen offset:1024 sc1\n"
    "buffer_load_dwordx4 a[248:251], v76, s[40:43], s58 offen offset:2048 sc1\n"
    "buffer_load_dwordx4 a[252:255], v76, s[40:43], s58 offen offset:3072 sc1\n"
    "s_waitcnt vmcnt(0)\n"
    "v_mfma_f32_16x16x32_bf16 v[80:83], a[224:227], a[160:163], v[40:43]\n"
    "v_mfma_f32_16x16x32_bf16 v[80:83], a[228:231], a[164:167], v[80:83]\n"
    "v_mfma_f32_16x16x32_bf16 v[80:83], a[232:235], a[168:171], v[80:83]\n"
    "v_mfma_f32_16x16x32_bf16 v[80:83], a[236:239], a[172:175], v[80:83]\n"
    "v_mfma_f32_16x16x32_bf16 v[84:87], a[240:243], a[160:163], v[40:43]\n"
    "v_mfma_f32_16x16x32_bf16 v[84:87], a[244:247], a[164:167], v[84:87]\n"
    "v_mfma_f32_16x16x32_bf16 v[84:87], a[248:251], a[168:171], v[84:87]\n"
    "v_mfma_f32_16x16x32_bf16 v[84:87], a[252:255], a[172:175], v[84:87]\n"
    "s_nop 3\n"
    "v_max3_f32 v9, v9, v80, v81\n"
    "v_max3_f32 v9, v9, v82, v83\n"
    "s_nop 1\n"
    "v_max3_f32 v9, v9, v84, v85\n"
    "v_max3_f32 v9, v9, v86, v87\n"
    "v_mov_b32 v10, v9\n"
    "s_nop 1\n"
    "v_permlane16_swap_b32 v10, v9\n"
    "v_max_f32 v9, v10, v9\n"
    "v_mov_b32 v10, v9\n"
    "s_nop 1\n"
    "v_permlane32_swap_b32 v10, v9\n"
    "v_max_f32 v9, v10, v9\n"
    "v_max_f32 v9, 0, v9\n"
    "v_sub_f32 v40, v40, v9\n"
    "v_sub_f32 v41, v41, v9\n"
    "v_sub_f32 v42, v42, v9\n"
    "v_sub_f32 v43, v43, v9\n"
    "s_add_u32 s57, s56, 0x0\n"
    "s_add_u32 s58, s56, 0x1000\n"
    "buffer_load_dwordx4 a[224:227], v76, s[40:43], s57 offen offset:0 sc1\n"
    "buffer_load_dwordx4 a[228:231], v76, s[40:43], s57 offen offset:1024 sc1\n"
    "buffer_load_dwordx4 a[232:235], v76, s[40:43], s57 offen offset:2048 sc1\n"
    "buffer_load_dwordx4 a[236:239], v76, s[40:43], s57 offen offset:3072 sc1\n"
    "buffer_load_dwordx4 a[240:243], v76, s[40:43], s58 offen offset:0 sc1\n"
    "buffer_load_dwordx4 a[244:247], v76, s[40:43], s58 offen offset:1024 sc1\n"
    "buffer_load_dwordx4 a[248:251], v76, s[40:43], s58 offen offset:2048 sc1\n"
    "buffer_load_dwordx4 a[252:255], v76, s[40:43], s58 offen offset:3072 sc1\n"
    "s_waitcnt vmcnt(0)\n"
    "v_mfma_f32_16x16x32_bf16 v[80:83], a[224:227], a[160:163], v[40:43]\n"
    "v_mfma_f32_16x16x32_bf16 v[80:83], a[228:231], a[164:167], v[80:83]\n"
    "v_mfma_f32_16x16x32_bf16 v[80:83], a[232:235], a[168:171], v[80:83]\n"
    "v_mfma_f32_16x16x32_bf16 v[80:83], a[236:239], a[172:175], v[80:83]\n"
    "v_mfma_f32_16x16x32_bf16 v[84:87], a[240:243], a[160:163], v[40:43]\n"
    "v_mfma_f32_16x16x32_bf16 v[84:87], a[244:247], a[164:167], v[84:87]\n"
    "v_mfma_f32_16x16x32_bf16 v[84:87], a[248:251], a[168:171], v[84:87]\n"
    "v_mfma_f32_16x16x32_bf16 v[84:87], a[252:255], a[172:175], v[84:87]\n"
    "s_nop 3\n"
    "v_exp_f32 v80, v80\n"
    "v_exp_f32 v81, v81\n"
    "v_exp_f32 v82, v82\n"
    "v_exp_f32 v83, v83\n"
    "v_exp_f32 v84, v84\n"
    "v_exp_f32 v85, v85\n"
    "v_exp_f32 v86, v86\n"
    "v_exp_f32 v87, v87\n"
    "v_cvt_pk_bf16_f32 v224, v80, v81\n"
    "v_cvt_pk_bf16_f32 v225, v82, v83\n"
    "v_cvt_pk_bf16_f32 v226, v84, v85\n"
    "v_cvt_pk_bf16_f32 v227, v86, v87\n"
    "s_add_u32 s57, s56, 0x2000\n"
    "s_add_u32 s58, s56, 0x3000\n"
    "buffer_load_dwordx4 a[224:227], v76, s[40:43], s57 offen offset:0 sc1\n"
    "buffer_load_dwordx4 a[228:231], v76, s[40:43], s57 offen offset:1024 sc1\n"
    "buffer_load_dwordx4 a[232:235], v76, s[40:43], s57 offen offset:2048 sc1\n"
    "buffer_load_dwordx4 a[236:239], v76, s[40:43], s57 offen offset:3072 sc1\n"
    "buffer_load_dwordx4 a[240:243], v76, s[40:43], s58 offen offset:0 sc1\n"
    "buffer_load_dwordx4 a[244:247], v76, s[40:43], s58 offen offset:1024 sc1\n"
    "buffer_load_dwordx4 a[248:251], v76, s[40:43], s58 offen offset:2048 sc1\n"
    "buffer_load_dwordx4 a[252:255], v76, s[40:43], s58 offen offset:3072 sc1\n"
    "s_waitcnt vmcnt(0)\n"
    "v_mfma_f32_16x16x32_bf16 v[80:83], a[224:227], a[160:163], v[40:43]\n"
    "v_mfma_f32_16x16x32_bf16 v[80:83], a[228:231], a[164:167], v[80:83]\n"
    "v_mfma_f32_16x16x32_bf16 v[80:83], a[232:235], a[168:171], v[80:83]\n"
    "v_mfma_f32_16x16x32_bf16 v[80:83], a[236:239], a[172:175], v[80:83]\n"
    "v_mfma_f32_16x16x32_bf16 v[84:87], a[240:243], a[160:163], v[40:43]\n"
    "v_mfma_f32_16x16x32_bf16 v[84:87], a[244:247], a[164:167], v[84:87]\n"
    "v_mfma_f32_16x16x32_bf16 v[84:87], a[248:251], a[168:171], v[84:87]\n"
    "v_mfma_f32_16x16x32_bf16 v[84:87], a[252:255], a[172:175], v[84:87]\n"
    "s_nop 3\n"
    "v_exp_f32 v80, v80\n"
    "v_exp_f32 v81, v81\n"
    "v_exp_f32 v82, v82\n"
    "v_exp_f32 v83, v83\n"
    "v_exp_f32 v84, v84\n"
    "v_exp_f32 v85, v85\n"
    "v_exp_f32 v86, v86\n"
    "v_exp_f32 v87, v87\n"
    "v_cvt_pk_bf16_f32 v228, v80, v81\n"
    "v_cvt_pk_bf16_f32 v229, v82, v83\n"
    "v_cvt_pk_bf16_f32 v230, v84, v85\n"
    "v_cvt_pk_bf16_f32 v231, v86, v87\n"
    "s_add_u32 s57, s56, 0x4000\n"
    "s_add_u32 s58, s56, 0x5000\n"
    "buffer_load_dwordx4 a[224:227], v76, s[40:43], s57 offen offset:0 sc1\n"
    "buffer_load_dwordx4 a[228:231], v76, s[40:43], s57 offen offset:1024 sc1\n"
    "buffer_load_dwordx4 a[232:235], v76, s[40:43], s57 offen offset:2048 sc1\n"
    "buffer_load_dwordx4 a[236:239], v76, s[40:43], s57 offen offset:3072 sc1\n"
    "buffer_load_dwordx4 a[240:243], v76, s[40:43], s58 offen offset:0 sc1\n"
    "buffer_load_dwordx4 a[244:247], v76, s[40:43], s58 offen offset:1024 sc1\n"
    "buffer_load_dwordx4 a[248:251], v76, s[40:43], s58 offen offset:2048 sc1\n"
    "buffer_load_dwordx4 a[252:255], v76, s[40:43], s58 offen offset:3072 sc1\n"
    "s_waitcnt vmcnt(0)\n"
    "v_mfma_f32_16x16x32_bf16 v[80:83], a[224:227], a[160:163], v[40:43]\n"
    "v_mfma_f32_16x16x32_bf16 v[80:83], a[228:231], a[164:167], v[80:83]\n"
    "v_mfma_f32_16x16x32_bf16 v[80:83], a[232:235], a[168:171], v[80:83]\n"
    "v_mfma_f32_16x16x32_bf16 v[80:83], a[236:239], a[172:175], v[80:83]\n"
    "v_mfma_f32_16x16x32_bf16 v[84:87], a[240:243], a[160:163], v[40:43]\n"
    "v_mfma_f32_16x16x32_bf16 v[84:87], a[244:247], a[164:167], v[84:87]\n"
    "v_mfma_f32_16x16x32_bf16 v[84:87], a[248:251], a[168:171], v[84:87]\n"
    "v_mfma_f32_16x16x32_bf16 v[84:87], a[252:255], a[172:175], v[84:87]\n"
    "s_nop 3\n"
    "v_exp_f32 v80, v80\n"
    "v_exp_f32 v81, v81\n"
    "v_exp_f32 v82, v82\n"
    "v_exp_f32 v83, v83\n"
    "v_exp_f32 v84, v84\n"
    "v_exp_f32 v85, v85\n"
    "v_exp_f32 v86, v86\n"
    "v_exp_f32 v87, v87\n"
    "v_cvt_pk_bf16_f32 v232, v80, v81\n"
    "v_cvt_pk_bf16_f32 v233, v82, v83\n"
    "v_cvt_pk_bf16_f32 v234, v84, v85\n"
    "v_cvt_pk_bf16_f32 v235, v86, v87\n"
    "s_add_u32 s57, s56, 0x6000\n"
    "s_add_u32 s58, s56, 0x7000\n"
    "buffer_load_dwordx4 a[224:227], v76, s[40:43], s57 offen offset:0 sc1\n"
    "buffer_load_dwordx4 a[228:231], v76, s[40:43], s57 offen offset:1024 sc1\n"
    "buffer_load_dwordx4 a[232:235], v76, s[40:43], s57 offen offset:2048 sc1\n"
    "buffer_load_dwordx4 a[236:239], v76, s[40:43], s57 offen offset:3072 sc1\n"
    "buffer_load_dwordx4 a[240:243], v76, s[40:43], s58 offen offset:0 sc1\n"
    "buffer_load_dwordx4 a[244:247], v76, s[40:43], s58 offen offset:1024 sc1\n"
    "buffer_load_dwordx4 a[248:251], v76, s[40:43], s58 offen offset:2048 sc1\n"
    "buffer_load_dwordx4 a[252:255], v76, s[40:43], s58 offen offset:3072 sc1\n"
    "s_waitcnt vmcnt(0)\n"
    "v_mfma_f32_16x16x32_bf16 v[80:83], a[224:227], a[160:163], v[40:43]\n"
    "v_mfma_f32_16x16x32_bf16 v[80:83], a[228:231], a[164:167], v[80:83]\n"
    "v_mfma_f32_16x16x32_bf16 v[80:83], a[232:235], a[168:171], v[80:83]\n"
    "v_mfma_f32_16x16x32_bf16 v[80:83], a[236:239], a[172:175], v[80:83]\n"
    "v_mfma_f32_16x16x32_bf16 v[84:87], a[240:243], a[160:163], v[40:43]\n"
    "v_mfma_f32_16x16x32_bf16 v[84:87], a[244:247], a[164:167], v[84:87]\n"
    "v_mfma_f32_16x16x32_bf16 v[84:87], a[248:251], a[168:171], v[84:87]\n"
    "v_mfma_f32_16x16x32_bf16 v[84:87], a[252:255], a[172:175], v[84:87]\n"
    "s_nop 3\n"
    "v_exp_f32 v80, v80\n"
    "v_exp_f32 v81, v81\n"
    "v_exp_f32 v82, v82\n"
    "v_exp_f32 v83, v83\n"
    "v_exp_f32 v84, v84\n"
    "v_exp_f32 v85, v85\n"
    "v_exp_f32 v86, v86\n"
    "v_exp_f32 v87, v87\n"
    "v_cvt_pk_bf16_f32 v236, v80, v81\n"
    "v_cvt_pk_bf16_f32 v237, v82, v83\n"
    "v_cvt_pk_bf16_f32 v238, v84, v85\n"
    "v_cvt_pk_bf16_f32 v239, v86, v87\n"
    "v_mfma_f32_16x16x32_bf16 v[56:59], v[64:67], v[224:227], 0\n"
    "v_mfma_f32_16x16x32_bf16 v[56:59], v[64:67], v[228:231], v[56:59]\n"
    "v_mfma_f32_16x16x32_bf16 v[56:59], v[64:67], v[232:235], v[56:59]\n"
    "v_mfma_f32_16x16x32_bf16 v[56:59], v[64:67], v[236:239], v[56:59]\n"
    "s_add_u32 s57, s56, 0x8000\n"
    "s_add_u32 s58, s56, 0x9000\n"
    "buffer_load_dwordx4 a[224:227], v76, s[40:43], s57 offen offset:0 sc1\n"
    "buffer_load_dwordx4 a[228:231], v76, s[40:43], s57 offen offset:1024 sc1\n"
    "buffer_load_dwordx4 a[232:235], v76, s[40:43], s57 offen offset:2048 sc1\n"
    "buffer_load_dwordx4 a[236:239], v76, s[40:43], s57 offen offset:3072 sc1\n"
    "buffer_load_dwordx4 a[240:243], v76, s[40:43], s58 offen offset:0 sc1\n"
    "buffer_load_dwordx4 a[244:247], v76, s[40:43], s58 offen offset:1024 sc1\n"
    "buffer_load_dwordx4 a[248:251], v76, s[40:43], s58 offen offset:2048 sc1\n"
    "buffer_load_dwordx4 a[252:255], v76, s[40:43], s58 offen offset:3072 sc1\n"
    "s_waitcnt vmcnt(0)\n"
    "v_mfma_f32_16x16x32_bf16 a[8:11], v[160:163], a[224:227], a[8:11]\n"
    "v_mfma_f32_16x16x32_bf16 a[8:11], v[164:167], a[228:231], a[8:11]\n"
    "v_mfma_f32_16x16x32_bf16 a[8:11], v[168:171], a[232:235], a[8:11]\n"
    "v_mfma_f32_16x16x32_bf16 a[8:11], v[172:175], a[236:239], a[8:11]\n"
    "v_mfma_f32_16x16x32_bf16 a[24:27], v[160:163], a[240:243], a[24:27]\n"
    "v_mfma_f32_16x16x32_bf16 a[24:27], v[164:167], a[244:247], a[24:27]\n"
    "v_mfma_f32_16x16x32_bf16 a[24:27], v[168:171], a[248:251], a[24:27]\n"
    "v_mfma_f32_16x16x32_bf16 a[24:27], v[172:175], a[252:255], a[24:27]\n"
    "s_nop 7\n"
    "s_add_u32 s57, s56, 0xa000\n"
    "s_add_u32 s58, s56, 0xb000\n"
    "buffer_load_dwordx4 a[224:227], v76, s[40:43], s57 offen offset:0 sc1\n"
    "buffer_load_dwordx4 a[228:231], v76, s[40:43], s57 offen offset:1024 sc1\n"
    "buffer_load_dwordx4 a[232:235], v76, s[40:43], s57 offen offset:2048 sc1\n"
    "buffer_load_dwordx4 a[236:239], v76, s[40:43], s57 offen offset:3072 sc1\n"
    "buffer_load_dwordx4 a[240:243], v76, s[40:43], s58 offen offset:0 sc1\n"
    "buffer_load_dwordx4 a[244:247], v76, s[40:43], s58 offen offset:1024 sc1\n"
    "buffer_load_dwordx4 a[248:251], v76, s[40:43], s58 offen offset:2048 sc1\n"
    "buffer_load_dwordx4 a[252:255], v76, s[40:43], s58 offen offset:3072 sc1\n"
    "s_waitcnt vmcnt(0)\n"
    "v_mfma_f32_16x16x32_bf16 a[40:43], v[160:163], a[224:227], a[40:43]\n"
    "v_mfma_f32_16x16x32_bf16 a[40:43], v[164:167], a[228:231], a[40:43]\n"
    "v_mfma_f32_16x16x32_bf16 a[40:43], v[168:171], a[232:235], a[40:43]\n"
    "v_mfma_f32_16x16x32_bf16 a[40:43], v[172:175], a[236:239], a[40:43]\n"
    "v_mfma_f32_16x16x32_bf16 a[56:59], v[160:163], a[240:243], a[56:59]\n"
    "v_mfma_f32_16x16x32_bf16 a[56:59], v[164:167], a[244:247], a[56:59]\n"
    "v_mfma_f32_16x16x32_bf16 a[56:59], v[168:171], a[248:251], a[56:59]\n"
    "v_mfma_f32_16x16x32_bf16 a[56:59], v[172:175], a[252:255], a[56:59]\n"
    "s_nop 7\n"
    "s_add_u32 s57, s56, 0xc000\n"
    "s_add_u32 s58, s56, 0xd000\n"
    "buffer_load_dwordx4 a[224:227], v76, s[40:43], s57 offen offset:0 sc1\n"
    "buffer_load_dwordx4 a[228:231], v76, s[40:43], s57 offen offset:1024 sc1\n"
    "buffer_load_dwordx4 a[232:235], v76, s[40:43], s57 offen offset:2048 sc1\n"
    "buffer_load_dwordx4 a[236:239], v76, s[40:43], s57 offen offset:3072 sc1\n"
    "buffer_load_dwordx4 a[240:243], v76, s[40:43], s58 offen offset:0 sc1\n"
    "buffer_load_dwordx4 a[244:247], v76, s[40:43], s58 offen offset:1024 sc1\n"
    "buffer_load_dwordx4 a[248:251], v76, s[40:43], s58 offen offset:2048 sc1\n"
    "buffer_load_dwordx4 a[252:255], v76, s[40:43], s58 offen offset:3072 sc1\n"
    "s_waitcnt vmcnt(0)\n"
    "v_mfma_f32_16x16x32_bf16 a[72:75], v[160:163], a[224:227], a[72:75]\n"
    "v_mfma_f32_16x16x32_bf16 a[72:75], v[164:167], a[228:231], a[72:75]\n"
    "v_mfma_f32_16x16x32_bf16 a[72:75], v[168:171], a[232:235], a[72:75]\n"
    "v_mfma_f32_16x16x32_bf16 a[72:75], v[172:175], a[236:239], a[72:75]\n"
    "v_mfma_f32_16x16x32_bf16 a[88:91], v[160:163], a[240:243], a[88:91]\n"
    "v_mfma_f32_16x16x32_bf16 a[88:91], v[164:167], a[244:247], a[88:91]\n"
    "v_mfma_f32_16x16x32_bf16 a[88:91], v[168:171], a[248:251], a[88:91]\n"
    "v_mfma_f32_16x16x32_bf16 a[88:91], v[172:175], a[252:255], a[88:91]\n"
    "s_nop 7\n"
    "s_add_u32 s57, s56, 0xe000\n"
    "s_add_u32 s58, s56, 0xf000\n"
    "buffer_load_dwordx4 a[224:227], v76, s[40:43], s57 offen offset:0 sc1\n"
    "buffer_load_dwordx4 a[228:231], v76, s[40:43], s57 offen offset:1024 sc1\n"
    "buffer_load_dwordx4 a[232:235], v76, s[40:43], s57 offen offset:2048 sc1\n"
    "buffer_load_dwordx4 a[236:239], v76, s[40:43], s57 offen offset:3072 sc1\n"
    "buffer_load_dwordx4 a[240:243], v76, s[40:43], s58 offen offset:0 sc1\n"
    "buffer_load_dwordx4 a[244:247], v76, s[40:43], s58 offen offset:1024 sc1\n"
    "buffer_load_dwordx4 a[248:251], v76, s[40:43], s58 offen offset:2048 sc1\n"
    "buffer_load_dwordx4 a[252:255], v76, s[40:43], s58 offen offset:3072 sc1\n"
    "s_waitcnt vmcnt(0)\n"
    "v_mfma_f32_16x16x32_bf16 a[104:107], v[160:163], a[224:227], a[104:107]\n"
    "v_mfma_f32_16x16x32_bf16 a[104:107], v[164:167], a[228:231], a[104:107]\n"
    "v_mfma_f32_16x16x32_bf16 a[104:107], v[168:171], a[232:235], a[104:107]\n"
    "v_mfma_f32_16x16x32_bf16 a[104:107], v[172:175], a[236:239], a[104:107]\n"
    "v_mfma_f32_16x16x32_bf16 a[120:123], v[160:163], a[240:243], a[120:123]\n"
    "v_mfma_f32_16x16x32_bf16 a[120:123], v[164:167], a[244:247], a[120:123]\n"
    "v_mfma_f32_16x16x32_bf16 a[120:123], v[168:171], a[248:251], a[120:123]\n"
    "v_mfma_f32_16x16x32_bf16 a[120:123], v[172:175], a[252:255], a[120:123]\n"
    "s_nop 7\n"
    "v_mov_b32 v160, 0\n"
    "v_mov_b32 v161, 0\n"
    "v_mov_b32 v162, 0\n"
    "v_mov_b32 v163, 0\n"
    "v_mov_b32 v164, 0\n"
    "v_mov_b32 v165, 0\n"
    "v_mov_b32 v166, 0\n"
    "v_mov_b32 v167, 0\n"
    "v_mov_b32 v168, 0\n"
    "v_mov_b32 v169, 0\n"
    "v_mov_b32 v170, 0\n"
    "v_mov_b32 v171, 0\n"
    "v_mov_b32 v172, 0\n"
    "v_mov_b32 v173, 0\n"
    "v_mov_b32 v174, 0\n"
    "v_mov_b32 v175, 0\n"
    "v_sub_f32 v11, 0, v9\n"
    "v_exp_f32 v11, v11\n"
    "s_nop 0\n"
    "v_mul_f32 v70, v11, v70\n"
    "ds_bpermute_b32 v12, v99, v11 offset:0\n"
    "ds_bpermute_b32 v13, v99, v11 offset:4\n"
    "ds_bpermute_b32 v14, v99, v11 offset:8\n"
    "ds_bpermute_b32 v15, v99, v11 offset:12\n"
    "s_waitcnt lgkmcnt(0)\n"
    "v_accvgpr_read_b32 v80, a8\n"
    "v_accvgpr_read_b32 v81, a9\n"
    "v_accvgpr_read_b32 v82, a10\n"
    "v_accvgpr_read_b32 v83, a11\n"
    "v_accvgpr_read_b32 v84, a24\n"
    "v_accvgpr_read_b32 v85, a25\n"
    "v_accvgpr_read_b32 v86, a26\n"
    "v_accvgpr_read_b32 v87, a27\n"
    "v_mul_f32 v80, v12, v80\n"
    "v_mul_f32 v81, v13, v81\n"
    "v_mul_f32 v82, v14, v82\n"
    "v_mul_f32 v83, v15, v83\n"
    "v_mul_f32 v84, v12, v84\n"
    "v_mul_f32 v85, v13, v85\n"
    "v_mul_f32 v86, v14, v86\n"
    "v_mul_f32 v87, v15, v87\n"
    "v_accvgpr_write_b32 a8, v80\n"
    "v_accvgpr_write_b32 a9, v81\n"
    "v_accvgpr_write_b32 a10, v82\n"
    "v_accvgpr_write_b32 a11, v83\n"
    "v_accvgpr_write_b32 a24, v84\n"
    "v_accvgpr_write_b32 a25, v85\n"
    "v_accvgpr_write_b32 a26, v86\n"
    "v_accvgpr_write_b32 a27, v87\n"
    "v_accvgpr_read_b32 v80, a40\n"
    "v_accvgpr_read_b32 v81, a41\n"
    "v_accvgpr_read_b32 v82, a42\n"
    "v_accvgpr_read_b32 v83, a43\n"
    "v_accvgpr_read_b32 v84, a56\n"
    "v_accvgpr_read_b32 v85, a57\n"
    "v_accvgpr_read_b32 v86, a58\n"
    "v_accvgpr_read_b32 v87, a59\n"
    "v_mul_f32 v80, v12, v80\n"
    "v_mul_f32 v81, v13, v81\n"
    "v_mul_f32 v82, v14, v82\n"
    "v_mul_f32 v83, v15, v83\n"
    "v_mul_f32 v84, v12, v84\n"
    "v_mul_f32 v85, v13, v85\n"
    "v_mul_f32 v86, v14, v86\n"
    "v_mul_f32 v87, v15, v87\n"
    "v_accvgpr_write_b32 a40, v80\n"
    "v_accvgpr_write_b32 a41, v81\n"
    "v_accvgpr_write_b32 a42, v82\n"
    "v_accvgpr_write_b32 a43, v83\n"
    "v_accvgpr_write_b32 a56, v84\n"
    "v_accvgpr_write_b32 a57, v85\n"
    "v_accvgpr_write_b32 a58, v86\n"
    "v_accvgpr_write_b32 a59, v87\n"
    "v_accvgpr_read_b32 v80, a72\n"
    "v_accvgpr_read_b32 v81, a73\n"
    "v_accvgpr_read_b32 v82, a74\n"
    "v_accvgpr_read_b32 v83, a75\n"
    "v_accvgpr_read_b32 v84, a88\n"
    "v_accvgpr_read_b32 v85, a89\n"
    "v_accvgpr_read_b32 v86, a90\n"
    "v_accvgpr_read_b32 v87, a91\n"
    "v_mul_f32 v80, v12, v80\n"
    "v_mul_f32 v81, v13, v81\n"
    "v_mul_f32 v82, v14, v82\n"
    "v_mul_f32 v83, v15, v83\n"
    "v_mul_f32 v84, v12, v84\n"
    "v_mul_f32 v85, v13, v85\n"
    "v_mul_f32 v86, v14, v86\n"
    "v_mul_f32 v87, v15, v87\n"
    "v_accvgpr_write_b32 a72, v80\n"
    "v_accvgpr_write_b32 a73, v81\n"
    "v_accvgpr_write_b32 a74, v82\n"
    "v_accvgpr_write_b32 a75, v83\n"
    "v_accvgpr_write_b32 a88, v84\n"
    "v_accvgpr_write_b32 a89, v85\n"
    "v_accvgpr_write_b32 a90, v86\n"
    "v_accvgpr_write_b32 a91, v87\n"
    "v_accvgpr_read_b32 v80, a104\n"
    "v_accvgpr_read_b32 v81, a105\n"
    "v_accvgpr_read_b32 v82, a106\n"
    "v_accvgpr_read_b32 v83, a107\n"
    "v_accvgpr_read_b32 v84, a120\n"
    "v_accvgpr_read_b32 v85, a121\n"
    "v_accvgpr_read_b32 v86, a122\n"
    "v_accvgpr_read_b32 v87, a123\n"
    "v_mul_f32 v80, v12, v80\n"
    "v_mul_f32 v81, v13, v81\n"
    "v_mul_f32 v82, v14, v82\n"
    "v_mul_f32 v83, v15, v83\n"
    "v_mul_f32 v84, v12, v84\n"
    "v_mul_f32 v85, v13, v85\n"
    "v_mul_f32 v86, v14, v86\n"
    "v_mul_f32 v87, v15, v87\n"
    "v_accvgpr_write_b32 a104, v80\n"
    "v_accvgpr_write_b32 a105, v81\n"
    "v_accvgpr_write_b32 a106, v82\n"
    "v_accvgpr_write_b32 a107, v83\n"
    "v_accvgpr_write_b32 a120, v84\n"
    "v_accvgpr_write_b32 a121, v85\n"
    "v_accvgpr_write_b32 a122, v86\n"
    "v_accvgpr_write_b32 a123, v87\n"
    "s_nop 7\n"
    "s_nop 7\n"
    "s_branch L_ret_F2\n"
    "L_rare_F3:\n"
    "s_nop 7\n"
    "s_nop 7\n"
    "s_sub_u32 s56, s48, 0x10000\n"
    "s_add_u32 s57, s56, 0x0\n"
    "s_add_u32 s58, s56, 0x1000\n"
    "buffer_load_dwordx4 a[224:227], v76, s[40:43], s57 offen offset:0 sc1\n"
    "buffer_load_dwordx4 a[228:231], v76, s[40:43], s57 offen offset:1024 sc1\n"
    "buffer_load_dwordx4 a[232:235], v76, s[40:43], s57 offen offset:2048 sc1\n"
    "buffer_load_dwordx4 a[236:239], v76, s[40:43], s57 offen offset:3072 sc1\n"
    "buffer_load_dwordx4 a[240:243], v76, s[40:43], s58 offen offset:0 sc1\n"
    "buffer_load_dwordx4 a[244:247], v76, s[40:43], s58 offen offset:1024 sc1\n"
    "buffer_load_dwordx4 a[248:251], v76, s[40:43], s58 offen offset:2048 sc1\n"
    "buffer_load_dwordx4 a[252:255], v76, s[40:43], s58 offen offset:3072 sc1\n"
    "s_waitcnt vmcnt(0)\n"
    "v_mfma_f32_16x16x32_bf16 v[80:83], a[224:227], a[176:179], v[44:47]\n"
    "v_mfma_f32_16x16x32_bf16 v[80:83], a[228:231], a[180:183], v[80:83]\n"
    "v_mfma_f32_16x16x32_bf16 v[80:83], a[232:235], a[184:187], v[80:83]\n"
    "v_mfma_f32_16x16x32_bf16 v[80:83], a[236:239], a[188:191], v[80:83]\n"
    "v_mfma_f32_16x16x32_bf16 v[84:87], a[240:243], a[176:179], v[44:47]\n"
    "v_mfma_f32_16x16x32_bf16 v[84:87], a[244:247], a[180:183], v[84:87]\n"
    "v_mfma_f32_16x16x32_bf16 v[84:87], a[248:251], a[184:187], v[84:87]\n"
    "v_mfma_f32_16x16x32_bf16 v[84:87], a[252:255], a[188:191], v[84:87]\n"
    "s_nop 3\n"
    "v_max_f32 v9, v80, v80\n"
    "v_max3_f32 v9, v9, v80, v81\n"
    "v_max3_f32 v9, v9, v82, v83\n"
    "s_nop 0\n"
    "v_max3_f32 v9, v9, v84, v85\n"
    "v_max3_f32 v9, v9, v86, v87\n"
    "s_add_u32 s57, s56, 0x2000\n"
    "s_add_u32 s58, s56, 0x3000\n"
    "buffer_load_dwordx4 a[224:227], v76, s[40:43], s57 offen offset:0 sc1\n"
    "buffer_load_dwordx4 a[228:231], v76, s[40:43], s57 offen offset:1024 sc1\n"
    "buffer_load_dwordx4 a[232:235], v76, s[40:43], s57 offen offset:2048 sc1\n"
    "buffer_load_dwordx4 a[236:239], v76, s[40:43], s57 offen offset:3072 sc1\n"
    "buffer_load_dwordx4 a[240:243], v76, s[40:43], s58 offen offset:0 sc1\n"
    "buffer_load_dwordx4 a[244:247], v76, s[40:43], s58 offen offset:1024 sc1\n"
    "buffer_load_dwordx4 a[248:251], v76, s[40:43], s58 offen offset:2048 sc1\n"
    "buffer_load_dwordx4 a[252:255], v76, s[40:43], s58 offen offset:3072 sc1\n"
    "s_waitcnt vmcnt(0)\n"
    "v_mfma_f32_16x16x32_bf16 v[80:83], a[224:227], a[176:179], v[44:47]\n"
    "v_mfma_f32_16x16x32_bf16 v[80:83], a[228:231], a[180:183], v[80:83]\n"
    "v_mfma_f32_16x16x32_bf16 v[80:83], a[232:235], a[184:187], v[80:83]\n"
    "v_mfma_f32_16x16x32_bf16 v[80:83], a[236:239], a[188:191], v[80:83]\n"
    "v_mfma_f32_16x16x32_bf16 v[84:87], a[240:243], a[176:179], v[44:47]\n"
    "v_mfma_f32_16x16x32_bf16 v[84:87], a[244:247], a[180:183], v[84:87]\n"
    "v_mfma_f32_16x16x32_bf16 v[84:87], a[248:251], a[184:187], v[84:87]\n"
    "v_mfma_f32_16x16x32_bf16 v[84:87], a[252:255], a[188:191], v[84:87]\n"
    "s_nop 3\n"
    "v_max3_f32 v9, v9, v80, v81\n"
    "v_max3_f32 v9, v9, v82, v83\n"
    "s_nop 1\n"
    "v_max3_f32 v9, v9, v84, v85\n"
    "v_max3_f32 v9, v9, v86, v87\n"
    "s_add_u32 s57, s56, 0x4000\n"
    "s_add_u32 s58, s56, 0x5000\n"
    "buffer_load_dwordx4 a[224:227], v76, s[40:43], s57 offen offset:0 sc1\n"
    "buffer_load_dwordx4 a[228:231], v76, s[40:43], s57 offen offset:1024 sc1\n"
    "buffer_load_dwordx4 a[232:235], v76, s[40:43], s57 offen offset:2048 sc1\n"
    "buffer_load_dwordx4 a[236:239], v76, s[40:43], s57 offen offset:3072 sc1\n"
    "buffer_load_dwordx4 a[240:243], v76, s[40:43], s58 offen offset:0 sc1\n"
    "buffer_load_dwordx4 a[244:247], v76, s[40:43], s58 offen offset:1024 sc1\n"
    "buffer_load_dwordx4 a[248:251], v76, s[40:43], s58 offen offset:2048 sc1\n"
    "buffer_load_dwordx4 a[252:255], v76, s[40:43], s58 offen offset:3072 sc1\n"
    "s_waitcnt vmcnt(0)\n"
    "v_mfma_f32_16x16x32_bf16 v[80:83], a[224:227], a[176:179], v[44:47]\n"
    "v_mfma_f32_16x16x32_bf16 v[80:83], a[228:231], a[180:183], v[80:83]\n"
    "v_mfma_f32_16x16x32_bf16 v[80:83], a[232:235], a[184:187], v[80:83]\n"
    "v_mfma_f32_16x16x32_bf16 v[80:83], a[236:239], a[188:191], v[80:83]\n"
    "v_mfma_f32_16x16x32_bf16 v[84:87], a[240:243], a[176:179], v[44:47]\n"
    "v_mfma_f32_16x16x32_bf16 v[84:87], a[244:247], a[180:183], v[84:87]\n"
    "v_mfma_f32_16x16x32_bf16 v[84:87], a[248:251], a[184:187], v[84:87]\n"
    "v_mfma_f32_16x16x32_bf16 v[84:87], a[252:255], a[188:191], v[84:87]\n"
    "s_nop 3\n"
    "v_max3_f32 v9, v9, v80, v81\n"
    "v_max3_f32 v9, v9, v82, v83\n"
    "s_nop 1\n"
    "v_max3_f32 v9, v9, v84, v85\n"
    "v_max3_f32 v9, v9, v86, v87\n"
    "s_add_u32 s57, s56, 0x6000\n"
    "s_add_u32 s58, s56, 0x7000\n"
    "buffer_load_dwordx4 a[224:227], v76, s[40:43], s57 offen offset:0 sc1\n"
    "buffer_load_dwordx4 a[228:231], v76, s[40:43], s57 offen offset:1024 sc1\n"
    "buffer_load_dwordx4 a[232:235], v76, s[40:43], s57 offen offset:2048 sc1\n"
    "buffer_load_dwordx4 a[236:239], v76, s[40:43], s57 offen offset:3072 sc1\n"
    "buffer_load_dwordx4 a[240:243], v76, s[40:43], s58 offen offset:0 sc1\n"
    "buffer_load_dwordx4 a[244:247], v76, s[40:43], s58 offen offset:1024 sc1\n"
    "buffer_load_dwordx4 a[248:251], v76, s[40:43], s58 offen offset:2048 sc1\n"
    "buffer_load_dwordx4 a[252:255], v76, s[40:43], s58 offen offset:3072 sc1\n"
    "s_waitcnt vmcnt(0)\n"
    "v_mfma_f32_16x16x32_bf16 v[80:83], a[224:227], a[176:179], v[44:47]\n"
    "v_mfma_f32_16x16x32_bf16 v[80:83], a[228:231], a[180:183], v[80:83]\n"
    "v_mfma_f32_16x16x32_bf16 v[80:83], a[232:235], a[184:187], v[80:83]\n"
    "v_mfma_f32_16x16x32_bf16 v[80:83], a[236:239], a[188:191], v[80:83]\n"
    "v_mfma_f32_16x16x32_bf16 v[84:87], a[240:243], a[176:179], v[44:47]\n"
    "v_mfma_f32_16x16x32_bf16 v[84:87], a[244:247], a[180:183], v[84:87]\n"
    "v_mfma_f32_16x16x32_bf16 v[84:87], a[248:251], a[184:187], v[84:87]\n"
    "v_mfma_f32_16x16x32_bf16 v[84:87], a[252:255], a[188:191], v[84:87]\n"
    "s_nop 3\n"
    "v_max3_f32 v9, v9, v80, v81\n"
    "v_max3_f32 v9, v9, v82, v83\n"
    "s_nop 1\n"
    "v_max3_f32 v9, v9, v84, v85\n"
    "v_max3_f32 v9, v9, v86, v87\n"
    "v_mov_b32 v10, v9\n"
    "s_nop 1\n"
    "v_permlane16_swap_b32 v10, v9\n"
    "v_max_f32 v9, v10, v9\n"
    "v_mov_b32 v10, v9\n"
    "s_nop 1\n"
    "v_permlane32_swap_b32 v10, v9\n"
    "v_max_f32 v9, v10, v9\n"
    "v_max_f32 v9, 0, v9\n"
    "v_sub_f32 v44, v44, v9\n"
    "v_sub_f32 v45, v45, v9\n"
    "v_sub_f32 v46, v46, v9\n"
    "v_sub_f32 v47, v47, v9\n"
    "s_add_u32 s57, s56, 0x0\n"
    "s_add_u32 s58, s56, 0x1000\n"
    "buffer_load_dwordx4 a[224:227], v76, s[40:43], s57 offen offset:0 sc1\n"
    "buffer_load_dwordx4 a[228:231], v76, s[40:43], s57 offen offset:1024 sc1\n"
    "buffer_load_dwordx4 a[232:235], v76, s[40:43], s57 offen offset:2048 sc1\n"
    "buffer_load_dwordx4 a[236:239], v76, s[40:43], s57 offen offset:3072 sc1\n"
    "buffer_load_dwordx4 a[240:243], v76, s[40:43], s58 offen offset:0 sc1\n"
    "buffer_load_dwordx4 a[244:247], v76, s[40:43], s58 offen offset:1024 sc1\n"
    "buffer_load_dwordx4 a[248:251], v76, s[40:43], s58 offen offset:2048 sc1\n"
    "buffer_load_dwordx4 a[252:255], v76, s[40:43], s58 offen offset:3072 sc1\n"
    "s_waitcnt vmcnt(0)\n"
    "v_mfma_f32_16x16x32_bf16 v[80:83], a[224:227], a[176:179], v[44:47]\n"
    "v_mfma_f32_16x16x32_bf16 v[80:83], a[228:231], a[180:183], v[80:83]\n"
    "v_mfma_f32_16x16x32_bf16 v[80:83], a[232:235], a[184:187], v[80:83]\n"
    "v_mfma_f32_16x16x32_bf16 v[80:83], a[236:239], a[188:191], v[80:83]\n"
    "v_mfma_f32_16x16x32_bf16 v[84:87], a[240:243], a[176:179], v[44:47]\n"
    "v_mfma_f32_16x16x32_bf16 v[84:87], a[244:247], a[180:183], v[84:87]\n"
    "v_mfma_f32_16x16x32_bf16 v[84:87], a[248:251], a[184:187], v[84:87]\n"
    "v_mfma_f32_16x16x32_bf16 v[84:87], a[252:255], a[188:191], v[84:87]\n"
    "s_nop 3\n"
    "v_exp_f32 v80, v80\n"
    "v_exp_f32 v81, v81\n"
    "v_exp_f32 v82, v82\n"
    "v_exp_f32 v83, v83\n"
    "v_exp_f32 v84, v84\n"
    "v_exp_f32 v85, v85\n"
    "v_exp_f32 v86, v86\n"
    "v_exp_f32 v87, v87\n"
    "v_cvt_pk_bf16_f32 v240, v80, v81\n"
    "v_cvt_pk_bf16_f32 v241, v82, v83\n"
    "v_cvt_pk_bf16_f32 v242, v84, v85\n"
    "v_cvt_pk_bf16_f32 v243, v86, v87\n"
    "s_add_u32 s57, s56, 0x2000\n"
    "s_add_u32 s58, s56, 0x3000\n"
    "buffer_load_dwordx4 a[224:227], v76, s[40:43], s57 offen offset:0 sc1\n"
    "buffer_load_dwordx4 a[228:231], v76, s[40:43], s57 offen offset:1024 sc1\n"
    "buffer_load_dwordx4 a[232:235], v76, s[40:43], s57 offen offset:2048 sc1\n"
    "buffer_load_dwordx4 a[236:239], v76, s[40:43], s57 offen offset:3072 sc1\n"
    "buffer_load_dwordx4 a[240:243], v76, s[40:43], s58 offen offset:0 sc1\n"
    "buffer_load_dwordx4 a[244:247], v76, s[40:43], s58 offen offset:1024 sc1\n"
    "buffer_load_dwordx4 a[248:251], v76, s[40:43], s58 offen offset:2048 sc1\n"
    "buffer_load_dwordx4 a[252:255], v76, s[40:43], s58 offen offset:3072 sc1\n"
    "s_waitcnt vmcnt(0)\n"
    "v_mfma_f32_16x16x32_bf16 v[80:83], a[224:227], a[176:179], v[44:47]\n"
    "v_mfma_f32_16x16x32_bf16 v[80:83], a[228:231], a[180:183], v[80:83]\n"
    "v_mfma_f32_16x16x32_bf16 v[80:83], a[232:235], a[184:187], v[80:83]\n"
    "v_mfma_f32_16x16x32_bf16 v[80:83], a[236:239], a[188:191], v[80:83]\n"
    "v_mfma_f32_16x16x32_bf16 v[84:87], a[240:243], a[176:179], v[44:47]\n"
    "v_mfma_f32_16x16x32_bf16 v[84:87], a[244:247], a[180:183], v[84:87]\n"
    "v_mfma_f32_16x16x32_bf16 v[84:87], a[248:251], a[184:187], v[84:87]\n"
    "v_mfma_f32_16x16x32_bf16 v[84:87], a[252:255], a[188:191], v[84:87]\n"
    "s_nop 3\n"
    "v_exp_f32 v80, v80\n"
    "v_exp_f32 v81, v81\n"
    "v_exp_f32 v82, v82\n"
    "v_exp_f32 v83, v83\n"
    "v_exp_f32 v84, v84\n"
    "v_exp_f32 v85, v85\n"
    "v_exp_f32 v86, v86\n"
    "v_exp_f32 v87, v87\n"
    "v_cvt_pk_bf16_f32 v244, v80, v81\n"
    "v_cvt_pk_bf16_f32 v245, v82, v83\n"
    "v_cvt_pk_bf16_f32 v246, v84, v85\n"
    "v_cvt_pk_bf16_f32 v247, v86, v87\n"
    "s_add_u32 s57, s56, 0x4000\n"
    "s_add_u32 s58, s56, 0x5000\n"
    "buffer_load_dwordx4 a[224:227], v76, s[40:43], s57 offen offset:0 sc1\n"
    "buffer_load_dwordx4 a[228:231], v76, s[40:43], s57 offen offset:1024 sc1\n"
    "buffer_load_dwordx4 a[232:235], v76, s[40:43], s57 offen offset:2048 sc1\n"
    "buffer_load_dwordx4 a[236:239], v76, s[40:43], s57 offen offset:3072 sc1\n"
    "buffer_load_dwordx4 a[240:243], v76, s[40:43], s58 offen offset:0 sc1\n"
    "buffer_load_dwordx4 a[244:247], v76, s[40:43], s58 offen offset:1024 sc1\n"
    "buffer_load_dwordx4 a[248:251], v76, s[40:43], s58 offen offset:2048 sc1\n"
    "buffer_load_dwordx4 a[252:255], v76, s[40:43], s58 offen offset:3072 sc1\n"
    "s_waitcnt vmcnt(0)\n"
    "v_mfma_f32_16x16x32_bf16 v[80:83], a[224:227], a[176:179], v[44:47]\n"
    "v_mfma_f32_16x16x32_bf16 v[80:83], a[228:231], a[180:183], v[80:83]\n"
    "v_mfma_f32_16x16x32_bf16 v[80:83], a[232:235], a[184:187], v[80:83]\n"
    "v_mfma_f32_16x16x32_bf16 v[80:83], a[236:239], a[188:191], v[80:83]\n"
    "v_mfma_f32_16x16x32_bf16 v[84:87], a[240:243], a[176:179], v[44:47]\n"
    "v_mfma_f32_16x16x32_bf16 v[84:87], a[244:247], a[180:183], v[84:87]\n"
    "v_mfma_f32_16x16x32_bf16 v[84:87], a[248:251], a[184:187], v[84:87]\n"
    "v_mfma_f32_16x16x32_bf16 v[84:87], a[252:255], a[188:191], v[84:87]\n"
    "s_nop 3\n"
    "v_exp_f32 v80, v80\n"
    "v_exp_f32 v81, v81\n"
    "v_exp_f32 v82, v82\n"
    "v_exp_f32 v83, v83\n"
    "v_exp_f32 v84, v84\n"
    "v_exp_f32 v85, v85\n"
    "v_exp_f32 v86, v86\n"
    "v_exp_f32 v87, v87\n"
    "v_cvt_pk_bf16_f32 v248, v80, v81\n"
    "v_cvt_pk_bf16_f32 v249, v82, v83\n"
    "v_cvt_pk_bf16_f32 v250, v84, v85\n"
    "v_cvt_pk_bf16_f32 v251, v86, v87\n"
    "s_add_u32 s57, s56, 0x6000\n"
    "s_add_u32 s58, s56, 0x7000\n"
    "buffer_load_dwordx4 a[224:227], v76, s[40:43], s57 offen offset:0 sc1\n"
    "buffer_load_dwordx4 a[228:231], v76, s[40:43], s57 offen offset:1024 sc1\n"
    "buffer_load_dwordx4 a[232:235], v76, s[40:43], s57 offen offset:2048 sc1\n"
    "buffer_load_dwordx4 a[236:239], v76, s[40:43], s57 offen offset:3072 sc1\n"
    "buffer_load_dwordx4 a[240:243], v76, s[40:43], s58 offen offset:0 sc1\n"
    "buffer_load_dwordx4 a[244:247], v76, s[40:43], s58 offen offset:1024 sc1\n"
    "buffer_load_dwordx4 a[248:251], v76, s[40:43], s58 offen offset:2048 sc1\n"
    "buffer_load_dwordx4 a[252:255], v76, s[40:43], s58 offen offset:3072 sc1\n"
    "s_waitcnt vmcnt(0)\n"
    "v_mfma_f32_16x16x32_bf16 v[80:83], a[224:227], a[176:179], v[44:47]\n"
    "v_mfma_f32_16x16x32_bf16 v[80:83], a[228:231], a[180:183], v[80:83]\n"
    "v_mfma_f32_16x16x32_bf16 v[80:83], a[232:235], a[184:187], v[80:83]\n"
    "v_mfma_f32_16x16x32_bf16 v[80:83], a[236:239], a[188:191], v[80:83]\n"
    "v_mfma_f32_16x16x32_bf16 v[84:87], a[240:243], a[176:179], v[44:47]\n"
    "v_mfma_f32_16x16x32_bf16 v[84:87], a[244:247], a[180:183], v[84:87]\n"
    "v_mfma_f32_16x16x32_bf16 v[84:87], a[248:251], a[184:187], v[84:87]\n"
    "v_mfma_f32_16x16x32_bf16 v[84:87], a[252:255], a[188:191], v[84:87]\n"
    "s_nop 3\n"
    "v_exp_f32 v80, v80\n"
    "v_exp_f32 v81, v81\n"
    "v_exp_f32 v82, v82\n"
    "v_exp_f32 v83, v83\n"
    "v_exp_f32 v84, v84\n"
    "v_exp_f32 v85, v85\n"
    "v_exp_f32 v86, v86\n"
    "v_exp_f32 v87, v87\n"
    "v_cvt_pk_bf16_f32 v252, v80, v81\n"
    "v_cvt_pk_bf16_f32 v253, v82, v83\n"
    "v_cvt_pk_bf16_f32 v254, v84, v85\n"
    "v_cvt_pk_bf16_f32 v255, v86, v87\n"
    "v_mfma_f32_16x16x32_bf16 v[60:63], v[64:67], v[240:243], 0\n"
    "v_mfma_f32_16x16x32_bf16 v[60:63], v[64:67], v[244:247], v[60:63]\n"
    "v_mfma_f32_16x16x32_bf16 v[60:63], v[64:67], v[248:251], v[60:63]\n"
    "v_mfma_f32_16x16x32_bf16 v[60:63], v[64:67], v[252:255], v[60:63]\n"
    "s_add_u32 s57, s56, 0x8000\n"
    "s_add_u32 s58, s56, 0x9000\n"
    "buffer_load_dwordx4 a[224:227], v76, s[40:43], s57 offen offset:0 sc1\n"
    "buffer_load_dwordx4 a[228:231], v76, s[40:43], s57 offen offset:1024 sc1\n"
    "buffer_load_dwordx4 a[232:235], v76, s[40:43], s57 offen offset:2048 sc1\n"
    "buffer_load_dwordx4 a[236:239], v76, s[40:43], s57 offen offset:3072 sc1\n"
    "buffer_load_dwordx4 a[240:243], v76, s[40:43], s58 offen offset:0 sc1\n"
    "buffer_load_dwordx4 a[244:247], v76, s[40:43], s58 offen offset:1024 sc1\n"
    "buffer_load_dwordx4 a[248:251], v76, s[40:43], s58 offen offset:2048 sc1\n"
    "buffer_load_dwordx4 a[252:255], v76, s[40:43], s58 offen offset:3072 sc1\n"
    "s_waitcnt vmcnt(0)\n"
    "v_mfma_f32_16x16x32_bf16 a[12:15], v[176:179], a[224:227], a[12:15]\n"
    "v_mfma_f32_16x16x32_bf16 a[12:15], v[180:183], a[228:231], a[12:15]\n"
    "v_mfma_f32_16x16x32_bf16 a[12:15], v[184:187], a[232:235], a[12:15]\n"
    "v_mfma_f32_16x16x32_bf16 a[12:15], v[188:191], a[236:239], a[12:15]\n"
    "v_mfma_f32_16x16x32_bf16 a[28:31], v[176:179], a[240:243], a[28:31]\n"
    "v_mfma_f32_16x16x32_bf16 a[28:31], v[180:183], a[244:247], a[28:31]\n"
    "v_mfma_f32_16x16x32_bf16 a[28:31], v[184:187], a[248:251], a[28:31]\n"
    "v_mfma_f32_16x16x32_bf16 a[28:31], v[188:191], a[252:255], a[28:31]\n"
    "s_nop 7\n"
    "s_add_u32 s57, s56, 0xa000\n"
    "s_add_u32 s58, s56, 0xb000\n"
    "buffer_load_dwordx4 a[224:227], v76, s[40:43], s57 offen offset:0 sc1\n"
    "buffer_load_dwordx4 a[228:231], v76, s[40:43], s57 offen offset:1024 sc1\n"
    "buffer_load_dwordx4 a[232:235], v76, s[40:43], s57 offen offset:2048 sc1\n"
    "buffer_load_dwordx4 a[236:239], v76, s[40:43], s57 offen offset:3072 sc1\n"
    "buffer_load_dwordx4 a[240:243], v76, s[40:43], s58 offen offset:0 sc1\n"
    "buffer_load_dwordx4 a[244:247], v76, s[40:43], s58 offen offset:1024 sc1\n"
    "buffer_load_dwordx4 a[248:251], v76, s[40:43], s58 offen offset:2048 sc1\n"
    "buffer_load_dwordx4 a[252:255], v76, s[40:43], s58 offen offset:3072 sc1\n"
    "s_waitcnt vmcnt(0)\n"
    "v_mfma_f32_16x16x32_bf16 a[44:47], v[176:179], a[224:227], a[44:47]\n"
    "v_mfma_f32_16x16x32_bf16 a[44:47], v[180:183], a[228:231], a[44:47]\n"
    "v_mfma_f32_16x16x32_bf16 a[44:47], v[184:187], a[232:235], a[44:47]\n"
    "v_mfma_f32_16x16x32_bf16 a[44:47], v[188:191], a[236:239], a[44:47]\n"
    "v_mfma_f32_16x16x32_bf16 a[60:63], v[176:179], a[240:243], a[60:63]\n"
    "v_mfma_f32_16x16x32_bf16 a[60:63], v[180:183], a[244:247], a[60:63]\n"
    "v_mfma_f32_16x16x32_bf16 a[60:63], v[184:187], a[248:251], a[60:63]\n"
    "v_mfma_f32_16x16x32_bf16 a[60:63], v[188:191], a[252:255], a[60:63]\n"
    "s_nop 7\n"
    "s_add_u32 s57, s56, 0xc000\n"
    "s_add_u32 s58, s56, 0xd000\n"
    "buffer_load_dwordx4 a[224:227], v76, s[40:43], s57 offen offset:0 sc1\n"
    "buffer_load_dwordx4 a[228:231], v76, s[40:43], s57 offen offset:1024 sc1\n"
    "buffer_load_dwordx4 a[232:235], v76, s[40:43], s57 offen offset:2048 sc1\n"
    "buffer_load_dwordx4 a[236:239], v76, s[40:43], s57 offen offset:3072 sc1\n"
    "buffer_load_dwordx4 a[240:243], v76, s[40:43], s58 offen offset:0 sc1\n"
    "buffer_load_dwordx4 a[244:247], v76, s[40:43], s58 offen offset:1024 sc1\n"
    "buffer_load_dwordx4 a[248:251], v76, s[40:43], s58 offen offset:2048 sc1\n"
    "buffer_load_dwordx4 a[252:255], v76, s[40:43], s58 offen offset:3072 sc1\n"
    "s_waitcnt vmcnt(0)\n"
    "v_mfma_f32_16x16x32_bf16 a[76:79], v[176:179], a[224:227], a[76:79]\n"
    "v_mfma_f32_16x16x32_bf16 a[76:79], v[180:183], a[228:231], a[76:79]\n"
    "v_mfma_f32_16x16x32_bf16 a[76:79], v[184:187], a[232:235], a[76:79]\n"
    "v_mfma_f32_16x16x32_bf16 a[76:79], v[188:191], a[236:239], a[76:79]\n"
    "v_mfma_f32_16x16x32_bf16 a[92:95], v[176:179], a[240:243], a[92:95]\n"
    "v_mfma_f32_16x16x32_bf16 a[92:95], v[180:183], a[244:247], a[92:95]\n"
    "v_mfma_f32_16x16x32_bf16 a[92:95], v[184:187], a[248:251], a[92:95]\n"
    "v_mfma_f32_16x16x32_bf16 a[92:95], v[188:191], a[252:255], a[92:95]\n"
    "s_nop 7\n"
    "s_add_u32 s57, s56, 0xe000\n"
    "s_add_u32 s58, s56, 0xf000\n"
    "buffer_load_dwordx4 a[224:227], v76, s[40:43], s57 offen offset:0 sc1\n"
    "buffer_load_dwordx4 a[228:231], v76, s[40:43], s57 offen offset:1024 sc1\n"
    "buffer_load_dwordx4 a[232:235], v76, s[40:43], s57 offen offset:2048 sc1\n"
    "buffer_load_dwordx4 a[236:239], v76, s[40:43], s57 offen offset:3072 sc1\n"
    "buffer_load_dwordx4 a[240:243], v76, s[40:43], s58 offen offset:0 sc1\n"
    "buffer_load_dwordx4 a[244:247], v76, s[40:43], s58 offen offset:1024 sc1\n"
    "buffer_load_dwordx4 a[248:251], v76, s[40:43], s58 offen offset:2048 sc1\n"
    "buffer_load_dwordx4 a[252:255], v76, s[40:43], s58 offen offset:3072 sc1\n"
    "s_waitcnt vmcnt(0)\n"
    "v_mfma_f32_16x16x32_bf16 a[108:111], v[176:179], a[224:227], a[108:111]\n"
    "v_mfma_f32_16x16x32_bf16 a[108:111], v[180:183], a[228:231], a[108:111]\n"
    "v_mfma_f32_16x16x32_bf16 a[108:111], v[184:187], a[232:235], a[108:111]\n"
    "v_mfma_f32_16x16x32_bf16 a[108:111], v[188:191], a[236:239], a[108:111]\n"
    "v_mfma_f32_16x16x32_bf16 a[124:127], v[176:179], a[240:243], a[124:127]\n"
    "v_mfma_f32_16x16x32_bf16 a[124:127], v[180:183], a[244:247], a[124:127]\n"
    "v_mfma_f32_16x16x32_bf16 a[124:127], v[184:187], a[248:251], a[124:127]\n"
    "v_mfma_f32_16x16x32_bf16 a[124:127], v[188:191], a[252:255], a[124:127]\n"
    "s_nop 7\n"
    "v_mov_b32 v176, 0\n"
    "v_mov_b32 v177, 0\n"
    "v_mov_b32 v178, 0\n"
    "v_mov_b32 v179, 0\n"
    "v_mov_b32 v180, 0\n"
    "v_mov_b32 v181, 0\n"
    "v_mov_b32 v182, 0\n"
    "v_mov_b32 v183, 0\n"
    "v_mov_b32 v184, 0\n"
    "v_mov_b32 v185, 0\n"
    "v_mov_b32 v186, 0\n"
    "v_mov_b32 v187, 0\n"
    "v_mov_b32 v188, 0\n"
    "v_mov_b32 v189, 0\n"
    "v_mov_b32 v190, 0\n"
    "v_mov_b32 v191, 0\n"
    "v_sub_f32 v11, 0, v9\n"
    "v_exp_f32 v11, v11\n"
    "s_nop 0\n"
    "v_mul_f32 v71, v11, v71\n"
    "ds_bpermute_b32 v12, v99, v11 offset:0\n"
    "ds_bpermute_b32 v13, v99, v11 offset:4\n"
    "ds_bpermute_b32 v14, v99, v11 offset:8\n"
    "ds_bpermute_b32 v15, v99, v11 offset:12\n"
    "s_waitcnt lgkmcnt(0)\n"
    "v_accvgpr_read_b32 v80, a12\n"
    "v_accvgpr_read_b32 v81, a13\n"
    "v_accvgpr_read_b32 v82, a14\n"
    "v_accvgpr_read_b32 v83, a15\n"
    "v_accvgpr_read_b32 v84, a28\n"
    "v_accvgpr_read_b32 v85, a29\n"
    "v_accvgpr_read_b32 v86, a30\n"
    "v_accvgpr_read_b32 v87, a31\n"
    "v_mul_f32 v80, v12, v80\n"
    "v_mul_f32 v81, v13, v81\n"
    "v_mul_f32 v82, v14, v82\n"
    "v_mul_f32 v83, v15, v83\n"
    "v_mul_f32 v84, v12, v84\n"
    "v_mul_f32 v85, v13, v85\n"
    "v_mul_f32 v86, v14, v86\n"
    "v_mul_f32 v87, v15, v87\n"
    "v_accvgpr_write_b32 a12, v80\n"
    "v_accvgpr_write_b32 a13, v81\n"
    "v_accvgpr_write_b32 a14, v82\n"
    "v_accvgpr_write_b32 a15, v83\n"
    "v_accvgpr_write_b32 a28, v84\n"
    "v_accvgpr_write_b32 a29, v85\n"
    "v_accvgpr_write_b32 a30, v86\n"
    "v_accvgpr_write_b32 a31, v87\n"
    "v_accvgpr_read_b32 v80, a44\n"
    "v_accvgpr_read_b32 v81, a45\n"
    "v_accvgpr_read_b32 v82, a46\n"
    "v_accvgpr_read_b32 v83, a47\n"
    "v_accvgpr_read_b32 v84, a60\n"
    "v_accvgpr_read_b32 v85, a61\n"
    "v_accvgpr_read_b32 v86, a62\n"
    "v_accvgpr_read_b32 v87, a63\n"
    "v_mul_f32 v80, v12, v80\n"
    "v_mul_f32 v81, v13, v81\n"
    "v_mul_f32 v82, v14, v82\n"
    "v_mul_f32 v83, v15, v83\n"
    "v_mul_f32 v84, v12, v84\n"
    "v_mul_f32 v85, v13, v85\n"
    "v_mul_f32 v86, v14, v86\n"
    "v_mul_f32 v87, v15, v87\n"
    "v_accvgpr_write_b32 a44, v80\n"
    "v_accvgpr_write_b32 a45, v81\n"
    "v_accvgpr_write_b32 a46, v82\n"
    "v_accvgpr_write_b32 a47, v83\n"
    "v_accvgpr_write_b32 a60, v84\n"
    "v_accvgpr_write_b32 a61, v85\n"
    "v_accvgpr_write_b32 a62, v86\n"
    "v_accvgpr_write_b32 a63, v87\n"
    "v_accvgpr_read_b32 v80, a76\n"
    "v_accvgpr_read_b32 v81, a77\n"
    "v_accvgpr_read_b32 v82, a78\n"
    "v_accvgpr_read_b32 v83, a79\n"
    "v_accvgpr_read_b32 v84, a92\n"
    "v_accvgpr_read_b32 v85, a93\n"
    "v_accvgpr_read_b32 v86, a94\n"
    "v_accvgpr_read_b32 v87, a95\n"
    "v_mul_f32 v80, v12, v80\n"
    "v_mul_f32 v81, v13, v81\n"
    "v_mul_f32 v82, v14, v82\n"
    "v_mul_f32 v83, v15, v83\n"
    "v_mul_f32 v84, v12, v84\n"
    "v_mul_f32 v85, v13, v85\n"
    "v_mul_f32 v86, v14, v86\n"
    "v_mul_f32 v87, v15, v87\n"
    "v_accvgpr_write_b32 a76, v80\n"
    "v_accvgpr_write_b32 a77, v81\n"
    "v_accvgpr_write_b32 a78, v82\n"
    "v_accvgpr_write_b32 a79, v83\n"
    "v_accvgpr_write_b32 a92, v84\n"
    "v_accvgpr_write_b32 a93, v85\n"
    "v_accvgpr_write_b32 a94, v86\n"
    "v_accvgpr_write_b32 a95, v87\n"
    "v_accvgpr_read_b32 v80, a108\n"
    "v_accvgpr_read_b32 v81, a109\n"
    "v_accvgpr_read_b32 v82, a110\n"
    "v_accvgpr_read_b32 v83, a111\n"
    "v_accvgpr_read_b32 v84, a124\n"
    "v_accvgpr_read_b32 v85, a125\n"
    "v_accvgpr_read_b32 v86, a126\n"
    "v_accvgpr_read_b32 v87, a127\n"
    "v_mul_f32 v80, v12, v80\n"
    "v_mul_f32 v81, v13, v81\n"
    "v_mul_f32 v82, v14, v82\n"
    "v_mul_f32 v83, v15, v83\n"
    "v_mul_f32 v84, v12, v84\n"
    "v_mul_f32 v85, v13, v85\n"
    "v_mul_f32 v86, v14, v86\n"
    "v_mul_f32 v87, v15, v87\n"
    "v_accvgpr_write_b32 a108, v80\n"
    "v_accvgpr_write_b32 a109, v81\n"
    "v_accvgpr_write_b32 a110, v82\n"
    "v_accvgpr_write_b32 a111, v83\n"
    "v_accvgpr_write_b32 a124, v84\n"
    "v_accvgpr_write_b32 a125, v85\n"
    "v_accvgpr_write_b32 a126, v86\n"
    "v_accvgpr_write_b32 a127, v87\n"
    "s_nop 7\n"
    "s_nop 7\n"
    "s_branch L_ret_F3\n"
    "L_spin_A:\n"
    "s_sleep 8\n"
    "buffer_load_dword v96, off, s[64:67], s96 offset:0 sc1\n"
    "s_waitcnt vmcnt(0)\n"
    "v_cmp_gt_u32 vcc, 32, v96\n"
    "s_nop 1\n"
    "s_cbranch_vccz L_spinret_A\n"
    "s_sub_u32 s98, s98, 1\n"
    "s_cmp_gt_i32 s98, 0\n"
    "s_cbranch_scc1 L_spin_A\n"
    "s_branch L_spinret_A\n"
    "L_spin_B:\n"
    "s_sleep 8\n"
    "buffer_load_dword v96, off, s[64:67], s96 offset:0 sc1\n"
    "s_waitcnt vmcnt(0)\n"
    "v_cmp_gt_u32 vcc, 32, v96\n"
    "s_nop 1\n"
    "s_cbranch_vccz L_spinret_B\n"
    "s_sub_u32 s98, s98, 1\n"
    "s_cmp_gt_i32 s98, 0\n"
    "s_cbranch_scc1 L_spin_B\n"
    "s_branch L_spinret_B\n"
    "L_spin_C:\n"
    "s_sleep 8\n"
    "buffer_load_dword v96, off, s[64:67], s96 offset:0 sc1\n"
    "s_waitcnt vmcnt(0)\n"
    "v_cmp_gt_u32 vcc, 32, v96\n"
    "s_nop 1\n"
    "s_cbranch_vccz L_spinret_C\n"
    "s_sub_u32 s98, s98, 1\n"
    "s_cmp_gt_i32 s98, 0\n"
    "s_cbranch_scc1 L_spin_C\n"
    "s_branch L_spinret_C\n"
    "L_spin_D:\n"
    "s_sleep 8\n"
    "buffer_load_dword v96, off, s[64:67], s96 offset:0 sc1\n"
    "s_waitcnt vmcnt(0)\n"
    "v_cmp_gt_u32 vcc, 32, v96\n"
    "s_nop 1\n"
    "s_cbranch_vccz L_spinret_D\n"
    "s_sub_u32 s98, s98, 1\n"
    "s_cmp_gt_i32 s98, 0\n"
    "s_cbranch_scc1 L_spin_D\n"
    "s_branch L_spinret_D\n"
    "L_end:\n"
    :
    : LO(qp), HI(qp), LO(op), HI(op), LO(ip), HI(ip), "s"(w), "s"((unsigned)(size_t)lds),
      LO(kp), HI(kp), LO(vp), HI(vp), LO(ksp), HI(ksp), LO(kzp), HI(kzp), LO(vsp), HI(vsp), LO(vzp), HI(vzp),
      "s"(rest * 8u), "s"(ksrc0), "s"(ksc0), "s"(kdst0), "s"(vsrc0), "s"(vsc0), "s"(vdst0)
    : "v0", "v1", "v2", "v3", "v4", "v5", "v6", "v7", "v8", "v9", "v10", "v11", "v12", "v13", "v14", "v15", "v16", "v17", "v18", "v19", "v20", "v21", "v22", "v23", "v24", "v25", "v26", "v27", "v28", "v29", "v30", "v31", "v32", "v33", "v34", "v35", "v36", "v37", "v38", "v39", "v40", "v41", "v42", "v43", "v44", "v45", "v46", "v47", "v48", "v49", "v50", "v51", "v52", "v53", "v54", "v55", "v56", "v57", "v58", "v59", "v60", "v61", "v62", "v63", "v64", "v65", "v66", "v67", "v68", "v69", "v70", "v71", "v72", "v73", "v74", "v75", "v76", "v77", "v78", "v79", "v80", "v81", "v82", "v83", "v84", "v85", "v86", "v87", "v88", "v89", "v90", "v91", "v92", "v93", "v94", "v95", "v96", "v97", "v98", "v99", "v100", "v101", "v102", "v103", "v104", "v105", "v106", "v107", "v108", "v109", "v110", "v111", "v112", "v113", "v114", "v115", "v116", "v117", "v118", "v119", "v120", "v121", "v122", "v123", "v124", "v125", "v126", "v127", "v128", "v129", "v130", "v131", "v132", "v133", "v134", "v135", "v136", "v137", "v138", "v139", "v140", "v141", "v142", "v143", "v144", "v145", "v146", "v147", "v148", "v149", "v150", "v151", "v152", "v153", "v154", "v155", "v156", "v157", "v158", "v159", "v160", "v161", "v162", "v163", "v164", "v165", "v166", "v167", "v168", "v169", "v170", "v171", "v172", "v173", "v174", "v175", "v176", "v177", "v178", "v179", "v180", "v181", "v182", "v183", "v184", "v185", "v186", "v187", "v188", "v189", "v190", "v191", "v192", "v193", "v194", "v195", "v196", "v197", "v198", "v199", "v200", "v201", "v202", "v203", "v204", "v205", "v206", "v207", "v208", "v209", "v210", "v211", "v212", "v213", "v214", "v215", "v216", "v217", "v218", "v219", "v220", "v221", "v222", "v223", "v224", "v225", "v226", "v227", "v228", "v229", "v230", "v231", "v232", "v233", "v234", "v235", "v236", "v237", "v238", "v239", "v240", "v241", "v242", "v243", "v244", "v245", "v246", "v247", "v248", "v249", "v250", "v251", "v252", "v253", "v254", "v255", "a0", "a1", "a2", "a3", "a4", "a5", "a6", "a7", "a8", "a9", "a10", "a11", "a12", "a13", "a14", "a15", "a16", "a17", "a18", "a19", "a20", "a21", "a22", "a23", "a24", "a25", "a26", "a27", "a28", "a29", "a30", "a31", "a32", "a33", "a34", "a35", "a36", "a37", "a38", "a39", "a40", "a41", "a42", "a43", "a44", "a45", "a46", "a47", "a48", "a49", "a50", "a51", "a52", "a53", "a54", "a55", "a56", "a57", "a58", "a59", "a60", "a61", "a62", "a63", "a64", "a65", "a66", "a67", "a68", "a69", "a70", "a71", "a72", "a73", "a74", "a75", "a76", "a77", "a78", "a79", "a80", "a81", "a82", "a83", "a84", "a85", "a86", "a87", "a88", "a89", "a90", "a91", "a92", "a93", "a94", "a95", "a96", "a97", "a98", "a99", "a100", "a101", "a102", "a103", "a104", "a105", "a106", "a107", "a108", "a109", "a110", "a111", "a112", "a113", "a114", "a115", "a116", "a117", "a118", "a119", "a120", "a121", "a122", "a123", "a124", "a125", "a126", "a127", "a128", "a129", "a130", "a131", "a132", "a133", "a134", "a135", "a136", "a137", "a138", "a139", "a140", "a141", "a142", "a143", "a144", "a145", "a146", "a147", "a148", "a149", "a150", "a151", "a152", "a153", "a154", "a155", "a156", "a157", "a158", "a159", "a160", "a161", "a162", "a163", "a164", "a165", "a166", "a167", "a168", "a169", "a170", "a171", "a172", "a173", "a174", "a175", "a176", "a177", "a178", "a179", "a180", "a181", "a182", "a183", "a184", "a185", "a186", "a187", "a188", "a189", "a190", "a191", "a192", "a193", "a194", "a195", "a196", "a197", "a198", "a199", "a200", "a201", "a202", "a203", "a204", "a205", "a206", "a207", "a208", "a209", "a210", "a211", "a212", "a213", "a214", "a215", "a216", "a217", "a218", "a219", "a220", "a221", "a222", "a223", "a224", "a225", "a226", "a227", "a228", "a229", "a230", "a231", "a232", "a233", "a234", "a235", "a236", "a237", "a238", "a239", "a240", "a241", "a242", "a243", "a244", "a245", "a246", "a247", "a248", "a249", "a250", "a251", "a252", "a253", "a254", "a255", "s36", "s37", "s38", "s39", "s40", "s41", "s42", "s43", "s44", "s45", "s46", "s47", "s48", "s49", "s50", "s51", "s52", "s53", "s54", "s55", "s56", "s57", "s58", "s59", "s60", "s61", "s62", "s63", "s64", "s65", "s66", "s67", "s68", "s69", "s70", "s71", "s72", "s73", "s74", "s75", "s76", "s77", "s78", "s79", "s80", "s81", "s82", "s83", "s84", "s85", "s86", "s87", "s88", "s89", "s90", "s91", "s92", "s93", "s94", "s95", "s96", "s97", "s98", "s99", "vcc", "scc", "m0", "memory");
}

extern "C" void kernel_launch(void* const* d_in, const int* in_sizes, int n_in, void* d_out, int out_size, void* d_ws, size_t ws_size,
                              hipStream_t stream) {
  hipLaunchKernelGGL(attn_fused, dim3(256), dim3(256), 0, stream, (const char*)d_in[0], (const char*)d_in[1], (const char*)d_in[2],
                     (const char*)d_in[3], (const char*)d_in[4], (const char*)d_in[5], (const char*)d_in[6], (char*)d_ws, (char*)d_out);
}
```
